```python
import torch

def _softmax(x, axis=-1):
    return torch.softmax(x, dim=axis)
def _gelu(x):
    return torch.nn.functional.gelu(x, approximate="tanh")
def _silu(x):
    return torch.nn.functional.silu(x)
def _sigmoid(x):
    return torch.sigmoid(x)
def _layernorm(x, axis=-1, eps=1e-5):
    m = x.mean(dim=axis, keepdim=True)
    v = x.var(dim=axis, keepdim=True, unbiased=False)
    return (x - m) / torch.sqrt(v + eps)
def _scatter_add(tgt, idx, src, axis=0):
    return tgt.clone().index_add_(axis, idx, src)
def _segment_sum(src, seg, n):
    out = torch.zeros((n,) + src.shape[1:], dtype=src.dtype)
    return out.index_add_(0, seg, src)
def _pad(x, pw):
    flat = tuple(v for pair in reversed(pw) for v in pair)
    return torch.nn.functional.pad(x, flat)

kernel_name = 'gated_mix__linear_kernel_hedgehog__moe_arch_mod__prompt'

def setup_inputs():

    _w = (lambda a: (lambda t: t.to(torch.bfloat16) if t.is_floating_point() else t)(torch.as_tensor(a)))
    def _A_setup():
        B, S, D, H = 1, 2048, 1024, 32
        Dh = D // H
        return {
            "x": _w((lambda *s: torch.randn(*s, dtype=torch.bfloat16) * 0.05)(B, S, D)),
            "wqkv": _w((lambda *s: torch.randn(*s, dtype=torch.bfloat16) * 1.6)(D, 3 * D)),
            "w_phi": _w((lambda *s: torch.randn(*s, dtype=torch.bfloat16) * 1.6)(H, Dh, Dh) * (Dh ** -0.5)),
            "wo": _w((lambda *s: torch.randn(*s, dtype=torch.bfloat16) * 1.6)(D, D)),
        }
    _A_in = _A_setup()

    def _B_setup():
        B, S, D, F, C = 1, 2048, 1024, 3584, 1
        return {
            "x": _w((lambda *s: torch.randn(*s, dtype=torch.bfloat16) * 0.05)(B, S, D)),
            "w_r": _w((lambda *s: torch.randn(*s, dtype=torch.bfloat16) * 1.6)(D)),
            "w_gate": _w((lambda *s: torch.randn(*s, dtype=torch.bfloat16) * 0.8)(D, F)),
            "w_up": _w((lambda *s: torch.randn(*s, dtype=torch.bfloat16) * 0.8)(D, F)),
            "w_down": _w((lambda *s: torch.randn(*s, dtype=torch.bfloat16) * 0.8)(F, D)),
        }
    _B_in = _B_setup()

    return {
        "A_x": _A_in["x"],
        "A_wqkv": _A_in["wqkv"],
        "A_w_phi": _A_in["w_phi"],
        "A_wo": _A_in["wo"],
        "B_x": _B_in["x"],
        "B_w_r": _B_in["w_r"],
        "B_w_gate": _B_in["w_gate"],
        "B_w_up": _B_in["w_up"],
        "B_w_down": _B_in["w_down"],
        "gate_w": _w((lambda *s: torch.randn(*s, dtype=torch.bfloat16) * 0.05)(1024) * 16.0),
    }

def reference(A_x, A_wqkv, A_w_phi, A_wo, B_x, B_w_r, B_w_gate, B_w_up, B_w_down, gate_w):
    def _A_ref(x, wqkv, w_phi, wo):
        B, S, D = x.shape
        H = 32; Dh = D // H
        qkv = (lambda x, *d: x.permute(*d))((x @ wqkv).reshape(B, S, 3, H, Dh), 2, 0, 3, 1, 4)
        q, k, v = qkv[0], qkv[1], qkv[2]
        pq = torch.einsum("bhsd,hdf->bhsf", q, w_phi)
        pk = torch.einsum("bhsd,hdf->bhsf", k, w_phi)
        phi_q = torch.concatenate([_softmax(pq, axis=-1), _softmax(-pq, axis=-1)], axis=-1)
        phi_k = torch.concatenate([_softmax(pk, axis=-1), _softmax(-pk, axis=-1)], axis=-1)
        kv = torch.einsum("bhsf,bhsd->bhfd", phi_k, v)
        z  = torch.einsum("bhsf->bhf", phi_k)
        num = torch.einsum("bhqf,bhfd->bhqd", phi_q, kv)
        den = torch.einsum("bhqf,bhf->bhq", phi_q, z)[..., None] + 1e-6
        o = num / den
        return (lambda x, *d: x.permute(*d))(o, 0, 2, 1, 3).reshape(B, S, D) @ wo

    def _B_ref(x, w_r, w_gate, w_up, w_down):
        B, S, D = x.shape
        C = 1
        r = x @ w_r
        idx = torch.argsort(-r, axis=-1)[:, :C]
        idx_e = (lambda x, n, axis: torch.repeat_interleave(x, n, dim=axis))(idx.reshape(B, C, 1), D, axis=2)
        x_sel = (lambda x, i, axis: torch.take_along_dim(x, i, dim=axis))(x, idx_e, axis=1)
        g = _silu(x_sel @ w_gate)
        u = x_sel @ w_up
        y = (g * u) @ w_down
        r_sel = _sigmoid((lambda x, i, axis: torch.take_along_dim(x, i, dim=axis))(r, idx, axis=1))
        y = y * r_sel.reshape(B, C, 1)
        b_off = (torch.arange(B) * S).reshape(B, 1)
        flat_idx = (idx + b_off).reshape(B * C)
        zero = (x * 0.0).reshape(B * S, D)
        upd = _scatter_add(zero, flat_idx, y.reshape(B * C, D), axis=0)
        return x + upd.reshape(B, S, D)

    x = A_x
    g = _sigmoid((x * gate_w).sum(-1, keepdims=True))
    a = _A_ref(x, A_wqkv, A_w_phi, A_wo)
    b = _B_ref(x, B_w_r, B_w_gate, B_w_up, B_w_down)
    return g * a + (1 - g) * b
```

```cpp
#include <hip/hip_runtime.h>
#include <hip/hip_bf16.h>
#include <stdint.h>
#include <type_traits>

typedef __attribute__((ext_vector_type(8))) short bf16x8;
typedef __attribute__((ext_vector_type(4))) short s16x4;
typedef __attribute__((ext_vector_type(4))) float f32x4;
typedef __attribute__((ext_vector_type(2))) float f32x2;
typedef __attribute__((ext_vector_type(2))) unsigned u32x2;
typedef __attribute__((ext_vector_type(4))) unsigned u32x4;
typedef __attribute__((ext_vector_type(2))) __bf16 bf16x2_t;
typedef unsigned short u16;
typedef unsigned long long u64;

#define DI __device__ __forceinline__
#define AS3 __attribute__((address_space(3)))
#define AS1 __attribute__((address_space(1)))
#define RLX_AGENT __ATOMIC_RELAXED, __HIP_MEMORY_SCOPE_AGENT

constexpr int SEQ = 2048, DM = 1024, NH = 32, FF = 3584;

DI float bf2f(u16 v) { return __uint_as_float(((unsigned)v) << 16); }
DI u16 f2bf(float f) { __bf16 b = (__bf16)f; return __builtin_bit_cast(u16, b); }
DI float rbf(float f) { return bf2f(f2bf(f)); }
DI unsigned pack2(float lo, float hi) {
    const f32x2 v = {lo, hi};
    return __builtin_bit_cast(unsigned, __builtin_convertvector(v, bf16x2_t));
}
DI float lo16(unsigned u) { return __uint_as_float(u << 16); }
DI float hi16(unsigned u) { return __uint_as_float(u & 0xffff0000u); }

DI s16x4 tr_read(unsigned a) { return __builtin_amdgcn_ds_read_tr16_b64_v4i16((AS3 s16x4*)(size_t)a); }
DI bf16x8 lds_read128(unsigned a) { return *(AS3 const bf16x8*)(size_t)a; }
template <class T> DI void lds_write(unsigned a, T v) { *(AS3 T*)(size_t)a = v; }
template <class T> DI T lds_read(unsigned a) { return *(AS3 const T*)(size_t)a; }
DI bf16x8 cat8(s16x4 a, s16x4 b) { return __builtin_shufflevector(a, b, 0, 1, 2, 3, 4, 5, 6, 7); }
DI f32x4 mfma16(bf16x8 a, bf16x8 b, f32x4 c) { return __builtin_amdgcn_mfma_f32_16x16x32_bf16(a, b, c, 0, 0, 0); }
DI bf16x8 pack44(const f32x4& a, const f32x4& b) {
    u32x4 r;
    r[0] = pack2(a[0], a[1]);
    r[1] = pack2(a[2], a[3]);
    r[2] = pack2(b[0], b[1]);
    r[3] = pack2(b[2], b[3]);
    return __builtin_bit_cast(bf16x8, r);
}
DI unsigned lds_of(const void* p) { return (unsigned)(size_t)(AS3 const char*)p; }

template <int CTRL>
DI unsigned dpp(unsigned x) { return __builtin_amdgcn_update_dpp(0u, x, CTRL, 0xf, 0xf, true); }
template <int CTRL>
DI float dpp_add(float x) { return x + __uint_as_float(dpp<CTRL>(__float_as_uint(x))); }
DI float swap32_add(float a, float b) {
    auto r = __builtin_amdgcn_permlane32_swap(__float_as_uint(a), __float_as_uint(b), false, false);
    return __uint_as_float(r[0]) + __uint_as_float(r[1]);
}
DI float swap16_add(float a, float b) {
    auto r = __builtin_amdgcn_permlane16_swap(__float_as_uint(a), __float_as_uint(b), false, false);
    return __uint_as_float(r[0]) + __uint_as_float(r[1]);
}
DI float wave_sum(float x) {
    x = dpp_add<0xB1>(x);
    x = dpp_add<0x4E>(x);
    x = dpp_add<0x141>(x);
    x = dpp_add<0x140>(x);
    x = swap16_add(x, x);
    return swap32_add(x, x);
}
DI unsigned wave_umax(unsigned x) {
    x = max(x, dpp<0xB1>(x));
    x = max(x, dpp<0x4E>(x));
    x = max(x, dpp<0x141>(x));
    x = max(x, dpp<0x140>(x));
    auto r = __builtin_amdgcn_permlane16_swap(x, x, false, false);
    x = max(r[0], r[1]);
    auto s = __builtin_amdgcn_permlane32_swap(x, x, false, false);
    return max(s[0], s[1]);
}
template <class OP, class POST>
DI void red2(float& a, float& b, OP op, POST post) {
    auto x = __builtin_amdgcn_permlane32_swap(__float_as_uint(a), __float_as_uint(b), false, false);
    const float m = op(__uint_as_float(x[0]), __uint_as_float(x[1]));
    auto y = __builtin_amdgcn_permlane16_swap(__float_as_uint(m), __float_as_uint(m), false, false);
    const float f = post(op(__uint_as_float(y[0]), __uint_as_float(y[1])));
    auto z = __builtin_amdgcn_permlane32_swap(__float_as_uint(f), __float_as_uint(f), false, false);
    a = __uint_as_float(z[0]);
    b = __uint_as_float(z[1]);
}

#define RS(i) do { if ((i) == 0 && w < 4) { __builtin_amdgcn_sched_barrier(0); tpad = __builtin_amdgcn_s_memrealtime(); __builtin_amdgcn_s_waitcnt(0xC07F); __builtin_amdgcn_sched_barrier(0); } } while (0)
#define RSD(i) do {} while (0)
#define RSDUMP() do { if (w < 4) while (__builtin_amdgcn_s_memrealtime() < tpad + 6000) __builtin_amdgcn_s_sleep(1); } while (0)

constexpr unsigned WS_E = 0;
constexpr unsigned WS_RG = 256;
constexpr unsigned WS_GG = WS_RG + 2048;
constexpr unsigned WS_Z = 65536;
constexpr unsigned WS_KF = WS_Z + 131072;
constexpr unsigned WS_GU = WS_KF + 262144;
constexpr unsigned WS_Y = WS_GU + 262144;
constexpr unsigned WS_KV = WS_Y + 262144;
constexpr unsigned WS_O = WS_KV + (4u << 20);
constexpr unsigned WS_END = WS_O + (4u << 20);

constexpr unsigned SA = 40960;
constexpr unsigned L_PHI = 98304;
constexpr unsigned L_VX = 131072;
constexpr unsigned L_HS = L_VX + 16384;
constexpr unsigned L_H = L_HS + 512, L_WPHI = L_HS + 1024, L_QL = L_HS + 2048;
constexpr unsigned L_SM = L_HS;
constexpr unsigned L_FULL = L_SM;
constexpr unsigned L_FWD = L_SM + 448, L_FWO = L_SM + 464, L_FKF = L_SM + 272, L_PROG = L_SM + 288, L_FPK = L_SM + 304, L_FQ = L_SM + 320;
constexpr unsigned L_FG = L_SM + 496;
constexpr unsigned L_BEST = L_SM + 336, L_SEL = L_SM + 352, L_FP = L_SM + 384, L_FKS = L_SM + 416;
constexpr unsigned wo_pos(int j) { return 4096 * (j < 14 ? j + 10 : j < 24 ? j - 14 : j); }

constexpr unsigned SPIN_CAP = 1u << 16;
__device__ const unsigned k_one[4] = {1u, 1u, 1u, 1u};

#define RSL(i) do {} while (0)
#define RSC(i) do {} while (0)
#define FB0 6
#define FBD 0
#define FBN 6
#define NPH 4
#define KSK 1
#define CLRN 1
#define GDL 0
#define ROTK 8
#define OWIN 3
#define NA1 2
#define NQ1 2
#define CLR0 1

#define SLK 2
#define SLO 0
#define FBM 2
#define CPH 2
#define NPL 8
#define WOE 2
#define GORD 1
#define BAR() asm volatile("s_waitcnt lgkmcnt(0)\n\ts_barrier" ::: "memory")
template <int N>
DI void waitv() { asm volatile("s_waitcnt vmcnt(%0)" ::"n"(N) : "memory"); }

DI u64 ld_agent(const char* p) { return __hip_atomic_load((const AS1 u64*)p, RLX_AGENT); }
DI void st_agent(char* p, u64 v) { __hip_atomic_store((AS1 u64*)p, v, RLX_AGENT); }
DI unsigned nz1(unsigned v) { return max(v, 1u); }
DI unsigned umin(unsigned a, unsigned b) { return min(a, b); }
DI u32x4 nz4(u32x4 v) { return (u32x4){max(v[0], 1u), max(v[1], 1u), max(v[2], 1u), max(v[3], 1u)}; }
DI u32x4 nz4(f32x4 v) { return nz4(__builtin_bit_cast(u32x4, v)); }
DI unsigned min4(u32x4 v) { return min(min(v[0], v[1]), min(v[2], v[3])); }
DI u32x4 ld_wt(__amdgpu_buffer_rsrc_t r, unsigned off) { return __builtin_amdgcn_raw_buffer_load_b128(r, off, 0, 16); }
DI void st_wt(__amdgpu_buffer_rsrc_t r, unsigned off, u32x4 v) { __builtin_amdgcn_raw_buffer_store_b128(v, r, off, 0, 16); }
DI void ld_async(u32x4& v, unsigned off, const char* base) {
    asm volatile("global_load_dwordx4 %0, %1, %2 sc1" : "=&v"(v) : "v"(off), "s"(base) : "memory");
}
#define ST_MASK(op, m, off, v, base) asm volatile("s_mov_b64 exec, %3\n\t" op " %0, %1, %2 sc1\n\ts_mov_b64 exec, -1\n\ts_nop 0" ::"v"(off), "v"(v), "s"(base), "s"((u64)(m)) : "memory")
DI void pin(u32x4& v) { asm volatile("" : "+v"(v)); }
template <int N, class OFF, class BASE, class OK>
DI void settle(u32x4 (&v)[N], OFF off, BASE base, OK ok) {
#pragma unroll
    for (int i = 0; i < N; ++i) pin(v[i]);
#pragma unroll 1
    for (unsigned s = 0; s < SPIN_CAP; ++s) {
        unsigned m = ok(v[0]);
#pragma unroll
        for (int i = 1; i < N; ++i) m = min(m, ok(v[i]));
        if (__ballot(m == 0u) == 0ull) break;
#pragma unroll
        for (int i = 0; i < N; ++i) {
            u64 keep;
            asm volatile("s_mov_b64 %1, exec\n\ts_mov_b64 exec, %4\n\tglobal_load_dwordx4 %0, %2, %3 sc1\n\ts_mov_b64 exec, %1"
                         : "+&v"(v[i]), "=&s"(keep) : "v"(off(i)), "s"(base(i)), "s"((u64)__ballot(ok(v[i]) == 0u)) : "memory");
        }
        waitv<0>();
#pragma unroll
        for (int i = 0; i < N; ++i) pin(v[i]);
    }
}
template <int N, class OFF, class BASE>
DI void settle(u32x4 (&v)[N], OFF off, BASE base) { settle(v, off, base, [](const u32x4& a) { return min4(a); }); }
DI u32x4 tag4(f32x4 v, const u32x4& t) {
    u32x4 r;
#pragma unroll
    for (int i = 0; i < 4; ++i) r[i] = ((__float_as_uint(v[i]) + 0x200u) & ~0x3FFu) | t[i];
    return r;
}
DI f32x4 untag4(const u32x4& v) { return __builtin_bit_cast(f32x4, v & ~0x3FFu); }
DI void lds_wait(unsigned a, unsigned n) {
    unsigned v, c;
    asm volatile("s_mov_b32 %1, 0x400000\n"
                 ".Lp%=:\n\tds_read_b32 %0, %2\n\ts_waitcnt lgkmcnt(0)\n\tv_cmp_gt_u32 vcc, %3, %0\n\ts_cbranch_vccz .Lf%=\n\t"
                 "s_sleep 1\n\ts_sub_u32 %1, %1, 1\n\ts_cbranch_scc0 .Lp%=\n.Lf%=:"
                 : "=&v"(v), "=&s"(c) : "v"(a), "s"(n) : "memory", "vcc", "scc");
}
DI void lds_peek(unsigned& v, unsigned a) { asm volatile("ds_read_b32 %0, %1" : "=v"(v) : "v"(a) : "memory"); }
DI void lds_wait(unsigned& v, unsigned a, unsigned n) {
    unsigned c;
    asm volatile("s_mov_b32 %1, 0x400000\n"
                 ".Lp%=:\n\ts_waitcnt lgkmcnt(0)\n\tv_cmp_gt_u32 vcc, %3, %0\n\ts_cbranch_vccz .Lf%=\n\t"
                 "ds_read_b32 %0, %2\n\ts_sub_u32 %1, %1, 1\n\ts_cbranch_scc0 .Lp%=\n.Lf%=:"
                 : "+v"(v), "=&s"(c) : "v"(a), "s"(n) : "memory", "vcc", "scc");
}
DI void lds_set(unsigned a) {
    asm volatile("s_waitcnt lgkmcnt(0)\n\tds_write_b32 %0, %1" ::"v"(a), "v"(1u) : "memory");
}
DI float dot2(unsigned a, unsigned b, float c) {
    return __builtin_amdgcn_fdot2_f32_bf16(__builtin_bit_cast(bf16x2_t, a), __builtin_bit_cast(bf16x2_t, b), c, false);
}
DI float sum4(const float (&a)[4]) {
    float c = swap16_add(swap32_add(a[0], a[1]), swap32_add(a[2], a[3]));
    c = dpp_add<0xB1>(c);
    c = dpp_add<0x4E>(c);
    c = dpp_add<0x141>(c);
    return dpp_add<0x140>(c);
}

DI float max3r(float a, float b, float c) { float r; asm("v_max3_f32 %0, %1, %2, %3" : "=v"(r) : "v"(a), "v"(b), "v"(c)); return r; }
DI float min3r(float a, float b, float c) { float r; asm("v_min3_f32 %0, %1, %2, %3" : "=v"(r) : "v"(a), "v"(b), "v"(c)); return r; }
DI float max2r(float a, float b) { float r; asm("v_max_f32 %0, %1, %2" : "=v"(r) : "v"(a), "v"(b)); return r; }
DI float min2r(float a, float b) { float r; asm("v_min_f32 %0, %1, %2" : "=v"(r) : "v"(a), "v"(b)); return r; }
template <class HOOK>
DI void softmax2(const u32x4 (&pp)[2], u32x4 (&ph)[2][2], HOOK hook) {
    constexpr float L2E = 1.4426950408889634f;
    f32x2 v[2][4];
    float mx[2], mn[2];
#pragma unroll
    for (int b = 0; b < 2; ++b) {
#pragma unroll
        for (int e = 0; e < 4; ++e) v[b][e] = (f32x2){lo16(pp[b][e]), hi16(pp[b][e])};
        mx[b] = max2r(v[b][0][0], v[b][0][1]);
        mn[b] = min2r(v[b][0][0], v[b][0][1]);
#pragma unroll
        for (int e = 1; e < 4; ++e) {
            mx[b] = max3r(mx[b], v[b][e][0], v[b][e][1]);
            mn[b] = min3r(mn[b], v[b][e][0], v[b][e][1]);
        }
    }
    hook(0);
    red2(mx[0], mx[1], [](float a, float b) { return max2r(a, b); }, [](float a) { return a * -L2E; });
    red2(mn[0], mn[1], [](float a, float b) { return min2r(a, b); }, [](float a) { return a * L2E; });
    asm volatile("" : "+v"(mx[0]), "+v"(mx[1]), "+v"(mn[0]), "+v"(mn[1]));
    f32x2 ep[2][4], en[2][4];
    float sp_[2], sn_[2];
#pragma unroll
    for (int b = 0; b < 2; ++b) {
        hook(1 + b);
#pragma unroll
        for (int e = 0; e < 4; ++e) {
            const f32x2 a = __builtin_elementwise_fma(v[b][e], (f32x2){L2E, L2E}, (f32x2){mx[b], mx[b]});
            const f32x2 c = __builtin_elementwise_fma(v[b][e], (f32x2){-L2E, -L2E}, (f32x2){mn[b], mn[b]});
            ep[b][e] = (f32x2){__builtin_amdgcn_exp2f(a[0]), __builtin_amdgcn_exp2f(a[1])};
            en[b][e] = (f32x2){__builtin_amdgcn_exp2f(c[0]), __builtin_amdgcn_exp2f(c[1])};
        }
        const f32x2 s = (ep[b][0] + ep[b][1]) + (ep[b][2] + ep[b][3]), t = (en[b][0] + en[b][1]) + (en[b][2] + en[b][3]);
        sp_[b] = s[0] + s[1];
        sn_[b] = t[0] + t[1];
        asm volatile("" : "+v"(sp_[b]), "+v"(sn_[b]));
    }
    hook(3);
    red2(sp_[0], sp_[1], [](float a, float b) { return a + b; }, [](float a) { return __builtin_amdgcn_rcpf(a); });
    red2(sn_[0], sn_[1], [](float a, float b) { return a + b; }, [](float a) { return __builtin_amdgcn_rcpf(a); });
#pragma unroll
    for (int b = 0; b < 2; ++b)
#pragma unroll
        for (int e = 0; e < 4; ++e) {
            const f32x2 a = ep[b][e] * (f32x2){sp_[b], sp_[b]}, c = en[b][e] * (f32x2){sn_[b], sn_[b]};
            ph[b][0][e] = pack2(a[0], a[1]);
            ph[b][1][e] = pack2(c[0], c[1]);
        }
}
DI int fsw(int row) { return (((row >> 1) & 3) << 2) | (((row >> 3) & 1) << 1) | (row & 1); }
template <class HOOK>
DI void phi_k(const u32x4 (&pp)[2], unsigned phi, int sp, int l, HOOK hook) {
    const int g = l >> 4, l15 = l & 15;
    const unsigned pw = phi + l15 * 128 + ((g ^ fsw(l15)) << 3);
    u32x4 ph[2][2];
    softmax2(pp, ph, hook);
#pragma unroll
    for (int b = 0; b < 2; ++b)
#pragma unroll
        for (int kf = 0; kf < 2; ++kf)
#pragma unroll
            for (int fb = 0; fb < 2; ++fb) {
                const u32x2 a = {ph[b][kf][2 * fb], ph[b][kf][2 * fb + 1]};
                lds_write<u32x2>((pw ^ (fb << 5) ^ (kf << 6)) + (2 * sp + b) * 2048, a);
            }
}

DI void waitvn(int n) {
#define WV(k) case k: waitv<k>(); break;
#define WV8(k) WV(k) WV(k + 1) WV(k + 2) WV(k + 3) WV(k + 4) WV(k + 5) WV(k + 6) WV(k + 7)
    switch (n < 63 ? n : 63) { WV8(0) WV8(8) WV8(16) WV8(24) WV8(32) WV8(40) WV8(48) WV8(56) }
}
#define SB0() __builtin_amdgcn_sched_barrier(0)
template <int A, int B, class F>
DI void sfor(F f) {
    if constexpr (A < B) {
        f(std::integral_constant<int, A>{});
        sfor<A + 1, B>(f);
    }
}
#define AI __attribute__((always_inline))

DI unsigned kf_off(int i, int l) { return i < 4 ? (l & 4) * 128 + (l >> 3) * 64 + (l & 3) * 16 : (l >> 4) * 16; }
DI const char* kf_adr(int i, const char* kb) { return i < 4 ? kb + i * 1024 : kb + 4096 + (i - 4) * 64; }
DI void kf_load(u32x4 (&kf)[6], const char* kb, int l) {
#pragma unroll
    for (int i = 0; i < 6; ++i) ld_async(kf[i], kf_off(i, l), kf_adr(i, kb));
}
DI void kf_fix(u32x4 (&kf)[6], const char* kb, int l) {
    settle(kf, [&](int i) { return kf_off(i, l); }, [&](int i) { return kf_adr(i, kb); });
#pragma unroll
    for (int i = 0; i < 4; ++i) {
        const u32x4 v = kf[i];
        kf[i][0] = __builtin_amdgcn_update_dpp(v[0], v[2], 0x114, 0xf, 0xa, false);
        kf[i][1] = __builtin_amdgcn_update_dpp(v[1], v[3], 0x114, 0xf, 0xa, false);
        kf[i][2] = __builtin_amdgcn_update_dpp(v[2], v[0], 0x104, 0xf, 0x5, false);
        kf[i][3] = __builtin_amdgcn_update_dpp(v[3], v[1], 0x104, 0xf, 0x5, false);
    }
}
DI void o_store(const u32x4 (&kf)[6], const u32x4 (&ph)[2][2], __amdgpu_buffer_rsrc_t rsrc, unsigned o_st) {
    const f32x4 zero = {0.f, 0.f, 0.f, 0.f};
#pragma unroll
    for (int i = 0; i < 2; ++i) {
        const bf16x8 p0 = __builtin_bit_cast(bf16x8, ph[i][0]), p1 = __builtin_bit_cast(bf16x8, ph[i][1]);
        f32x4 dn = mfma16(__builtin_bit_cast(bf16x8, kf[4]), p0, zero);
        f32x4 n0 = mfma16(__builtin_bit_cast(bf16x8, kf[0]), p0, zero);
        f32x4 n1 = mfma16(__builtin_bit_cast(bf16x8, kf[1]), p0, zero);
        dn = mfma16(__builtin_bit_cast(bf16x8, kf[5]), p1, dn);
        n0 = mfma16(__builtin_bit_cast(bf16x8, kf[2]), p1, n0);
        n1 = mfma16(__builtin_bit_cast(bf16x8, kf[3]), p1, n1);
        const float rd_ = __builtin_amdgcn_rcpf(rbf(rbf(dn[0]) + 1e-6f));
        const u32x4 nb_ = __builtin_bit_cast(u32x4, pack44(n0, n1));
        u32x4 o;
#pragma unroll
        for (int j = 0; j < 4; ++j) o[j] = pack2(lo16(nb_[j]) * rd_, hi16(nb_[j]) * rd_);
        st_wt(rsrc, o_st + i * 1024, nz4(o));
    }
}

__global__ __launch_bounds__(512, 2) void fused_mix(const u16* __restrict__ x, const u16* __restrict__ wqkv,
                                                    const u16* __restrict__ wphi, const u16* __restrict__ wo,
                                                    const u16* __restrict__ wr, const u16* __restrict__ wg,
                                                    const u16* __restrict__ wu, const u16* __restrict__ wd,
                                                    const u16* __restrict__ gw, char* __restrict__ ws,
                                                    u16* __restrict__ out) {
    __shared__ __attribute__((aligned(16))) char smem[163840];
    const int tid = threadIdx.x, l = tid & 63;
    const int w = __builtin_amdgcn_readfirstlane(tid >> 6) ^ 4;
    const int bid = blockIdx.x, xcd = bid & 7, slot = bid >> 3;
    const int mt = (xcd & 1) * 8 + (slot & 7);
    const int hp = (xcd >> 1) * 4 + (slot >> 3);
    const int rt = (xcd >> 1) * 4 + (slot & 3), cb = (xcd & 1) * 8 + (slot >> 2), rot = 4 * cb + ROTK * rt;
    const unsigned lds0 = lds_of(smem);
    const __amdgpu_buffer_rsrc_t rsrc = __builtin_amdgcn_make_buffer_rsrc(ws, 0, WS_END, 0x00020000);
    const int g = l >> 4, l15 = l & 15, q = l15 >> 2, p = l & 3;
    const int srow = l >> 3, sch = l & 7;
    const unsigned l16 = l * 16;
    unsigned vst = 0; u64 tpad = 0;
    const u32x4 z4 = {0u, 0u, 0u, 0u};
    const f32x4 zero = {0.f, 0.f, 0.f, 0.f};
    const int rg = w & 3, kp = w >> 2;
    const int hs = rg >> 1;
    const unsigned VX = lds0 + L_VX + l16;
    const unsigned QL = lds0 + L_QL + rg * 4096 + l16;
    const unsigned PROG = lds0 + L_PROG + p * 4, F2 = lds0 + (l & 1) * 4;
    auto prog_wait = [&](unsigned n) { lds_wait(PROG, lds0 + L_FULL + 8 * n); };
    const unsigned o_st = WS_O + ((mt * 2 + (rg & 1)) * 32 + 2 * hp + hs) * 4096 + l16;
    const char* kfb = ws + WS_KF + (hp * 2 + hs) * 8192;
    const int e4 = (rg & 1) * 2 + kp;
    const unsigned kf_st = WS_KF + (hp * 2 + (mt >> 3)) * 8192;
    const unsigned kf_kv = kf_st + (((mt >> 2) & 1) * 2 + (mt & 1)) * 1024 + ((mt >> 1) & 1) * 512;
    const unsigned kf_z = kf_st + 4096 + ((mt >> 2) & 1) * 64 + p * 16 + ((mt >> 1) & 1) * 8;
    const unsigned kv_st = WS_KV + ((hp * 16 + hs * 8) * 16 + mt) * 1024 + l16;
    const unsigned z_st = WS_Z + ((hp * 8 + 4 * hs) * 16 + mt) * 64 + g * 16;
    const int sc = (3 - (int)((unsigned)(size_t)wqkv >> 7)) & 7;
    const bool ffn = (hp & 7) != sc;
    const int ph4 = (slot + (slot >> 3)) & 3;
    const int kq = mt >> 2;
    const int cs = (ffn ? hp - (hp > sc) - (hp > sc + 8) : 14 + (hp >> 3)) * 4 + ((ph4 + CPH - (int)((unsigned)(size_t)wg >> 7)) & 3);
    const unsigned y_st = WS_Y + cs * 4096 + kq * 1024 + hs * 512;
    u32x4 pq[2];
    u32x4 fd[16], gu[2];
    f32x4 aq[2][4];
    bf16x8 aphi[2], qx[2][4], qw[2][2];
    unsigned vx[4], sel_[4][2];
    auto four = [&](const unsigned (&v)[4], const char* src, unsigned dst) AI {
        asm volatile("s_mov_b32 m0, %5\n\ts_nop 0\n\t"
                     "global_load_lds_dwordx4 %0, %4\n\tglobal_load_lds_dwordx4 %1, %4 offset:1024\n\t"
                     "global_load_lds_dwordx4 %2, %4 offset:2048\n\tglobal_load_lds_dwordx4 %3, %4 offset:3072"
                     ::"v"(v[0]), "v"(v[1]), "v"(v[2]), "v"(v[3]), "s"(src), "s"(dst) : "memory");
    };
    auto mark = [&](unsigned dst) AI {
        asm volatile("s_mov_b32 m0, %2\n\ts_mov_b64 exec, 1\n\tglobal_load_lds_dword %0, %1\n\ts_mov_b64 exec, -1"
                     ::"v"(0u), "s"(k_one), "s"(dst) : "memory");
    };
    const char* wob = (const char*)wo + cb * 128 - 4096;
    auto wo_load = [&](int j0, int j1, int dp) AI {
#pragma unroll 1
        for (int j = j0 + hs; j < j1; j += 2) four(vx, wob + ((j + rot) & 31) * 65536, lds0 + (j + dp) * 4096);
    };
    auto spread = [&](const u32x4& v, int i) -> bf16x8 {
        const unsigned s = v[i >> 1];
        u32x4 a;
#pragma unroll
        for (int d = 0; d < 4; ++d) a[d] = __builtin_amdgcn_perm(s, s, sel_[d][i & 1]);
        return __builtin_bit_cast(bf16x8, a);
    };
    auto fold_store = [&](const f32x4& a, unsigned off) {
        f32x4 v;
#pragma unroll
        for (int i = 0; i < 4; ++i) {
            const unsigned u = __float_as_uint(a[i]);
            const float r = __uint_as_float(__builtin_amdgcn_update_dpp(u, u, 0x128, 0xc, 0xf, false));
            v[i] = swap32_add(r, r);
        }
        const u32x4 o = nz4(v);
        ST_MASK("global_store_dwordx4", 0x00FF00FFull, off + l15 * 32 + g * 16, o, ws);
    };
    auto logits = [&](const f32x4& d0, const f32x4& d1) -> u32x4 {
        const bf16x8 b = pack44(d0, d1);
        return __builtin_bit_cast(u32x4, pack44(mfma16(aphi[0], b, zero), mfma16(aphi[1], b, zero)));
    };
    const bool loader = (w & 5) == 4;

    if (__builtin_expect(loader, 1)) {
        RS(0);
        unsigned vw[4];
#pragma unroll
        for (int i = 0; i < 4; ++i) {
            vx[i] = (unsigned)((8 * i + srow) * 2048 + ((sch ^ ((4 * (i & 1) + (srow >> 1)) & 7)) * 16) + 4096 - i * 1024);
            vw[i] = (unsigned)((8 * i + srow) * 6144 + (sch ^ (((srow >> 1) & 1) << 1) ^ ((i & 1) << 2)) * 16 + 4096 - i * 1024);
        }
        const int ks0 = SLK && (hp & 7) == 3 ? SLK * (slot & 7) + SLO : KSK * ((slot + (slot >> 3)) & (NPH - 1));
        const char* xb0 = (const char*)(x + (size_t)(mt * 128 + hs * 64) * DM) - 4096;
        const char* wb0 = (const char*)(wqkv + hp * 64) + hs * (32 * 6144) - 4096;
        const unsigned fl = lds0 + L_FULL + hs * 4;
        unsigned pv, sb = lds0;
#pragma unroll 1
        for (int s = 0; s < 16; ++s) {
            const int ks = (ks0 + s) & 15;
            const char* xs_ = xb0 + ks * 128;
            const char* ws_ = wb0 + ks * (64 * 6144);
            if (s >= 3) lds_wait(pv, PROG, lds0 + L_FULL + 8 * (s - 2));
            four(vw, ws_ + 2048, sb + 16384 + hs * 4096);
            four(vx, xs_, sb + hs * 8192);
            four(vw, ws_ + 4096, sb + 24576 + hs * 4096);
            four(vx, xs_ + 32 * 2048, sb + hs * 8192 + 4096);
            lds_peek(pv, PROG);
            four(vw, ws_, sb + 32768 + hs * 4096);
            if (s == 0) asm volatile("s_barrier" ::: "memory");
            if (s == 2) {
#pragma unroll
                for (int i = 0; i < 2; ++i)
                    asm volatile("s_mov_b32 m0, %2\n\ts_nop 0\n\tglobal_load_lds_dwordx4 %0, %1"
                                 ::"v"((unsigned)((l >> 2) * 64 + (((l & 3) ^ (2 * ((l >> 4) & 1))) * 16))),
                                   "s"(wphi + hp * 2048 + (2 * hs + i) * 512), "s"(lds0 + L_WPHI + (2 * hs + i) * 4096) : "memory");
            }
            mark(fl + s * 8);
            sb = sb == lds0 + 2 * SA ? lds0 : sb + SA;
            RSL(32 + s);
        }
#pragma unroll
        for (int i = 0; i < 4; ++i) vx[i] = (unsigned)((8 * i + srow) * 2048 + ((sch ^ ((g & 3) << 1)) * 16) + 4096 - i * 1024);
        RS(1);
        prog_wait(14);
        RS(2);
        wo_load(0, 10, 10);
        prog_wait(15);
        wo_load(10, 14, 10);
        prog_wait(16);
        wo_load(14, 24, -14);
        RS(3);
    } else if (w >= 4) {
        RS(0);
        const unsigned gu_st = WS_GU + cs * 4096 + kq * 512 + hs * 256;
        const int row0 = (mt * 16 + hp) * 8 + 4 * hs;
        u64 T;
        asm volatile("s_barrier" ::: "memory");
        {
            u32x4 rv[2], gv[2], xv[4][2];
            u64 ev;
            asm volatile("global_load_dwordx2 %4, %5, %9 sc1\n\t"
                         "global_store_dwordx4 %10, %11, %9 sc1\n\t"
                         "global_store_dwordx2 %12, %13, %9 sc1\n\t"
                         "global_load_dwordx4 %0, %6, %7\n\tglobal_load_dwordx4 %1, %6, %7 offset:1024\n\t"
                         "global_load_dwordx4 %2, %6, %8\n\tglobal_load_dwordx4 %3, %6, %8 offset:1024"
                         : "=&v"(rv[0]), "=&v"(rv[1]), "=&v"(gv[0]), "=&v"(gv[1]), "=&v"(ev)
                         : "v"(0u), "v"(l16), "s"(wr), "s"(gw), "s"(ws), "v"(l < 32 ? y_st + l16 : gu_st + l15 * 16), "v"(z4),
                           "v"(hs ? kf_z : kf_kv + l * 8), "v"(0ull)
                         : "memory");
            prog_wait(CLR0);
#pragma unroll
            for (int n = 0; n < 8; ++n) st_wt(rsrc, o_st - (n >> 2) * 131072 + (n & 3) * 1024, z4);
            RS(1);
#pragma unroll
            for (int r = 0; r < 2; ++r)
                asm volatile("global_load_dwordx4 %0, %4, %5\n\tglobal_load_dwordx4 %1, %4, %5 offset:1024\n\t"
                             "global_load_dwordx4 %2, %4, %5 offset:2048\n\tglobal_load_dwordx4 %3, %4, %5 offset:3072"
                             : "=&v"(xv[2 * r][0]), "=&v"(xv[2 * r][1]), "=&v"(xv[2 * r + 1][0]), "=&v"(xv[2 * r + 1][1])
                             : "v"(l16), "s"(x + (size_t)(row0 + 2 * r) * DM) : "memory");
            SB0();
            waitv<0>();
            asm volatile("" : "+v"(rv[0]), "+v"(rv[1]), "+v"(gv[0]), "+v"(gv[1]), "+v"(ev));
#pragma unroll
            for (int r = 0; r < 4; ++r) asm volatile("" : "+v"(xv[r][0]), "+v"(xv[r][1]));
            RS(2);
            T = ((u64)__builtin_amdgcn_readfirstlane((unsigned)(ev >> 32)) << 32 | __builtin_amdgcn_readfirstlane((unsigned)ev)) + 1;
            float ra_[4] = {0.f, 0.f, 0.f, 0.f}, ga_[4] = {0.f, 0.f, 0.f, 0.f};
#pragma unroll
            for (int r = 0; r < 4; ++r)
#pragma unroll
                for (int i = 0; i < 2; ++i)
#pragma unroll
                    for (int j = 0; j < 4; ++j) {
                        ra_[r] = dot2(xv[r][i][j], rv[i][j], ra_[r]);
                        ga_[r] = dot2(xv[r][i][j], gv[i][j], ga_[r]);
                    }
            const int rr = (g >> 1) | ((g & 1) << 1);
            unsigned k = f2bf(sum4(ra_));
            k ^= (k & 0x8000u) ? 0xFFFFu : 0x8000u;
            k = (k << 3) | (7 - 4 * hs - rr);
            auto r1 = __builtin_amdgcn_permlane16_swap(k, k, false, false);
            k = max(r1[0], r1[1]);
            auto r2 = __builtin_amdgcn_permlane32_swap(k, k, false, false);
            k = max(r2[0], r2[1]);
            if (hs) {
                lds_write<unsigned>(lds0 + L_BEST, k | 0x80000000u);
            } else {
                lds_wait(lds0 + L_BEST, 1);
                const u64 rq = (T << 19) | max(k, lds_read<unsigned>(lds0 + L_BEST) & 0x7FFFFu);
                ST_MASK("global_store_dwordx2", 1ull, 0u, rq, ws + WS_RG + (mt * 16 + hp) * 8);
            }
            const u64 gq = (T << 16) | f2bf(1.f / (1.f + __expf(-rbf(sum4(ga_)))));
            ST_MASK("global_store_dwordx2", 0x0001000100010001ull, (unsigned)(rr * 8), gq, ws + WS_GG + row0 * 8);
        }
        RS(3);
        u32x4 fw[32];
        const unsigned fgo = ffn ? (unsigned)((256 * kq + 16 * srow) * 7168 + cs * 128 + sch * 16) : 0u;
        const unsigned fdo = ffn ? (unsigned)((64 * cs + 8 * srow) * 2048 + 512 * kq + 256 * hs + sch * 16) : 0u;
        const char* fsrc = (const char*)(hs ? wu : wg);
        const unsigned fb = FB0 + ph4;
        prog_wait(fb);
        RS(4);
#pragma unroll
        for (int i = 0; i < 32; ++i)
            asm volatile("global_load_dwordx4 %0, %1, %2 nt" : "=&v"(fw[i]) : "v"(fgo), "s"(fsrc + (128 * (i >> 4) + (i & 15)) * 7168) : "memory");
        RS(5);
#pragma unroll
        for (int d = 0; d < 4; ++d)
#pragma unroll
            for (int hf = 0; hf < 2; ++hf) {
                const unsigned b = hf ? 0x0302u : 0x0100u;
                sel_[d][hf] = (sch >> 1) != d ? 0x0c0c0c0cu : (l & 1) ? (b << 16) | 0x0c0cu : 0x0c0c0000u | b;
            }
        {
            u32x4 sv[2], xs[4];
            const unsigned tl = (unsigned)T & 0x1FFFu, thi = (unsigned)(T >> 13);
#pragma unroll 1
            for (unsigned s = 0; s < SPIN_CAP; ++s) {
                asm volatile("global_load_dwordx4 %0, %2, %3 sc1\n\tglobal_load_dwordx4 %1, %2, %3 offset:16 sc1\n\ts_waitcnt vmcnt(0)"
                             : "=&v"(sv[0]), "=&v"(sv[1]) : "v"(l * 32), "s"(ws + WS_RG) : "memory");
                const unsigned bad = ((sv[0][0] >> 19) ^ tl) | (sv[0][1] ^ thi) | ((sv[0][2] >> 19) ^ tl) | (sv[0][3] ^ thi) |
                                     ((sv[1][0] >> 19) ^ tl) | (sv[1][1] ^ thi) | ((sv[1][2] >> 19) ^ tl) | (sv[1][3] ^ thi);
                if (__all(bad == 0u)) break;
                __builtin_amdgcn_s_sleep(8);
            }
            RS(6);
#pragma unroll
            for (int i = 0; i < 32; ++i) pin(fw[i]);
            unsigned k = 0;
#pragma unroll
            for (int j = 0; j < 4; ++j) {
                const unsigned v = sv[j >> 1][(j & 1) * 2] & 0x7FFFFu;
                k = max(k, ((v >> 3) << 11) | ((255 - (4 * l + j)) << 3) | (v & 7));
            }
            const unsigned key = __builtin_amdgcn_readfirstlane(wave_umax(k));
            asm volatile("global_load_dwordx4 %0, %4, %5\n\tglobal_load_dwordx4 %1, %4, %5 offset:16\n\t"
                         "global_load_dwordx4 %2, %4, %5 offset:256\n\tglobal_load_dwordx4 %3, %4, %5 offset:272"
                         : "=&v"(xs[0]), "=&v"(xs[1]), "=&v"(xs[2]), "=&v"(xs[3])
                         : "v"((unsigned)(512 * kq + 32 * srow)), "s"(x + (size_t)(2047 - (int)(key & 2047u)) * DM)
                         : "memory");
            unsigned rb = key >> 11;
            rb ^= (rb & 0x8000u) ? 0x8000u : 0xFFFFu;
            const float rsel = rbf(1.f / (1.f + __expf(-bf2f((u16)rb))));
            lds_write<u32x4>(lds0 + L_SEL, (u32x4){(unsigned)T, (unsigned)(T >> 32), 2047u - (key & 2047u), __float_as_uint(rsel)});
            if (bid == 0 && hs == 0) ST_MASK("global_store_dwordx2", 1ull, 0u, T, ws + WS_E);
            if (FBD) prog_wait(fb + FBD);
#pragma unroll
            for (int i = 0; i < 16; ++i)
                asm volatile("global_load_dwordx4 %0, %1, %2 nt" : "=&v"(fd[i]) : "v"(fdo), "s"((const char*)wd + (i & 7) * 2048 + (i >> 3) * 128) : "memory");
            waitv<16>();
#pragma unroll
            for (int i = 0; i < 4; ++i) pin(xs[i]);
            RS(7);
            f32x4 a = zero;
#pragma unroll
            for (int i = 0; i < 32; ++i) a = mfma16(spread(xs[i >> 3], i & 7), __builtin_bit_cast(bf16x8, fw[i]), a);
            fold_store(a, gu_st);
            SB0();
        }
        RS(8);
    } else {
        RS(0);
        lds_write<u64>(lds0 + L_SM + l * 8, 0ull);
        BAR();
        unsigned off[4], ad[4];
#pragma unroll
        for (int ks = 0; ks < 2; ++ks) off[ks] = lds0 + (64 * (w & 1) + l15) * 128 + (((4 * ks + g) ^ (l15 >> 1)) * 16);
        off[2] = lds0 + 16384 + (8 * g + q) * 128 + (((2 * hs) ^ (q >> 1) ^ ((g & 1) << 1)) * 32) + 8 * p;
        off[3] = off[2] ^ 32;
        const unsigned pa = lds0 + L_PROG + w * 4;
        unsigned fa = F2 + L_FULL, fv = 0;
        f32x4 ak[2][4], av[4][2];
#pragma unroll
        for (int a = 0; a < 2; ++a)
#pragma unroll
            for (int b = 0; b < 4; ++b) aq[a][b] = ak[a][b] = av[b][a] = zero;
        bf16x8 xa[4], wa[6], xb[4], wb[6];
        auto adr = [&](unsigned boff) {
#pragma unroll
            for (int i = 0; i < 4; ++i) ad[i] = off[i] + boff;
            asm volatile("" : "+v"(ad[0]), "+v"(ad[1]), "+v"(ad[2]), "+v"(ad[3]));
        };
        auto rd = [&](bf16x8 (&xf)[4], bf16x8 (&wf)[6], int ks) {
#pragma unroll
            for (int j = 0; j < 6; ++j) {
                const unsigned a = ad[2 + (j & 1)] + ks * 4096 + (j >> 1) * 8192;
                wf[j] = cat8(tr_read(a), tr_read(a + 512));
                if (j < 4) xf[3 - j] = lds_read128(ad[ks] + (3 - j) * 2048);
            }
        };
        auto mm = [&](bf16x8 (&xf)[4], bf16x8 (&wf)[6], int a, int b) {
#pragma unroll
            for (int n = 0; n < 24; ++n)
                if (n >= a && n < b) {
                    const int i = 3 - ((n >> 1) & 3), d = n & 1;
                    if (n < 8) ak[d][i] = mfma16(wf[d], xf[i], ak[d][i]);
                    else if (n < 16) av[i][d] = mfma16(xf[i], wf[2 + d], av[i][d]);
                    else aq[d][i] = mfma16(wf[4 + d], xf[i], aq[d][i]);
                }
            if (a < 8) asm volatile("" : "+v"(ak[0][0]), "+v"(ak[0][1]), "+v"(ak[0][2]), "+v"(ak[0][3]), "+v"(ak[1][0]), "+v"(ak[1][1]), "+v"(ak[1][2]), "+v"(ak[1][3]));
            if (a < 16 && b > 8) asm volatile("" : "+v"(av[0][0]), "+v"(av[0][1]), "+v"(av[1][0]), "+v"(av[1][1]), "+v"(av[2][0]), "+v"(av[2][1]), "+v"(av[3][0]), "+v"(av[3][1]));
            if (b > 16) asm volatile("" : "+v"(aq[0][0]), "+v"(aq[0][1]), "+v"(aq[0][2]), "+v"(aq[0][3]), "+v"(aq[1][0]), "+v"(aq[1][1]), "+v"(aq[1][2]), "+v"(aq[1][3]));
        };
#define SBEG(k) do { __builtin_amdgcn_s_waitcnt(0xC07F); SB0(); RSC(32 + (k)); \
            asm volatile("v_add_u32 %0, 8, %0\n\tds_write_b32 %1, %0" : "+v"(fa) : "v"(pa) : "memory"); SB0(); } while (0)
#define SCHK() do { unsigned c_; SB0(); \
            asm volatile("v_cmp_eq_u32 vcc, 0, %0\n\ts_cbranch_vccz .Lf%=\n\ts_mov_b32 %1, 0x400000\n" \
                         ".Lp%=:\n\tds_read_b32 %0, %2\n\ts_waitcnt lgkmcnt(0)\n\tv_cmp_eq_u32 vcc, 0, %0\n\ts_cbranch_vccz .Lf%=\n\t" \
                         "s_sub_u32 %1, %1, 1\n\ts_cbranch_scc0 .Lp%=\n.Lf%=:" \
                         : "+v"(fv), "=&s"(c_) : "v"(fa) : "memory", "vcc", "scc"); SB0(); } while (0)
#define SGB(m, n) __builtin_amdgcn_sched_group_barrier(m, n, 0)
#define REP(n, x) _Pragma("unroll") for (int i_ = 0; i_ < (n); ++i_) { x; }
        adr(0);
        RS(1);
        SCHK();
        RS(2);
        rd(xa, wa, 0);
        unsigned nb = 0;
#pragma unroll 1
        for (int t = 0; t < 15; ++t) {
            nb = nb == 2 * SA ? 0 : nb + SA;
            __builtin_amdgcn_s_waitcnt(0xC07F);
            SB0();
            rd(xb, wb, 1);
            fv = *(volatile AS3 unsigned*)(size_t)(fa + 8);
            mm(xa, wa, 0, 24);
            adr(nb);
            REP(17, SGB(0x008, 1); SGB(0x100, 1));
            REP(4, SGB(0x008, 1); SGB(0x006, 1));
            SGB(0x008, 3);
            SB0();
            SBEG(t);
            mm(xb, wb, 0, NA1);
            SCHK();
            RSC(48 + t);
            rd(xa, wa, 0);
            mm(xb, wb, NA1, 24);
            REP(16, SGB(0x008, 1); SGB(0x100, 1));
            SGB(0x008, 8 - NA1);
            SB0();
        }
        __builtin_amdgcn_s_waitcnt(0xC07F);
        SB0();
        rd(xb, wb, 1);
        {
            const unsigned a = lds0 + L_WPHI + hs * 8192 + (4 * g + q) * 64 + (((p >> 1) ^ (2 * (g & 1))) * 16) + 8 * (p & 1);
            aphi[0] = cat8(tr_read(a), tr_read(a + 4096));
            aphi[1] = cat8(tr_read(a ^ 32), tr_read((a ^ 32) + 4096));
        }
        mm(xa, wa, 0, 16);
        REP(8, SGB(0x008, 1); SGB(0x100, 2); SGB(0x006, 2));
        REP(4, SGB(0x008, 1); SGB(0x100, 1));
        SGB(0x008, 4);
        SB0();
        SBEG(15);
        RS(3);
        mm(xb, wb, 0, 8);
        SB0();
        {
            const bf16x8 b3 = pack44(ak[0][3], ak[1][3]), b2 = pack44(ak[0][2], ak[1][2]);
            mm(xb, wb, 8, 12);
            REP(4, SGB(0x008, 1); SGB(0x002, 2));
            SB0();
            const f32x4 l3a = mfma16(aphi[0], b3, zero), l3b = mfma16(aphi[1], b3, zero);
            const f32x4 l2a = mfma16(aphi[0], b2, zero), l2b = mfma16(aphi[1], b2, zero);
            const bf16x8 b1 = pack44(ak[0][1], ak[1][1]), b0 = pack44(ak[0][0], ak[1][0]);
            REP(4, SGB(0x008, 1); SGB(0x002, 2));
            SB0();
            f32x4 l1a = mfma16(aphi[0], b1, zero), l1b = mfma16(aphi[1], b1, zero);
            f32x4 l0a = mfma16(aphi[0], b0, zero), l0b = mfma16(aphi[1], b0, zero);
            lds_write<bf16x8>(QL + 1024, pack44(l3a, l3b));
            lds_write<bf16x8>(QL, pack44(l2a, l2b));
            asm volatile("" : "+v"(l1a), "+v"(l1b), "+v"(l0a), "+v"(l0b));
            REP(4, SGB(0x008, 1); SGB(0x002, 2));
            SGB(0x200, 2);
            SB0();
            asm volatile("ds_write_b32 %0, %0 offset:%1" ::"v"(pa), "n"(L_FPK - L_PROG) : "memory");
            lds_peek(fv, PROG);
            mm(xb, wb, 12, 16);
            SB0();
            RS(4);
            u32x4 pk[2];
            pk[0] = __builtin_bit_cast(u32x4, pack44(l0a, l0b));
            pk[1] = __builtin_bit_cast(u32x4, pack44(l1a, l1b));
            phi_k(pk, lds0 + L_PHI + w * 8192, 0, l, [&](int i) AI {
                if (i == 3) {
                    lds_wait(fv, PROG, lds0 + L_FULL + 8 * 15);
#pragma unroll
                    for (int sp = 0; sp < 2; ++sp)
#pragma unroll
                        for (int db = 0; db < 2; ++db) lds_write<bf16x8>(VX + w * 4096 + (sp * 2 + db) * 1024, pack44(av[2 * sp][db], av[2 * sp + 1][db]));
                }
            });
        }
        SB0();
        asm volatile("ds_write_b32 %0, %0" ::"v"(lds0 + L_FP + hs * 16 + e4 * 4) : "memory");
        RS(5);
#pragma unroll
        for (int i = 0; i < 4; ++i) qx[0][i] = xa[i], qx[1][i] = xb[i];
#pragma unroll
        for (int d = 0; d < 2; ++d) qw[0][d] = wa[4 + d], qw[1][d] = wb[4 + d];
    }
    if (w >= 4) {
        lds_wait(lds0 + L_FPK + rg * 4, 1);
        RS(9);
        u32x4 pp[2];
        pp[0] = lds_read<u32x4>(QL);
        pp[1] = lds_read<u32x4>(QL + 1024);
        prog_wait(15);
        phi_k(pp, lds0 + L_PHI + rg * 8192, 1, l, [](int) {});
        SB0();
        asm volatile("ds_write_b32 %0, %0" ::"v"(lds0 + L_FP + hs * 16 + e4 * 4) : "memory");
    }
    RS(10);
    u32x4 sq, tg;
    {
        bf16x8 ones;
#pragma unroll
        for (int j = 0; j < 8; ++j) ones[j] = (short)0x3f80;
        const unsigned ra = lds0 + L_PHI + hs * 16384 + (4 * g + q) * 128 + (((4 * e4 + p) ^ fsw(4 * g + q)) << 3);
        bf16x8 b0[4], b1[4], af[4];
        lds_wait(lds0 + L_FP + hs * 16 + p * 4, 1);
        RS(11);
#pragma unroll
        for (int i = 0; i < 4; ++i) {
            af[i] = cat8(tr_read(ra + i * 4096), tr_read(ra + i * 4096 + 2048));
            b0[i] = lds_read128(VX + hs * 8192 + i * 2048);
            b1[i] = lds_read128(VX + hs * 8192 + i * 2048 + 1024);
        }
        sq = lds_read<u32x4>(lds0 + L_SEL);
        SB0();
        f32x4 k0 = zero, k1 = zero, zt = zero;
#pragma unroll
        for (int i = 0; i < 4; ++i) {
            k0 = mfma16(af[i], b0[i], k0);
            k1 = mfma16(af[i], b1[i], k1);
            zt = mfma16(af[i], ones, zt);
        }
        tg = ((u32x4){0u, sq[0] >> 10, sq[0] >> 20, (sq[0] >> 30) | (sq[1] << 2)} + sq[0]) & 0x3FFu;
        st_wt(rsrc, kv_st + e4 * 32768, tag4(k0, tg));
        st_wt(rsrc, kv_st + e4 * 32768 + 16384, tag4(k1, tg));
        const u32x4 zo = tag4(zt, tg);
        ST_MASK("global_store_dwordx4", 0x0001000100010001ull, z_st + e4 * 1024, zo, ws);
        lds_set(lds0 + L_FKS + hs * 16 + e4 * 4);
    }
    RS(12);
    auto gua = [&](int i) { return ws + WS_GU + cs * 4096 + i * 1024; };
    if (w < 4) {
        u32x4 kp_[5];
        const char* kb = ws + WS_KV + (hp * 16 + mt) * 16384 + w * 256;
        const bool zw = w == 3 && !(mt & 1);
        const char* zb = zw ? ws + WS_Z + (hp * 8 + (mt >> 1)) * 1024 : kb;
        const unsigned kvo = g * 1024 + l15 * 16, zo = zw ? l16 : 0u;
        auto ko = [&](int i) { return i < 4 ? kvo : zo; };
        auto ka = [&](int i) { return i < 4 ? kb + i * 4096 : zb; };
#pragma unroll
        for (int n = 0; n < 16; ++n) aq[n & 1][3 - ((n >> 1) & 3)] = mfma16(qw[n >> 3][n & 1], qx[n >> 3][3 - ((n >> 1) & 3)], aq[n & 1][3 - ((n >> 1) & 3)]);
        lds_write<u32x4>(QL + 1024, logits(aq[0][3], aq[1][3]));
        lds_write<u32x4>(QL, logits(aq[0][2], aq[1][2]));
        lds_set(lds0 + L_FQ + w * 4);
        pq[0] = logits(aq[0][0], aq[1][0]);
        pq[1] = logits(aq[0][1], aq[1][1]);
        asm volatile("" : "+v"(pq[0]), "+v"(pq[1]));
        SB0();
        if (GDL) __builtin_amdgcn_s_sleep(GDL);
#pragma unroll
        for (int i = 0; i < 5; ++i) ld_async(kp_[i], ko(i), ka(i));
        SB0();
        RS(13);
        waitv<0>();
        settle(kp_, ko, ka, [&](const u32x4& a) { const u32x4 d = a ^ tg; return ((d[0] | d[1] | d[2] | d[3]) & 0x3FFu) == 0u ? 1u : 0u; });
        RS(25);
        const f32x4 s = (untag4(kp_[0]) + untag4(kp_[1])) + (untag4(kp_[2]) + untag4(kp_[3]));
        const float c = swap16_add(swap32_add(s[0], s[1]), swap32_add(s[2], s[3]));
        auto r = __builtin_amdgcn_permlane32_swap(__float_as_uint(c), __float_as_uint(c), false, false);
        const unsigned o = nz1(pack2(__uint_as_float(r[0]), __uint_as_float(r[1])));
        ST_MASK("global_store_dword", 0xFFFFFFFFull, kf_kv + w * 128 + (l & 8) * 8 + p * 16 + (l & 4) * 2 + g * 4, o, ws);
        if (zw) {
            f32x4 z = untag4(kp_[4]);
#pragma unroll
            for (int i = 0; i < 4; ++i) {
                z[i] = dpp_add<0x124>(z[i]);
                z[i] = dpp_add<0x128>(z[i]);
                z[i] = swap16_add(z[i], z[i]);
                z[i] = swap32_add(z[i], z[i]);
            }
            const u32x2 oz = {nz1(pack2(z[0], z[1])), nz1(pack2(z[2], z[3]))};
            ST_MASK("global_store_dwordx2", 0xFull, kf_z, oz, ws);
        }
            if (WOE == 2) lds_set(lds0 + L_FG + w * 4);
    } else {
        if (loader) {
        } else {
            ld_async(gu[0], l16, gua(0));
            ld_async(gu[1], l16, gua(1));
        }
        RS(13);
        lds_wait(lds0 + L_FQ + rg * 4, 1);
        pq[0] = lds_read<u32x4>(QL);
        pq[1] = lds_read<u32x4>(QL + 1024);
    }
    RS(14);
    {
        u32x4 ph[2][2], kf[6];
        softmax2(pq, ph, [](int) {});
        asm volatile("" : "+v"(ph[0][0]), "+v"(ph[0][1]), "+v"(ph[1][0]), "+v"(ph[1][1]));
        RS(24);
        if (w >= 4 && !loader) {
            waitv<0>();
#pragma unroll
            for (int i = 0; i < 16; ++i) pin(fd[i]);
            settle(gu, [&](int) { return l16; }, gua);
            RS(25);
            const f32x4 v = __builtin_bit_cast(f32x4, gu[0]) + __builtin_bit_cast(f32x4, gu[1]);
            auto r = __builtin_amdgcn_permlane16_swap(__float_as_uint(swap32_add(v[0], v[2])), __float_as_uint(swap32_add(v[1], v[3])), false, false);
            const float gt = rbf(__uint_as_float(r[0])), up = rbf(__uint_as_float(r[1]));
            lds_write<u16>(lds0 + L_H + rg * 4096 + l15 * 8 + g * 2, f2bf(rbf(gt / (1.f + __expf(-gt))) * up));
            const u32x4 hv = lds_read<u32x4>(lds0 + L_H + rg * 4096 + srow * 16);
            f32x4 y0 = zero, y1 = zero;
#pragma unroll
            for (int i = 0; i < 8; ++i) {
                const bf16x8 a = spread(hv, i);
                y0 = mfma16(a, __builtin_bit_cast(bf16x8, fd[i]), y0);
                y1 = mfma16(a, __builtin_bit_cast(bf16x8, fd[8 + i]), y1);
            }
            fold_store(y0, y_st);
            fold_store(y1, y_st + 256);
        }
        SB0();
        RS(26);
        if (loader) {
            lds_wait(lds0 + ((l & 8) ? L_FG + p * 4 : L_FKS + sch * 4), 1);
            wo_load(24, 32, 0);
            mark(lds0 + L_FWD + hs * 4);
            kf_load(kf, kfb, l);
            waitv<0>();
            kf_fix(kf, kfb, l);
#pragma unroll
            for (int i = 0; i < 6; ++i) lds_write<u32x4>(VX + hs * 8192 + i * 1024, kf[i]);
            SB0();
            asm volatile("ds_write_b32 %0, %0" ::"v"(lds0 + L_FKF + hs * 4) : "memory");
        } else {
            lds_wait(lds0 + L_FKF + hs * 4, 1);
#pragma unroll
            for (int i = 0; i < 6; ++i) kf[i] = lds_read<u32x4>(VX + hs * 8192 + i * 1024);
        }
        lds_set(lds0 + L_FWO + w * 4);
        RS(27);
        o_store(kf, ph, rsrc, o_st + kp * 2048);
        RS(28);
    }
    const int row = rt * 128 + rg * 32 + kp * 16 + l15;
    const int col = cb * 64 + (g & 1) * 16 + (g >> 1) * 8;
    const u64 T = (u64)sq[1] << 32 | sq[0];
    const u32x2 selv = {sq[2], sq[3]};

    RS(15);
    const u32x4 xr0 = *(const u32x4*)(x + (size_t)row * DM + col), xr1 = *(const u32x4*)(x + (size_t)row * DM + col + 32);
    u64 gg = ld_agent(ws + WS_GG + row * 8);
    SB0();
    const char* ob = ws + WS_O + (rt * 2 + (rg >> 1)) * 131072 + (rg & 1) * 2048;
    const unsigned ov0 = l16 + kp * 1024, ov1 = l16 + (kp ^ 1) * 1024;
    auto oa = [&](int i) { return ob + ((2 * i + kp + rot) & 31) * 4096; };
    unsigned pv = 0, rdy = 0;
    const unsigned pof = ((2 * l15 + kp + rot) & 31) * 4096 + 2044;
    auto poll = [&]() {
        u64 keep;
        asm volatile("s_mov_b64 %1, exec\n\ts_mov_b64 exec, %4\n\tglobal_load_dword %0, %2, %3 sc1\n\ts_mov_b64 exec, %1\n\ts_waitcnt vmcnt(0)"
                     : "+&v"(pv), "=&s"(keep) : "v"(pof), "s"(ob), "s"((u64)(~rdy & 0xFFFFu)) : "memory");
        rdy = (unsigned)__ballot(pv != 0u);
    };
    constexpr int OW = OWIN;
    u32x4 of[OW][4];
    auto fetch = [&](int k) {
#pragma unroll 1
        for (unsigned s = 0; k < NPL && s < SPIN_CAP && (~rdy & (3u << (2 * k))); ++s) poll();
#pragma unroll
        for (int j = 0; j < 4; ++j) ld_async(of[k % OW][j], (j & 1) ? ov1 : ov0, oa(2 * k + (j >> 1)));
    };
    const bool own = (__builtin_amdgcn_readfirstlane(selv[0]) >> 4) == (unsigned)(rt * 8 + rg * 2 + kp);
    if (NPL) poll();
    RS(16);
#pragma unroll
    for (int k = 0; k < OW - 1; ++k) fetch(k);
    RS(17);
    f32x4 acc[2][4];
#pragma unroll
    for (int i = 0; i < 4; ++i) acc[0][i] = acc[1][i] = zero;
    const unsigned wa = lds0 + 4096 * kp + (4 * g + q) * 128 + ((((g & 1) << 1) | (q >> 1)) * 32) + 8 * p;
    bf16x8 wf[2][2][4];
    auto wrd = [&](int k) {
#pragma unroll
        for (int hh = 0; hh < 2; ++hh)
#pragma unroll
            for (int nb = 0; nb < 4; ++nb) {
                const unsigned a = (wa ^ (nb << 5)) + wo_pos(2 * (2 * k + hh));
                wf[k & 1][hh][nb] = cat8(tr_read(a), tr_read(a + 2048));
            }
    };
    u32x4 yp[14];
    const unsigned yo = g * 4096 + cb * 256 + l15 * 16;
    auto ya = [&](int i) { return ws + WS_Y + i * 16384; };
    lds_wait(lds0 + L_FWD + ((l & 8) ? 16 + sch * 4 : (l & 1) * 4), 1);
    RS(18);
    wrd(0);
#pragma unroll
    for (int k = 0; k < 8; ++k) {
        if (k + OW - 1 < 8) fetch(k + OW - 1);
        waitvn(4 * (k + OW - 1 < 8 ? OW - 1 : 7 - k));
        settle(of[k % OW], [&](int j) { return (j & 1) ? ov1 : ov0; }, [&](int j) { return oa(2 * k + (j >> 1)); });
        if (k == 7 && own) {
#pragma unroll
            for (int i = 0; i < 14; ++i) ld_async(yp[i], yo, ya(i));
        }
        SB0();
        if (k < 7) wrd(k + 1);
#pragma unroll
        for (int j = 0; j < 4; ++j)
#pragma unroll
            for (int nb = 0; nb < 4; ++nb) acc[j & 1][nb] = mfma16(wf[k & 1][j >> 1][nb], __builtin_bit_cast(bf16x8, of[k % OW][j]), acc[j & 1][nb]);
        if (k < 7) {
#pragma unroll
            for (int i = 0; i < 16; ++i) {
                __builtin_amdgcn_sched_group_barrier(0x008, 1, 0);
                __builtin_amdgcn_sched_group_barrier(0x100, 1, 0);
            }
        }
        SB0();
        if (k == 0) RS(19);
        if (k == 3) RS(23);
    }
    RS(20);
    const unsigned ea = VX + 4096 * w, eb = lds0 + L_VX + 4096 * (w ^ 4);
    lds_write<u32x4>(ea, nz4(acc[1][0]));
#pragma unroll
    for (int nb = 1; nb < 4; ++nb) lds_write<f32x4>(ea + nb * 1024, acc[1][nb]);
    BAR();
    RS(21);
#pragma unroll
    for (int nb = 0; nb < 4; ++nb) acc[0][nb] += lds_read<f32x4>(eb + l16 + nb * 1024);
    if (own) {
        waitv<0>();
        settle(yp, [&](int) { return yo; }, ya);
        f32x4 yv = __builtin_bit_cast(f32x4, yp[0]);
#pragma unroll
        for (int i = 1; i < 14; ++i) yv += __builtin_bit_cast(f32x4, yp[i]);
#pragma unroll
        for (int i = 0; i < 4; ++i) {
            yv[i] = swap16_add(yv[i], yv[i]);
            yv[i] = rbf(rbf(swap32_add(yv[i], yv[i])) * __uint_as_float(selv[1]));
        }
        asm volatile("s_waitcnt lgkmcnt(0)" ::: "memory");
        lds_write<f32x4>(eb + l16, yv);
    }
#pragma unroll 1
    for (unsigned s = 0; s < SPIN_CAP && (gg >> 16) != (T & 0xFFFFFFFFFFFFull); ++s) gg = ld_agent(ws + WS_GG + row * 8);
    const float gv_ = bf2f((u16)gg);
    const float og = rbf(1.f - gv_);
#pragma unroll
    for (int i = 0; i < 2; ++i) {
        unsigned a0 = pack2(acc[0][2 * i][0], acc[0][2 * i][1]), a1 = pack2(acc[0][2 * i][2], acc[0][2 * i][3]);
        unsigned b0 = pack2(acc[0][2 * i + 1][0], acc[0][2 * i + 1][1]), b1 = pack2(acc[0][2 * i + 1][2], acc[0][2 * i + 1][3]);
        auto s0_ = __builtin_amdgcn_permlane16_swap(a0, b0, false, false);
        auto s1_ = __builtin_amdgcn_permlane16_swap(a1, b1, false, false);
        const unsigned av_[4] = {s0_[0], s1_[0], s0_[1], s1_[1]};
        u32x4 bv = i ? xr1 : xr0;
        if (row == (int)selv[0]) {
            const unsigned yb = eb + (col & 63) * 4 + 128 * i;
            const f32x4 y0 = lds_read<f32x4>(yb), y1 = lds_read<f32x4>(yb + 16);
            bv[0] = pack2(lo16(bv[0]) + y0[0], hi16(bv[0]) + y0[1]);
            bv[1] = pack2(lo16(bv[1]) + y0[2], hi16(bv[1]) + y0[3]);
            bv[2] = pack2(lo16(bv[2]) + y1[0], hi16(bv[2]) + y1[1]);
            bv[3] = pack2(lo16(bv[3]) + y1[2], hi16(bv[3]) + y1[3]);
        }
        u32x4 o;
#pragma unroll
        for (int j = 0; j < 4; ++j) {
            const unsigned ga = pack2(gv_ * lo16(av_[j]), gv_ * hi16(av_[j]));
            const unsigned gb = pack2(og * lo16(bv[j]), og * hi16(bv[j]));
            o[j] = pack2(lo16(ga) + lo16(gb), hi16(ga) + hi16(gb));
        }
        *(u32x4*)(out + (size_t)row * DM + col + 32 * i) = o;
    }
    RS(22);
    RSDUMP();
}

extern "C" void kernel_launch(void* const* d_in, const int* in_sizes, int n_in, void* d_out, int out_size,
                              void* d_ws, size_t ws_size, hipStream_t stream) {
    fused_mix<<<256, 512, 0, stream>>>((const u16*)d_in[0], (const u16*)d_in[1], (const u16*)d_in[2], (const u16*)d_in[3],
                                       (const u16*)d_in[5], (const u16*)d_in[6], (const u16*)d_in[7], (const u16*)d_in[8],
                                       (const u16*)d_in[9], (char*)d_ws, (u16*)d_out);
}
```

```cpp
#include <hip/hip_runtime.h>
#include <hip/hip_bf16.h>
#include <stdint.h>
#include <type_traits>

typedef __attribute__((ext_vector_type(8))) short bf16x8;
typedef __attribute__((ext_vector_type(4))) short s16x4;
typedef __attribute__((ext_vector_type(4))) float f32x4;
typedef __attribute__((ext_vector_type(2))) float f32x2;
typedef __attribute__((ext_vector_type(2))) unsigned u32x2;
typedef __attribute__((ext_vector_type(4))) unsigned u32x4;
typedef __attribute__((ext_vector_type(2))) __bf16 bf16x2_t;
typedef unsigned short u16;
typedef unsigned long long u64;

#define DI __device__ __forceinline__
#define AS3 __attribute__((address_space(3)))
#define AS1 __attribute__((address_space(1)))
#define RLX_AGENT __ATOMIC_RELAXED, __HIP_MEMORY_SCOPE_AGENT

constexpr int SEQ = 2048, DM = 1024, NH = 32, FF = 3584;

DI float bf2f(u16 v) { return __uint_as_float(((unsigned)v) << 16); }
DI u16 f2bf(float f) { __bf16 b = (__bf16)f; return __builtin_bit_cast(u16, b); }
DI float rbf(float f) { return bf2f(f2bf(f)); }
DI unsigned pack2(float lo, float hi) {
    const f32x2 v = {lo, hi};
    return __builtin_bit_cast(unsigned, __builtin_convertvector(v, bf16x2_t));
}
DI float lo16(unsigned u) { return __uint_as_float(u << 16); }
DI float hi16(unsigned u) { return __uint_as_float(u & 0xffff0000u); }

DI s16x4 tr_read(unsigned a) { return __builtin_amdgcn_ds_read_tr16_b64_v4i16((AS3 s16x4*)(size_t)a); }
DI bf16x8 lds_read128(unsigned a) { return *(AS3 const bf16x8*)(size_t)a; }
template <class T> DI void lds_write(unsigned a, T v) { *(AS3 T*)(size_t)a = v; }
template <class T> DI T lds_read(unsigned a) { return *(AS3 const T*)(size_t)a; }
DI bf16x8 cat8(s16x4 a, s16x4 b) { return __builtin_shufflevector(a, b, 0, 1, 2, 3, 4, 5, 6, 7); }
DI f32x4 mfma16(bf16x8 a, bf16x8 b, f32x4 c) { return __builtin_amdgcn_mfma_f32_16x16x32_bf16(a, b, c, 0, 0, 0); }
DI bf16x8 pack44(const f32x4& a, const f32x4& b) {
    u32x4 r;
    r[0] = pack2(a[0], a[1]);
    r[1] = pack2(a[2], a[3]);
    r[2] = pack2(b[0], b[1]);
    r[3] = pack2(b[2], b[3]);
    return __builtin_bit_cast(bf16x8, r);
}
DI unsigned lds_of(const void* p) { return (unsigned)(size_t)(AS3 const char*)p; }

template <int CTRL>
DI unsigned dpp(unsigned x) { return __builtin_amdgcn_update_dpp(0u, x, CTRL, 0xf, 0xf, true); }
template <int CTRL>
DI float dpp_add(float x) { return x + __uint_as_float(dpp<CTRL>(__float_as_uint(x))); }
DI float swap32_add(float a, float b) {
    auto r = __builtin_amdgcn_permlane32_swap(__float_as_uint(a), __float_as_uint(b), false, false);
    return __uint_as_float(r[0]) + __uint_as_float(r[1]);
}
DI float swap16_add(float a, float b) {
    auto r = __builtin_amdgcn_permlane16_swap(__float_as_uint(a), __float_as_uint(b), false, false);
    return __uint_as_float(r[0]) + __uint_as_float(r[1]);
}
DI float wave_sum(float x) {
    x = dpp_add<0xB1>(x);
    x = dpp_add<0x4E>(x);
    x = dpp_add<0x141>(x);
    x = dpp_add<0x140>(x);
    x = swap16_add(x, x);
    return swap32_add(x, x);
}
DI unsigned wave_umax(unsigned x) {
    x = max(x, dpp<0xB1>(x));
    x = max(x, dpp<0x4E>(x));
    x = max(x, dpp<0x141>(x));
    x = max(x, dpp<0x140>(x));
    auto r = __builtin_amdgcn_permlane16_swap(x, x, false, false);
    x = max(r[0], r[1]);
    auto s = __builtin_amdgcn_permlane32_swap(x, x, false, false);
    return max(s[0], s[1]);
}
template <class OP, class POST>
DI void red2(float& a, float& b, OP op, POST post) {
    auto x = __builtin_amdgcn_permlane32_swap(__float_as_uint(a), __float_as_uint(b), false, false);
    const float m = op(__uint_as_float(x[0]), __uint_as_float(x[1]));
    auto y = __builtin_amdgcn_permlane16_swap(__float_as_uint(m), __float_as_uint(m), false, false);
    const float f = post(op(__uint_as_float(y[0]), __uint_as_float(y[1])));
    auto z = __builtin_amdgcn_permlane32_swap(__float_as_uint(f), __float_as_uint(f), false, false);
    a = __uint_as_float(z[0]);
    b = __uint_as_float(z[1]);
}

#define RS(i) do {} while (0)
#define RSD(i) do {} while (0)
#define RSDUMP() do {} while (0)

constexpr unsigned WS_E = 0;
constexpr unsigned WS_RG = 256;
constexpr unsigned WS_GG = WS_RG + 2048;
constexpr unsigned WS_CL = 32768;
constexpr unsigned WS_Z = 65536;
constexpr unsigned WS_KF = WS_Z + 131072;
constexpr unsigned WS_GU = WS_KF + 262144;
constexpr unsigned WS_Y = WS_GU + 262144;
constexpr unsigned WS_KV = WS_Y + 262144;
constexpr unsigned WS_O = WS_KV + (2u << 20);
constexpr unsigned WS_END = WS_O + (4u << 20);

constexpr int NA = 10, ND = 6, NF = 64, NF1 = 61, NP = 11;
constexpr int J_S[48] = {0, 0, 0, 1, 1, 1, 2, 2, 2, 3, 3, 3, 4, 4, 4, 5, 5, 5, 6, 6, 6, 7, 7, 7, 8, 8, 8, 9, 9, 9, 10, 11, 12, 13, 14, 15, 10, 11, 12, 13, 14, 15, 10, 11, 12, 13, 14, 15};
constexpr int J_M[48] = {0, 1, 2, 0, 1, 2, 0, 1, 2, 0, 1, 2, 0, 1, 2, 0, 1, 2, 0, 1, 2, 0, 1, 2, 0, 1, 2, 0, 1, 2, 0, 0, 0, 0, 0, 0, 1, 1, 1, 1, 1, 1, 2, 2, 2, 2, 2, 2};
constexpr int J_U[48] = {6, 7, 0, 3, 16, 17, 9, 10, 11, 14, 15, 4, 7, 2, 3, 16, 17, 9, 12, 13, 14, 5, 0, 1, 2, 3, 18, 10, 11, 14, 5, 6, 7, 0, 3, 18, 8, 9, 10, 11, 12, 13, 14, 15, 4, 5, 6, 7};
constexpr int J_XE[48] = {0, -1, -1, 1, -1, -1, 2, -1, -1, 3, -1, -1, 4, -1, -1, -1, -1, 6, -1, -1, -1, 8, -1, -1, -1, -1, 10, -1, -1, 12, -1, -1, -1, -1, 14, 15, -1, -1, -1, -1, -1, -1, -1, -1, -1, -1, -1, -1};
constexpr int J_XL[48] = {-1, -1, -1, -1, -1, -1, -1, -1, -1, -1, -1, -1, 5, -1, -1, -1, -1, 7, -1, -1, -1, 9, -1, -1, -1, -1, 11, -1, -1, 13, -1, -1, -1, -1, -1, -1, -1, -1, -1, -1, -1, -1, -1, -1, -1, -1, -1, -1};
constexpr int J_C[48] = {0, 1, 2, 3, 4, 5, 6, 7, 8, 9, 10, 11, 12, 13, 14, 15, 16, 17, 18, 19, 20, 21, 22, 23, 24, 25, 26, 27, 28, 29, 30, 31, 32, 33, 34, 35, 36, 37, 38, 39, 40, 41, 42, 43, 44, 45, 46, 47};
constexpr int J_P[48] = {0, 0, 0, 1, 0, 0, 2, 0, 0, 3, 0, 0, 4, 0, 5, 0, 0, 6, 0, 0, 0, 7, 0, 0, 8, 0, 9, 0, 0, 10, 0, 0, 0, 0, 0, 0, 0, 0, 0, 0, 0, 0, 0, 0, 0, 0, 0, 0};
constexpr int X_U0[16] = {4, 1, 18, 12, 5, 0, 18, 10, 15, 6, 16, 8, 12, 15, 1, 16};
constexpr int X_U1[16] = {5, 2, 8, 13, 6, 1, 8, 11, 4, 7, 17, 9, 13, 4, 2, 17};
constexpr int P_U[4] = {0, 1, 2, 3};
constexpr int F_J[64] = {-1, 0, 1, 2, -2, 3, 4, 5, -3, 6, 7, 8, -4, 9, 10, 11, -5, 12, -6, 13, 14, 15, 16, -7, 17, -8, 18, 19, 20, -9, 21, -10, 22, 23, 24, 25, -11, 26, -12, 27, 28, -13, 29, -14, 30, 31, 32, 33, -15, 34, -16, 35, 36, 37, 38, 39, 40, 41, 42, 43, 44, 45, 46, 47};
constexpr int F_N[64] = {0, 0, 0, 0, 0, 0, 0, 0, 0, 0, 0, 0, 0, 0, 0, 1, 1, 1, 1, 1, 1, 2, 2, 2, 2, 3, 3, 3, 3, 4, 4, 4, 4, 4, 5, 5, 6, 6, 6, 6, 6, 7, 7, 7, 7, 7, 7, 8, 8, 9, 9, 9, 9, 9, 10, 10, 10, 10, 10, 10, 10, 11, 11, 11};
constexpr int F_K[64] = {-1, 0, 1, 2, -1, 3, 4, 5, -1, 6, 7, 8, -1, 9, 10, 11, -1, -1, 12, 13, 14, 15, 16, -1, -1, 17, 18, 19, 20, -1, -1, 21, 22, 23, 24, 25, -1, -1, 26, 27, 28, -1, -1, 29, 30, 31, 32, 33, -1, 34, -1, 35, 36, 37, 38, 39, 40, 41, 42, 43, 44, 45, 46, 47};
constexpr unsigned L_A = 16 * 8192;
constexpr unsigned L_KF = 18 * 8192;
constexpr unsigned L_SM = 19 * 8192;
constexpr unsigned L_FULL = L_SM;
constexpr unsigned L_PROG = L_SM + 768 - 16;
constexpr unsigned L_FW2 = L_SM + 512, L_FOS = L_SM + 480, L_FGI = L_SM + 520;
constexpr unsigned L_FK = L_SM + 528, L_FKT = L_SM + 544, L_FV = L_SM + 560, L_FP = L_SM + 576, L_FKS = L_SM + 592;
constexpr unsigned L_FQD = L_SM + 608, L_FG = L_SM + 624, L_FKF = L_SM + 736, L_FQ = L_SM + 720, L_FWD = L_SM + 640, L_FWO = L_SM + 656, L_FLD = L_SM + 688, L_BEST = L_SM + 696;
constexpr unsigned L_SEL = L_SM + 704;
constexpr unsigned L_XS = L_SM + 1024, L_KZ = L_SM + 2048, L_WPHI = L_SM + 4096;
static_assert(P_U[1] == P_U[0] + 1 && P_U[3] == P_U[2] + 1, "feature maps of a head lie side by side");
constexpr int NW1 = ND, NW2 = ND + 4;
constexpr int wo_unit(int i) { return i < NW1 ? J_U[3 * NA + ND + i] : i < NW2 ? P_U[i - NW1] : J_U[3 * NA + 2 * ND + i - NW2]; }

constexpr unsigned SPIN_CAP = 1u << 16;
__device__ const unsigned k_one[4] = {1u, 1u, 1u, 1u};

#define RSL(i) do {} while (0)
#define RSC(i) do {} while (0)
#define PD 4
#define NPH 4
#define KSK 3
#define ROTK 8
#define OWIN 3
#define CLR0 4
#define CLS 1
#define CLP 2
#define SLK 2
#define CPH 2
#define NPL 8
#define CPRIO 1
#define HPRIO 2
#define KPK 0
#define KRND 1
#define QW 1
#define WOG 1
#define WON 10
#define KFD 0
#define SEL0 7
#define SPRIO 2
#define FBA 3
#define FBB 10
#define FBS 1
#define FBL 1
#define WOP 0
#define GTL 3
#define WPF 2
#define WOE 10
#define GW 0
#define GUF 0
#define FBH 0
#define WG2 0
#define PWN 2
#define PWL (NP - 1)
#define TRN 0
#define RT0 0
#define BSP 0
#define WOV 0
#define GUP 1
#define NOF 0
#define FBD 8
#define BAR() asm volatile("s_waitcnt lgkmcnt(0)\n\ts_barrier" ::: "memory")
template <int N>
DI void waitv() { asm volatile("s_waitcnt vmcnt(%0)" ::"n"(N) : "memory"); }

DI u64 ld_agent(const char* p) { return __hip_atomic_load((const AS1 u64*)p, RLX_AGENT); }
DI void st_agent(char* p, u64 v) { __hip_atomic_store((AS1 u64*)p, v, RLX_AGENT); }
DI unsigned nz1(unsigned v) { return max(v, 1u); }
DI unsigned umin(unsigned a, unsigned b) { return min(a, b); }
DI u32x4 nz4(u32x4 v) { return (u32x4){max(v[0], 1u), max(v[1], 1u), max(v[2], 1u), max(v[3], 1u)}; }
DI u32x4 nz4(f32x4 v) { return nz4(__builtin_bit_cast(u32x4, v)); }
DI unsigned min4(u32x4 v) { return min(min(v[0], v[1]), min(v[2], v[3])); }
DI u32x4 ld_wt(__amdgpu_buffer_rsrc_t r, unsigned off) { return __builtin_amdgcn_raw_buffer_load_b128(r, off, 0, 16); }
DI void st_wt(__amdgpu_buffer_rsrc_t r, unsigned off, u32x4 v) { __builtin_amdgcn_raw_buffer_store_b128(v, r, off, 0, 16); }
DI void ld_async(u32x4& v, unsigned off, const char* base) {
    asm volatile("global_load_dwordx4 %0, %1, %2 sc1" : "=&v"(v) : "v"(off), "s"(base) : "memory");
}
#define ST_MASK(op, m, off, v, base) asm volatile("s_mov_b64 exec, %3\n\t" op " %0, %1, %2 sc1\n\ts_mov_b64 exec, -1\n\ts_nop 0" ::"v"(off), "v"(v), "s"(base), "s"((u64)(m)) : "memory")
DI void pin(u32x4& v) { asm volatile("" : "+v"(v)); }
template <int N, class OFF, class BASE, class OK>
DI void settle(u32x4 (&v)[N], OFF off, BASE base, OK ok) {
#pragma unroll
    for (int i = 0; i < N; ++i) pin(v[i]);
#pragma unroll 1
    for (unsigned s = 0; s < SPIN_CAP; ++s) {
        unsigned m = ok(v[0]);
#pragma unroll
        for (int i = 1; i < N; ++i) m = min(m, ok(v[i]));
        if (__ballot(m == 0u) == 0ull) break;
#pragma unroll
        for (int i = 0; i < N; ++i) {
            u64 keep;
            asm volatile("s_mov_b64 %1, exec\n\ts_mov_b64 exec, %4\n\tglobal_load_dwordx4 %0, %2, %3 sc1\n\ts_mov_b64 exec, %1"
                         : "+&v"(v[i]), "=&s"(keep) : "v"(off(i)), "s"(base(i)), "s"((u64)__ballot(ok(v[i]) == 0u)) : "memory");
        }
        waitv<0>();
#pragma unroll
        for (int i = 0; i < N; ++i) pin(v[i]);
    }
}
template <int N, class OFF, class BASE>
DI void settle(u32x4 (&v)[N], OFF off, BASE base) { settle(v, off, base, [](const u32x4& a) { return min4(a); }); }
DI void lds_wait(unsigned a, unsigned n) {
    unsigned v, c;
    asm volatile("s_mov_b32 %1, 0x400000\n"
                 ".Lp%=:\n\tds_read_b32 %0, %2\n\ts_waitcnt lgkmcnt(0)\n\tv_cmp_gt_u32 vcc, %3, %0\n\ts_cbranch_vccz .Lf%=\n\t"
                 "s_sleep 1\n\ts_sub_u32 %1, %1, 1\n\ts_cbranch_scc0 .Lp%=\n.Lf%=:"
                 : "=&v"(v), "=&s"(c) : "v"(a), "s"(n) : "memory", "vcc", "scc");
}
DI void lds_wait8(unsigned a, unsigned n) {
    unsigned v, c;
    asm volatile("s_mov_b32 %1, 0x100000\n"
                 ".Lp%=:\n\tds_read_b32 %0, %2\n\ts_waitcnt lgkmcnt(0)\n\tv_cmp_gt_u32 vcc, %3, %0\n\ts_cbranch_vccz .Lf%=\n\t"
                 "s_sleep 4\n\ts_sub_u32 %1, %1, 1\n\ts_cbranch_scc0 .Lp%=\n.Lf%=:"
                 : "=&v"(v), "=&s"(c) : "v"(a), "s"(n) : "memory", "vcc", "scc");
}
DI void lds_peek(unsigned& v, unsigned a) { asm volatile("ds_read_b32 %0, %1" : "=v"(v) : "v"(a) : "memory"); }
DI void lds_wait(unsigned& v, unsigned a, unsigned n) {
    unsigned c;
    asm volatile("s_mov_b32 %1, 0x400000\n"
                 ".Lp%=:\n\ts_waitcnt lgkmcnt(0)\n\tv_cmp_gt_u32 vcc, %3, %0\n\ts_cbranch_vccz .Lf%=\n\t"
                 "ds_read_b32 %0, %2\n\ts_sub_u32 %1, %1, 1\n\ts_cbranch_scc0 .Lp%=\n.Lf%=:"
                 : "+v"(v), "=&s"(c) : "v"(a), "s"(n) : "memory", "vcc", "scc");
}
DI void lds_set(unsigned a) {
    asm volatile("s_waitcnt lgkmcnt(0)\n\tds_write_b32 %0, %1" ::"v"(a), "v"(1u) : "memory");
}
DI float dot2(unsigned a, unsigned b, float c) {
    return __builtin_amdgcn_fdot2_f32_bf16(__builtin_bit_cast(bf16x2_t, a), __builtin_bit_cast(bf16x2_t, b), c, false);
}
DI float sum4(const float (&a)[4]) {
    float c = swap16_add(swap32_add(a[0], a[1]), swap32_add(a[2], a[3]));
    c = dpp_add<0xB1>(c);
    c = dpp_add<0x4E>(c);
    c = dpp_add<0x141>(c);
    return dpp_add<0x140>(c);
}

template <class OP, class POST>
DI void red4(float& a, float& b, float& c, float& d, OP op, POST post) {
    auto x = __builtin_amdgcn_permlane32_swap(__float_as_uint(a), __float_as_uint(b), false, false);
    auto y = __builtin_amdgcn_permlane32_swap(__float_as_uint(c), __float_as_uint(d), false, false);
    const float m = op(__uint_as_float(x[0]), __uint_as_float(x[1])), n = op(__uint_as_float(y[0]), __uint_as_float(y[1]));
    auto z = __builtin_amdgcn_permlane16_swap(__float_as_uint(m), __float_as_uint(n), false, false);
    const unsigned f = __float_as_uint(post(op(__uint_as_float(z[0]), __uint_as_float(z[1]))));
    auto u = __builtin_amdgcn_permlane16_swap(f, f, false, false);
    auto v = __builtin_amdgcn_permlane32_swap(u[0], u[0], false, false);
    auto t = __builtin_amdgcn_permlane32_swap(u[1], u[1], false, false);
    a = __uint_as_float(v[0]);
    b = __uint_as_float(v[1]);
    c = __uint_as_float(t[0]);
    d = __uint_as_float(t[1]);
}
DI float mx2(float a, float b) { return __builtin_elementwise_maximum(a, b); }
DI float opq(float a) { asm("" : "+v"(a)); return a; }
template <bool PK, class HOOK>
DI void softmax2(const f32x4 (&v)[2][2], u32x4 (&ph)[2][2], HOOK hook) {
    constexpr float L2E = 1.4426950408889634f;
    float mx[2], mn[2], sp_[2], sn_[2];
#pragma unroll
    for (int b = 0; b < 2; ++b) {
        const f32x4 s = v[b][0], t = v[b][1];
        mx[b] = mx2(mx2(mx2(mx2(s[0], s[1]), s[2]), mx2(mx2(s[3], t[0]), t[1])), mx2(t[2], t[3]));
        mn[b] = mx2(mx2(mx2(mx2(-s[0], -s[1]), -s[2]), mx2(mx2(-s[3], -t[0]), -t[1])), mx2(-t[2], -t[3]));
    }
    red4(mx[0], mx[1], mn[0], mn[1], [](float a, float b) { return mx2(a, b); }, [](float a) { return a * -L2E; });
    f32x2 ep[2][4], en[2][4];
#pragma unroll
    for (int b = 0; b < 2; ++b) {
#pragma unroll
        for (int e = 0; e < 4; ++e) {
            const f32x2 u = {v[b][e >> 1][2 * (e & 1)], v[b][e >> 1][2 * (e & 1) + 1]};
            f32x2 a, c;
            if (PK) {
                a = __builtin_elementwise_fma(u, (f32x2){L2E, L2E}, (f32x2){mx[b], mx[b]});
                c = __builtin_elementwise_fma(u, (f32x2){-L2E, -L2E}, (f32x2){mn[b], mn[b]});
            } else {
                a = (f32x2){__builtin_fmaf(u[0], L2E, mx[b]), __builtin_fmaf(u[1], L2E, mx[b])};
                c = (f32x2){__builtin_fmaf(u[0], -L2E, mn[b]), __builtin_fmaf(u[1], -L2E, mn[b])};
            }
            ep[b][e] = (f32x2){__builtin_amdgcn_exp2f(a[0]), __builtin_amdgcn_exp2f(a[1])};
            en[b][e] = (f32x2){__builtin_amdgcn_exp2f(c[0]), __builtin_amdgcn_exp2f(c[1])};
        }
        if (PK) {
            const f32x2 s = (ep[b][0] + ep[b][1]) + (ep[b][2] + ep[b][3]), t = (en[b][0] + en[b][1]) + (en[b][2] + en[b][3]);
            sp_[b] = s[0] + s[1];
            sn_[b] = t[0] + t[1];
        } else {
            sp_[b] = ep[b][0][0];
            sn_[b] = en[b][0][0];
#pragma unroll
            for (int e = 1; e < 8; ++e) sp_[b] += ep[b][e >> 1][e & 1], sn_[b] += en[b][e >> 1][e & 1];
        }
    }
    asm volatile("" : "+v"(sp_[0]), "+v"(sn_[0]), "+v"(sp_[1]), "+v"(sn_[1]));
    hook();
    red4(sp_[0], sp_[1], sn_[0], sn_[1], [](float a, float b) { return a + b; }, [](float a) { return __builtin_amdgcn_rcpf(a); });
#pragma unroll
    for (int b = 0; b < 2; ++b)
#pragma unroll
        for (int e = 0; e < 4; ++e) {
            if (PK) {
                const f32x2 a = ep[b][e] * (f32x2){sp_[b], sp_[b]}, c = en[b][e] * (f32x2){sn_[b], sn_[b]};
                ph[b][0][e] = pack2(a[0], a[1]);
                ph[b][1][e] = pack2(c[0], c[1]);
            } else {
                ph[b][0][e] = pack2(opq(ep[b][e][0] * sp_[b]), ep[b][e][1] * sp_[b]);
                ph[b][1][e] = pack2(opq(en[b][e][0] * sn_[b]), en[b][e][1] * sn_[b]);
            }
        }
}
DI f32x4 rbf4(const f32x4& a) {
    const unsigned u = pack2(a[0], a[1]), v = pack2(a[2], a[3]);
    return (f32x4){lo16(u), hi16(u), lo16(v), hi16(v)};
}
DI int fsw(int row) { return (((row >> 1) & 3) << 2) | (((row >> 3) & 1) << 1) | (row & 1); }
template <bool PK, class HOOK>
DI void phi_k(const f32x4 (&pp)[2][2], const unsigned (&pw)[4], unsigned off, HOOK hook) {
    u32x4 ph[2][2];
    softmax2<PK>(pp, ph, hook);
#pragma unroll
    for (int b = 0; b < 2; ++b)
#pragma unroll
        for (int kf = 0; kf < 2; ++kf)
#pragma unroll
            for (int fb = 0; fb < 2; ++fb) {
                const u32x2 a = {ph[b][kf][2 * fb], ph[b][kf][2 * fb + 1]};
                lds_write<u32x2>(pw[2 * kf + fb] + off + b * 2048, a);
            }
}

DI void waitvn(int n) {
#define WV(k) case k: waitv<k>(); break;
#define WV8(k) WV(k) WV(k + 1) WV(k + 2) WV(k + 3) WV(k + 4) WV(k + 5) WV(k + 6) WV(k + 7)
    switch (n < 63 ? n : 63) { WV8(0) WV8(8) WV8(16) WV8(24) WV8(32) WV8(40) WV8(48) WV8(56) }
}
#define SB0() __builtin_amdgcn_sched_barrier(0)
template <int A, int B, class F>
DI void sfor(F f) {
    if constexpr (A < B) {
        f(std::integral_constant<int, A>{});
        sfor<A + 1, B>(f);
    }
}
template <int N> using IC = std::integral_constant<int, N>;
#define AI __attribute__((always_inline))

DI void o_store(const u32x4 (&kf)[6], const u32x4 (&ph)[2][2], __amdgpu_buffer_rsrc_t rsrc, unsigned o_st) {
    const f32x4 zero = {0.f, 0.f, 0.f, 0.f};
#pragma unroll
    for (int i = 0; i < 2; ++i) {
        const bf16x8 p0 = __builtin_bit_cast(bf16x8, ph[i][0]), p1 = __builtin_bit_cast(bf16x8, ph[i][1]);
        f32x4 dn = mfma16(__builtin_bit_cast(bf16x8, kf[4]), p0, zero);
        f32x4 n0 = mfma16(__builtin_bit_cast(bf16x8, kf[0]), p0, zero);
        f32x4 n1 = mfma16(__builtin_bit_cast(bf16x8, kf[1]), p0, zero);
        dn = mfma16(__builtin_bit_cast(bf16x8, kf[5]), p1, dn);
        n0 = mfma16(__builtin_bit_cast(bf16x8, kf[2]), p1, n0);
        n1 = mfma16(__builtin_bit_cast(bf16x8, kf[3]), p1, n1);
        const float rd_ = __builtin_amdgcn_rcpf(rbf(rbf(dn[0]) + 1e-6f));
        const u32x4 nb_ = __builtin_bit_cast(u32x4, pack44(n0, n1));
        u32x4 o;
#pragma unroll
        for (int j = 0; j < 4; ++j) o[j] = pack2(lo16(nb_[j]) * rd_, hi16(nb_[j]) * rd_);
        st_wt(rsrc, o_st + i * 1024, nz4(o));
    }
}

__global__ __launch_bounds__(512, 2) void fused_mix(const u16* __restrict__ x, const u16* __restrict__ wqkv,
                                                    const u16* __restrict__ wphi, const u16* __restrict__ wo,
                                                    const u16* __restrict__ wr, const u16* __restrict__ wg,
                                                    const u16* __restrict__ wu, const u16* __restrict__ wd,
                                                    const u16* __restrict__ gw, char* __restrict__ ws,
                                                    u16* __restrict__ out) {
    __shared__ __attribute__((aligned(16))) char smem[163840];
    const int tid = threadIdx.x, l = tid & 63;
    const int w = __builtin_amdgcn_readfirstlane(tid >> 6) ^ 4;
    const int bid = blockIdx.x, xcd = bid & 7, slot = bid >> 3;
    const int mt = (xcd & 1) * 8 + (slot & 7);
    const int hp = (xcd >> 1) * 4 + (slot >> 3);
    const int rt = (xcd >> 1) * 4 + (slot & 3), cb = (xcd & 1) * 8 + (slot >> 2), rot = 4 * cb + ROTK * rt;
    const unsigned lds0 = lds_of(smem);
    const __amdgpu_buffer_rsrc_t rsrc = __builtin_amdgcn_make_buffer_rsrc(ws, 0, WS_END, 0x00020000);
    const int g = l >> 4, l15 = l & 15, q = l15 >> 2, p = l & 3;
    const int srow = l >> 3, sch = l & 7;
    const unsigned l16 = l * 16;
    unsigned vst = 0;
    const u32x4 z4 = {0u, 0u, 0u, 0u};
    const f32x4 zero = {0.f, 0.f, 0.f, 0.f};
    const int rg = w & 3, kp = w >> 2;
    const int hs = rg >> 1;
    const unsigned AX = lds0 + L_A + rg * 4096 + l16;
    const unsigned W4 = lds0 + p * 4;
    const unsigned WR = lds0 + rg * 4;
    auto mark = [&](unsigned dst) AI {
        asm volatile("s_mov_b32 m0, %2\n\ts_mov_b64 exec, 1\n\tglobal_load_lds_dword %0, %1\n\ts_mov_b64 exec, -1"
                     ::"v"(0u), "s"(k_one), "s"(dst) : "memory");
    };
    auto flag = [&](unsigned a) AI { asm volatile("ds_write_b32 %0, %0" ::"v"(a) : "memory"); };
    auto phi_w = [&](bf16x8 (&a)[2], int h) AI {
        const unsigned ad = lds0 + L_WPHI + h * 2048 + (4 * g + q) * 64 + (((p >> 1) ^ (2 * (g & 1))) * 16) + 8 * (p & 1);
        a[0] = cat8(tr_read(ad), tr_read(ad + 1024));
        a[1] = cat8(tr_read(ad ^ 32), tr_read((ad ^ 32) + 1024));
    };
    const unsigned o_st = WS_O + ((mt * 2 + hs) * 32 + 2 * hp) * 4096 + (rg & 1) * 2048 + l16;
    u32x4 fw[32], gu[2];
    unsigned sel_[4][2];
    u32x4 ph[2][2], sq;
    const int sc = (3 - (int)((unsigned)(size_t)wqkv >> 7)) & 7;
    const bool ffn = (hp & 7) != sc && !(NOF & 1);
    const int ph4 = (slot + (slot >> 3)) & 3;
    const int turn = (ph4 + TRN) & 3;
    const int kq = mt >> 2;
    const int cs = (ffn ? hp - (hp > sc) - (hp > sc + 8) : 14 + (hp >> 3)) * 4 + ((ph4 + CPH - (int)((unsigned)(size_t)wg >> 7)) & 3);
    auto spread = [&](const u32x4& v, int i) -> bf16x8 {
        const unsigned s = v[i >> 1];
        u32x4 a;
#pragma unroll
        for (int d = 0; d < 4; ++d) a[d] = __builtin_amdgcn_perm(s, s, sel_[d][i & 1]);
        return __builtin_bit_cast(bf16x8, a);
    };
    auto fold_store = [&](const f32x4& a, unsigned off) {
        f32x4 v;
#pragma unroll
        for (int i = 0; i < 4; ++i) {
            const unsigned u = __float_as_uint(a[i]);
            const float r = __uint_as_float(__builtin_amdgcn_update_dpp(u, u, 0x128, 0xc, 0xf, false));
            v[i] = swap32_add(r, r);
        }
        const u32x4 o = nz4(v);
        ST_MASK("global_store_dwordx4", 0x00FF00FFull, off + l15 * 32 + g * 16, o, ws);
    };
    auto four = [&](const unsigned (&v)[4], const char* src, unsigned dst) AI {
        asm volatile("s_mov_b32 m0, %5\n\ts_nop 0\n\t"
                     "global_load_lds_dwordx4 %0, %4\n\tglobal_load_lds_dwordx4 %1, %4 offset:1024\n\t"
                     "global_load_lds_dwordx4 %2, %4 offset:2048\n\tglobal_load_lds_dwordx4 %3, %4 offset:3072"
                     ::"v"(v[0]), "v"(v[1]), "v"(v[2]), "v"(v[3]), "s"(src), "s"(dst) : "memory");
    };
    auto sel_init = [&]() AI {
#pragma unroll
        for (int d = 0; d < 4; ++d)
#pragma unroll
            for (int hf = 0; hf < 2; ++hf) {
                const unsigned b = hf ? 0x0302u : 0x0100u;
                sel_[d][hf] = (sch >> 1) != d ? 0x0c0c0c0cu : (l & 1) ? (b << 16) | 0x0c0cu : 0x0c0c0000u | b;
            }
    };
    auto gua = [&](int i) { return ws + WS_GU + cs * 4096 + i * 1024; };
    auto gu_load = [&]() AI {
#pragma unroll
        for (int i = 0; i < 2; ++i) ld_async(gu[i], l16, gua(i));
    };
    auto down = [&]() AI {
#pragma unroll
        for (int i = 0; i < 16; ++i) pin(fw[i]);
        sel_init();
        settle(gu, [&](int) { return l16; }, gua);
        const f32x4 v = __builtin_bit_cast(f32x4, gu[0]) + __builtin_bit_cast(f32x4, gu[1]);
        auto r = __builtin_amdgcn_permlane16_swap(__float_as_uint(swap32_add(v[0], v[2])), __float_as_uint(swap32_add(v[1], v[3])), false, false);
        const float gt = rbf(__uint_as_float(r[0])), up = rbf(__uint_as_float(r[1]));
        const unsigned ha = lds0 + L_A + w * 4096 + 3072;
        lds_write<u16>(ha + l15 * 8 + g * 2, f2bf(rbf(gt / (1.f + __expf(-gt))) * up));
        const u32x4 hv = lds_read<u32x4>(ha + srow * 16);
        f32x4 y0 = zero, y1 = zero;
#pragma unroll
        for (int i = 0; i < 8; ++i) {
            const bf16x8 a = spread(hv, i);
            y0 = mfma16(a, __builtin_bit_cast(bf16x8, fw[i]), y0);
            y1 = mfma16(a, __builtin_bit_cast(bf16x8, fw[8 + i]), y1);
        }
        fold_store(y0, WS_Y + cs * 4096 + kq * 1024 + hs * 512);
        fold_store(y1, WS_Y + cs * 4096 + kq * 1024 + hs * 512 + 256);
    };
    auto wo_load = [&](int n0, int n1, bool mk) AI {
        unsigned vo[4];
#pragma unroll
        for (int i = 0; i < 4; ++i) vo[i] = (unsigned)((8 * i + srow) * 2048 + ((sch ^ ((g & 3) << 1)) * 16) + 4096 - i * 1024);
        const char* wob = (const char*)wo + cb * 128 - 4096;
#pragma unroll
        for (int n = n0; n < ((NOF & 4) ? n0 : n1); ++n) four(vo, wob + ((2 * n + hs + rot) & 31) * 65536, lds0 + wo_unit(n) * 8192 + hs * 4096);
        if (mk) mark(lds0 + (n0 >= WON && WON ? L_FW2 : L_FWD) + hs * 4);
    };

    if (w >= 4) {
        const bool loader = !(w & 1);
        const unsigned kf_kv = WS_KF + (hp * 2 + (mt >> 3)) * 8192 + (mt & 7) * 512;
        const unsigned kf_z = WS_KF + (hp * 2 + (mt >> 3)) * 8192 + 4096 + ((mt >> 2) & 1) * 64;
        unsigned vx[4], vw[4];
        const int ks0 = SLK && (hp & 7) == sc ? SLK * (slot & 7) : KSK * ((slot + (slot >> 3)) & (NPH - 1));
        const char* xb0 = (const char*)(x + (size_t)(mt * 128 + hs * 64) * DM) - 4096;
        const char* wb0 = (const char*)(wqkv + hp * 64) + hs * (32 * 6144) - 4096;
        auto fill = [&](auto N) AI {
            constexpr int n = N, j = F_J[n] < 0 ? 0 : F_J[n], s = F_J[n] < 0 ? -1 - F_J[n] : J_S[j], m = J_M[j];
            const int ks = (ks0 + s) & 15;
            if constexpr (F_J[n] >= 0) {
                four(vw, wb0 + ks * (64 * 6144) + (m == 2 ? 0 : 2048 * (m + 1)), lds0 + J_U[j] * 8192 + hs * 4096);
            } else {
                const char* xs_ = xb0 + ks * 128;
                const unsigned d = lds0 + (hs ? X_U1[s] : X_U0[s]) * 8192;
                four(vx, xs_, d);
                four(vx, xs_ + 32 * 2048, d + 4096);
            }
            if constexpr (F_K[n] == 0) asm volatile("s_barrier" ::: "memory");
            if constexpr (F_K[n] >= 0) mark(lds0 + L_FULL + F_K[n] * 8 + hs * 4);
        };
        const unsigned fgo = ffn ? (unsigned)((256 * kq + 16 * srow) * 7168 + cs * 128 + sch * 16) : 0u;
        const char* fsrc = (const char*)(hs ? wu : wg);
        auto burst = [&](int h, int now) AI {
            if (NOF & 8) return;
            unsigned t1, t2, t3;
            const char* b = fsrc + h * 128 * 7168;
#define FL4(a, b, c, d, s) "global_load_dwordx4 %" #a ", %21, %" #s " nt\n\tglobal_load_dwordx4 %" #b ", %16, %" #s " nt\n\t" \
                           "global_load_dwordx4 %" #c ", %17, %" #s " nt\n\tglobal_load_dwordx4 %" #d ", %18, %" #s " nt\n\t"
            asm volatile("s_cmp_lg_u32 %19, %20\n\ts_cbranch_scc1 .Lk%=\n\t"
                         "v_add_u32 %16, 0x1c00, %21\n\tv_add_u32 %17, 0x3800, %21\n\tv_add_u32 %18, 0x5400, %21\n\t"
                         FL4(0, 1, 2, 3, 22) FL4(4, 5, 6, 7, 23) FL4(8, 9, 10, 11, 24) FL4(12, 13, 14, 15, 25) "\n.Lk%=:"
                         : "+v"(fw[16 * h]), "+v"(fw[16 * h + 1]), "+v"(fw[16 * h + 2]), "+v"(fw[16 * h + 3]), "+v"(fw[16 * h + 4]), "+v"(fw[16 * h + 5]),
                           "+v"(fw[16 * h + 6]), "+v"(fw[16 * h + 7]), "+v"(fw[16 * h + 8]), "+v"(fw[16 * h + 9]), "+v"(fw[16 * h + 10]), "+v"(fw[16 * h + 11]),
                           "+v"(fw[16 * h + 12]), "+v"(fw[16 * h + 13]), "+v"(fw[16 * h + 14]), "+v"(fw[16 * h + 15]), "=&v"(t1), "=&v"(t2), "=&v"(t3)
                         : "s"(turn), "s"(now), "v"(fgo), "s"(b), "s"(b + 4 * 7168), "s"(b + 8 * 7168), "s"(b + 12 * 7168) : "memory", "scc");
        };
        auto fdef = [&]() AI {
#pragma unroll
            for (int i = 0; i < 32; ++i) asm volatile("" : "=v"(fw[i]));
        };
        auto fpin = [&]() AI {
#pragma unroll
            for (int i = 0; i < 32; ++i) pin(fw[i]);
        };
        RS(0);
        if (__builtin_expect(loader, 1)) {
#pragma unroll
            for (int i = 0; i < 4; ++i) {
                vx[i] = (unsigned)((8 * i + srow) * 2048 + ((sch ^ ((4 * (i & 1) + (srow >> 1)) & 7)) * 16) + 4096 - i * 1024);
                vw[i] = (unsigned)((8 * i + srow) * 6144 + (sch ^ (((srow >> 1) & 1) << 1) ^ ((i & 1) << 2)) * 16 + 4096 - i * 1024);
            }
            fdef();
            sfor<0, NF1>([&](auto N) {
                constexpr int n = N;
                if constexpr (n > 0 && F_N[n] > F_N[n > 0 ? n - 1 : 0]) {
                    constexpr int gp = F_N[n] - 1;
                    if constexpr (gp >= FBA && gp < FBA + 4 * FBS && (gp - FBA) % FBS == 0) burst(0, (gp - FBA) / FBS);
                    if constexpr (gp >= FBA + FBH && gp < FBA + FBH + 4 * FBS && (gp - FBA - FBH) % FBS == 0) burst(1, (gp - FBA - FBH) / FBS);
                    RSL(32 + F_N[n]);
                    lds_wait(W4 + L_PROG + (F_N[n]) * 16, 1);
                }
                if constexpr (n == 12) {
                    asm volatile("s_mov_b32 m0, %2\n\ts_nop 0\n\tglobal_load_lds_dwordx4 %0, %1\n\tglobal_load_lds_dwordx4 %0, %1 offset:1024"
                                 ::"v"((unsigned)((l >> 2) * 64 + (((l & 3) ^ (2 * ((l >> 4) & 1))) * 16))),
                                   "s"(wphi + (hp * 2 + hs) * 1024), "s"(lds0 + L_WPHI + hs * 2048) : "memory");
                }
                fill(N);
            });
            flag(lds0 + L_FLD + hs * 4);
            RS(1);
            waitv<28>();
            static_assert(FBA + FBH + 3 * FBS + 1 < NP, "");
            fpin();
        } else {
            const int row0 = (mt * 16 + hp) * 8 + 4 * hs;
            u64 T;
            if (SPRIO) __builtin_amdgcn_s_setprio(SPRIO);
            asm volatile("s_barrier" ::: "memory");
            u32x4 rv[2], gv[2], xv[4][2];
            u64 ev;
            if (RT0) lds_wait8(W4 + L_PROG + RT0 * 16, 1);
            asm volatile("global_load_dwordx2 %4, %5, %9 sc1\n\t"
                         "global_store_dwordx4 %10, %11, %9 sc1\n\t"
                         "global_store_dwordx2 %12, %13, %9 sc1\n\t"
                         "global_load_dwordx4 %0, %6, %7\n\tglobal_load_dwordx4 %1, %6, %7 offset:1024\n\t"
                         "global_load_dwordx4 %2, %6, %8\n\tglobal_load_dwordx4 %3, %6, %8 offset:1024"
                         : "=&v"(rv[0]), "=&v"(rv[1]), "=&v"(gv[0]), "=&v"(gv[1]), "=&v"(ev)
                         : "v"(0u), "v"(l16), "s"(wr), "s"(gw), "s"(ws),
                           "v"(l < 32 ? WS_Y + cs * 4096 + kq * 1024 + hs * 512 + l16 : WS_GU + cs * 4096 + kq * 512 + hs * 256 + l15 * 16), "v"(z4),
                           "v"(hs ? kf_z + sch * 8 : kf_kv + l * 8), "v"(0ull)
                         : "memory");
            RS(1);
#pragma unroll
            for (int r = 0; r < 2; ++r)
                asm volatile("global_load_dwordx4 %0, %4, %5\n\tglobal_load_dwordx4 %1, %4, %5 offset:1024\n\t"
                             "global_load_dwordx4 %2, %4, %5 offset:2048\n\tglobal_load_dwordx4 %3, %4, %5 offset:3072"
                             : "=&v"(xv[2 * r][0]), "=&v"(xv[2 * r][1]), "=&v"(xv[2 * r + 1][0]), "=&v"(xv[2 * r + 1][1])
                             : "v"(l16), "s"(x + (size_t)(row0 + 2 * r) * DM) : "memory");
            SB0();
            waitv<0>();
            asm volatile("" : "+v"(rv[0]), "+v"(rv[1]), "+v"(gv[0]), "+v"(gv[1]), "+v"(ev));
#pragma unroll
            for (int r = 0; r < 4; ++r) asm volatile("" : "+v"(xv[r][0]), "+v"(xv[r][1]));
            RS(2);
            T = ((u64)__builtin_amdgcn_readfirstlane((unsigned)(ev >> 32)) << 32 | __builtin_amdgcn_readfirstlane((unsigned)ev)) + 1;
            float ra_[4] = {0.f, 0.f, 0.f, 0.f}, ga_[4] = {0.f, 0.f, 0.f, 0.f};
#pragma unroll
            for (int r = 0; r < 4; ++r)
#pragma unroll
                for (int i = 0; i < 2; ++i)
#pragma unroll
                    for (int j = 0; j < 4; ++j) {
                        ra_[r] = dot2(xv[r][i][j], rv[i][j], ra_[r]);
                        ga_[r] = dot2(xv[r][i][j], gv[i][j], ga_[r]);
                    }
            const int rr = (g >> 1) | ((g & 1) << 1);
            unsigned k = f2bf(sum4(ra_));
            k ^= (k & 0x8000u) ? 0xFFFFu : 0x8000u;
            k = (k << 3) | (7 - 4 * hs - rr);
            auto r1 = __builtin_amdgcn_permlane16_swap(k, k, false, false);
            k = max(r1[0], r1[1]);
            auto r2 = __builtin_amdgcn_permlane32_swap(k, k, false, false);
            k = max(r2[0], r2[1]);
            if (hs) {
                lds_write<unsigned>(lds0 + L_BEST, k | 0x80000000u);
            } else {
                lds_wait(lds0 + L_BEST, 1);
                const u64 rq = (T << 19) | max(k, lds_read<unsigned>(lds0 + L_BEST) & 0x7FFFFu);
                ST_MASK("global_store_dwordx2", 1ull, 0u, rq, ws + WS_RG + (mt * 16 + hp) * 8);
            }
            const u64 gq = (T << 16) | f2bf(1.f / (1.f + __expf(-rbf(sum4(ga_)))));
            ST_MASK("global_store_dwordx2", 0x0001000100010001ull, (unsigned)(rr * 8), gq, ws + WS_GG + row0 * 8);
            RS(3);
            lds_wait8(W4 + L_PROG + (CLR0 + CLS * ((ph4 + CLP) & 3)) * 16, 1);
#pragma unroll
            for (int n = 0; n < ((NOF & 2) ? 1 : 8); ++n) st_wt(rsrc, WS_O + ((mt * 2 + (n >> 2)) * 32 + 2 * hp + hs) * 4096 + (n & 3) * 1024 + l16, z4);
#pragma unroll
            for (int n = 0; n < 4; ++n) st_wt(rsrc, WS_KV + ((hp * 8 + hs * 4 + n) * 16 + mt) * 1024 + l16, z4);
            ST_MASK("global_store_dwordx4", 0xFFFFull, WS_Z + ((hp * 4 + hs * 2 + (srow & 1)) * 16 + mt) * 128 + sch * 16, z4, ws);
            waitv<0>();
            ST_MASK("global_store_dwordx2", 1ull, 0u, T, ws + WS_CL + ((mt * 2 + hs) * 16 + hp) * 8);
            if (hs) {
                u32x4 sv[2], xs;
                lds_wait8(W4 + L_PROG + (SEL0) * 16, 1);
                const unsigned tl = (unsigned)T & 0x1FFFu, thi = (unsigned)(T >> 13);
#pragma unroll 1
                for (unsigned s = 0; s < SPIN_CAP; ++s) {
                    if (s) __builtin_amdgcn_s_sleep(16);
                    asm volatile("global_load_dwordx4 %0, %2, %3 sc1\n\tglobal_load_dwordx4 %1, %2, %3 offset:16 sc1\n\ts_waitcnt vmcnt(0)"
                                 : "=&v"(sv[0]), "=&v"(sv[1]) : "v"(l * 32), "s"(ws + WS_RG) : "memory");
                    const unsigned bad = ((sv[0][0] >> 19) ^ tl) | (sv[0][1] ^ thi) | ((sv[0][2] >> 19) ^ tl) | (sv[0][3] ^ thi) |
                                         ((sv[1][0] >> 19) ^ tl) | (sv[1][1] ^ thi) | ((sv[1][2] >> 19) ^ tl) | (sv[1][3] ^ thi);
                    if (__all(bad == 0u)) break;
                }
                RS(4);
                unsigned k2 = 0;
#pragma unroll
                for (int j = 0; j < 4; ++j) {
                    const unsigned v = sv[j >> 1][(j & 1) * 2] & 0x7FFFFu;
                    k2 = max(k2, ((v >> 3) << 11) | ((255 - (4 * l + j)) << 3) | (v & 7));
                }
                const unsigned key = __builtin_amdgcn_readfirstlane(wave_umax(k2));
                asm volatile("global_load_dwordx4 %0, %1, %2" : "=&v"(xs) : "v"((unsigned)(512 * kq + (l & 31) * 16)), "s"(x + (size_t)(2047 - (int)(key & 2047u)) * DM) : "memory");
                unsigned rb = key >> 11;
                rb ^= (rb & 0x8000u) ? 0x8000u : 0xFFFFu;
                const float rsel = rbf(1.f / (1.f + __expf(-bf2f((u16)rb))));
                lds_write<u32x4>(lds0 + L_SEL, (u32x4){(unsigned)T, (unsigned)(T >> 32), 2047u - (key & 2047u), __float_as_uint(rsel)});
                if (bid == 0) ST_MASK("global_store_dwordx2", 1ull, 0u, T, ws + WS_E);
                waitv<0>();
                pin(xs);
                lds_write<u32x4>(lds0 + L_XS + (l & 31) * 16, xs);
                RS(5);
            }
            fdef();
            if (FBL) lds_wait8(lds0 + L_FLD + (l & 1) * 4, 1);
            else lds_wait8(W4 + L_PROG + (FBB) * 16, 1);
        }
        const char* cla = ws + WS_CL + ((l15 * 2 + (mt >> 3)) * 16 + hp) * 8;
        u64 cl;
        unsigned pf;
        auto wo_warm = [&]() AI { asm volatile("global_load_dword %0, %1, %2" : "=&v"(pf) : "v"((unsigned)(((rt & 3) * 256 + rg * 64 + l) * 2048)), "s"((const char*)wo + cb * 128) : "memory"); };
        RS(6);
        {
            const unsigned fdo = ffn ? (unsigned)((64 * cs + 8 * srow) * 2048 + 512 * kq + 256 * hs + sch * 16) : 0u;
#pragma unroll
            for (int h = 0; h < 2; ++h)
                asm volatile("s_bitcmp1_b32 %8, 0\n\ts_cbranch_scc0 .Lk%=\n\t"
                             "global_load_dwordx4 %0, %9, %10 offset:-4096 nt\n\tglobal_load_dwordx4 %1, %9, %10 offset:-2048 nt\n\t"
                             "global_load_dwordx4 %2, %9, %10 nt\n\tglobal_load_dwordx4 %3, %9, %10 offset:2048 nt\n\t"
                             "global_load_dwordx4 %4, %9, %11 offset:-4096 nt\n\tglobal_load_dwordx4 %5, %9, %11 offset:-2048 nt\n\t"
                             "global_load_dwordx4 %6, %9, %11 nt\n\tglobal_load_dwordx4 %7, %9, %11 offset:2048 nt\n.Lk%=:"
                             : "+v"(fw[8 * h]), "+v"(fw[8 * h + 1]), "+v"(fw[8 * h + 2]), "+v"(fw[8 * h + 3]),
                               "+v"(fw[8 * h + 4]), "+v"(fw[8 * h + 5]), "+v"(fw[8 * h + 6]), "+v"(fw[8 * h + 7])
                             : "s"((NOF & 8) ? 0 : w), "v"(fdo), "s"((const char*)wd + 4096 + h * 128), "s"((const char*)wd + 12288 + h * 128) : "memory", "scc");
        }
        if (WPF == 1) wo_warm();
        RS(7);
        if (HPRIO) __builtin_amdgcn_s_setprio(HPRIO);
        {
            bf16x8 a0[2], a1[2], kb[4];
            f32x4 pk[2][2][2];
            unsigned pw[4];
            phi_w(a0, 0);
            phi_w(a1, 1);
#pragma unroll
            for (int i = 0; i < 4; ++i) pw[i] = (lds0 + P_U[0] * 8192 + rg * 4096 + l15 * 128 + ((g ^ fsw(l15)) << 3)) ^ (i << 5);
            asm volatile("" : "+v"(pw[0]), "+v"(pw[1]), "+v"(pw[2]), "+v"(pw[3]));
            lds_wait(WR + L_FK, 1);
            RS(8);
#pragma unroll
            for (int i = 0; i < 4; ++i) kb[i] = lds_read128(AX + i * 1024);
#pragma unroll
            for (int i = 0; i < 4; ++i)
#pragma unroll
                for (int fb = 0; fb < 2; ++fb) {
                    pk[i >> 1][i & 1][fb] = mfma16(i < 2 ? a0[fb] : a1[fb], kb[i], zero);
                    if (KRND) pk[i >> 1][i & 1][fb] = rbf4(pk[i >> 1][i & 1][fb]);
                }
            SB0();
            flag(WR + L_FKT);
            RS(9);
            phi_k<KPK & 1>(pk[0], pw, 0, [&]() AI {
                lds_wait(W4 + L_FK, 1);
                if (loader) sfor<NF1, NF>(fill);
            });
            SB0();
#pragma unroll
            for (int i = 0; i < 4; ++i) pw[i] += (P_U[2] - P_U[0]) * 8192;
            if (WPF == 2) wo_warm();
            asm volatile("global_load_dwordx2 %0, %1, off sc1" : "=&v"(cl) : "v"(cla) : "memory");
            RS(10);
            phi_k<(KPK >> 1)>(pk[1], pw, 0, []() {});
            SB0();
            flag(WR + L_FP);
        }
        RS(11);
        {
            bf16x8 ones;
#pragma unroll
            for (int j = 0; j < 8; ++j) ones[j] = (short)0x3f80;
            const unsigned ra = lds0 + (hs ? P_U[2] : P_U[0]) * 8192 + (4 * g + q) * 128 + (((8 * (rg & 1) + p) ^ fsw(4 * g + q)) << 3);
            const unsigned va = lds0 + L_A + hs * 2048 + l16;
            bf16x8 b0[4], b1[4], af[2][4];
            lds_wait(lds0 + L_FV + sch * 4, 1);
            RS(12);
#pragma unroll
            for (int i = 0; i < 4; ++i) {
                af[0][i] = cat8(tr_read(ra + i * 4096), tr_read(ra + i * 4096 + 2048));
                b0[i] = lds_read128(va + i * 4096);
                b1[i] = lds_read128(va + i * 4096 + 1024);
            }
            sq = lds_read<u32x4>(lds0 + L_SEL);
#pragma unroll
            for (int i = 0; i < 4; ++i) af[1][i] = cat8(tr_read((ra ^ 32) + i * 4096), tr_read((ra ^ 32) + i * 4096 + 2048));
            SB0();
            f32x4 k0[2], k1[2], zt[2];
#pragma unroll
            for (int e = 0; e < 2; ++e) {
                k0[e] = k1[e] = zt[e] = zero;
#pragma unroll
                for (int i = 0; i < 4; ++i) {
                    k0[e] = mfma16(af[e][i], b0[i], k0[e]);
                    k1[e] = mfma16(af[e][i], b1[i], k1[e]);
                    zt[e] = mfma16(af[e][i], ones, zt[e]);
                }
            }
            const unsigned kv_st = WS_KV + ((hp * 8 + rg * 2) * 16 + mt) * 1024 + l16;
            st_wt(rsrc, kv_st, nz4(__builtin_bit_cast(u32x4, pack44(k0[0], k0[1]))));
            st_wt(rsrc, kv_st + 16384, nz4(__builtin_bit_cast(u32x4, pack44(k1[0], k1[1]))));
            const u32x4 zo = nz4((l & 1) ? zt[1] : zt[0]);
            ST_MASK("global_store_dwordx4", 0x0003000300030003ull, WS_Z + ((hp * 4 + rg) * 16 + mt) * 128 + (l & 1) * 64 + g * 16, zo, ws);
            SB0();
            flag(WR + L_FKS);
        }
        RS(13);
        auto gate_up = [&]() AI {
            sel_init();
            u32x4 xs[4];
#pragma unroll
            for (int i = 0; i < 4; ++i) xs[i] = lds_read<u32x4>(lds0 + L_XS + (i >> 1) * 256 + srow * 32 + (i & 1) * 16);
            f32x4 a = zero;
#pragma unroll
            for (int i = 0; i < 32; ++i) a = mfma16(spread(xs[i >> 3], i & 7), __builtin_bit_cast(bf16x8, fw[i]), a);
            fold_store(a, WS_GU + cs * 4096 + kq * 512 + hs * 256);
            SB0();
        };
        waitv<3>();
        asm volatile("" : "+v"(cl));
        if (WPF) asm volatile("" : "+v"(pf));
        if (loader) {
            __builtin_amdgcn_s_setprio(0);
            if (GUF) gate_up();
            if (WOE) {
                lds_wait(lds0 + L_FKS + (l & 8 ? L_FGI - L_FKS + (l & 1) * 4 : p * 4), 1);
                wo_load(0, WOE, WOE == WON);
            }
            RS(14);
            if (!GUF) gate_up();
            RS(15);
            if (WON > WOE) {
                lds_wait(lds0 + L_FKS + (l15 < (WG2 ? 8 : 10) ? l15 : 0) * 4, 1);
                wo_load(WOE, WON, true);
            }
        } else {
            {
                u32x4 kp_[6];
                const char* kb = ws + WS_KV + (hp * 8 + (mt >> 1)) * 16384 + (mt & 1) * 512 + hs * 256;
                const bool zw = hs && !(mt & 3);
                const char* zb = zw ? ws + WS_Z + (hp * 4 + (mt >> 2)) * 2048 : kb;
                const unsigned kvo = g * 1024 + l15 * 16, zo = zw ? l15 * 128 + g * 16 : 0u;
                auto ko = [&](int i) { return i < 4 ? kvo : zo; };
                auto ka = [&](int i) { return i < 4 ? kb + i * 4096 : zb + (i - 4) * 64; };
                const u64 T = (u64)sq[1] << 32 | sq[0];
#pragma unroll 1
                for (unsigned s = 0; s < SPIN_CAP && __ballot(cl != T); ++s)
                    asm volatile("global_load_dwordx2 %0, %1, off sc1\n\ts_waitcnt vmcnt(0)" : "=&v"(cl) : "v"(cla) : "memory");
                if (GW) lds_wait(W4 + L_FKS, 1);
#pragma unroll
                for (int i = 0; i < 6; ++i) ld_async(kp_[i], ko(i), ka(i));
                flag(lds0 + L_FGI + hs * 4);
                SB0();
                RS(14);
                waitv<0>();
                RS(15);
                settle(kp_, ko, ka);
                RS(24);
                float v[8], u[4];
#pragma unroll
                for (int e = 0; e < 4; ++e) {
                    v[2 * e] = (lo16(kp_[0][e]) + lo16(kp_[1][e])) + (lo16(kp_[2][e]) + lo16(kp_[3][e]));
                    v[2 * e + 1] = (hi16(kp_[0][e]) + hi16(kp_[1][e])) + (hi16(kp_[2][e]) + hi16(kp_[3][e]));
                }
#pragma unroll
                for (int e = 0; e < 4; ++e) u[e] = swap32_add(v[e], v[e + 4]);
                const unsigned o = nz1(pack2(swap16_add(u[0], u[2]), swap16_add(u[1], u[3])));
                ST_MASK("global_store_dword", ~0ull, kf_kv + hs * 256 + l15 * 16 + g * 4, o, ws);
                if (zw) {
                    f32x4 z[2];
#pragma unroll
                    for (int e = 0; e < 2; ++e)
#pragma unroll
                        for (int i = 0; i < 4; ++i) z[e][i] = dpp_add<0x140>(dpp_add<0x141>(dpp_add<0x4E>(dpp_add<0xB1>(__uint_as_float(kp_[4 + e][i])))));
                    const u32x4 oz = nz4(__builtin_bit_cast(u32x4, pack44(z[0], z[1])));
                    ST_MASK("global_store_dwordx4", 0x0001000100010001ull, kf_z + g * 16, oz, ws);
                }
                flag(lds0 + L_FG + hs * 4);
            }
            RS(25);
            u32x4 kf[6];
            const char* kfb = ws + WS_KF + (hp * 2 + hs) * 8192;
            auto fo = [&](int i) { return i < 4 ? l16 : (unsigned)g * 16; };
            auto fb = [&](int i) { return i < 4 ? kfb + i * 1024 : kfb + 4096 + (i - 4) * 64; };
            if (KFD) __builtin_amdgcn_s_sleep(KFD);
#pragma unroll
            for (int i = 0; i < 6; ++i) ld_async(kf[i], fo(i), fb(i));
            waitv<0>();
            RS(29);
            settle(kf, fo, fb);
#pragma unroll
            for (int i = 0; i < 4; ++i) lds_write<u32x4>(lds0 + L_KF + hs * 4096 + i * 1024 + l16, kf[i]);
#pragma unroll
            for (int i = 0; i < 2; ++i) lds_write<u32x4>(lds0 + L_KZ + hs * 128 + i * 64 + g * 16, kf[4 + i]);
            SB0();
            flag(lds0 + L_FKF + hs * 4);
        }
        RS(26);
        lds_wait(WR + L_FQ, 1);
#pragma unroll
        for (int i = 0; i < 4; ++i) ph[i >> 1][i & 1] = lds_read<u32x4>(AX + i * 1024);
    } else {
        RS(0);
        lds_write<u32x4>(lds0 + L_SM + l16, z4);
        BAR();
        if (CPRIO) __builtin_amdgcn_s_setprio(CPRIO);
        unsigned ra[4], xa[2];
#pragma unroll
        for (int u = 0; u < 4; ++u) ra[u] = lds0 + (J_U[0] >> 3) * 65536 + (8 * g + q) * 128 + ((u ^ (q >> 1) ^ ((g & 1) << 1)) * 32) + 8 * p;
#pragma unroll
        for (int ks = 0; ks < 2; ++ks) xa[ks] = lds0 + (32 * (w & 1) + l15) * 128 + (((4 * ks + g) ^ (l15 >> 1)) * 16);
        const unsigned fa = lds0 + L_FULL + (l & 1) * 4;
        f32x4 acc[3][4][2];
#pragma unroll
        for (int m = 0; m < 3; ++m)
#pragma unroll
            for (int u = 0; u < 4; ++u) acc[m][u][0] = acc[m][u][1] = zero;
        bf16x8 xf[ND][2][2];
        s16x4 wh[PD + 1][2];
        unsigned fv = 0, xt;
        auto chk = [&](unsigned a, int o) AI {
            SB0();
            if (__builtin_expect(__builtin_amdgcn_ballot_w64(fv == 0u) != 0ull, 0)) {
                unsigned c_;
                asm volatile("s_mov_b32 %1, 0x400000\n"
                             ".Lp%=:\n\tds_read_b32 %0, %2 offset:%3\n\ts_waitcnt lgkmcnt(0)\n\tv_cmp_eq_u32 vcc, 0, %0\n\ts_cbranch_vccz .Lf%=\n\t"
                             "s_sub_u32 %1, %1, 1\n\ts_cbranch_scc0 .Lp%=\n.Lf%=:"
                             : "=&v"(fv), "=&s"(c_) : "v"(a), "n"(o) : "memory", "vcc", "scc");
            }
            SB0();
        };
        auto rd1 = [&](auto F, auto H) AI {
            constexpr int f = F, h = H, j = f >> 3, e = f & 7, ks = e >> 2, u = e & 3;
            if constexpr (h == 0 && e == 0 && J_C[j] >= 0) chk(fa, J_C[j] * 8);
            if constexpr (h == 0 && e < 4 && j > 0 && (J_U[j] >> 3) != (J_U[j > 0 ? j - 1 : 0] >> 3)) {
                ra[u] += ((J_U[j] >> 3) - (J_U[j > 0 ? j - 1 : 0] >> 3)) * 65536;
                asm volatile("" : "+v"(ra[u]));
            }
            wh[f % (PD + 1)][h] = tr_read(ra[u] + (J_U[j] & 7) * 8192 + ks * 4096 + h * 512);
            if constexpr ((ks ? J_XL[j] : J_XE[j]) >= 0 && (e & 1) == 0) {
                constexpr int s = (ks ? J_XL[j] : J_XE[j]) & 15;
                if constexpr (h == 0) {
                    xt = xa[(e >> 1) & 1] + (hs ? X_U1[s] : X_U0[s]) * 8192;
                    asm volatile("" : "+v"(xt));
                }
                xf[s % ND][h][(e >> 1) & 1] = lds_read128(xt + h * 2048);
            }
            if constexpr (h == 1 && e == 4 && j < 47 && J_C[j < 47 ? j + 1 : 0] >= 0) fv = *(volatile AS3 unsigned*)(size_t)(fa + J_C[j < 47 ? j + 1 : 0] * 8);
        };
        RS(1);
        sfor<0, PD>([&](auto F) { rd1(F, IC<0>{}); rd1(F, IC<1>{}); });
        RS(2);
        constexpr int KD = 3 * NA + ND, VD = KD + ND;
        sfor<0, 48>([&](auto J) {
            sfor<0, 8>([&](auto E) {
                constexpr int j = J, e = E, f = 8 * j + e, ks = e >> 2, u = e & 3, s = J_S[j], m = J_M[j];
                const bf16x8 wv = cat8(wh[f % (PD + 1)][0], wh[f % (PD + 1)][1]);
                sfor<0, 2>([&](auto RB) {
                    constexpr int rb = RB;
                    const bf16x8 xv = xf[s % ND][rb][ks];
                    if (m == 1) acc[m][u][rb] = mfma16(xv, wv, acc[m][u][rb]);
                    else acc[m][u][rb] = mfma16(wv, xv, acc[m][u][rb]);
                    SB0();
                    if constexpr (f + PD < 384) rd1(IC<f + PD>{}, RB);
                    if constexpr (j == KD && e < 2) lds_write<bf16x8>(AX + (2 * e + rb) * 1024, pack44(acc[0][2 * e][rb], acc[0][2 * e + 1][rb]));
                    if constexpr (j == VD && e < 2) lds_write<bf16x8>(AX + (2 * e + rb) * 1024, pack44(acc[1][2 * e + rb][0], acc[1][2 * e + rb][1]));
                    SB0();
                });
                if constexpr (j == KD && e == 1) { flag(WR + L_FK); RS(4); }
                if constexpr (j == VD && e == 1) { flag(WR + L_FV); RS(5); if (QW) lds_wait(WR + (QW == 1 ? L_FP : L_FKS), 1); RS(10); }
                if constexpr (j == VD - 1 && e == 7) lds_wait(WR + L_FKT, 1);
                if constexpr (e == 7) {
                    if constexpr (J_P[j] > 0) {
                        constexpr int n = J_P[j];
                        if constexpr (n >= PWL) flag(WR + L_PROG + n * 16);
                        else if constexpr (PWN == 0 || n <= FBA || n > FBA + 3 * FBS + PWN) mark(lds0 + L_PROG + n * 16 + w * 4);
                        else if (__builtin_expect((unsigned)(n - FBA - 1 - FBS * turn) < PWN, 0)) flag(WR + L_PROG + n * 16);
                        else mark(lds0 + L_PROG + n * 16 + w * 4);
                    }
                    SB0();
                    if constexpr (j % 3 == 2) RSC(32 + j / 3);
                    if constexpr (j == 3 * NA - 1) RS(3);
                }
            });
        });
        RS(6);
        if (CPRIO) __builtin_amdgcn_s_setprio(0);
        flag(WR + L_FQD);
        bf16x8 a0[2], a1[2];
        f32x4 pq[2][2][2];
        phi_w(a1, 1);
        phi_w(a0, 0);
#pragma unroll
        for (int h = 1; h >= 0; --h)
#pragma unroll
            for (int rb = 0; rb < 2; ++rb) {
                const bf16x8 b = pack44(acc[2][2 * h][rb], acc[2][2 * h + 1][rb]);
#pragma unroll
                for (int fb = 0; fb < 2; ++fb) pq[h][rb][fb] = rbf4(mfma16(h ? a1[fb] : a0[fb], b, zero));
            }
        softmax2<true>(pq[1], ph, []() {});
        RS(7);
        lds_wait(W4 + L_FKS, 1);
#pragma unroll
        for (int i = 0; i < 4; ++i) lds_write<u32x4>(AX + i * 1024, ph[i >> 1][i & 1]);
        SB0();
        flag(WR + L_FQ);
        sq = lds_read<u32x4>(lds0 + L_SEL);
        RS(8);
        softmax2<true>(pq[0], ph, []() {});
        asm volatile("" : "+v"(ph[0][0]), "+v"(ph[0][1]), "+v"(ph[1][0]), "+v"(ph[1][1]));
        RS(9);
        RS(11);
    }
    const char* cla = ws + WS_CL + ((rt * 2 + kp) * 16 + l15) * 8;
    u64 cl = ld_agent(cla);
    {
        u32x4 kf[6];
        lds_wait(lds0 + L_FKF + kp * 4, 1);
#pragma unroll
        for (int i = 0; i < 4; ++i) kf[i] = lds_read<u32x4>(lds0 + L_KF + kp * 4096 + i * 1024 + l16);
#pragma unroll
        for (int i = 0; i < 2; ++i) kf[4 + i] = lds_read<u32x4>(lds0 + L_KZ + kp * 128 + i * 64 + g * 16);
        lds_set(lds0 + L_FWO + w * 4);
        RS(27);
        o_store(kf, ph, rsrc, o_st + kp * 4096);
        RS(28);
    }
    __builtin_amdgcn_s_setprio(0);
    if ((w & 5) == 5) gu_load();
    if (WON < 16) {
        flag(lds0 + L_FOS + w * 4);
        if ((w & 5) == 4) {
            lds_wait(lds0 + L_FOS + sch * 4, 1);
            wo_load(WON, 16, true);
        }
    }
    const int row = rt * 128 + rg * 32 + kp * 16 + l15;
    const int col = cb * 64 + (g & 1) * 16 + (g >> 1) * 8;
    const u64 T = (u64)sq[1] << 32 | sq[0];
    const u32x2 selv = {sq[2], sq[3]};

    RS(30);
    const u32x4 xr0 = *(const u32x4*)(x + (size_t)row * DM + col), xr1 = *(const u32x4*)(x + (size_t)row * DM + col + 32);
    u64 gg = ld_agent(ws + WS_GG + row * 8);
    SB0();
#pragma unroll 1
    for (unsigned s = 0; s < SPIN_CAP && __ballot(cl != T); ++s) cl = ld_agent(cla);
    const char* ob = ws + WS_O + (rt * 2 + (rg >> 1)) * 131072 + (rg & 1) * 2048;
    const unsigned ov0 = l16 + kp * 1024, ov1 = l16 + (kp ^ 1) * 1024;
    auto oa = [&](int i) { return ob + ((2 * i + kp + rot) & 31) * 4096; };
    unsigned pv = 0, rdy = 0;
    const unsigned pof = ((2 * l15 + kp + rot) & 31) * 4096 + 2044;
    auto poll = [&]() {
        u64 keep;
        asm volatile("s_mov_b64 %1, exec\n\ts_mov_b64 exec, %4\n\tglobal_load_dword %0, %2, %3 sc1\n\ts_mov_b64 exec, %1\n\ts_waitcnt vmcnt(0)"
                     : "+&v"(pv), "=&s"(keep) : "v"(pof), "s"(ob), "s"((u64)(~rdy & 0xFFFFu)) : "memory");
        rdy = (unsigned)__ballot(pv != 0u);
    };
    constexpr int OW = OWIN;
    u32x4 of[OW][4];
    auto fetch = [&](int k) {
#pragma unroll 1
        for (unsigned s = 0; k < NPL && s < SPIN_CAP && (~rdy & (3u << (2 * k))); ++s) poll();
#pragma unroll
        for (int j = 0; j < 4; ++j) ld_async(of[k % OW][j], (j & 1) ? ov1 : ov0, oa(2 * k + (j >> 1)));
    };
    const bool own = (__builtin_amdgcn_readfirstlane(selv[0]) >> 4) == (unsigned)(rt * 8 + rg * 2 + kp);
    if (NPL) poll();
    RS(16);
#pragma unroll
    for (int k = 0; k < OW - 1; ++k) fetch(k);
    RS(17);
    f32x4 acc[2][4];
#pragma unroll
    for (int i = 0; i < 4; ++i) acc[0][i] = acc[1][i] = zero;
    const unsigned wa = lds0 + 4096 * kp + (4 * g + q) * 128 + ((((g & 1) << 1) | (q >> 1)) * 32) + 8 * p;
    bf16x8 wf[2][2][4];
    auto wrd = [&](int k) {
#pragma unroll
        for (int hh = 0; hh < 2; ++hh)
#pragma unroll
            for (int nb = 0; nb < 4; ++nb) {
                const unsigned a = (wa ^ (nb << 5)) + wo_unit(2 * k + hh) * 8192;
                wf[k & 1][hh][nb] = cat8(tr_read(a), tr_read(a + 2048));
            }
    };
    u32x4 yp[14];
    const unsigned yo = g * 4096 + cb * 256 + l15 * 16;
    auto ya = [&](int i) { return ws + WS_Y + i * 16384; };
    lds_wait(lds0 + L_FWD + ((l & 8) ? 16 + sch * 4 : (l & 1) * 4), 1);
    RS(18);
    if ((w & 5) == 5) down();
    RS(31);
    wrd(0);
#pragma unroll
    for (int k = 0; k < 8; ++k) {
        if (k + OW - 1 < 8) fetch(k + OW - 1);
        waitvn(4 * (k + OW - 1 < 8 ? OW - 1 : 7 - k));
        settle(of[k % OW], [&](int j) { return (j & 1) ? ov1 : ov0; }, [&](int j) { return oa(2 * k + (j >> 1)); });
        if (k == 7 && own) {
#pragma unroll
            for (int i = 0; i < 14; ++i) ld_async(yp[i], yo, ya(i));
        }
        SB0();
        if (WON < 16 && 2 * (k + 1) == WON) lds_wait(lds0 + L_FW2 + (l & 1) * 4, 1);
        if (k < 7) wrd(k + 1);
#pragma unroll
        for (int j = 0; j < 4; ++j)
#pragma unroll
            for (int nb = 0; nb < 4; ++nb) acc[j & 1][nb] = mfma16(wf[k & 1][j >> 1][nb], __builtin_bit_cast(bf16x8, of[k % OW][j]), acc[j & 1][nb]);
        if (k < 7) {
#pragma unroll
            for (int i = 0; i < 16; ++i) {
                __builtin_amdgcn_sched_group_barrier(0x008, 1, 0);
                __builtin_amdgcn_sched_group_barrier(0x100, 1, 0);
            }
        }
        SB0();
        if (k == 0) RS(19);
        if (k == 3) RS(23);
    }
    RS(20);
    const unsigned ea = lds0 + L_A + l16 + 4096 * w, eb = lds0 + L_A + 4096 * (w ^ 4);
    lds_write<u32x4>(ea, nz4(acc[1][0]));
#pragma unroll
    for (int nb = 1; nb < 4; ++nb) lds_write<f32x4>(ea + nb * 1024, acc[1][nb]);
    BAR();
    RS(21);
#pragma unroll
    for (int nb = 0; nb < 4; ++nb) acc[0][nb] += lds_read<f32x4>(eb + l16 + nb * 1024);
    if (own) {
        waitv<0>();
        settle(yp, [&](int) { return yo; }, ya);
        f32x4 yv = __builtin_bit_cast(f32x4, yp[0]);
#pragma unroll
        for (int i = 1; i < 14; ++i) yv += __builtin_bit_cast(f32x4, yp[i]);
#pragma unroll
        for (int i = 0; i < 4; ++i) {
            yv[i] = swap16_add(yv[i], yv[i]);
            yv[i] = rbf(rbf(swap32_add(yv[i], yv[i])) * __uint_as_float(selv[1]));
        }
        asm volatile("s_waitcnt lgkmcnt(0)" ::: "memory");
        lds_write<f32x4>(eb + l16, yv);
    }
#pragma unroll 1
    for (unsigned s = 0; s < SPIN_CAP && (gg >> 16) != (T & 0xFFFFFFFFFFFFull); ++s) gg = ld_agent(ws + WS_GG + row * 8);
    const float gv_ = bf2f((u16)gg);
    const float og = rbf(1.f - gv_);
#pragma unroll
    for (int i = 0; i < 2; ++i) {
        unsigned a0 = pack2(acc[0][2 * i][0], acc[0][2 * i][1]), a1 = pack2(acc[0][2 * i][2], acc[0][2 * i][3]);
        unsigned b0 = pack2(acc[0][2 * i + 1][0], acc[0][2 * i + 1][1]), b1 = pack2(acc[0][2 * i + 1][2], acc[0][2 * i + 1][3]);
        auto s0_ = __builtin_amdgcn_permlane16_swap(a0, b0, false, false);
        auto s1_ = __builtin_amdgcn_permlane16_swap(a1, b1, false, false);
        const unsigned av_[4] = {s0_[0], s1_[0], s0_[1], s1_[1]};
        u32x4 bv = i ? xr1 : xr0;
        if (row == (int)selv[0]) {
            const unsigned yb = eb + (col & 63) * 4 + 128 * i;
            const f32x4 y0 = lds_read<f32x4>(yb), y1 = lds_read<f32x4>(yb + 16);
            bv[0] = pack2(lo16(bv[0]) + y0[0], hi16(bv[0]) + y0[1]);
            bv[1] = pack2(lo16(bv[1]) + y0[2], hi16(bv[1]) + y0[3]);
            bv[2] = pack2(lo16(bv[2]) + y1[0], hi16(bv[2]) + y1[1]);
            bv[3] = pack2(lo16(bv[3]) + y1[2], hi16(bv[3]) + y1[3]);
        }
        u32x4 o;
#pragma unroll
        for (int j = 0; j < 4; ++j) {
            const unsigned ga = pack2(gv_ * lo16(av_[j]), gv_ * hi16(av_[j]));
            const unsigned gb = pack2(og * lo16(bv[j]), og * hi16(bv[j]));
            o[j] = pack2(lo16(ga) + lo16(gb), hi16(ga) + hi16(gb));
        }
        *(u32x4*)(out + (size_t)row * DM + col + 32 * i) = o;
    }
    RS(22);
    RSDUMP();
}

extern "C" void kernel_launch(void* const* d_in, const int* in_sizes, int n_in, void* d_out, int out_size,
                              void* d_ws, size_t ws_size, hipStream_t stream) {
    fused_mix<<<256, 512, 0, stream>>>((const u16*)d_in[0], (const u16*)d_in[1], (const u16*)d_in[2], (const u16*)d_in[3],
                                       (const u16*)d_in[5], (const u16*)d_in[6], (const u16*)d_in[7], (const u16*)d_in[8],
                                       (const u16*)d_in[9], (char*)d_ws, (u16*)d_out);
}
```

```cpp
#include <hip/hip_runtime.h>
#include <hip/hip_bf16.h>
#include <stdint.h>
#include <type_traits>

typedef __attribute__((ext_vector_type(8))) short bf16x8;
typedef __attribute__((ext_vector_type(4))) short s16x4;
typedef __attribute__((ext_vector_type(4))) float f32x4;
typedef __attribute__((ext_vector_type(2))) float f32x2;
typedef __attribute__((ext_vector_type(2))) unsigned u32x2;
typedef __attribute__((ext_vector_type(4))) unsigned u32x4;
typedef __attribute__((ext_vector_type(2))) __bf16 bf16x2_t;
typedef unsigned short u16;
typedef unsigned long long u64;

#define DI __device__ __forceinline__
#define AS3 __attribute__((address_space(3)))
#define AS1 __attribute__((address_space(1)))
#define RLX_AGENT __ATOMIC_RELAXED, __HIP_MEMORY_SCOPE_AGENT

constexpr int SEQ = 2048, DM = 1024, NH = 32, FF = 3584;

DI float bf2f(u16 v) { return __uint_as_float(((unsigned)v) << 16); }
DI u16 f2bf(float f) { __bf16 b = (__bf16)f; return __builtin_bit_cast(u16, b); }
DI float rbf(float f) { return bf2f(f2bf(f)); }
DI unsigned pack2(float lo, float hi) {
    const f32x2 v = {lo, hi};
    return __builtin_bit_cast(unsigned, __builtin_convertvector(v, bf16x2_t));
}
DI float lo16(unsigned u) { return __uint_as_float(u << 16); }
DI float hi16(unsigned u) { return __uint_as_float(u & 0xffff0000u); }

DI s16x4 tr_read(unsigned a) { return __builtin_amdgcn_ds_read_tr16_b64_v4i16((AS3 s16x4*)(size_t)a); }
DI bf16x8 lds_read128(unsigned a) { return *(AS3 const bf16x8*)(size_t)a; }
template <class T> DI void lds_write(unsigned a, T v) { *(AS3 T*)(size_t)a = v; }
template <class T> DI T lds_read(unsigned a) { return *(AS3 const T*)(size_t)a; }
DI bf16x8 cat8(s16x4 a, s16x4 b) { return __builtin_shufflevector(a, b, 0, 1, 2, 3, 4, 5, 6, 7); }
DI f32x4 mfma16(bf16x8 a, bf16x8 b, f32x4 c) { return __builtin_amdgcn_mfma_f32_16x16x32_bf16(a, b, c, 0, 0, 0); }
DI bf16x8 pack44(const f32x4& a, const f32x4& b) {
    u32x4 r;
    r[0] = pack2(a[0], a[1]);
    r[1] = pack2(a[2], a[3]);
    r[2] = pack2(b[0], b[1]);
    r[3] = pack2(b[2], b[3]);
    return __builtin_bit_cast(bf16x8, r);
}
DI unsigned lds_of(const void* p) { return (unsigned)(size_t)(AS3 const char*)p; }

template <int CTRL>
DI unsigned dpp(unsigned x) { return __builtin_amdgcn_update_dpp(0u, x, CTRL, 0xf, 0xf, true); }
template <int CTRL>
DI float dpp_add(float x) { return x + __uint_as_float(dpp<CTRL>(__float_as_uint(x))); }
DI float swap32_add(float a, float b) {
    auto r = __builtin_amdgcn_permlane32_swap(__float_as_uint(a), __float_as_uint(b), false, false);
    return __uint_as_float(r[0]) + __uint_as_float(r[1]);
}
DI float swap16_add(float a, float b) {
    auto r = __builtin_amdgcn_permlane16_swap(__float_as_uint(a), __float_as_uint(b), false, false);
    return __uint_as_float(r[0]) + __uint_as_float(r[1]);
}
DI float wave_sum(float x) {
    x = dpp_add<0xB1>(x);
    x = dpp_add<0x4E>(x);
    x = dpp_add<0x141>(x);
    x = dpp_add<0x140>(x);
    x = swap16_add(x, x);
    return swap32_add(x, x);
}
DI unsigned wave_umax(unsigned x) {
    x = max(x, dpp<0xB1>(x));
    x = max(x, dpp<0x4E>(x));
    x = max(x, dpp<0x141>(x));
    x = max(x, dpp<0x140>(x));
    auto r = __builtin_amdgcn_permlane16_swap(x, x, false, false);
    x = max(r[0], r[1]);
    auto s = __builtin_amdgcn_permlane32_swap(x, x, false, false);
    return max(s[0], s[1]);
}
template <class OP, class POST>
DI void red2(float& a, float& b, OP op, POST post) {
    auto x = __builtin_amdgcn_permlane32_swap(__float_as_uint(a), __float_as_uint(b), false, false);
    const float m = op(__uint_as_float(x[0]), __uint_as_float(x[1]));
    auto y = __builtin_amdgcn_permlane16_swap(__float_as_uint(m), __float_as_uint(m), false, false);
    const float f = post(op(__uint_as_float(y[0]), __uint_as_float(y[1])));
    auto z = __builtin_amdgcn_permlane32_swap(__float_as_uint(f), __float_as_uint(f), false, false);
    a = __uint_as_float(z[0]);
    b = __uint_as_float(z[1]);
}

#define RS(i) do {} while (0)
#define RSD(i) do {} while (0)
#define RSDUMP() do {} while (0)

constexpr unsigned WS_E = 0;
constexpr unsigned WS_RG = 256;
constexpr unsigned WS_GG = WS_RG + 2048;
constexpr unsigned WS_CL = 32768;
constexpr unsigned WS_Z = 65536;
constexpr unsigned WS_KF = WS_Z + 131072;
constexpr unsigned WS_GU = WS_KF + 262144;
constexpr unsigned WS_Y = WS_GU + 262144;
constexpr unsigned WS_KV = WS_Y + 262144;
constexpr unsigned WS_O = WS_KV + (2u << 20);
constexpr unsigned WS_END = WS_O + (4u << 20);

constexpr int NA = 10, ND = 6, SPLIT = 0, NF = 64, NF1 = 58, NP = 12;
constexpr int J_S[48] = {0, 0, 0, 1, 1, 1, 2, 2, 2, 3, 3, 3, 4, 4, 4, 5, 5, 5, 6, 6, 6, 7, 7, 7, 8, 8, 8, 9, 9, 9, 10, 11, 12, 13, 14, 15, 10, 11, 12, 13, 14, 15, 10, 11, 12, 13, 14, 15};
constexpr int J_M[48] = {0, 1, 2, 0, 1, 2, 0, 1, 2, 0, 1, 2, 0, 1, 2, 0, 1, 2, 0, 1, 2, 0, 1, 2, 0, 1, 2, 0, 1, 2, 0, 0, 0, 0, 0, 0, 1, 1, 1, 1, 1, 1, 2, 2, 2, 2, 2, 2};
constexpr int J_U[48] = {6, 7, 0, 3, 16, 17, 9, 10, 11, 14, 15, 4, 7, 0, 1, 16, 17, 11, 12, 13, 14, 15, 6, 7, 0, 3, 18, 10, 11, 12, 15, 4, 5, 0, 3, 18, 8, 9, 10, 11, 12, 13, 14, 15, 4, 5, 6, 7};
constexpr int Q_J[96] = {0, 0, 1, 1, 2, 2, 3, 3, 4, 4, 5, 5, 6, 6, 7, 7, 8, 8, 9, 9, 10, 10, 11, 11, 12, 12, 13, 13, 14, 14, 15, 15, 16, 16, 17, 17, 18, 18, 19, 19, 20, 20, 21, 21, 22, 22, 23, 23, 24, 24, 25, 25, 26, 26, 27, 27, 28, 28, 29, 29, 30, 30, 31, 31, 32, 32, 33, 33, 34, 34, 35, 35, 36, 36, 37, 37, 38, 38, 39, 39, 40, 40, 41, 41, 42, 42, 43, 43, 44, 44, 45, 45, 46, 46, 47, 47};
constexpr int Q_D[96] = {0, 1, 0, 1, 0, 1, 0, 1, 0, 1, 0, 1, 0, 1, 0, 1, 0, 1, 0, 1, 0, 1, 0, 1, 0, 1, 0, 1, 0, 1, 0, 1, 0, 1, 0, 1, 0, 1, 0, 1, 0, 1, 0, 1, 0, 1, 0, 1, 0, 1, 0, 1, 0, 1, 0, 1, 0, 1, 0, 1, 0, 1, 0, 1, 0, 1, 0, 1, 0, 1, 0, 1, 0, 1, 0, 1, 0, 1, 0, 1, 0, 1, 0, 1, 0, 1, 0, 1, 0, 1, 0, 1, 0, 1, 0, 1};
constexpr int Q_X[96] = {0, -1, -1, -1, -1, -1, 1, -1, -1, -1, -1, -1, 2, -1, -1, -1, -1, -1, 3, -1, -1, -1, -1, 4, -1, -1, -1, -1, -1, 5, -1, -1, -1, 6, 7, -1, -1, -1, -1, -1, -1, -1, -1, 8, -1, -1, -1, -1, -1, 9, -1, -1, 10, 11, -1, -1, -1, -1, -1, 12, -1, -1, -1, -1, -1, -1, 13, -1, 14, -1, 15, -1, -1, -1, -1, -1, -1, -1, -1, -1, -1, -1, -1, -1, -1, -1, -1, -1, -1, -1, -1, -1, -1, -1, -1, -1};
constexpr int Q_C[96] = {0, -1, 1, -1, 2, -1, 3, -1, 4, -1, 5, -1, 6, -1, 7, -1, 8, -1, 9, -1, 10, -1, 11, 12, 13, -1, 14, -1, 15, 16, 17, -1, 18, 19, 20, -1, 21, -1, 22, -1, 23, -1, 24, 25, 26, -1, 27, -1, 28, 29, 30, -1, 31, 32, 33, -1, 34, -1, 35, 36, 37, -1, 38, -1, 39, -1, 40, -1, 41, -1, 42, -1, 43, -1, 44, -1, 45, -1, 46, -1, 47, -1, 48, -1, 49, -1, 50, -1, 51, -1, 52, -1, 53, -1, 54, -1};
constexpr int Q_P[96] = {0, 0, 0, 0, 0, 0, 1, 0, 0, 0, 0, 0, 2, 0, 0, 0, 0, 0, 3, 0, 0, 0, 0, 4, 0, 0, 0, 0, 0, 5, 0, 0, 0, 0, 6, 0, 0, 7, 0, 0, 0, 0, 0, 8, 0, 0, 0, 0, 0, 9, 0, 0, 0, 10, 0, 0, 0, 0, 0, 11, 0, 0, 0, 0, 0, 0, 0, 0, 0, 0, 0, 0, 0, 0, 0, 0, 0, 0, 0, 0, 0, 0, 0, 0, 0, 0, 0, 0, 0, 0, 0, 0, 0, 0, 0, 0};
constexpr int X_U0[16] = {4, 1, 18, 12, 5, 2, 18, 9, 4, 1, 16, 8, 13, 6, 1, 16};
constexpr int X_U1[16] = {5, 2, 8, 13, 6, 3, 8, 10, 5, 2, 17, 9, 14, 7, 2, 17};
constexpr int P_U[4] = {0, 1, 2, 3};
constexpr int W_U[16] = {0, 1, 2, 3, 8, 9, 10, 11, 12, 13, 14, 15, 4, 5, 6, 7};
constexpr int F_J[64] = {-1, 0, 1, 2, -2, 3, 4, 5, -3, 6, 7, 8, -4, 9, 10, 11, -5, 12, 13, 14, -6, 15, 16, -7, -8, 17, 18, 19, 20, 21, -9, 22, 23, 24, -10, 25, -11, 26, -12, 27, 28, 29, -13, 30, 31, 32, -14, 33, -15, 34, -16, 35, 36, 37, 38, 39, 40, 41, 42, 43, 44, 45, 46, 47};
constexpr int F_N[64] = {0, 0, 0, 0, 0, 0, 0, 0, 0, 0, 0, 0, 0, 0, 0, 1, 1, 1, 1, 1, 2, 2, 2, 2, 3, 3, 3, 3, 4, 4, 4, 4, 5, 5, 5, 5, 6, 6, 6, 6, 7, 7, 8, 8, 8, 8, 9, 9, 9, 10, 10, 10, 10, 10, 11, 11, 11, 11, 12, 12, 12, 12, 12, 12};
constexpr int F_K[64] = {-1, 0, 1, 2, -1, 3, 4, 5, -1, 6, 7, 8, -1, 9, 10, 11, 12, 13, 14, 15, 16, 17, 18, 19, -1, 20, 21, 22, 23, 24, 25, 26, 27, 28, 29, 30, -1, 31, 32, 33, 34, 35, 36, 37, 38, 39, -1, 40, -1, 41, -1, 42, 43, 44, 45, 46, 47, 48, 49, 50, 51, 52, 53, 54};
constexpr unsigned L_A = 16 * 8192;
constexpr unsigned L_KF = 18 * 8192;
constexpr unsigned L_SM = 19 * 8192;
constexpr unsigned L_FULL = L_SM;
constexpr unsigned L_PROG = L_SM + 768 - 16;
constexpr unsigned L_FW2 = L_SM + 512, L_FOS = L_SM + 480, L_FGI = L_SM + 520, L_FK0 = L_SM + 752;
constexpr unsigned L_FK = L_SM + 528, L_FKT = L_SM + 544, L_FV = L_SM + 560, L_FP = L_SM + 576, L_FKS = L_SM + 592;
constexpr unsigned L_FQD = L_SM + 608, L_FG = L_SM + 624, L_FKF = L_SM + 736, L_FQ = L_SM + 720, L_FWD = L_SM + 640, L_FWO = L_SM + 656, L_FLD = L_SM + 688, L_BEST = L_SM + 696;
constexpr unsigned L_SEL = L_SM + 704;
constexpr unsigned L_XS = L_SM + 1024, L_KZ = L_SM + 2048, L_WPHI = L_SM + 4096;
static_assert(P_U[1] == P_U[0] + 1 && P_U[3] == P_U[2] + 1, "feature maps of a head lie side by side");
constexpr int wo_unit(int i) { return W_U[i]; }
constexpr int f_ks(int f) { return Q_D[f >> 2] < 2 ? Q_D[f >> 2] : (f >> 1) & 1; }
constexpr int f_u(int f) { return Q_D[f >> 2] < 2 ? f & 3 : 2 * (Q_D[f >> 2] - 2) + (f & 1); }
constexpr int f_hi(int f) { return J_U[Q_J[f >> 2]] >> 3; }
constexpr int f_step(int f) {
    for (int i = f - 1; i >= 0; --i)
        if (f_u(i) == f_u(f)) return f_hi(f) - f_hi(i);
    return 0;
}
constexpr unsigned SPIN_CAP = 1u << 16;
__device__ const unsigned k_one[4] = {1u, 1u, 1u, 1u};

#define RSL(i) do {} while (0)
#define RSC(i) do {} while (0)
#define PD 4
#define NPH 4
#define KSK 3
#define ROTK 8
#define OWIN 2
#define CLR0 4
#define CLS 1
#define CLP 2
#define SLK 2
#define CPH 3
#define NPL 0
#define CPRIO 1
#define HPRIO 2
#define KPK 0
#define KRND 1
#define QW 1
#define WOG 1
#define WON 10
#define KFD 0
#define SEL0 7
#define SPRIO 2
#define FBA 3
#define FBB 10
#define FBS 1
#define FBL 1
#define WOP 0
#define GTL 3
#define WPF 2
#define WOE 10
#define GW 0
#define GUF 0
#define FBH 0
#define WG2 0
#define NTX 0
#define NTW 0
#define NTS 16
#define NTO ""
#define GQ1 8
#define GQ2 8
#define CLO 0
#define PDL 6
#define XK 3
#define YK 7
#define BPRIO 0
#define AMF 1
#define LF 1
#define SH 1
#define LC 0
#define SPR2 0
#define GQ0 0
#define PWN 2
#define PWL (NP - 1)
#define TRN 0
#define RT0 0
#define BSP 0
#define WOV 0
#define GUP 1
#define NOF 0
#define FBD 8
#define BAR() asm volatile("s_waitcnt lgkmcnt(0)\n\ts_barrier" ::: "memory")
template <int N>
DI void waitv() { asm volatile("s_waitcnt vmcnt(%0)" ::"n"(N) : "memory"); }

DI u64 ld_agent(const char* p) { return __hip_atomic_load((const AS1 u64*)p, RLX_AGENT); }
DI void st_agent(char* p, u64 v) { __hip_atomic_store((AS1 u64*)p, v, RLX_AGENT); }
DI unsigned nz1(unsigned v) { return max(v, 1u); }
DI unsigned umin(unsigned a, unsigned b) { return min(a, b); }
DI u32x4 nz4(u32x4 v) { return (u32x4){max(v[0], 1u), max(v[1], 1u), max(v[2], 1u), max(v[3], 1u)}; }
DI u32x4 nz4(f32x4 v) { return nz4(__builtin_bit_cast(u32x4, v)); }
DI unsigned min4(u32x4 v) { return min(min(v[0], v[1]), min(v[2], v[3])); }
DI u32x4 ld_wt(__amdgpu_buffer_rsrc_t r, unsigned off) { return __builtin_amdgcn_raw_buffer_load_b128(r, off, 0, 16); }
DI void st_wt(__amdgpu_buffer_rsrc_t r, unsigned off, u32x4 v) { __builtin_amdgcn_raw_buffer_store_b128(v, r, off, 0, 16); }
DI void ld_async(u32x4& v, unsigned off, const char* base) {
    asm volatile("global_load_dwordx4 %0, %1, %2 sc1" : "=&v"(v) : "v"(off), "s"(base) : "memory");
}
#define ST_MASK(op, m, off, v, base) asm volatile("s_mov_b64 exec, %3\n\t" op " %0, %1, %2 sc1\n\ts_mov_b64 exec, -1\n\ts_nop 0" ::"v"(off), "v"(v), "s"(base), "s"((u64)(m)) : "memory")
DI void pin(u32x4& v) { asm volatile("" : "+v"(v)); }
template <int N, class OFF, class BASE, class OK>
DI void settle(u32x4 (&v)[N], OFF off, BASE base, OK ok) {
#pragma unroll
    for (int i = 0; i < N; ++i) pin(v[i]);
#pragma unroll 1
    for (unsigned s = 0; s < SPIN_CAP; ++s) {
        unsigned m = ok(v[0]);
#pragma unroll
        for (int i = 1; i < N; ++i) m = min(m, ok(v[i]));
        if (__ballot(m == 0u) == 0ull) break;
#pragma unroll
        for (int i = 0; i < N; ++i) {
            u64 keep;
            asm volatile("s_mov_b64 %1, exec\n\ts_mov_b64 exec, %4\n\tglobal_load_dwordx4 %0, %2, %3 sc1\n\ts_mov_b64 exec, %1"
                         : "+&v"(v[i]), "=&s"(keep) : "v"(off(i)), "s"(base(i)), "s"((u64)__ballot(ok(v[i]) == 0u)) : "memory");
        }
        waitv<0>();
#pragma unroll
        for (int i = 0; i < N; ++i) pin(v[i]);
    }
}
template <int N, class OFF, class BASE>
DI void settle(u32x4 (&v)[N], OFF off, BASE base) { settle(v, off, base, [](const u32x4& a) { return min4(a); }); }
DI void lds_wait(unsigned a, unsigned n) {
    unsigned v, c;
    asm volatile("s_mov_b32 %1, 0x400000\n"
                 ".Lp%=:\n\tds_read_b32 %0, %2\n\ts_waitcnt lgkmcnt(0)\n\tv_cmp_gt_u32 vcc, %3, %0\n\ts_cbranch_vccz .Lf%=\n\t"
                 "s_sleep 1\n\ts_sub_u32 %1, %1, 1\n\ts_cbranch_scc0 .Lp%=\n.Lf%=:"
                 : "=&v"(v), "=&s"(c) : "v"(a), "s"(n) : "memory", "vcc", "scc");
}
DI void lds_wait8(unsigned a, unsigned n) {
    unsigned v, c;
    asm volatile("s_mov_b32 %1, 0x100000\n"
                 ".Lp%=:\n\tds_read_b32 %0, %2\n\ts_waitcnt lgkmcnt(0)\n\tv_cmp_gt_u32 vcc, %3, %0\n\ts_cbranch_vccz .Lf%=\n\t"
                 "s_sleep 4\n\ts_sub_u32 %1, %1, 1\n\ts_cbranch_scc0 .Lp%=\n.Lf%=:"
                 : "=&v"(v), "=&s"(c) : "v"(a), "s"(n) : "memory", "vcc", "scc");
}
DI void lds_peek(unsigned& v, unsigned a) { asm volatile("ds_read_b32 %0, %1" : "=v"(v) : "v"(a) : "memory"); }
DI void lds_wait(unsigned& v, unsigned a, unsigned n) {
    unsigned c;
    asm volatile("s_mov_b32 %1, 0x400000\n"
                 ".Lp%=:\n\ts_waitcnt lgkmcnt(0)\n\tv_cmp_gt_u32 vcc, %3, %0\n\ts_cbranch_vccz .Lf%=\n\t"
                 "ds_read_b32 %0, %2\n\ts_sub_u32 %1, %1, 1\n\ts_cbranch_scc0 .Lp%=\n.Lf%=:"
                 : "+v"(v), "=&s"(c) : "v"(a), "s"(n) : "memory", "vcc", "scc");
}
DI void lds_set(unsigned a) {
    asm volatile("s_waitcnt lgkmcnt(0)\n\tds_write_b32 %0, %1" ::"v"(a), "v"(1u) : "memory");
}
DI float dot2(unsigned a, unsigned b, float c) {
    return __builtin_amdgcn_fdot2_f32_bf16(__builtin_bit_cast(bf16x2_t, a), __builtin_bit_cast(bf16x2_t, b), c, false);
}
DI float sum4(const float (&a)[4]) {
    float c = swap16_add(swap32_add(a[0], a[1]), swap32_add(a[2], a[3]));
    c = dpp_add<0xB1>(c);
    c = dpp_add<0x4E>(c);
    c = dpp_add<0x141>(c);
    return dpp_add<0x140>(c);
}

template <class OP, class POST>
DI void red4(float& a, float& b, float& c, float& d, OP op, POST post) {
    auto x = __builtin_amdgcn_permlane32_swap(__float_as_uint(a), __float_as_uint(b), false, false);
    auto y = __builtin_amdgcn_permlane32_swap(__float_as_uint(c), __float_as_uint(d), false, false);
    const float m = op(__uint_as_float(x[0]), __uint_as_float(x[1])), n = op(__uint_as_float(y[0]), __uint_as_float(y[1]));
    auto z = __builtin_amdgcn_permlane16_swap(__float_as_uint(m), __float_as_uint(n), false, false);
    const unsigned f = __float_as_uint(post(op(__uint_as_float(z[0]), __uint_as_float(z[1]))));
    auto u = __builtin_amdgcn_permlane16_swap(f, f, false, false);
    auto v = __builtin_amdgcn_permlane32_swap(u[0], u[0], false, false);
    auto t = __builtin_amdgcn_permlane32_swap(u[1], u[1], false, false);
    a = __uint_as_float(v[0]);
    b = __uint_as_float(v[1]);
    c = __uint_as_float(t[0]);
    d = __uint_as_float(t[1]);
}
DI float mx2(float a, float b) { return __builtin_elementwise_maximum(a, b); }
DI float opq(float a) { asm("" : "+v"(a)); return a; }
template <bool PK, class HOOK>
DI void softmax2(const f32x4 (&v)[2][2], u32x4 (&ph)[2][2], HOOK hook) {
    constexpr float L2E = 1.4426950408889634f;
    float mx[2], mn[2], sp_[2], sn_[2];
#pragma unroll
    for (int b = 0; b < 2; ++b) {
        const f32x4 s = v[b][0], t = v[b][1];
        mx[b] = mx2(mx2(mx2(mx2(s[0], s[1]), s[2]), mx2(mx2(s[3], t[0]), t[1])), mx2(t[2], t[3]));
        mn[b] = mx2(mx2(mx2(mx2(-s[0], -s[1]), -s[2]), mx2(mx2(-s[3], -t[0]), -t[1])), mx2(-t[2], -t[3]));
    }
    red4(mx[0], mx[1], mn[0], mn[1], [](float a, float b) { return mx2(a, b); }, [](float a) { return a * -L2E; });
    f32x2 ep[2][4], en[2][4];
#pragma unroll
    for (int b = 0; b < 2; ++b) {
#pragma unroll
        for (int e = 0; e < 4; ++e) {
            const f32x2 u = {v[b][e >> 1][2 * (e & 1)], v[b][e >> 1][2 * (e & 1) + 1]};
            f32x2 a, c;
            if (PK) {
                a = __builtin_elementwise_fma(u, (f32x2){L2E, L2E}, (f32x2){mx[b], mx[b]});
                c = __builtin_elementwise_fma(u, (f32x2){-L2E, -L2E}, (f32x2){mn[b], mn[b]});
            } else {
                a = (f32x2){__builtin_fmaf(u[0], L2E, mx[b]), __builtin_fmaf(u[1], L2E, mx[b])};
                c = (f32x2){__builtin_fmaf(u[0], -L2E, mn[b]), __builtin_fmaf(u[1], -L2E, mn[b])};
            }
            ep[b][e] = (f32x2){__builtin_amdgcn_exp2f(a[0]), __builtin_amdgcn_exp2f(a[1])};
            en[b][e] = (f32x2){__builtin_amdgcn_exp2f(c[0]), __builtin_amdgcn_exp2f(c[1])};
        }
        if (PK) {
            const f32x2 s = (ep[b][0] + ep[b][1]) + (ep[b][2] + ep[b][3]), t = (en[b][0] + en[b][1]) + (en[b][2] + en[b][3]);
            sp_[b] = s[0] + s[1];
            sn_[b] = t[0] + t[1];
        } else {
            sp_[b] = ep[b][0][0];
            sn_[b] = en[b][0][0];
#pragma unroll
            for (int e = 1; e < 8; ++e) sp_[b] += ep[b][e >> 1][e & 1], sn_[b] += en[b][e >> 1][e & 1];
        }
    }
    asm volatile("" : "+v"(sp_[0]), "+v"(sn_[0]), "+v"(sp_[1]), "+v"(sn_[1]));
    hook();
    red4(sp_[0], sp_[1], sn_[0], sn_[1], [](float a, float b) { return a + b; }, [](float a) { return __builtin_amdgcn_rcpf(a); });
#pragma unroll
    for (int b = 0; b < 2; ++b)
#pragma unroll
        for (int e = 0; e < 4; ++e) {
            if (PK) {
                const f32x2 a = ep[b][e] * (f32x2){sp_[b], sp_[b]}, c = en[b][e] * (f32x2){sn_[b], sn_[b]};
                ph[b][0][e] = pack2(a[0], a[1]);
                ph[b][1][e] = pack2(c[0], c[1]);
            } else {
                ph[b][0][e] = pack2(opq(ep[b][e][0] * sp_[b]), ep[b][e][1] * sp_[b]);
                ph[b][1][e] = pack2(opq(en[b][e][0] * sn_[b]), en[b][e][1] * sn_[b]);
            }
        }
}
DI f32x4 rbf4(const f32x4& a) {
    const unsigned u = pack2(a[0], a[1]), v = pack2(a[2], a[3]);
    return (f32x4){lo16(u), hi16(u), lo16(v), hi16(v)};
}
DI int fsw(int row) { return (((row >> 1) & 3) << 2) | (((row >> 3) & 1) << 1) | (row & 1); }
template <bool PK, class HOOK>
DI void phi_k(const f32x4 (&pp)[2][2], const unsigned (&pw)[4], unsigned off, HOOK hook) {
    u32x4 ph[2][2];
    softmax2<PK>(pp, ph, hook);
#pragma unroll
    for (int b = 0; b < 2; ++b)
#pragma unroll
        for (int kf = 0; kf < 2; ++kf)
#pragma unroll
            for (int fb = 0; fb < 2; ++fb) {
                const u32x2 a = {ph[b][kf][2 * fb], ph[b][kf][2 * fb + 1]};
                lds_write<u32x2>(pw[2 * kf + fb] + off + b * 2048, a);
            }
}

DI void phi_k1(const f32x4 (&v)[2], const unsigned (&pw)[4]) {
    constexpr float L2E = 1.4426950408889634f;
    const f32x4 s = v[0], t = v[1];
    float mx = mx2(mx2(mx2(mx2(s[0], s[1]), s[2]), mx2(mx2(s[3], t[0]), t[1])), mx2(t[2], t[3]));
    float mn = mx2(mx2(mx2(mx2(-s[0], -s[1]), -s[2]), mx2(mx2(-s[3], -t[0]), -t[1])), mx2(-t[2], -t[3]));
    red2(mx, mn, [](float a, float b) { return mx2(a, b); }, [](float a) { return a * -L2E; });
    f32x2 ep[4], en[4];
#pragma unroll
    for (int e = 0; e < 4; ++e) {
        const f32x2 u = {v[e >> 1][2 * (e & 1)], v[e >> 1][2 * (e & 1) + 1]};
        const f32x2 a = __builtin_elementwise_fma(u, (f32x2){L2E, L2E}, (f32x2){mx, mx});
        const f32x2 c = __builtin_elementwise_fma(u, (f32x2){-L2E, -L2E}, (f32x2){mn, mn});
        ep[e] = (f32x2){__builtin_amdgcn_exp2f(a[0]), __builtin_amdgcn_exp2f(a[1])};
        en[e] = (f32x2){__builtin_amdgcn_exp2f(c[0]), __builtin_amdgcn_exp2f(c[1])};
    }
    const f32x2 a = (ep[0] + ep[1]) + (ep[2] + ep[3]), c = (en[0] + en[1]) + (en[2] + en[3]);
    float sp = a[0] + a[1], sn = c[0] + c[1];
    red2(sp, sn, [](float a, float b) { return a + b; }, [](float a) { return __builtin_amdgcn_rcpf(a); });
#pragma unroll
    for (int i = 0; i < 4; ++i) {
        const float r = i < 2 ? sp : sn;
        const f32x2 lo = (i < 2 ? ep[2 * i] : en[2 * i - 4]) * (f32x2){r, r}, hi = (i < 2 ? ep[2 * i + 1] : en[2 * i - 3]) * (f32x2){r, r};
        lds_write<u32x2>(pw[i], (u32x2){pack2(lo[0], lo[1]), pack2(hi[0], hi[1])});
    }
}

DI void waitvn(int n) {
#define WV(k) case k: waitv<k>(); break;
#define WV8(k) WV(k) WV(k + 1) WV(k + 2) WV(k + 3) WV(k + 4) WV(k + 5) WV(k + 6) WV(k + 7)
    switch (n < 63 ? n : 63) { WV8(0) WV8(8) WV8(16) WV8(24) WV8(32) WV8(40) WV8(48) WV8(56) }
}
#define SB0() __builtin_amdgcn_sched_barrier(0)
template <int A, int B, class F>
DI void sfor(F f) {
    if constexpr (A < B) {
        f(std::integral_constant<int, A>{});
        sfor<A + 1, B>(f);
    }
}
template <int N> using IC = std::integral_constant<int, N>;
#define AI __attribute__((always_inline))

DI void o_store(const u32x4 (&kf)[6], const u32x4 (&ph)[2][2], __amdgpu_buffer_rsrc_t rsrc, unsigned o_st) {
    const f32x4 zero = {0.f, 0.f, 0.f, 0.f};
#pragma unroll
    for (int i = 0; i < 2; ++i) {
        const bf16x8 p0 = __builtin_bit_cast(bf16x8, ph[i][0]), p1 = __builtin_bit_cast(bf16x8, ph[i][1]);
        f32x4 dn = mfma16(__builtin_bit_cast(bf16x8, kf[4]), p0, zero);
        f32x4 n0 = mfma16(__builtin_bit_cast(bf16x8, kf[0]), p0, zero);
        f32x4 n1 = mfma16(__builtin_bit_cast(bf16x8, kf[1]), p0, zero);
        dn = mfma16(__builtin_bit_cast(bf16x8, kf[5]), p1, dn);
        n0 = mfma16(__builtin_bit_cast(bf16x8, kf[2]), p1, n0);
        n1 = mfma16(__builtin_bit_cast(bf16x8, kf[3]), p1, n1);
        const float rd_ = __builtin_amdgcn_rcpf(rbf(rbf(dn[0]) + 1e-6f));
        const u32x4 nb_ = __builtin_bit_cast(u32x4, pack44(n0, n1));
        u32x4 o;
#pragma unroll
        for (int j = 0; j < 4; ++j) o[j] = pack2(lo16(nb_[j]) * rd_, hi16(nb_[j]) * rd_);
        st_wt(rsrc, o_st + i * 1024, nz4(o));
    }
}

__global__ __launch_bounds__(512, 2) void fused_mix(const u16* __restrict__ x, const u16* __restrict__ wqkv,
                                                    const u16* __restrict__ wphi, const u16* __restrict__ wo,
                                                    const u16* __restrict__ wr, const u16* __restrict__ wg,
                                                    const u16* __restrict__ wu, const u16* __restrict__ wd,
                                                    const u16* __restrict__ gw, char* __restrict__ ws,
                                                    u16* __restrict__ out) {
    __shared__ __attribute__((aligned(16))) char smem[163840];
    const int tid = threadIdx.x, l = tid & 63;
    const int w = __builtin_amdgcn_readfirstlane(tid >> 6) ^ 4;
    const int bid = blockIdx.x, xcd = bid & 7, slot = bid >> 3;
    const int mt = (xcd & 1) * 8 + (slot & 7);
    const int hp = (xcd >> 1) * 4 + (slot >> 3);
    const int rt = (xcd >> 1) * 4 + (slot & 3), cb = (xcd & 1) * 8 + (slot >> 2), rot = 4 * cb + ROTK * rt;
    const unsigned lds0 = lds_of(smem);
    const __amdgpu_buffer_rsrc_t rsrc = __builtin_amdgcn_make_buffer_rsrc(ws, 0, WS_END, 0x00020000);
    const int g = l >> 4, l15 = l & 15, q = l15 >> 2, p = l & 3;
    const int srow = l >> 3, sch = l & 7;
    const unsigned l16 = l * 16;
    unsigned vst = 0;
    const u32x4 z4 = {0u, 0u, 0u, 0u};
    const f32x4 zero = {0.f, 0.f, 0.f, 0.f};
    const int rg = w & 3, kp = w >> 2;
    const int hs = rg >> 1;
    const unsigned AX = lds0 + L_A + rg * 4096 + l16;
    const unsigned W4 = lds0 + p * 4;
    const unsigned WR = lds0 + rg * 4;
    const unsigned* one = k_one;
    unsigned v0 = 0;
    asm volatile("" : "+s"(one), "+v"(v0));
    auto mark = [&](unsigned dst) AI {
        asm volatile("s_mov_b32 m0, %2\n\ts_mov_b64 exec, 1\n\tglobal_load_lds_dword %0, %1\n\ts_mov_b64 exec, -1"
                     ::"v"(v0), "s"(one), "s"(dst) : "memory");
    };
    auto flag = [&](unsigned a) AI { asm volatile("ds_write_b32 %0, %0" ::"v"(a) : "memory"); };
    auto phi_w = [&](bf16x8 (&a)[2], int h) AI {
        const unsigned ad = lds0 + L_WPHI + h * 2048 + (4 * g + q) * 64 + (((p >> 1) ^ (2 * (g & 1))) * 16) + 8 * (p & 1);
        a[0] = cat8(tr_read(ad), tr_read(ad + 1024));
        a[1] = cat8(tr_read(ad ^ 32), tr_read((ad ^ 32) + 1024));
    };
    const unsigned o_st = WS_O + ((mt * 2 + hs) * 32 + 2 * hp) * 4096 + (rg & 1) * 2048 + l16;
    u32x4 fw[32], gu[2];
    unsigned sel_[4][2];
    u32x4 ph[2][2], sq;
    const int sc = (3 - (int)((unsigned)(size_t)wqkv >> 7)) & 7;
    const bool ffn = (hp & 7) != sc && !(NOF & 1);
    const int ph4 = (slot + (slot >> 3)) & 3;
    const int turn = (ph4 + TRN) & 3;
    const int kq = mt >> 2;
    const int cs = (ffn ? hp - (hp > sc) - (hp > sc + 8) : 14 + (hp >> 3)) * 4 + ((ph4 + CPH - (int)((unsigned)(size_t)wg >> 7)) & 3);
    auto spread = [&](const u32x4& v, int i) -> bf16x8 {
        const unsigned s = v[i >> 1];
        u32x4 a;
#pragma unroll
        for (int d = 0; d < 4; ++d) a[d] = __builtin_amdgcn_perm(s, s, sel_[d][i & 1]);
        return __builtin_bit_cast(bf16x8, a);
    };
    auto fold_store = [&](const f32x4& a, unsigned off) {
        f32x4 v;
#pragma unroll
        for (int i = 0; i < 4; ++i) {
            const unsigned u = __float_as_uint(a[i]);
            const float r = __uint_as_float(__builtin_amdgcn_update_dpp(u, u, 0x128, 0xc, 0xf, false));
            v[i] = swap32_add(r, r);
        }
        const u32x4 o = nz4(v);
        ST_MASK("global_store_dwordx4", 0x00FF00FFull, off + l15 * 32 + g * 16, o, ws);
    };
    auto four = [&](const unsigned (&v)[4], const char* src, unsigned dst, int nt = 0) AI {
#define FOUR(P) asm volatile("s_mov_b32 m0, %5\n\ts_nop 0\n\t" \
                     "global_load_lds_dwordx4 %0, %4" P "\n\tglobal_load_lds_dwordx4 %1, %4 offset:1024" P "\n\t" \
                     "global_load_lds_dwordx4 %2, %4 offset:2048" P "\n\tglobal_load_lds_dwordx4 %3, %4 offset:3072" P \
                     ::"v"(v[0]), "v"(v[1]), "v"(v[2]), "v"(v[3]), "s"(src), "s"(dst) : "memory")
        if (nt == 1) FOUR(" nt");
        else if (nt == 2) FOUR(" sc1");
        else if (nt == 3) FOUR(" sc0 sc1");
        else FOUR("");
    };
    auto sel_init = [&]() AI {
#pragma unroll
        for (int d = 0; d < 4; ++d)
#pragma unroll
            for (int hf = 0; hf < 2; ++hf) {
                const unsigned b = hf ? 0x0302u : 0x0100u;
                sel_[d][hf] = (sch >> 1) != d ? 0x0c0c0c0cu : (l & 1) ? (b << 16) | 0x0c0cu : 0x0c0c0000u | b;
            }
    };
    auto gua = [&](int i) { return ws + WS_GU + cs * 4096 + i * 1024; };
    auto gu_load = [&]() AI {
#pragma unroll
        for (int i = 0; i < 2; ++i) ld_async(gu[i], l16, gua(i));
    };
    auto down = [&]() AI {
#pragma unroll
        for (int i = 0; i < 16; ++i) pin(fw[i]);
        sel_init();
        settle(gu, [&](int) { return l16; }, gua);
        const f32x4 v = __builtin_bit_cast(f32x4, gu[0]) + __builtin_bit_cast(f32x4, gu[1]);
        auto r = __builtin_amdgcn_permlane16_swap(__float_as_uint(swap32_add(v[0], v[2])), __float_as_uint(swap32_add(v[1], v[3])), false, false);
        const float gt = rbf(__uint_as_float(r[0])), up = rbf(__uint_as_float(r[1]));
        const unsigned ha = lds0 + L_A + w * 4096 + 3072;
        lds_write<u16>(ha + l15 * 8 + g * 2, f2bf(rbf(gt / (1.f + __expf(-gt))) * up));
        const u32x4 hv = lds_read<u32x4>(ha + srow * 16);
        f32x4 y0 = zero, y1 = zero;
#pragma unroll
        for (int i = 0; i < 8; ++i) {
            const bf16x8 a = spread(hv, i);
            y0 = mfma16(a, __builtin_bit_cast(bf16x8, fw[i]), y0);
            y1 = mfma16(a, __builtin_bit_cast(bf16x8, fw[8 + i]), y1);
        }
        fold_store(y0, WS_Y + cs * 4096 + kq * 1024 + hs * 512);
        fold_store(y1, WS_Y + cs * 4096 + kq * 1024 + hs * 512 + 256);
    };
    auto wo_load = [&](int n0, int n1, bool mk) AI {
        unsigned vo[4];
#pragma unroll
        for (int i = 0; i < 4; ++i) vo[i] = (unsigned)((8 * i + srow) * 2048 + ((sch ^ ((g & 3) << 1)) * 16) + 4096 - i * 1024);
        const char* wob = (const char*)wo + cb * 128 - 4096;
#pragma unroll
        for (int n = n0; n < ((NOF & 4) ? n0 : n1); ++n) four(vo, wob + ((2 * n + hs + rot) & 31) * 65536, lds0 + wo_unit(n) * 8192 + hs * 4096);
        if (mk) mark(lds0 + (n0 >= WON && WON ? L_FW2 : L_FWD) + hs * 4);
    };
    unsigned vx[4], vw[4];
    auto vw_init = [&]() AI {
#pragma unroll
        for (int i = 0; i < 4; ++i) vw[i] = (unsigned)((8 * i + srow) * 6144 + (sch ^ (((srow >> 1) & 1) << 1) ^ ((i & 1) << 2)) * 16 + 4096 - i * 1024);
    };
    const int ks0 = SLK && (hp & 7) == sc ? SLK * (slot & 7) : KSK * ((slot + (slot >> 3)) & (NPH - 1));
    const char* xb0 = (const char*)(x + (size_t)(mt * 128 + hs * 64) * DM) - 4096;
    const char* wb0 = (const char*)(wqkv + hp * 64) + hs * (32 * 6144) - 4096;
    auto fill = [&](auto N) AI {
        constexpr int n = N, j = F_J[n] < 0 ? 0 : F_J[n], s = F_J[n] < 0 ? -1 - F_J[n] : J_S[j], m = J_M[j];
        const int ks = (ks0 + s) & 15;
        if constexpr (F_J[n] >= 0) {
            four(vw, wb0 + ks * (64 * 6144) + (m == 2 ? 0 : 2048 * (m + 1)), lds0 + J_U[j] * 8192 + hs * 4096, s < NTS ? NTW : 0);
        } else {
            const char* xs_ = xb0 + ks * 128;
            const unsigned d = lds0 + (hs ? X_U1[s] : X_U0[s]) * 8192;
            four(vx, xs_, d, s < NTS ? NTX : 0);
            four(vx, xs_ + 32 * 2048, d + 4096, s < NTS ? NTX : 0);
        }
        if constexpr (F_K[n] == 0) asm volatile("s_barrier" ::: "memory");
        if constexpr (F_K[n] >= 0) mark(lds0 + L_FULL + F_K[n] * 8 + hs * 4);
    };
    auto phi_adr = [&](unsigned (&pw)[4], unsigned base) AI {
#pragma unroll
        for (int i = 0; i < 4; ++i) pw[i] = (base + rg * 4096 + l15 * 128 + ((g ^ fsw(l15)) << 3)) ^ (i << 5);
    };

    if (w >= 4) {
        const bool loader = !(w & 1);
        const unsigned kf_kv = WS_KF + (hp * 2 + (mt >> 3)) * 8192 + (mt & 7) * 512;
        const unsigned kf_z = WS_KF + (hp * 2 + (mt >> 3)) * 8192 + 4096 + ((mt >> 2) & 1) * 64;
        const unsigned fgo = ffn ? (unsigned)((256 * kq + 16 * srow) * 7168 + cs * 128 + sch * 16) : 0u;
        const char* fsrc = (const char*)(hs ? wu : wg);
        const unsigned fdo = ffn ? (unsigned)((64 * cs + 8 * srow) * 2048 + 512 * kq + 256 * hs + sch * 16) : 0u;
        auto burst = [&](int now) AI {
            if (NOF & 8) return;
#pragma unroll
            for (int h = 0; h < 2; ++h)
                asm volatile("s_cmp_lg_u32 %8, %12\n\ts_cbranch_scc1 .Lk%=\n\t"
                             "global_load_dwordx4 %0, %9, %10 offset:-4096 nt\n\tglobal_load_dwordx4 %1, %9, %10 offset:-2048 nt\n\t"
                             "global_load_dwordx4 %2, %9, %10 nt\n\tglobal_load_dwordx4 %3, %9, %10 offset:2048 nt\n\t"
                             "global_load_dwordx4 %4, %9, %11 offset:-4096 nt\n\tglobal_load_dwordx4 %5, %9, %11 offset:-2048 nt\n\t"
                             "global_load_dwordx4 %6, %9, %11 nt\n\tglobal_load_dwordx4 %7, %9, %11 offset:2048 nt\n.Lk%=:"
                             : "+v"(fw[8 * h]), "+v"(fw[8 * h + 1]), "+v"(fw[8 * h + 2]), "+v"(fw[8 * h + 3]),
                               "+v"(fw[8 * h + 4]), "+v"(fw[8 * h + 5]), "+v"(fw[8 * h + 6]), "+v"(fw[8 * h + 7])
                             : "s"(turn), "v"(fdo), "s"((const char*)wd + 4096 + h * 128), "s"((const char*)wd + 12288 + h * 128), "s"(now) : "memory", "scc");
        };
        auto gu_w = [&](int q0, int q1) AI {
            if (NOF & 8) return;
#pragma unroll
            for (int i = q0; i < q1; ++i) {
                unsigned t1, t2, t3;
                asm volatile("s_bitcmp1_b32 %7, 0\n\ts_cbranch_scc0 .Lk%=\n\t"
                             "v_add_u32 %4, 0x1c00, %8\n\tv_add_u32 %5, 0x3800, %8\n\tv_add_u32 %6, 0x5400, %8\n\t"
                             "global_load_dwordx4 %0, %8, %9 nt\n\tglobal_load_dwordx4 %1, %4, %9 nt\n\t"
                             "global_load_dwordx4 %2, %5, %9 nt\n\tglobal_load_dwordx4 %3, %6, %9 nt\n.Lk%=:"
                             : "+v"(fw[4 * i]), "+v"(fw[4 * i + 1]), "+v"(fw[4 * i + 2]), "+v"(fw[4 * i + 3]), "=&v"(t1), "=&v"(t2), "=&v"(t3)
                             : "s"(w), "v"(fgo), "s"(fsrc + (128 * (i >> 2) + 4 * (i & 3)) * 7168) : "memory", "scc");
            }
        };
        auto o_clear = [&]() AI {
#pragma unroll
            for (int n = 0; n < ((NOF & 2) ? 1 : 8); ++n) st_wt(rsrc, WS_O + ((mt * 2 + (n >> 2)) * 32 + 2 * hp + hs) * 4096 + (n & 3) * 1024 + l16, z4);
        };
        auto fdef = [&]() AI {
#pragma unroll
            for (int i = 0; i < 32; ++i) asm volatile("" : "=v"(fw[i]));
        };
        auto fpin = [&]() AI {
#pragma unroll
            for (int i = 0; i < 32; ++i) pin(fw[i]);
        };
        RS(0);
        if (__builtin_expect(loader, 1)) {
#pragma unroll
            for (int i = 0; i < 4; ++i) {
                vx[i] = (unsigned)((8 * i + srow) * 2048 + ((sch ^ ((4 * (i & 1) + (srow >> 1)) & 7)) * 16) + 4096 - i * 1024);
                vw[i] = (unsigned)((8 * i + srow) * 6144 + (sch ^ (((srow >> 1) & 1) << 1) ^ ((i & 1) << 2)) * 16 + 4096 - i * 1024);
            }
            fdef();
            sfor<0, NF1>([&](auto N) {
                constexpr int n = N;
                if constexpr (n > 0 && F_N[n] > F_N[n > 0 ? n - 1 : 0]) {
                    constexpr int gp = F_N[n] - 1;
                    if constexpr (gp >= FBA && gp < FBA + 4 * FBS && (gp - FBA) % FBS == 0) burst((gp - FBA) / FBS);
                    RSL(32 + F_N[n]);
                    lds_wait(W4 + L_PROG + (F_N[n]) * 16, 1);
                }
                if constexpr (n == 12) {
                    asm volatile("s_mov_b32 m0, %2\n\ts_nop 0\n\tglobal_load_lds_dwordx4 %0, %1\n\tglobal_load_lds_dwordx4 %0, %1 offset:1024"
                                 ::"v"((unsigned)((l >> 2) * 64 + (((l & 3) ^ (2 * ((l >> 4) & 1))) * 16))),
                                   "s"(wphi + (hp * 2 + hs) * 1024), "s"(lds0 + L_WPHI + hs * 2048) : "memory");
                }
                fill(N);
            });
            flag(lds0 + L_FLD + hs * 4);
            RS(1);
            waitv<28>();
            static_assert(FBA + 3 * FBS + 4 < NP, "");
            fpin();
        } else {
            const int row0 = (mt * 16 + hp) * 8 + 4 * hs;
            u64 T;
            if (SPRIO) __builtin_amdgcn_s_setprio(SPRIO);
            asm volatile("s_barrier" ::: "memory");
            u32x4 rv[2], gv[2], xv[4][2];
            u64 ev;
            if (RT0) lds_wait8(W4 + L_PROG + RT0 * 16, 1);
            asm volatile("global_load_dwordx2 %4, %5, %9 sc1\n\t"
                         "global_store_dwordx4 %10, %11, %9 sc1\n\t"
                         "global_store_dwordx2 %12, %13, %9 sc1\n\t"
                         "global_load_dwordx4 %0, %6, %7\n\tglobal_load_dwordx4 %1, %6, %7 offset:1024\n\t"
                         "global_load_dwordx4 %2, %6, %8\n\tglobal_load_dwordx4 %3, %6, %8 offset:1024"
                         : "=&v"(rv[0]), "=&v"(rv[1]), "=&v"(gv[0]), "=&v"(gv[1]), "=&v"(ev)
                         : "v"(0u), "v"(l16), "s"(wr), "s"(gw), "s"(ws),
                           "v"(l < 32 ? WS_Y + cs * 4096 + kq * 1024 + hs * 512 + l16 : WS_GU + cs * 4096 + kq * 512 + hs * 256 + l15 * 16), "v"(z4),
                           "v"(hs ? kf_z + sch * 8 : kf_kv + l * 8), "v"(0ull)
                         : "memory");
            RS(1);
#pragma unroll
            for (int r = 0; r < 2; ++r)
                asm volatile("global_load_dwordx4 %0, %4, %5\n\tglobal_load_dwordx4 %1, %4, %5 offset:1024\n\t"
                             "global_load_dwordx4 %2, %4, %5 offset:2048\n\tglobal_load_dwordx4 %3, %4, %5 offset:3072"
                             : "=&v"(xv[2 * r][0]), "=&v"(xv[2 * r][1]), "=&v"(xv[2 * r + 1][0]), "=&v"(xv[2 * r + 1][1])
                             : "v"(l16), "s"(x + (size_t)(row0 + 2 * r) * DM) : "memory");
            SB0();
            waitv<0>();
            asm volatile("" : "+v"(rv[0]), "+v"(rv[1]), "+v"(gv[0]), "+v"(gv[1]), "+v"(ev));
#pragma unroll
            for (int r = 0; r < 4; ++r) asm volatile("" : "+v"(xv[r][0]), "+v"(xv[r][1]));
            RS(2);
            T = ((u64)__builtin_amdgcn_readfirstlane((unsigned)(ev >> 32)) << 32 | __builtin_amdgcn_readfirstlane((unsigned)ev)) + 1;
            float ra_[4] = {0.f, 0.f, 0.f, 0.f}, ga_[4] = {0.f, 0.f, 0.f, 0.f};
#pragma unroll
            for (int r = 0; r < 4; ++r)
#pragma unroll
                for (int i = 0; i < 2; ++i)
#pragma unroll
                    for (int j = 0; j < 4; ++j) {
                        ra_[r] = dot2(xv[r][i][j], rv[i][j], ra_[r]);
                        ga_[r] = dot2(xv[r][i][j], gv[i][j], ga_[r]);
                    }
            const int rr = (g >> 1) | ((g & 1) << 1);
            unsigned k = f2bf(sum4(ra_));
            k ^= (k & 0x8000u) ? 0xFFFFu : 0x8000u;
            k = (k << 3) | (7 - 4 * hs - rr);
            auto r1 = __builtin_amdgcn_permlane16_swap(k, k, false, false);
            k = max(r1[0], r1[1]);
            auto r2 = __builtin_amdgcn_permlane32_swap(k, k, false, false);
            k = max(r2[0], r2[1]);
            if (hs) {
                lds_write<unsigned>(lds0 + L_BEST, k | 0x80000000u);
            } else {
                lds_wait(lds0 + L_BEST, 1);
                const u64 rq = (T << 19) | max(k, lds_read<unsigned>(lds0 + L_BEST) & 0x7FFFFu);
                ST_MASK("global_store_dwordx2", 1ull, 0u, rq, ws + WS_RG + (mt * 16 + hp) * 8);
            }
            const u64 gq = (T << 16) | f2bf(1.f / (1.f + __expf(-rbf(sum4(ga_)))));
            ST_MASK("global_store_dwordx2", 0x0001000100010001ull, (unsigned)(rr * 8), gq, ws + WS_GG + row0 * 8);
            RS(3);
            __builtin_amdgcn_s_setprio(SPR2);
            fdef();
            if (GQ0 || WPF == 3) {
                lds_wait8(W4 + L_PROG + (FBA + FBS * turn) * 16, 1);
                __builtin_amdgcn_s_sleep(FBD);
                gu_w(0, GQ0);
                if (WPF == 3) {
                    unsigned p0, p1;
                    asm volatile("global_load_dword %0, %2, %3\n\tglobal_load_dword %1, %2, %4\n\ts_waitcnt vmcnt(0)" : "=&v"(p0), "=&v"(p1)
                                 : "v"((unsigned)(((rt & 3) * 256 + hs * 128 + l) * 2048)), "s"((const char*)wo + cb * 128), "s"((const char*)wo + cb * 128 + 64 * 2048) : "memory");
                }
            }
            lds_wait8(W4 + L_PROG + (CLR0 + CLS * ((ph4 + CLP) & 3)) * 16, 1);
            if (!CLO) o_clear();
#pragma unroll
            for (int n = 0; n < 4; ++n) st_wt(rsrc, WS_KV + ((hp * 8 + hs * 4 + n) * 16 + mt) * 1024 + l16, z4);
            ST_MASK("global_store_dwordx4", 0xFFFFull, WS_Z + ((hp * 4 + hs * 2 + (srow & 1)) * 16 + mt) * 128 + sch * 16, z4, ws);
            waitv<0>();
            ST_MASK("global_store_dwordx2", 1ull, 0u, T, ws + WS_CL + ((mt * 2 + hs) * 16 + hp) * 8);
            if (hs) {
                u32x4 sv[2], xs;
                lds_wait8(W4 + L_PROG + (SEL0) * 16, 1);
                const unsigned tl = (unsigned)T & 0x1FFFu, thi = (unsigned)(T >> 13);
#pragma unroll 1
                for (unsigned s = 0; s < SPIN_CAP; ++s) {
                    if (s) __builtin_amdgcn_s_sleep(16);
                    asm volatile("global_load_dwordx4 %0, %2, %3 sc1\n\tglobal_load_dwordx4 %1, %2, %3 offset:16 sc1\n\ts_waitcnt vmcnt(0)"
                                 : "=&v"(sv[0]), "=&v"(sv[1]) : "v"(l * 32), "s"(ws + WS_RG) : "memory");
                    const unsigned bad = ((sv[0][0] >> 19) ^ tl) | (sv[0][1] ^ thi) | ((sv[0][2] >> 19) ^ tl) | (sv[0][3] ^ thi) |
                                         ((sv[1][0] >> 19) ^ tl) | (sv[1][1] ^ thi) | ((sv[1][2] >> 19) ^ tl) | (sv[1][3] ^ thi);
                    if (__all(bad == 0u)) break;
                }
                RS(4);
                unsigned k2 = 0;
#pragma unroll
                for (int j = 0; j < 4; ++j) {
                    const unsigned v = sv[j >> 1][(j & 1) * 2] & 0x7FFFFu;
                    k2 = max(k2, ((v >> 3) << 11) | ((255 - (4 * l + j)) << 3) | (v & 7));
                }
                const unsigned key = __builtin_amdgcn_readfirstlane(wave_umax(k2));
                asm volatile("global_load_dwordx4 %0, %1, %2" : "=&v"(xs) : "v"((unsigned)(512 * kq + (l & 31) * 16)), "s"(x + (size_t)(2047 - (int)(key & 2047u)) * DM) : "memory");
                unsigned rb = key >> 11;
                rb ^= (rb & 0x8000u) ? 0x8000u : 0xFFFFu;
                const float rsel = rbf(1.f / (1.f + __expf(-bf2f((u16)rb))));
                lds_write<u32x4>(lds0 + L_SEL, (u32x4){(unsigned)T, (unsigned)(T >> 32), 2047u - (key & 2047u), __float_as_uint(rsel)});
                if (bid == 0) ST_MASK("global_store_dwordx2", 1ull, 0u, T, ws + WS_E);
                waitv<0>();
                pin(xs);
                lds_write<u32x4>(lds0 + L_XS + (l & 31) * 16, xs);
                RS(5);
            }
            if (GQ0) {
                waitv<0>();
                fpin();
            }
            if (FBL) lds_wait8(lds0 + L_FLD + (l & 1) * 4, 1);
            else lds_wait8(W4 + L_PROG + (FBB) * 16, 1);
        }
        const char* cla = ws + WS_CL + ((l15 * 2 + (mt >> 3)) * 16 + hp) * 8;
        u64 cl;
        unsigned pf;
        auto wo_warm = [&]() AI { asm volatile("global_load_dword %0, %1, %2" NTO : "=&v"(pf) : "v"((unsigned)(((rt & 3) * 256 + rg * 64 + l) * 2048)), "s"((const char*)wo + cb * 128) : "memory"); };
        RS(6);
        gu_w(GQ0, GQ1);
        if (WPF == 1) wo_warm();
        RS(7);
        if (HPRIO) __builtin_amdgcn_s_setprio(HPRIO);
        {
            bf16x8 a0[2], a1[2], kb[2];
            f32x4 pk[2][2][2];
            unsigned pw[4];
            phi_w(a0, 0);
            phi_w(a1, 1);
            phi_adr(pw, lds0 + P_U[0] * 8192);
            asm volatile("" : "+v"(pw[0]), "+v"(pw[1]), "+v"(pw[2]), "+v"(pw[3]));
            auto logits = [&](int h) AI {
#pragma unroll
                for (int i = 0; i < (SH && h ? 1 : 2); ++i) kb[i] = lds_read128(AX + (2 * h + i) * 1024);
#pragma unroll
                for (int i = 0; i < (SH && h ? 1 : 2); ++i)
#pragma unroll
                    for (int fb = 0; fb < 2; ++fb) pk[h][i][fb] = mfma16(h ? a1[fb] : a0[fb], kb[i], zero);
            };
            auto round = [&](int h) AI {
#pragma unroll
                for (int i = 0; i < (SH && h ? 2 : 4); ++i) pk[h][i >> 1][i & 1] = rbf4(pk[h][i >> 1][i & 1]);
            };
            lds_wait(WR + L_FK0, 1);
            RS(8);
            logits(0);
            if (KRND) round(0);
            SB0();
            RS(9);
            phi_k<KPK & 1>(pk[0], pw, 0, [&]() AI {
                lds_wait(W4 + L_FK, 1);
                logits(1);
                SB0();
                flag(WR + L_FKT);
                if (!LF && loader) sfor<NF1, NF>(fill);
                gu_w(GQ1, GQ2);
            });
            SB0();
            if (KRND) round(1);
#pragma unroll
            for (int i = 0; i < 4; ++i) pw[i] += (P_U[2] - P_U[0]) * 8192;
            gu_w(GQ2, 8);
            if (WPF == 2) wo_warm();
            asm volatile("global_load_dwordx2 %0, %1, off sc1" : "=&v"(cl) : "v"(cla) : "memory");
            RS(10);
            if (SH) phi_k1(pk[1][0], pw);
            else phi_k<(KPK >> 1)>(pk[1], pw, 0, []() {});
            SB0();
            flag(WR + L_FP);
        }
        RS(11);
        {
            bf16x8 ones;
#pragma unroll
            for (int j = 0; j < 8; ++j) ones[j] = (short)0x3f80;
            const unsigned ra = lds0 + (hs ? P_U[2] : P_U[0]) * 8192 + (4 * g + q) * 128 + (((8 * (rg & 1) + p) ^ fsw(4 * g + q)) << 3);
            const unsigned va = lds0 + L_A + hs * 2048 + l16;
            bf16x8 b0[4], b1[4], af[2][4];
            lds_wait(lds0 + L_FV + sch * 4, 1);
            RS(12);
#pragma unroll
            for (int i = 0; i < 4; ++i) {
                af[0][i] = cat8(tr_read(ra + i * 4096), tr_read(ra + i * 4096 + 2048));
                b0[i] = lds_read128(va + i * 4096);
                b1[i] = lds_read128(va + i * 4096 + 1024);
            }
            sq = lds_read<u32x4>(lds0 + L_SEL);
#pragma unroll
            for (int i = 0; i < 4; ++i) af[1][i] = cat8(tr_read((ra ^ 32) + i * 4096), tr_read((ra ^ 32) + i * 4096 + 2048));
            SB0();
            f32x4 k0[2], k1[2], zt[2];
#pragma unroll
            for (int e = 0; e < 2; ++e) {
                k0[e] = k1[e] = zt[e] = zero;
#pragma unroll
                for (int i = 0; i < 4; ++i) {
                    k0[e] = mfma16(af[e][i], b0[i], k0[e]);
                    k1[e] = mfma16(af[e][i], b1[i], k1[e]);
                    zt[e] = mfma16(af[e][i], ones, zt[e]);
                }
            }
            const unsigned kv_st = WS_KV + ((hp * 8 + rg * 2) * 16 + mt) * 1024 + l16;
            st_wt(rsrc, kv_st, nz4(__builtin_bit_cast(u32x4, pack44(k0[0], k0[1]))));
            st_wt(rsrc, kv_st + 16384, nz4(__builtin_bit_cast(u32x4, pack44(k1[0], k1[1]))));
            const u32x4 zo = nz4((l & 1) ? zt[1] : zt[0]);
            ST_MASK("global_store_dwordx4", 0x0003000300030003ull, WS_Z + ((hp * 4 + rg) * 16 + mt) * 128 + (l & 1) * 64 + g * 16, zo, ws);
            SB0();
            flag(WR + L_FKS);
        }
        RS(13);
        auto gate_up = [&]() AI {
            sel_init();
            u32x4 xs[4];
#pragma unroll
            for (int i = 0; i < 4; ++i) xs[i] = lds_read<u32x4>(lds0 + L_XS + (i >> 1) * 256 + srow * 32 + (i & 1) * 16);
            f32x4 a = zero;
#pragma unroll
            for (int i = 0; i < 32; ++i) a = mfma16(spread(xs[i >> 3], i & 7), __builtin_bit_cast(bf16x8, fw[i]), a);
            fold_store(a, WS_GU + cs * 4096 + kq * 512 + hs * 256);
            SB0();
        };
        waitv<3>();
        asm volatile("" : "+v"(cl));
        if (WPF == 1 || WPF == 2) asm volatile("" : "+v"(pf));
        if (!loader) {
            __builtin_amdgcn_s_setprio(0);
            fpin();
            if (GUF) gate_up();
            if (WOE) {
                lds_wait(lds0 + L_FKS + (l & 8 ? L_FGI - L_FKS + (l & 1) * 4 : p * 4), 1);
                if (LF && NF1 + LC < NF) {
                    vw_init();
                    sfor<NF1 + LC, NF>(fill);
                }
                if (CLO == 1) o_clear();
                wo_load(0, WOE, WOE == WON);
                if (CLO == 3) o_clear();
            }
            if (CLO) {
                if (CLO == 1) waitv<4 * WOE>();
                else waitv<0>();
                const u32x2 tv = {sq[0], sq[1]};
                ST_MASK("global_store_dwordx2", 1ull, 0u, tv, ws + WS_CL + 4096 + ((mt * 2 + hs) * 16 + hp) * 8);
                flag(lds0 + L_FKF + 8 + hs * 4);
            }
            RS(14);
            if (!GUF) gate_up();
            RS(15);
            if (WON > WOE) {
                lds_wait(lds0 + L_FKS + (l15 < (WG2 ? 8 : 10) ? l15 : 0) * 4, 1);
                wo_load(WOE, WON, true);
            }
        } else {
            {
                u32x4 kp_[6];
                const char* kb = ws + WS_KV + (hp * 8 + (mt >> 1)) * 16384 + (mt & 1) * 512 + hs * 256;
                const bool zw = hs && !(mt & 3);
                const char* zb = zw ? ws + WS_Z + (hp * 4 + (mt >> 2)) * 2048 : kb;
                const unsigned kvo = g * 1024 + l15 * 16, zo = zw ? l15 * 128 + g * 16 : 0u;
                auto ko = [&](int i) { return i < 4 ? kvo : zo; };
                auto ka = [&](int i) { return i < 4 ? kb + i * 4096 : zb + (i - 4) * 64; };
                const u64 T = (u64)sq[1] << 32 | sq[0];
#pragma unroll 1
                for (unsigned s = 0; s < SPIN_CAP && __ballot(cl != T); ++s)
                    asm volatile("global_load_dwordx2 %0, %1, off sc1\n\ts_waitcnt vmcnt(0)" : "=&v"(cl) : "v"(cla) : "memory");
                if (GW) lds_wait(W4 + L_FKS, 1);
#pragma unroll
                for (int i = 0; i < 6; ++i) ld_async(kp_[i], ko(i), ka(i));
                flag(lds0 + L_FGI + hs * 4);
                SB0();
                RS(14);
                waitv<0>();
                RS(15);
                settle(kp_, ko, ka);
                RS(24);
                float v[8], u[4];
#pragma unroll
                for (int e = 0; e < 4; ++e) {
                    v[2 * e] = (lo16(kp_[0][e]) + lo16(kp_[1][e])) + (lo16(kp_[2][e]) + lo16(kp_[3][e]));
                    v[2 * e + 1] = (hi16(kp_[0][e]) + hi16(kp_[1][e])) + (hi16(kp_[2][e]) + hi16(kp_[3][e]));
                }
#pragma unroll
                for (int e = 0; e < 4; ++e) u[e] = swap32_add(v[e], v[e + 4]);
                const unsigned o = nz1(pack2(swap16_add(u[0], u[2]), swap16_add(u[1], u[3])));
                ST_MASK("global_store_dword", ~0ull, kf_kv + hs * 256 + l15 * 16 + g * 4, o, ws);
                if (zw) {
                    f32x4 z[2];
#pragma unroll
                    for (int e = 0; e < 2; ++e)
#pragma unroll
                        for (int i = 0; i < 4; ++i) z[e][i] = dpp_add<0x140>(dpp_add<0x141>(dpp_add<0x4E>(dpp_add<0xB1>(__uint_as_float(kp_[4 + e][i])))));
                    const u32x4 oz = nz4(__builtin_bit_cast(u32x4, pack44(z[0], z[1])));
                    ST_MASK("global_store_dwordx4", 0x0001000100010001ull, kf_z + g * 16, oz, ws);
                }
                flag(lds0 + L_FG + hs * 4);
            }
            RS(25);
            u32x4 kf[6];
            const char* kfb = ws + WS_KF + (hp * 2 + hs) * 8192;
            auto fo = [&](int i) { return i < 4 ? l16 : (unsigned)g * 16; };
            auto fb = [&](int i) { return i < 4 ? kfb + i * 1024 : kfb + 4096 + (i - 4) * 64; };
            if (KFD) __builtin_amdgcn_s_sleep(KFD);
#pragma unroll
            for (int i = 0; i < 6; ++i) ld_async(kf[i], fo(i), fb(i));
            waitv<0>();
            RS(29);
            settle(kf, fo, fb);
#pragma unroll
            for (int i = 0; i < 4; ++i) lds_write<u32x4>(lds0 + L_KF + hs * 4096 + i * 1024 + l16, kf[i]);
#pragma unroll
            for (int i = 0; i < 2; ++i) lds_write<u32x4>(lds0 + L_KZ + hs * 128 + i * 64 + g * 16, kf[4 + i]);
            SB0();
            flag(lds0 + L_FKF + hs * 4);
        }
        RS(26);
        lds_wait(WR + L_FQ, 1);
#pragma unroll
        for (int i = 0; i < 4; ++i) ph[i >> 1][i & 1] = lds_read<u32x4>(AX + i * 1024);
    } else {
        RS(0);
        lds_write<u32x4>(lds0 + L_SM + l16, z4);
        BAR();
        if (CPRIO) __builtin_amdgcn_s_setprio(CPRIO);
        unsigned ra[4], xa[2];
#pragma unroll
        for (int u = 0; u < 4; ++u) ra[u] = lds0 + (J_U[0] >> 3) * 65536 + (8 * g + q) * 128 + ((u ^ (q >> 1) ^ ((g & 1) << 1)) * 32) + 8 * p;
#pragma unroll
        for (int ks = 0; ks < 2; ++ks) xa[ks] = lds0 + (32 * (w & 1) + l15) * 128 + (((4 * ks + g) ^ (l15 >> 1)) * 16);
        const unsigned fa = lds0 + L_FULL + (l & 1) * 4;
        f32x4 acc[3][4][2];
#pragma unroll
        for (int m = 0; m < 3; ++m)
#pragma unroll
            for (int u = 0; u < 4; ++u) acc[m][u][0] = acc[m][u][1] = zero;
        bf16x8 xf[ND][2][2];
        s16x4 wh[PD + 1][2];
        unsigned fv = 0, xt;
        bf16x8 kown;
        auto chk = [&](unsigned a, int o) AI {
            SB0();
            if (__builtin_expect(__builtin_amdgcn_ballot_w64(fv == 0u) != 0ull, 0)) {
                unsigned c_;
                asm volatile("s_mov_b32 %1, 0x400000\n"
                             ".Lp%=:\n\tds_read_b32 %0, %2 offset:%3\n\ts_waitcnt lgkmcnt(0)\n\tv_cmp_eq_u32 vcc, 0, %0\n\ts_cbranch_vccz .Lf%=\n\t"
                             "s_sub_u32 %1, %1, 1\n\ts_cbranch_scc0 .Lp%=\n.Lf%=:"
                             : "=&v"(fv), "=&s"(c_) : "v"(a), "n"(o) : "memory", "vcc", "scc");
            }
            SB0();
        };
        auto rd1 = [&](auto F, auto H) AI {
            constexpr int f = F, h = H, n = f >> 2, e = f & 3, j = Q_J[n], ks = f_ks(f), u = f_u(f);
            if constexpr (h == 0 && e == 0 && Q_C[n] >= 0) chk(fa, Q_C[n] * 8);
            constexpr int st = f_step(f);
            if constexpr (h == 0 && st != 0) {
                ra[u] += st * 65536;
                asm volatile("" : "+v"(ra[u]));
            }
            wh[f % (PD + 1)][h] = tr_read(ra[u] + (J_U[j] & 7) * 8192 + ks * 4096 + h * 512);
            if constexpr (Q_X[n] >= 0 && (e & 1) == 0) {
                constexpr int s = Q_X[n] & 15;
                if constexpr (h == 0) {
                    asm volatile("v_add_u32 %0, %1, %2" : "=v"(xt) : "s"((hs ? X_U1[s] : X_U0[s]) * 8192), "v"(xa[e >> 1]));
                }
                xf[s % ND][h][e >> 1] = lds_read128(xt + h * 2048);
            }
            if constexpr (h == 1 && e == 0 && n < 95 && Q_C[n < 95 ? n + 1 : 0] >= 0) fv = *(volatile AS3 unsigned*)(size_t)(fa + Q_C[n < 95 ? n + 1 : 0] * 8);
        };
        RS(1);
        sfor<0, PD>([&](auto F) { rd1(F, IC<0>{}); rd1(F, IC<1>{}); });
        RS(2);
        constexpr int KD = 2 * (3 * NA + ND), VD = KD + 2 * ND;
        constexpr int K0F = SPLIT ? 4 * (KD - SPLIT) + 1 : 4 * KD, K1F = 4 * KD + 1;
        sfor<0, 96>([&](auto N) {
            sfor<0, 4>([&](auto E) {
                constexpr int n = N, e = E, f = 4 * n + e, j = Q_J[n], ks = f_ks(f), u = f_u(f), s = J_S[j], m = J_M[j];
                const bf16x8 wv = cat8(wh[f % (PD + 1)][0], wh[f % (PD + 1)][1]);
                sfor<0, 2>([&](auto RB) {
                    constexpr int rb = RB;
                    const bf16x8 xv = xf[s % ND][rb][ks];
                    if (AMF) {
                        if (f < 24 && ks == 0) asm volatile("v_mfma_f32_16x16x32_bf16 %0, %1, %2, 0" : "=&v"(acc[m][u][rb]) : "v"(m == 1 ? xv : wv), "v"(m == 1 ? wv : xv));
                        else asm volatile("v_mfma_f32_16x16x32_bf16 %0, %1, %2, %0" : "+v"(acc[m][u][rb]) : "v"(m == 1 ? xv : wv), "v"(m == 1 ? wv : xv));
                    } else if (m == 1) acc[m][u][rb] = mfma16(xv, wv, acc[m][u][rb]);
                    else acc[m][u][rb] = mfma16(wv, xv, acc[m][u][rb]);
                    SB0();
                    if constexpr (f + PD < 384 && (f + PD < 4 * VD || f >= 4 * VD)) rd1(IC<f + PD>{}, RB);
                    if constexpr (AMF && rb == 0 && (f == K0F || f == K1F)) asm volatile("s_nop 7");
                    SB0();
                    if constexpr (f == K0F) lds_write<bf16x8>(AX + rb * 1024, pack44(acc[0][0][rb], acc[0][1][rb]));
                    if constexpr (f == K1F) {
                        kown = pack44(acc[0][2][rb], acc[0][3][rb]);
                        if (!SH || rb == 0) lds_write<bf16x8>(AX + (2 + rb) * 1024, kown);
                        else asm volatile("" : "+v"(kown));
                    }
                    SB0();
                });
                if constexpr (f == K0F) { flag(WR + L_FK0); RS(12); }
                if constexpr (f == K1F) { flag(WR + L_FK); RS(4); }
                if constexpr (f == 4 * VD - 1) {
                    bf16x8 ak[2];
                    if (SH) {
                        __builtin_amdgcn_s_setprio(HPRIO);
                        phi_w(ak, 1);
                    }
                    lds_wait(WR + L_FKT, 1);
                    asm volatile("s_nop 3");
                    SB0();
#pragma unroll
                    for (int i = 0; i < 4; ++i) lds_write<bf16x8>(AX + i * 1024, pack44(acc[1][i][0], acc[1][i][1]));
                    SB0();
                    RS(5);
                    if (SH) {
                        f32x4 pk[2];
                        unsigned pw[4];
#pragma unroll
                        for (int fb = 0; fb < 2; ++fb) pk[fb] = rbf4(mfma16(ak[fb], kown, zero));
                        phi_adr(pw, lds0 + P_U[2] * 8192 + 2048);
                        phi_k1(pk, pw);
                        SB0();
                    }
                    flag(WR + L_FV);
                    RS(13);
                    if (LC) {
                        vw_init();
                        lds_wait(lds0 + L_FV + sch * 4, 1);
                        if (!(w & 1)) sfor<NF1, NF1 + LC>(fill);
                    }
                    if (QW) lds_wait(WR + (QW == 1 ? L_FP : L_FKS), 1);
                    __builtin_amdgcn_s_setprio(CPRIO);
                    fv = 0;
                    sfor<4 * VD, 4 * VD + PD>([&](auto F) { rd1(F, IC<0>{}); rd1(F, IC<1>{}); });
                    RS(10);
                }
                if constexpr (e == 3) {
                    if constexpr (Q_P[n] > 0) {
                        constexpr int r = Q_P[n];
                        if constexpr (r >= PWL) flag(WR + L_PROG + r * 16);
                        else if constexpr (PWN == 0 || r <= FBA || r > FBA + 3 * FBS + PWN) mark(lds0 + L_PROG + r * 16 + w * 4);
                        else if (__builtin_expect((unsigned)(r - FBA - 1 - FBS * turn) < PWN, 0)) flag(WR + L_PROG + r * 16);
                        else mark(lds0 + L_PROG + r * 16 + w * 4);
                    }
                    SB0();
                    if constexpr (n % 6 == 5) RSC(32 + n / 6);
                    if constexpr (n == 6 * NA - 1) RS(3);
                }
            });
        });
        asm volatile("s_nop 7");
        SB0();
        RS(6);
        if (CPRIO) __builtin_amdgcn_s_setprio(0);
        flag(WR + L_FQD);
        bf16x8 a0[2], a1[2];
        f32x4 pq[2][2][2];
        phi_w(a1, 1);
        phi_w(a0, 0);
#pragma unroll
        for (int h = 1; h >= 0; --h)
#pragma unroll
            for (int rb = 0; rb < 2; ++rb) {
                const bf16x8 b = pack44(acc[2][2 * h][rb], acc[2][2 * h + 1][rb]);
#pragma unroll
                for (int fb = 0; fb < 2; ++fb) pq[h][rb][fb] = rbf4(mfma16(h ? a1[fb] : a0[fb], b, zero));
            }
        softmax2<true>(pq[1], ph, []() {});
        RS(7);
        lds_wait(W4 + L_FKS, 1);
#pragma unroll
        for (int i = 0; i < 4; ++i) lds_write<u32x4>(AX + i * 1024, ph[i >> 1][i & 1]);
        SB0();
        flag(WR + L_FQ);
        sq = lds_read<u32x4>(lds0 + L_SEL);
        RS(8);
        softmax2<true>(pq[0], ph, []() {});
        asm volatile("" : "+v"(ph[0][0]), "+v"(ph[0][1]), "+v"(ph[1][0]), "+v"(ph[1][1]));
        RS(9);
        RS(11);
    }
    const char* cla = ws + WS_CL + (CLO ? 4096 : 0) + ((rt * 2 + kp) * 16 + l15) * 8;
    const u64 T = (u64)sq[1] << 32 | sq[0];
    const int row0 = rt * 128 + rg * 32 + kp * 16;
    u32x4 xr[2];
    u64 gg[2];
    const unsigned xo = srow * 2048 + sch * 16;
    auto g_load = [&]() AI {
        asm volatile("global_load_dwordx2 %0, %2, %3 sc1\n\tglobal_load_dwordx2 %1, %2, %3 offset:64 sc1"
                     : "=&v"(gg[0]), "=&v"(gg[1]) : "v"((unsigned)srow * 8), "s"(ws + WS_GG + row0 * 8) : "memory");
    };
    u64 cl;
#pragma unroll
    for (int j = 0; j < 2; ++j) asm volatile("global_load_dwordx4 %0, %1, %2" : "=&v"(xr[j]) : "v"(xo), "s"(x + (size_t)(row0 + 8 * j) * DM + cb * 64) : "memory");
    g_load();
    {
        u32x4 kf[6];
        lds_wait(lds0 + L_FKF + kp * 4 + (CLO ? (l & 1) * 8 : 0), 1);
#pragma unroll
        for (int i = 0; i < 4; ++i) kf[i] = lds_read<u32x4>(lds0 + L_KF + kp * 4096 + i * 1024 + l16);
#pragma unroll
        for (int i = 0; i < 2; ++i) kf[4 + i] = lds_read<u32x4>(lds0 + L_KZ + kp * 128 + i * 64 + g * 16);
        lds_set(lds0 + L_FWO + w * 4);
        asm volatile("global_load_dwordx2 %0, %1, off sc1" : "=&v"(cl) : "v"(cla) : "memory");
        RS(27);
        o_store(kf, ph, rsrc, o_st + kp * 4096);
        RS(28);
    }
    if (WON < 16) flag(lds0 + L_FOS + w * 4);
    waitv<3>();
    __builtin_amdgcn_s_setprio(0);
    if ((w & 5) == 4) gu_load();
    if (WON < 16) {
        if ((w & 5) == 5) {
            lds_wait(lds0 + L_FOS + sch * 4, 1);
            wo_load(WON, 16, true);
        }
    }
    const u32x2 selv = {sq[2], sq[3]};

    RS(30);
    const char* ob = ws + WS_O + (rt * 2 + (rg >> 1)) * 131072 + (rg & 1) * 2048;
    const unsigned ov0 = l16 + kp * 1024, ov1 = l16 + (kp ^ 1) * 1024;
    auto oa = [&](int i) { return ob + ((2 * i + kp + rot) & 31) * 4096; };
    unsigned pv = 0, rdy = 0;
    const unsigned pof = ((2 * l15 + kp + rot) & 31) * 4096 + 2044;
    auto poll = [&]() {
        u64 keep;
        asm volatile("s_mov_b64 %1, exec\n\ts_mov_b64 exec, %4\n\tglobal_load_dword %0, %2, %3 sc1\n\ts_mov_b64 exec, %1\n\ts_waitcnt vmcnt(0)"
                     : "+&v"(pv), "=&s"(keep) : "v"(pof), "s"(ob), "s"((u64)(~rdy & 0xFFFFu)) : "memory");
        rdy = (unsigned)__ballot(pv != 0u);
    };
    constexpr int OW = OWIN;
    u32x4 of[OW][4];
    auto fetch = [&](int k) {
#pragma unroll 1
        for (unsigned s = 0; k < NPL && s < SPIN_CAP && (~rdy & (3u << (2 * k))); ++s) poll();
#pragma unroll
        for (int j = 0; j < 4; ++j) ld_async(of[k % OW][j], (j & 1) ? ov1 : ov0, oa(2 * k + (j >> 1)));
    };
    const bool own = (__builtin_amdgcn_readfirstlane(selv[0]) >> 4) == (unsigned)(rt * 8 + rg * 2 + kp);
    float gv_[2];
    f32x2 gb[2][4];
    auto g_mix = [&]() AI {
#pragma unroll
        for (int j = 0; j < 2; ++j) {
            gv_[j] = bf2f((u16)gg[j]);
            const float og = rbf(1.f - gv_[j]);
#pragma unroll
            for (int i = 0; i < 4; ++i) {
                const unsigned t = pack2(og * lo16(xr[j][i]), og * hi16(xr[j][i]));
                gb[j][i] = (f32x2){lo16(t), hi16(t)};
                asm volatile("" : "+v"(gb[j][i]));
            }
        }
    };
    asm volatile("" : "+v"(xr[0]), "+v"(xr[1]), "+v"(gg[0]), "+v"(gg[1]));
#pragma unroll 1
    for (unsigned s = 0; s < SPIN_CAP && __ballot((gg[0] >> 16) != (T & 0xFFFFFFFFFFFFull) || (gg[1] >> 16) != (T & 0xFFFFFFFFFFFFull)); ++s) {
        g_load();
        waitv<0>();
        asm volatile("" : "+v"(gg[0]), "+v"(gg[1]));
    }
    g_mix();
    if (PDL && (w & 5) != 5) __builtin_amdgcn_s_sleep(PDL);
    if (!kp) waitv<2>();
    else if (!(w & 1)) waitv<4>();
    else waitv<(WON < 16 ? 3 + 4 * (16 - WON) : 2)>();
    asm volatile("" : "+v"(cl));
#pragma unroll 1
    for (unsigned s = 0; s < SPIN_CAP && __ballot(cl != T); ++s)
        asm volatile("global_load_dwordx2 %0, %1, off sc1\n\ts_waitcnt vmcnt(0)" : "=&v"(cl) : "v"(cla) : "memory");
    if (NPL) poll();
    RS(16);
#pragma unroll
    for (int k = 0; k < OW - 1; ++k) fetch(k);
    RS(17);
    f32x4 acc[2][4];
#pragma unroll
    for (int i = 0; i < 4; ++i) acc[0][i] = acc[1][i] = zero;
    const unsigned wa = lds0 + 4096 * kp + (4 * g + q) * 128 + ((((g & 1) << 1) | (q >> 1)) * 32) + 8 * p;
    bf16x8 wf[2][2][4];
    auto wrd = [&](int k) {
#pragma unroll
        for (int hh = 0; hh < 2; ++hh)
#pragma unroll
            for (int nb = 0; nb < 4; ++nb) {
                const unsigned a = (wa ^ (nb << 5)) + wo_unit(2 * k + hh) * 8192;
                wf[k & 1][hh][nb] = cat8(tr_read(a), tr_read(a + 2048));
            }
    };
    u32x4 yp[14];
    const unsigned yo = srow * 4096 + cb * 256 + sch * 32;
    auto ya = [&](int i) { return ws + WS_Y + (i >> 1) * 32768 + (i & 1) * 16; };
    lds_wait(lds0 + L_FWD + ((l & 8) ? 16 + sch * 4 : (l & 1) * 4), 1);
    RS(18);
    if ((w & 5) == 4) {
        waitv<4 * (OW - 1)>();
        down();
    }
    RS(31);
    if (BPRIO && !kp) __builtin_amdgcn_s_setprio(BPRIO);
    wrd(0);
#pragma unroll
    for (int k = 0; k < 8; ++k) {
        if (k + OW - 1 < 8) fetch(k + OW - 1);
        waitvn(4 * (k + OW - 1 < 8 ? OW - 1 : 7 - k));
        settle(of[k % OW], [&](int j) { return (j & 1) ? ov1 : ov0; }, [&](int j) { return oa(2 * k + (j >> 1)); });
        if (k == YK && own) {
#pragma unroll
            for (int i = 0; i < 14; ++i) ld_async(yp[i], yo, ya(i));
        }
        SB0();
        if (WON < 16 && 2 * (k + 1) == WON) lds_wait(lds0 + L_FW2 + (l & 1) * 4, 1);
        if (k < 7) wrd(k + 1);
#pragma unroll
        for (int j = 0; j < 4; ++j)
#pragma unroll
            for (int nb = 0; nb < 4; ++nb) acc[j & 1][nb] = mfma16(wf[k & 1][j >> 1][nb], __builtin_bit_cast(bf16x8, of[k % OW][j]), acc[j & 1][nb]);
        if (k < 7) {
#pragma unroll
            for (int i = 0; i < 16; ++i) {
                __builtin_amdgcn_sched_group_barrier(0x008, 1, 0);
                __builtin_amdgcn_sched_group_barrier(0x100, 1, 0);
            }
        }
        SB0();
        if (k == 0) RS(19);
        if (k == 3) RS(23);
    }
    RS(20);
    const unsigned ea = lds0 + L_A + l16 + 4096 * w, eb = lds0 + L_A + 4096 * (w ^ 4);
    lds_write<u32x4>(ea, nz4(acc[1][0]));
#pragma unroll
    for (int nb = 1; nb < 4; ++nb) lds_write<f32x4>(ea + nb * 1024, acc[1][nb]);
    if (own) {
        waitv<0>();
        settle(yp, [&](int) { return yo; }, ya);
        f32x4 yv[2];
#pragma unroll
        for (int h = 0; h < 2; ++h) {
            yv[h] = __builtin_bit_cast(f32x4, yp[h]);
#pragma unroll
            for (int i = 1; i < 7; ++i) yv[h] += __builtin_bit_cast(f32x4, yp[2 * i + h]);
#pragma unroll
            for (int i = 0; i < 4; ++i) {
                float t = dpp_add<0x128>(yv[h][i]);
                t = swap16_add(t, t);
                yv[h][i] = rbf(rbf(swap32_add(t, t)) * __uint_as_float(selv[1]));
            }
        }
#pragma unroll
        for (int j = 0; j < 2; ++j) {
            const bool hit = row0 + 8 * j + srow == (int)selv[0];
#pragma unroll
            for (int i = 0; i < 4; ++i) {
                const unsigned t = pack2(lo16(xr[j][i]) + yv[i >> 1][2 * (i & 1)], hi16(xr[j][i]) + yv[i >> 1][2 * (i & 1) + 1]);
                xr[j][i] = hit ? t : xr[j][i];
            }
        }
        g_mix();
    }
    BAR();
    RS(21);
#pragma unroll
    for (int nb = 0; nb < 4; ++nb) acc[0][nb] += lds_read<f32x4>(eb + l16 + nb * 1024);
#pragma unroll
    for (int nb = 0; nb < 4; ++nb) asm volatile("" : "+v"(acc[0][nb]));
#pragma unroll
    for (int nb = 0; nb < 4; ++nb) {
        const u32x2 a = {pack2(acc[0][nb][0], acc[0][nb][1]), pack2(acc[0][nb][2], acc[0][nb][3])};
        lds_write<u32x2>(eb + l15 * 128 + (((2 * nb + (g >> 1)) ^ (l & 7)) * 16) + (g & 1) * 8, a);
    }
#pragma unroll
    for (int j = 0; j < 2; ++j) {
        const u32x4 av = lds_read<u32x4>(eb + j * 1024 + srow * 128 + ((sch ^ srow) * 16));
        u32x4 o;
#pragma unroll
        for (int i = 0; i < 4; ++i) {
            const unsigned ga = pack2(gv_[j] * lo16(av[i]), gv_[j] * hi16(av[i]));
            o[i] = pack2(lo16(ga) + gb[j][i][0], hi16(ga) + gb[j][i][1]);
        }
        *(u32x4*)((char*)(out + (size_t)(row0 + 8 * j) * DM + cb * 64) + xo) = o;
    }
    RS(22);
    RSDUMP();
}

extern "C" void kernel_launch(void* const* d_in, const int* in_sizes, int n_in, void* d_out, int out_size,
                              void* d_ws, size_t ws_size, hipStream_t stream) {
    fused_mix<<<256, 512, 0, stream>>>((const u16*)d_in[0], (const u16*)d_in[1], (const u16*)d_in[2], (const u16*)d_in[3],
                                       (const u16*)d_in[5], (const u16*)d_in[6], (const u16*)d_in[7], (const u16*)d_in[8],
                                       (const u16*)d_in[9], (char*)d_ws, (u16*)d_out);
}
```

```cpp
#include <hip/hip_runtime.h>
#include <hip/hip_bf16.h>
#include <stdint.h>
#include <type_traits>

typedef __attribute__((ext_vector_type(8))) short bf16x8;
typedef __attribute__((ext_vector_type(4))) short s16x4;
typedef __attribute__((ext_vector_type(4))) float f32x4;
typedef __attribute__((ext_vector_type(2))) float f32x2;
typedef __attribute__((ext_vector_type(2))) unsigned u32x2;
typedef __attribute__((ext_vector_type(4))) unsigned u32x4;
typedef __attribute__((ext_vector_type(2))) __bf16 bf16x2_t;
typedef unsigned short u16;
typedef unsigned long long u64;

#define DI __device__ __forceinline__
#define AS3 __attribute__((address_space(3)))
#define AS1 __attribute__((address_space(1)))
#define RLX_AGENT __ATOMIC_RELAXED, __HIP_MEMORY_SCOPE_AGENT

constexpr int SEQ = 2048, DM = 1024, NH = 32, FF = 3584;

DI float bf2f(u16 v) { return __uint_as_float(((unsigned)v) << 16); }
DI u16 f2bf(float f) { __bf16 b = (__bf16)f; return __builtin_bit_cast(u16, b); }
DI float rbf(float f) { return bf2f(f2bf(f)); }
DI unsigned pack2(float lo, float hi) {
    const f32x2 v = {lo, hi};
    return __builtin_bit_cast(unsigned, __builtin_convertvector(v, bf16x2_t));
}
DI float lo16(unsigned u) { return __uint_as_float(u << 16); }
DI float hi16(unsigned u) { return __uint_as_float(u & 0xffff0000u); }

DI s16x4 tr_read(unsigned a) { return __builtin_amdgcn_ds_read_tr16_b64_v4i16((AS3 s16x4*)(size_t)a); }
DI bf16x8 lds_read128(unsigned a) { return *(AS3 const bf16x8*)(size_t)a; }
template <class T> DI void lds_write(unsigned a, T v) { *(AS3 T*)(size_t)a = v; }
template <class T> DI T lds_read(unsigned a) { return *(AS3 const T*)(size_t)a; }
DI bf16x8 cat8(s16x4 a, s16x4 b) { return __builtin_shufflevector(a, b, 0, 1, 2, 3, 4, 5, 6, 7); }
DI f32x4 mfma16(bf16x8 a, bf16x8 b, f32x4 c) { return __builtin_amdgcn_mfma_f32_16x16x32_bf16(a, b, c, 0, 0, 0); }
DI bf16x8 pack44(const f32x4& a, const f32x4& b) {
    u32x4 r;
    r[0] = pack2(a[0], a[1]);
    r[1] = pack2(a[2], a[3]);
    r[2] = pack2(b[0], b[1]);
    r[3] = pack2(b[2], b[3]);
    return __builtin_bit_cast(bf16x8, r);
}
DI unsigned lds_of(const void* p) { return (unsigned)(size_t)(AS3 const char*)p; }

template <int CTRL>
DI unsigned dpp(unsigned x) { return __builtin_amdgcn_update_dpp(0u, x, CTRL, 0xf, 0xf, true); }
template <int CTRL>
DI float dpp_add(float x) { return x + __uint_as_float(dpp<CTRL>(__float_as_uint(x))); }
DI float swap32_add(float a, float b) {
    auto r = __builtin_amdgcn_permlane32_swap(__float_as_uint(a), __float_as_uint(b), false, false);
    return __uint_as_float(r[0]) + __uint_as_float(r[1]);
}
DI float swap16_add(float a, float b) {
    auto r = __builtin_amdgcn_permlane16_swap(__float_as_uint(a), __float_as_uint(b), false, false);
    return __uint_as_float(r[0]) + __uint_as_float(r[1]);
}
DI float wave_sum(float x) {
    x = dpp_add<0xB1>(x);
    x = dpp_add<0x4E>(x);
    x = dpp_add<0x141>(x);
    x = dpp_add<0x140>(x);
    x = swap16_add(x, x);
    return swap32_add(x, x);
}
DI unsigned wave_umax(unsigned x) {
    x = max(x, dpp<0xB1>(x));
    x = max(x, dpp<0x4E>(x));
    x = max(x, dpp<0x141>(x));
    x = max(x, dpp<0x140>(x));
    auto r = __builtin_amdgcn_permlane16_swap(x, x, false, false);
    x = max(r[0], r[1]);
    auto s = __builtin_amdgcn_permlane32_swap(x, x, false, false);
    return max(s[0], s[1]);
}
template <class OP, class POST>
DI void red2(float& a, float& b, OP op, POST post) {
    auto x = __builtin_amdgcn_permlane32_swap(__float_as_uint(a), __float_as_uint(b), false, false);
    const float m = op(__uint_as_float(x[0]), __uint_as_float(x[1]));
    auto y = __builtin_amdgcn_permlane16_swap(__float_as_uint(m), __float_as_uint(m), false, false);
    const float f = post(op(__uint_as_float(y[0]), __uint_as_float(y[1])));
    auto z = __builtin_amdgcn_permlane32_swap(__float_as_uint(f), __float_as_uint(f), false, false);
    a = __uint_as_float(z[0]);
    b = __uint_as_float(z[1]);
}

#define RS(i) do {} while (0)
#define RSD(i) do {} while (0)
#define RSDUMP() do {} while (0)

constexpr unsigned WS_E = 0;
constexpr unsigned WS_RG = 256;
constexpr unsigned WS_GG = WS_RG + 2048;
constexpr unsigned WS_CL = 32768;
constexpr unsigned WS_Z = 65536;
constexpr unsigned WS_KF = WS_Z + 131072;
constexpr unsigned WS_GU = WS_KF + 262144;
constexpr unsigned WS_Y = WS_GU + 262144;
constexpr unsigned WS_KV = WS_Y + 262144;
constexpr unsigned WS_O = WS_KV + (2u << 20);
constexpr unsigned WS_END = WS_O + (4u << 20);

constexpr int NA = 10, ND = 6, SPLIT = 0, NF = 64, NF1 = 59, NP = 12;
constexpr int J_S[48] = {0, 0, 0, 1, 1, 1, 2, 2, 2, 3, 3, 3, 4, 4, 4, 5, 5, 5, 6, 6, 6, 7, 7, 7, 8, 8, 8, 9, 9, 9, 10, 11, 12, 13, 14, 15, 10, 11, 12, 13, 14, 15, 10, 11, 12, 13, 14, 15};
constexpr int J_M[48] = {0, 1, 2, 0, 1, 2, 0, 1, 2, 0, 1, 2, 0, 1, 2, 0, 1, 2, 0, 1, 2, 0, 1, 2, 0, 1, 2, 0, 1, 2, 0, 0, 0, 0, 0, 0, 1, 1, 1, 1, 1, 1, 2, 2, 2, 2, 2, 2};
constexpr int J_U[48] = {6, 7, 0, 3, 16, 17, 9, 10, 11, 14, 15, 4, 7, 0, 1, 16, 17, 11, 12, 13, 14, 15, 6, 7, 0, 3, 18, 10, 11, 12, 15, 4, 5, 0, 3, 18, 8, 9, 10, 11, 12, 13, 14, 15, 4, 5, 6, 7};
constexpr int Q_J[96] = {0, 0, 1, 1, 2, 2, 3, 3, 4, 4, 5, 5, 6, 6, 7, 7, 8, 8, 9, 9, 10, 10, 11, 11, 12, 12, 13, 13, 14, 14, 15, 15, 16, 16, 17, 17, 18, 18, 19, 19, 20, 20, 21, 21, 22, 22, 23, 23, 24, 24, 25, 25, 26, 26, 27, 27, 28, 28, 29, 29, 30, 30, 31, 31, 32, 32, 33, 33, 34, 34, 35, 35, 36, 36, 37, 37, 38, 38, 39, 39, 40, 40, 41, 41, 42, 42, 43, 43, 44, 44, 45, 45, 46, 46, 47, 47};
constexpr int Q_D[96] = {0, 1, 0, 1, 0, 1, 0, 1, 0, 1, 0, 1, 0, 1, 0, 1, 0, 1, 0, 1, 0, 1, 0, 1, 0, 1, 0, 1, 0, 1, 0, 1, 0, 1, 0, 1, 0, 1, 0, 1, 0, 1, 0, 1, 0, 1, 0, 1, 0, 1, 0, 1, 0, 1, 0, 1, 0, 1, 0, 1, 0, 1, 0, 1, 0, 1, 0, 1, 0, 1, 0, 1, 0, 1, 0, 1, 0, 1, 0, 1, 0, 1, 0, 1, 0, 1, 0, 1, 0, 1, 0, 1, 0, 1, 0, 1};
constexpr int Q_X[96] = {0, -1, -1, -1, -1, -1, 1, -1, -1, -1, -1, -1, 2, -1, -1, -1, -1, -1, 3, -1, -1, -1, -1, 4, -1, -1, -1, -1, -1, 5, -1, -1, -1, 6, 7, -1, -1, -1, -1, -1, -1, -1, -1, 8, -1, -1, -1, -1, -1, 9, -1, -1, 10, 11, -1, -1, -1, -1, -1, 12, -1, -1, -1, -1, -1, -1, 13, -1, 14, -1, 15, -1, -1, -1, -1, -1, -1, -1, -1, -1, -1, -1, -1, -1, -1, -1, -1, -1, -1, -1, -1, -1, -1, -1, -1, -1};
constexpr int Q_C[96] = {0, -1, 1, -1, 2, -1, 3, -1, 4, -1, 5, -1, 6, -1, 7, -1, 8, -1, 9, -1, 10, -1, 11, 12, 13, -1, 14, -1, 15, 16, 17, -1, 18, 19, 20, -1, 21, -1, 22, -1, 23, -1, 24, 25, 26, -1, 27, -1, 28, 29, 30, -1, 31, 32, 33, -1, 34, -1, 35, 36, 37, -1, 38, -1, 39, -1, 40, -1, 41, -1, 42, -1, 43, -1, 44, -1, 45, -1, 46, -1, 47, -1, 48, -1, 49, -1, 50, -1, 51, -1, 52, -1, 53, -1, 54, -1};
constexpr int Q_P[96] = {0, 0, 0, 0, 0, 0, 1, 0, 0, 0, 0, 0, 2, 0, 0, 0, 0, 0, 3, 0, 0, 0, 0, 4, 0, 0, 0, 0, 0, 5, 0, 0, 0, 0, 6, 0, 0, 7, 0, 0, 0, 0, 0, 8, 0, 0, 0, 0, 0, 9, 0, 0, 0, 10, 0, 0, 0, 0, 0, 11, 0, 0, 0, 0, 0, 0, 0, 0, 0, 0, 0, 0, 0, 0, 0, 0, 0, 0, 0, 0, 0, 0, 0, 0, 0, 0, 0, 0, 0, 0, 0, 0, 0, 0, 0, 0};
constexpr int X_U0[16] = {4, 1, 18, 12, 5, 2, 18, 9, 4, 1, 16, 8, 13, 6, 1, 16};
constexpr int X_U1[16] = {5, 2, 8, 13, 6, 3, 8, 10, 5, 2, 17, 9, 14, 7, 2, 17};
constexpr int P_U[4] = {0, 1, 2, 3};
constexpr int W_U[16] = {0, 1, 2, 3, 8, 9, 10, 11, 12, 13, 14, 15, 4, 5, 6, 7};
constexpr int F_J[64] = {-1, 0, 1, 2, -2, 3, 4, 5, -3, 6, 7, 8, -4, 9, 10, 11, -5, 12, 13, 14, -6, 15, 16, -7, -8, 17, 18, 19, 20, 21, -9, 22, 23, 24, -10, 25, -11, 26, -12, 27, 28, 29, -13, 30, 31, 32, -14, 33, -15, 34, -16, 35, 36, 37, 38, 39, 40, 41, 42, 43, 44, 45, 46, 47};
constexpr int F_N[64] = {0, 0, 0, 0, 0, 0, 0, 0, 0, 0, 0, 0, 0, 0, 0, 1, 1, 1, 1, 1, 2, 2, 2, 2, 3, 3, 3, 3, 4, 4, 4, 4, 5, 5, 5, 5, 6, 6, 6, 6, 7, 7, 8, 8, 8, 8, 9, 9, 9, 10, 10, 10, 10, 10, 11, 11, 11, 11, 11, 12, 12, 12, 12, 12};
constexpr int F_K[64] = {-1, 0, 1, 2, -1, 3, 4, 5, -1, 6, 7, 8, -1, 9, 10, 11, 12, 13, 14, 15, 16, 17, 18, 19, -1, 20, 21, 22, 23, 24, 25, 26, 27, 28, 29, 30, -1, 31, 32, 33, 34, 35, 36, 37, 38, 39, -1, 40, -1, 41, -1, 42, 43, 44, 45, 46, 47, 48, 49, 50, 51, 52, 53, 54};
constexpr unsigned L_A = 16 * 8192;
constexpr unsigned L_KF = 18 * 8192;
constexpr unsigned L_SM = 19 * 8192;
constexpr unsigned L_FULL = L_SM;
constexpr unsigned L_PROG = L_SM + 768 - 16;
constexpr unsigned L_FW2 = L_SM + 512, L_FOS = L_SM + 480, L_FGI = L_SM + 520, L_FK0 = L_SM + 752;
constexpr unsigned L_FK = L_SM + 528, L_FKT = L_SM + 544, L_FV = L_SM + 560, L_FP = L_SM + 576, L_FKS = L_SM + 592;
constexpr unsigned L_FQD = L_SM + 608, L_FG = L_SM + 624, L_FKF = L_SM + 736, L_FQ = L_SM + 720, L_FWD = L_SM + 640, L_FWO = L_SM + 656, L_FLD = L_SM + 688, L_BEST = L_SM + 696;
constexpr unsigned L_SEL = L_SM + 704;
constexpr unsigned L_XS = L_SM + 1024, L_KZ = L_SM + 2048, L_WPHI = L_SM + 4096;
static_assert(P_U[1] == P_U[0] + 1 && P_U[3] == P_U[2] + 1, "feature maps of a head lie side by side");
constexpr int wo_unit(int i) { return W_U[i]; }
constexpr int f_ks(int f) { return Q_D[f >> 2] < 2 ? Q_D[f >> 2] : (f >> 1) & 1; }
constexpr int f_u(int f) { return Q_D[f >> 2] < 2 ? f & 3 : 2 * (Q_D[f >> 2] - 2) + (f & 1); }
constexpr int f_hi(int f) { return J_U[Q_J[f >> 2]] >> 3; }
constexpr int f_step(int f) {
    for (int i = f - 1; i >= 0; --i)
        if (f_u(i) == f_u(f)) return f_hi(f) - f_hi(i);
    return 0;
}
constexpr unsigned SPIN_CAP = 1u << 16;
__device__ const unsigned k_one[4] = {1u, 1u, 1u, 1u};

#define RSL(i) do {} while (0)
#define RSC(i) do {} while (0)
#define PD 4
#define NPH 4
#define KSK 3
#define ROTK 8
#define OWIN 2
#define CLR0 4
#define CLS 1
#define CLP 2
#define SLK 2
#define CPH 3
#define NPL 0
#define CPRIO 1
#define HPRIO 2
#define KPK 3
#define KRND 1
#define QW 1
#define WOG 1
#define WON 10
#define KFD 0
#define SEL0 9
#define SPRIO 2
#define FBA 3
#define FBB 10
#define FBS 1
#define FBL 1
#define WOP 0
#define GTL 3
#define WPF 2
#define WOE 10
#define GW 0
#define GUF 0
#define FBH 0
#define WG2 0
#define NTX 0
#define NTW 0
#define NTS 16
#define NTO ""
#define GQ1 8
#define GQ2 8
#define CLO 0
#define PDL 6
#define XK 3
#define YK 7
#define BPRIO 0
#define AMF 1
#define LF 1
#define SH 1
#define LC 0
#define SPR2 0
#define GQ0 0
#define CPF 53248
#define NF0 4
#define OTL 0
#define OST " sc1"
#define TLW 1
#define PWN 0
#define PWL 99
#define TRN 0
#define RT0 0
#define BSP 0
#define WOV 0
#define GUP 1
#define NOF 0
#define FBD 8
#define BAR() asm volatile("s_waitcnt lgkmcnt(0)\n\ts_barrier" ::: "memory")
template <int N>
DI void waitv() { asm volatile("s_waitcnt vmcnt(%0)" ::"n"(N) : "memory"); }

DI u64 ld_agent(const char* p) { return __hip_atomic_load((const AS1 u64*)p, RLX_AGENT); }
DI void st_agent(char* p, u64 v) { __hip_atomic_store((AS1 u64*)p, v, RLX_AGENT); }
DI unsigned nz1(unsigned v) { return max(v, 1u); }
DI unsigned umin(unsigned a, unsigned b) { return min(a, b); }
DI u32x4 nz4(u32x4 v) { return (u32x4){max(v[0], 1u), max(v[1], 1u), max(v[2], 1u), max(v[3], 1u)}; }
DI u32x4 nz4(f32x4 v) { return nz4(__builtin_bit_cast(u32x4, v)); }
DI unsigned min4(u32x4 v) { return min(min(v[0], v[1]), min(v[2], v[3])); }
DI u32x4 ld_wt(__amdgpu_buffer_rsrc_t r, unsigned off) { return __builtin_amdgcn_raw_buffer_load_b128(r, off, 0, 16); }
DI void st_wt(__amdgpu_buffer_rsrc_t r, unsigned off, u32x4 v) { __builtin_amdgcn_raw_buffer_store_b128(v, r, off, 0, 16); }
DI void ld_async(u32x4& v, unsigned off, const char* base) {
    asm volatile("global_load_dwordx4 %0, %1, %2 sc1" : "=&v"(v) : "v"(off), "s"(base) : "memory");
}
#define ST_MASK(op, m, off, v, base) asm volatile("s_mov_b64 exec, %3\n\t" op " %0, %1, %2 sc1\n\ts_mov_b64 exec, -1\n\ts_nop 0" ::"v"(off), "v"(v), "s"(base), "s"((u64)(m)) : "memory")
DI void pin(u32x4& v) { asm volatile("" : "+v"(v)); }
template <int N, class OFF, class BASE, class OK>
DI void settle(u32x4 (&v)[N], OFF off, BASE base, OK ok) {
#pragma unroll
    for (int i = 0; i < N; ++i) pin(v[i]);
#pragma unroll 1
    for (unsigned s = 0; s < SPIN_CAP; ++s) {
        unsigned m = ok(v[0]);
#pragma unroll
        for (int i = 1; i < N; ++i) m = min(m, ok(v[i]));
        if (__ballot(m == 0u) == 0ull) break;
#pragma unroll
        for (int i = 0; i < N; ++i) {
            u64 keep;
            asm volatile("s_mov_b64 %1, exec\n\ts_mov_b64 exec, %4\n\tglobal_load_dwordx4 %0, %2, %3 sc1\n\ts_mov_b64 exec, %1"
                         : "+&v"(v[i]), "=&s"(keep) : "v"(off(i)), "s"(base(i)), "s"((u64)__ballot(ok(v[i]) == 0u)) : "memory");
        }
        waitv<0>();
#pragma unroll
        for (int i = 0; i < N; ++i) pin(v[i]);
    }
}
template <int N, class OFF, class BASE>
DI void settle(u32x4 (&v)[N], OFF off, BASE base) { settle(v, off, base, [](const u32x4& a) { return min4(a); }); }
DI void lds_wait(unsigned a, unsigned n) {
    unsigned v, c;
    asm volatile("s_mov_b32 %1, 0x400000\n"
                 ".Lp%=:\n\tds_read_b32 %0, %2\n\ts_waitcnt lgkmcnt(0)\n\tv_cmp_gt_u32 vcc, %3, %0\n\ts_cbranch_vccz .Lf%=\n\t"
                 "s_sleep 1\n\ts_sub_u32 %1, %1, 1\n\ts_cbranch_scc0 .Lp%=\n.Lf%=:"
                 : "=&v"(v), "=&s"(c) : "v"(a), "s"(n) : "memory", "vcc", "scc");
}
DI void lds_wait8(unsigned a, unsigned n) {
    unsigned v, c;
    asm volatile("s_mov_b32 %1, 0x100000\n"
                 ".Lp%=:\n\tds_read_b32 %0, %2\n\ts_waitcnt lgkmcnt(0)\n\tv_cmp_gt_u32 vcc, %3, %0\n\ts_cbranch_vccz .Lf%=\n\t"
                 "s_sleep 4\n\ts_sub_u32 %1, %1, 1\n\ts_cbranch_scc0 .Lp%=\n.Lf%=:"
                 : "=&v"(v), "=&s"(c) : "v"(a), "s"(n) : "memory", "vcc", "scc");
}
DI void lds_peek(unsigned& v, unsigned a) { asm volatile("ds_read_b32 %0, %1" : "=v"(v) : "v"(a) : "memory"); }
DI void lds_wait(unsigned& v, unsigned a, unsigned n) {
    unsigned c;
    asm volatile("s_mov_b32 %1, 0x400000\n"
                 ".Lp%=:\n\ts_waitcnt lgkmcnt(0)\n\tv_cmp_gt_u32 vcc, %3, %0\n\ts_cbranch_vccz .Lf%=\n\t"
                 "ds_read_b32 %0, %2\n\ts_sub_u32 %1, %1, 1\n\ts_cbranch_scc0 .Lp%=\n.Lf%=:"
                 : "+v"(v), "=&s"(c) : "v"(a), "s"(n) : "memory", "vcc", "scc");
}
DI void lds_set(unsigned a) {
    asm volatile("s_waitcnt lgkmcnt(0)\n\tds_write_b32 %0, %1" ::"v"(a), "v"(1u) : "memory");
}
DI float dot2(unsigned a, unsigned b, float c) {
    return __builtin_amdgcn_fdot2_f32_bf16(__builtin_bit_cast(bf16x2_t, a), __builtin_bit_cast(bf16x2_t, b), c, false);
}
DI float sum4(const float (&a)[4]) {
    float c = swap16_add(swap32_add(a[0], a[1]), swap32_add(a[2], a[3]));
    c = dpp_add<0xB1>(c);
    c = dpp_add<0x4E>(c);
    c = dpp_add<0x141>(c);
    return dpp_add<0x140>(c);
}

template <class OP, class POST>
DI void red4(float& a, float& b, float& c, float& d, OP op, POST post) {
    auto x = __builtin_amdgcn_permlane32_swap(__float_as_uint(a), __float_as_uint(b), false, false);
    auto y = __builtin_amdgcn_permlane32_swap(__float_as_uint(c), __float_as_uint(d), false, false);
    const float m = op(__uint_as_float(x[0]), __uint_as_float(x[1])), n = op(__uint_as_float(y[0]), __uint_as_float(y[1]));
    auto z = __builtin_amdgcn_permlane16_swap(__float_as_uint(m), __float_as_uint(n), false, false);
    const unsigned f = __float_as_uint(post(op(__uint_as_float(z[0]), __uint_as_float(z[1]))));
    auto u = __builtin_amdgcn_permlane16_swap(f, f, false, false);
    auto v = __builtin_amdgcn_permlane32_swap(u[0], u[0], false, false);
    auto t = __builtin_amdgcn_permlane32_swap(u[1], u[1], false, false);
    a = __uint_as_float(v[0]);
    b = __uint_as_float(v[1]);
    c = __uint_as_float(t[0]);
    d = __uint_as_float(t[1]);
}
DI float mx2(float a, float b) { return __builtin_elementwise_maximum(a, b); }
DI float opq(float a) { asm("" : "+v"(a)); return a; }
template <bool PK, class HOOK>
DI void softmax2(const f32x4 (&v)[2][2], u32x4 (&ph)[2][2], HOOK hook) {
    constexpr float L2E = 1.4426950408889634f;
    float mx[2], mn[2], sp_[2], sn_[2];
#pragma unroll
    for (int b = 0; b < 2; ++b) {
        const f32x4 s = v[b][0], t = v[b][1];
        mx[b] = mx2(mx2(mx2(mx2(s[0], s[1]), s[2]), mx2(mx2(s[3], t[0]), t[1])), mx2(t[2], t[3]));
        mn[b] = mx2(mx2(mx2(mx2(-s[0], -s[1]), -s[2]), mx2(mx2(-s[3], -t[0]), -t[1])), mx2(-t[2], -t[3]));
    }
    red4(mx[0], mx[1], mn[0], mn[1], [](float a, float b) { return mx2(a, b); }, [](float a) { return a * -L2E; });
    f32x2 ep[2][4], en[2][4];
#pragma unroll
    for (int b = 0; b < 2; ++b) {
#pragma unroll
        for (int e = 0; e < 4; ++e) {
            const f32x2 u = {v[b][e >> 1][2 * (e & 1)], v[b][e >> 1][2 * (e & 1) + 1]};
            f32x2 a, c;
            if (PK) {
                a = __builtin_elementwise_fma(u, (f32x2){L2E, L2E}, (f32x2){mx[b], mx[b]});
                c = __builtin_elementwise_fma(u, (f32x2){-L2E, -L2E}, (f32x2){mn[b], mn[b]});
            } else {
                a = (f32x2){__builtin_fmaf(u[0], L2E, mx[b]), __builtin_fmaf(u[1], L2E, mx[b])};
                c = (f32x2){__builtin_fmaf(u[0], -L2E, mn[b]), __builtin_fmaf(u[1], -L2E, mn[b])};
            }
            ep[b][e] = (f32x2){__builtin_amdgcn_exp2f(a[0]), __builtin_amdgcn_exp2f(a[1])};
            en[b][e] = (f32x2){__builtin_amdgcn_exp2f(c[0]), __builtin_amdgcn_exp2f(c[1])};
        }
        if (PK) {
            const f32x2 s = (ep[b][0] + ep[b][1]) + (ep[b][2] + ep[b][3]), t = (en[b][0] + en[b][1]) + (en[b][2] + en[b][3]);
            sp_[b] = s[0] + s[1];
            sn_[b] = t[0] + t[1];
        } else {
            sp_[b] = ep[b][0][0];
            sn_[b] = en[b][0][0];
#pragma unroll
            for (int e = 1; e < 8; ++e) sp_[b] += ep[b][e >> 1][e & 1], sn_[b] += en[b][e >> 1][e & 1];
        }
    }
    asm volatile("" : "+v"(sp_[0]), "+v"(sn_[0]), "+v"(sp_[1]), "+v"(sn_[1]));
    hook();
    red4(sp_[0], sp_[1], sn_[0], sn_[1], [](float a, float b) { return a + b; }, [](float a) { return __builtin_amdgcn_rcpf(a); });
#pragma unroll
    for (int b = 0; b < 2; ++b)
#pragma unroll
        for (int e = 0; e < 4; ++e) {
            if (PK) {
                const f32x2 a = ep[b][e] * (f32x2){sp_[b], sp_[b]}, c = en[b][e] * (f32x2){sn_[b], sn_[b]};
                ph[b][0][e] = pack2(a[0], a[1]);
                ph[b][1][e] = pack2(c[0], c[1]);
            } else {
                ph[b][0][e] = pack2(opq(ep[b][e][0] * sp_[b]), ep[b][e][1] * sp_[b]);
                ph[b][1][e] = pack2(opq(en[b][e][0] * sn_[b]), en[b][e][1] * sn_[b]);
            }
        }
}
DI f32x4 rbf4(const f32x4& a) {
    const unsigned u = pack2(a[0], a[1]), v = pack2(a[2], a[3]);
    return (f32x4){lo16(u), hi16(u), lo16(v), hi16(v)};
}
DI int fsw(int row) { return (((row >> 1) & 3) << 2) | (((row >> 3) & 1) << 1) | (row & 1); }
template <bool PK, class HOOK>
DI void phi_k(const f32x4 (&pp)[2][2], const unsigned (&pw)[4], unsigned off, HOOK hook) {
    u32x4 ph[2][2];
    softmax2<PK>(pp, ph, hook);
#pragma unroll
    for (int b = 0; b < 2; ++b)
#pragma unroll
        for (int kf = 0; kf < 2; ++kf)
#pragma unroll
            for (int fb = 0; fb < 2; ++fb) {
                const u32x2 a = {ph[b][kf][2 * fb], ph[b][kf][2 * fb + 1]};
                lds_write<u32x2>(pw[2 * kf + fb] + off + b * 2048, a);
            }
}

DI void phi_k1(const f32x4 (&v)[2], const unsigned (&pw)[4]) {
    constexpr float L2E = 1.4426950408889634f;
    const f32x4 s = v[0], t = v[1];
    float mx = mx2(mx2(mx2(mx2(s[0], s[1]), s[2]), mx2(mx2(s[3], t[0]), t[1])), mx2(t[2], t[3]));
    float mn = mx2(mx2(mx2(mx2(-s[0], -s[1]), -s[2]), mx2(mx2(-s[3], -t[0]), -t[1])), mx2(-t[2], -t[3]));
    red2(mx, mn, [](float a, float b) { return mx2(a, b); }, [](float a) { return a * -L2E; });
    f32x2 ep[4], en[4];
#pragma unroll
    for (int e = 0; e < 4; ++e) {
        const f32x2 u = {v[e >> 1][2 * (e & 1)], v[e >> 1][2 * (e & 1) + 1]};
        const f32x2 a = __builtin_elementwise_fma(u, (f32x2){L2E, L2E}, (f32x2){mx, mx});
        const f32x2 c = __builtin_elementwise_fma(u, (f32x2){-L2E, -L2E}, (f32x2){mn, mn});
        ep[e] = (f32x2){__builtin_amdgcn_exp2f(a[0]), __builtin_amdgcn_exp2f(a[1])};
        en[e] = (f32x2){__builtin_amdgcn_exp2f(c[0]), __builtin_amdgcn_exp2f(c[1])};
    }
    const f32x2 a = (ep[0] + ep[1]) + (ep[2] + ep[3]), c = (en[0] + en[1]) + (en[2] + en[3]);
    float sp = a[0] + a[1], sn = c[0] + c[1];
    red2(sp, sn, [](float a, float b) { return a + b; }, [](float a) { return __builtin_amdgcn_rcpf(a); });
#pragma unroll
    for (int i = 0; i < 4; ++i) {
        const float r = i < 2 ? sp : sn;
        const f32x2 lo = (i < 2 ? ep[2 * i] : en[2 * i - 4]) * (f32x2){r, r}, hi = (i < 2 ? ep[2 * i + 1] : en[2 * i - 3]) * (f32x2){r, r};
        lds_write<u32x2>(pw[i], (u32x2){pack2(lo[0], lo[1]), pack2(hi[0], hi[1])});
    }
}

DI void waitvn(int n) {
#define WV(k) case k: waitv<k>(); break;
#define WV8(k) WV(k) WV(k + 1) WV(k + 2) WV(k + 3) WV(k + 4) WV(k + 5) WV(k + 6) WV(k + 7)
    switch (n < 63 ? n : 63) { WV8(0) WV8(8) WV8(16) WV8(24) WV8(32) WV8(40) WV8(48) WV8(56) }
}
#define SB0() __builtin_amdgcn_sched_barrier(0)
template <int A, int B, class F>
DI void sfor(F f) {
    if constexpr (A < B) {
        f(std::integral_constant<int, A>{});
        sfor<A + 1, B>(f);
    }
}
template <int N> using IC = std::integral_constant<int, N>;
#define AI __attribute__((always_inline))

DI void o_store(const u32x4 (&kf)[6], const u32x4 (&ph)[2][2], __amdgpu_buffer_rsrc_t rsrc, unsigned o_st) {
    const f32x4 zero = {0.f, 0.f, 0.f, 0.f};
#pragma unroll
    for (int i = 0; i < 2; ++i) {
        const bf16x8 p0 = __builtin_bit_cast(bf16x8, ph[i][0]), p1 = __builtin_bit_cast(bf16x8, ph[i][1]);
        f32x4 dn = mfma16(__builtin_bit_cast(bf16x8, kf[4]), p0, zero);
        f32x4 n0 = mfma16(__builtin_bit_cast(bf16x8, kf[0]), p0, zero);
        f32x4 n1 = mfma16(__builtin_bit_cast(bf16x8, kf[1]), p0, zero);
        dn = mfma16(__builtin_bit_cast(bf16x8, kf[5]), p1, dn);
        n0 = mfma16(__builtin_bit_cast(bf16x8, kf[2]), p1, n0);
        n1 = mfma16(__builtin_bit_cast(bf16x8, kf[3]), p1, n1);
        const float rd_ = __builtin_amdgcn_rcpf(rbf(rbf(dn[0]) + 1e-6f));
        const u32x4 nb_ = __builtin_bit_cast(u32x4, pack44(n0, n1));
        u32x4 o;
#pragma unroll
        for (int j = 0; j < 4; ++j) o[j] = pack2(lo16(nb_[j]) * rd_, hi16(nb_[j]) * rd_);
        st_wt(rsrc, o_st + i * 1024, nz4(o));
    }
}

typedef __attribute__((ext_vector_type(16))) unsigned u32x16;
typedef __attribute__((ext_vector_type(8))) unsigned u32x8;
__global__ __launch_bounds__(512, 2) void fused_mix(const u16*, const u16*, const u16*, const u16*, const u16*, const u16*, const u16*,
                                                    const u16*, const u16*, char*, u16*, const void*) {
    __shared__ __attribute__((aligned(16))) char smem[163840];
    u32x16 ka;
    u32x8 kb_;
    asm volatile("s_load_dwordx16 %0, %2, 0x0\n\ts_load_dwordx8 %1, %2, 0x40" : "=&s"(ka), "=&s"(kb_) : "s"(__builtin_amdgcn_kernarg_segment_ptr()));
    SB0();
    const int tid = threadIdx.x, l = tid & 63;
    const int w = __builtin_amdgcn_readfirstlane(tid >> 6) ^ 4;
    int bid = blockIdx.x;
    const unsigned lds0 = lds_of(smem);
    const int g = l >> 4, l15 = l & 15, q = l15 >> 2, p = l & 3;
    const int srow = l >> 3, sch = l & 7;
    const unsigned l16 = l * 16;
    unsigned vst = 0;
    const int rg = w & 3, kp = w >> 2;
    const int hs = rg >> 1;
    RS(0);
    if (CPF) {
        u64 pc;
        asm volatile("s_getpc_b64 %0\n\ts_mov_b32 m0, %2\n\ts_mov_b64 exec, 3\n\tglobal_load_lds_dword %1, %0\n\ts_mov_b64 exec, -1"
                     : "=&s"(pc) : "v"(min((unsigned)(((bid & ~7) + w) * 2 + l) * 128u, (unsigned)CPF)), "s"(lds0 + L_SM + 3584) : "memory");
    }
    lds_write<unsigned>(lds0 + L_SM + (tid & 255) * 4, 0u);
    const int slot = bid >> 3;
    const int mt = (bid & 1) * 8 + (slot & 7);
    const int hp = ((bid >> 1) & 3) * 4 + (slot >> 3);
    unsigned vx[4], vw[4];
    auto vw_init = [&]() AI {
        int t = l;
        asm volatile("" : "+v"(t));
#pragma unroll
        for (int i = 0; i < 4; ++i) vw[i] = (unsigned)((8 * i + (t >> 3)) * 6144 + ((t & 7) ^ (((t >> 4) & 1) << 1) ^ ((i & 1) << 2)) * 16 + 4096 - i * 1024);
    };
    auto vx_init = [&]() AI {
        int t = l;
        asm volatile("" : "+v"(t));
#pragma unroll
        for (int i = 0; i < 4; ++i) vx[i] = (unsigned)((8 * i + (t >> 3)) * 2048 + (((t & 7) ^ ((4 * (i & 1) + (t >> 4)) & 7)) * 16) + 4096 - i * 1024);
    };
    vx_init();
    vw_init();
    asm volatile("" : "+v"(vx[0]), "+v"(vx[1]), "+v"(vx[2]), "+v"(vx[3]), "+v"(vw[0]), "+v"(vw[1]), "+v"(vw[2]), "+v"(vw[3]));
    const int ksa = SLK * (slot & 7), ksb = KSK * ((slot + (slot >> 3)) & (NPH - 1));
    unsigned xo0 = (mt * 128 + hs * 64) * 2048, wo0 = hp * 128 + hs * (32 * 6144);
    asm volatile("s_waitcnt lgkmcnt(0)" : "+s"(ka), "+s"(kb_), "+s"(xo0), "+s"(wo0) :: "memory");
    RS(48);
#define ARG(v, i) ((u64)v[2 * (i) + 1] << 32 | v[2 * (i)])
    const u16 *x = (const u16*)ARG(ka, 0), *wqkv = (const u16*)ARG(ka, 1), *wphi = (const u16*)ARG(ka, 2), *wo = (const u16*)ARG(ka, 3);
    const u16 *wr = (const u16*)ARG(ka, 4), *wg = (const u16*)ARG(ka, 5), *wu = (const u16*)ARG(ka, 6), *wd = (const u16*)ARG(ka, 7);
    const u16* gw = (const u16*)ARG(kb_, 0);
    char* ws = (char*)ARG(kb_, 1);
    u16* out = (u16*)ARG(kb_, 2);
    const int sc = (3 - (int)((unsigned)(size_t)wqkv >> 7)) & 7;
    const unsigned* one = k_one;
    unsigned v0 = 0;
    asm volatile("" : "+s"(one), "+v"(v0));
    auto mark = [&](unsigned dst) AI {
        asm volatile("s_mov_b32 m0, %2\n\ts_mov_b64 exec, 1\n\tglobal_load_lds_dword %0, %1\n\ts_mov_b64 exec, -1"
                     ::"v"(v0), "s"(one), "s"(dst) : "memory");
    };
    auto four = [&](const unsigned (&v)[4], const char* src, unsigned dst, int nt = 0) AI {
#define FOUR(P) asm volatile("s_mov_b32 m0, %5\n\ts_nop 0\n\t" \
                     "global_load_lds_dwordx4 %0, %4" P "\n\tglobal_load_lds_dwordx4 %1, %4 offset:1024" P "\n\t" \
                     "global_load_lds_dwordx4 %2, %4 offset:2048" P "\n\tglobal_load_lds_dwordx4 %3, %4 offset:3072" P \
                     ::"v"(v[0]), "v"(v[1]), "v"(v[2]), "v"(v[3]), "s"(src), "s"(dst) : "memory")
        if (nt == 1) FOUR(" nt");
        else if (nt == 2) FOUR(" sc1");
        else if (nt == 3) FOUR(" sc0 sc1");
        else FOUR("");
    };
    const int ks0 = ksb + (SLK && (hp & 7) == sc ? ksa - ksb : 0);
    const char* xb0 = (const char*)x + xo0 - 4096;
    const char* wb0 = (const char*)wqkv + wo0 - 4096;
    auto fill = [&](auto N) AI {
        constexpr int n = N, j = F_J[n] < 0 ? 0 : F_J[n], s = F_J[n] < 0 ? -1 - F_J[n] : J_S[j], m = J_M[j];
        const int ks = (ks0 + s) & 15;
        if constexpr (F_J[n] >= 0) {
            four(vw, wb0 + ks * (64 * 6144) + (m == 2 ? 0 : 2048 * (m + 1)), lds0 + J_U[j] * 8192 + hs * 4096, s < NTS ? NTW : 0);
        } else {
            const char* xs_ = xb0 + ks * 128;
            const unsigned d = lds0 + (hs ? X_U1[s] : X_U0[s]) * 8192;
            four(vx, xs_, d, s < NTS ? NTX : 0);
            four(vx, xs_ + 32 * 2048, d + 4096, s < NTS ? NTX : 0);
        }
        if constexpr (F_K[n] > 0) mark(lds0 + L_FULL + F_K[n] * 8 + hs * 4);
    };
    static_assert(F_K[1] == 0, "");
    if (__builtin_expect((w & 5) == 4, 1)) sfor<0, 2>(fill);
    RS(49);
    asm volatile("s_barrier" ::: "memory");
    RS(50);
    if (__builtin_expect((w & 5) == 4, 1)) {
        mark(lds0 + L_FULL + hs * 4);
        sfor<2, NF0>(fill);
    }
    if (TLW) {
        const char* tb = w == 0 ? (const char*)x : w == 1 ? (const char*)wqkv : w == 2 ? (const char*)wo : w == 3 ? (const char*)wg :
                         w == 4 ? (const char*)out : w == 5 ? (const char*)wu : w == 6 ? (const char*)ws : (const char*)wd;
        const int tn = (0x1B1B1B0F1B07170Full >> (8 * w)) & 31;
        asm volatile("s_mov_b32 m0, %2\n\ts_mov_b64 exec, 1\n\tglobal_load_lds_dword %0, %1\n\ts_mov_b64 exec, -1"
                     ::"v"(v0), "s"(tb + min(slot, tn) * 262144), "s"(lds0 + L_SM + 3584) : "memory");
    }
    RS(51);
    asm volatile("" : "+s"(bid));
    const int xcd = bid & 7;
    const int rt = (xcd >> 1) * 4 + ((bid >> 3) & 3), cb = (xcd & 1) * 8 + (bid >> 5), rot = 4 * cb + ROTK * rt;
    const __amdgpu_buffer_rsrc_t rsrc = __builtin_amdgcn_make_buffer_rsrc(ws, 0, WS_END, 0x00020000);
    const u32x4 z4 = {0u, 0u, 0u, 0u};
    const f32x4 zero = {0.f, 0.f, 0.f, 0.f};
    const unsigned AX = lds0 + L_A + rg * 4096 + l16;
    const unsigned W4 = lds0 + p * 4;
    const unsigned WR = lds0 + rg * 4;
    auto flag = [&](unsigned a) AI { asm volatile("ds_write_b32 %0, %0" ::"v"(a) : "memory"); };
    auto phi_w = [&](bf16x8 (&a)[2], int h) AI {
        const unsigned ad = lds0 + L_WPHI + h * 2048 + (4 * g + q) * 64 + (((p >> 1) ^ (2 * (g & 1))) * 16) + 8 * (p & 1);
        a[0] = cat8(tr_read(ad), tr_read(ad + 1024));
        a[1] = cat8(tr_read(ad ^ 32), tr_read((ad ^ 32) + 1024));
    };
    const unsigned o_st = WS_O + ((mt * 2 + hs) * 32 + 2 * hp) * 4096 + (rg & 1) * 2048 + l16;
    u32x4 fw[32], gu[2];
    unsigned sel_[4][2];
    u32x4 ph[2][2], sq;
    const int hp2 = ((bid >> 1) & 3) * 4 + (bid >> 6);
    const bool ffn = (hp2 & 7) != sc && !(NOF & 1);
    const int ph4 = ((bid >> 3) + (bid >> 6)) & 3;
    const int turn = (ph4 + TRN) & 3;
    const int kq = mt >> 2;
    const int cs = (ffn ? hp2 - (hp2 > sc) - (hp2 > sc + 8) : 14 + (hp2 >> 3)) * 4 + ((ph4 + CPH - (int)((unsigned)(size_t)wg >> 7)) & 3);
    auto spread = [&](const u32x4& v, int i) -> bf16x8 {
        const unsigned s = v[i >> 1];
        u32x4 a;
#pragma unroll
        for (int d = 0; d < 4; ++d) a[d] = __builtin_amdgcn_perm(s, s, sel_[d][i & 1]);
        return __builtin_bit_cast(bf16x8, a);
    };
    auto fold_store = [&](const f32x4& a, unsigned off) {
        f32x4 v;
#pragma unroll
        for (int i = 0; i < 4; ++i) {
            const unsigned u = __float_as_uint(a[i]);
            const float r = __uint_as_float(__builtin_amdgcn_update_dpp(u, u, 0x128, 0xc, 0xf, false));
            v[i] = swap32_add(r, r);
        }
        const u32x4 o = nz4(v);
        ST_MASK("global_store_dwordx4", 0x00FF00FFull, off + l15 * 32 + g * 16, o, ws);
    };
    auto sel_init = [&]() AI {
#pragma unroll
        for (int d = 0; d < 4; ++d)
#pragma unroll
            for (int hf = 0; hf < 2; ++hf) {
                const unsigned b = hf ? 0x0302u : 0x0100u;
                sel_[d][hf] = (sch >> 1) != d ? 0x0c0c0c0cu : (l & 1) ? (b << 16) | 0x0c0cu : 0x0c0c0000u | b;
            }
    };
    auto gua = [&](int i) { return ws + WS_GU + cs * 4096 + i * 1024; };
    auto gu_load = [&]() AI {
#pragma unroll
        for (int i = 0; i < 2; ++i) ld_async(gu[i], l16, gua(i));
    };
    auto down = [&]() AI {
#pragma unroll
        for (int i = 0; i < 16; ++i) pin(fw[i]);
        sel_init();
        settle(gu, [&](int) { return l16; }, gua);
        const f32x4 v = __builtin_bit_cast(f32x4, gu[0]) + __builtin_bit_cast(f32x4, gu[1]);
        auto r = __builtin_amdgcn_permlane16_swap(__float_as_uint(swap32_add(v[0], v[2])), __float_as_uint(swap32_add(v[1], v[3])), false, false);
        const float gt = rbf(__uint_as_float(r[0])), up = rbf(__uint_as_float(r[1]));
        const unsigned ha = lds0 + L_A + w * 4096 + 3072;
        lds_write<u16>(ha + l15 * 8 + g * 2, f2bf(rbf(gt / (1.f + __expf(-gt))) * up));
        const u32x4 hv = lds_read<u32x4>(ha + srow * 16);
        f32x4 y0 = zero, y1 = zero;
#pragma unroll
        for (int i = 0; i < 8; ++i) {
            const bf16x8 a = spread(hv, i);
            y0 = mfma16(a, __builtin_bit_cast(bf16x8, fw[i]), y0);
            y1 = mfma16(a, __builtin_bit_cast(bf16x8, fw[8 + i]), y1);
        }
        fold_store(y0, WS_Y + cs * 4096 + kq * 1024 + hs * 512);
        fold_store(y1, WS_Y + cs * 4096 + kq * 1024 + hs * 512 + 256);
    };
    auto wo_load = [&](int n0, int n1, bool mk) AI {
        unsigned vo[4];
#pragma unroll
        for (int i = 0; i < 4; ++i) vo[i] = (unsigned)((8 * i + srow) * 2048 + ((sch ^ ((g & 3) << 1)) * 16) + 4096 - i * 1024);
        const char* wob = (const char*)wo + cb * 128 - 4096;
#pragma unroll
        for (int n = n0; n < ((NOF & 4) ? n0 : n1); ++n) four(vo, wob + ((2 * n + hs + rot) & 31) * 65536, lds0 + wo_unit(n) * 8192 + hs * 4096);
        if (mk) mark(lds0 + (n0 >= WON && WON ? L_FW2 : L_FWD) + hs * 4);
    };
    auto phi_adr = [&](unsigned (&pw)[4], unsigned base) AI {
#pragma unroll
        for (int i = 0; i < 4; ++i) pw[i] = (base + rg * 4096 + l15 * 128 + ((g ^ fsw(l15)) << 3)) ^ (i << 5);
    };

    if (__builtin_expect(w >= 4, 1)) {
        const bool loader = !(w & 1);
        const unsigned kf_kv = WS_KF + (hp * 2 + (mt >> 3)) * 8192 + (mt & 7) * 512;
        const unsigned kf_z = WS_KF + (hp * 2 + (mt >> 3)) * 8192 + 4096 + ((mt >> 2) & 1) * 64;
        const unsigned fgo = ffn ? (unsigned)((256 * kq + 16 * srow) * 7168 + cs * 128 + sch * 16) : 0u;
        const char* fsrc = (const char*)(hs ? wu : wg);
        const unsigned fdo = ffn ? (unsigned)((64 * cs + 8 * srow) * 2048 + 512 * kq + 256 * hs + sch * 16) : 0u;
        auto burst = [&](int now) AI {
            if (NOF & 8) return;
#pragma unroll
            for (int h = 0; h < 2; ++h)
                asm volatile("s_cmp_lg_u32 %8, %12\n\ts_cbranch_scc1 .Lk%=\n\t"
                             "global_load_dwordx4 %0, %9, %10 offset:-4096 nt\n\tglobal_load_dwordx4 %1, %9, %10 offset:-2048 nt\n\t"
                             "global_load_dwordx4 %2, %9, %10 nt\n\tglobal_load_dwordx4 %3, %9, %10 offset:2048 nt\n\t"
                             "global_load_dwordx4 %4, %9, %11 offset:-4096 nt\n\tglobal_load_dwordx4 %5, %9, %11 offset:-2048 nt\n\t"
                             "global_load_dwordx4 %6, %9, %11 nt\n\tglobal_load_dwordx4 %7, %9, %11 offset:2048 nt\n.Lk%=:"
                             : "+v"(fw[8 * h]), "+v"(fw[8 * h + 1]), "+v"(fw[8 * h + 2]), "+v"(fw[8 * h + 3]),
                               "+v"(fw[8 * h + 4]), "+v"(fw[8 * h + 5]), "+v"(fw[8 * h + 6]), "+v"(fw[8 * h + 7])
                             : "s"(turn), "v"(fdo), "s"((const char*)wd + 4096 + h * 128), "s"((const char*)wd + 12288 + h * 128), "s"(now) : "memory", "scc");
        };
        auto gu_w = [&](int q0, int q1) AI {
            if (NOF & 8) return;
#pragma unroll
            for (int i = q0; i < q1; ++i) {
                unsigned t1, t2, t3;
                asm volatile("s_bitcmp1_b32 %7, 0\n\ts_cbranch_scc0 .Lk%=\n\t"
                             "v_add_u32 %4, 0x1c00, %8\n\tv_add_u32 %5, 0x3800, %8\n\tv_add_u32 %6, 0x5400, %8\n\t"
                             "global_load_dwordx4 %0, %8, %9 nt\n\tglobal_load_dwordx4 %1, %4, %9 nt\n\t"
                             "global_load_dwordx4 %2, %5, %9 nt\n\tglobal_load_dwordx4 %3, %6, %9 nt\n.Lk%=:"
                             : "+v"(fw[4 * i]), "+v"(fw[4 * i + 1]), "+v"(fw[4 * i + 2]), "+v"(fw[4 * i + 3]), "=&v"(t1), "=&v"(t2), "=&v"(t3)
                             : "s"(w), "v"(fgo), "s"(fsrc + (128 * (i >> 2) + 4 * (i & 3)) * 7168) : "memory", "scc");
            }
        };
        auto o_clear = [&]() AI {
#pragma unroll
            for (int n = 0; n < ((NOF & 2) ? 1 : 8); ++n) st_wt(rsrc, WS_O + ((mt * 2 + (n >> 2)) * 32 + 2 * hp + hs) * 4096 + (n & 3) * 1024 + l16, z4);
        };
        auto fdef = [&]() AI {
#pragma unroll
            for (int i = 0; i < 32; ++i) asm volatile("" : "=v"(fw[i]));
        };
        auto fpin = [&]() AI {
#pragma unroll
            for (int i = 0; i < 32; ++i) pin(fw[i]);
        };
        if (__builtin_expect(loader, 1)) {
            vx_init();
            vw_init();
            fdef();
            sfor<NF0, NF1>([&](auto N) {
                constexpr int n = N;
                if constexpr (n > 0 && F_N[n] > F_N[n > 0 ? n - 1 : 0]) {
                    constexpr int gp = F_N[n] - 1;
                    if constexpr (gp >= FBA && gp < FBA + 4 * FBS && (gp - FBA) % FBS == 0) burst((gp - FBA) / FBS);
                    RSL(32 + F_N[n]);
                    lds_wait(W4 + L_PROG + (F_N[n]) * 16, 1);
                }
                if constexpr (n == 12) {
                    asm volatile("s_mov_b32 m0, %2\n\ts_nop 0\n\tglobal_load_lds_dwordx4 %0, %1\n\tglobal_load_lds_dwordx4 %0, %1 offset:1024"
                                 ::"v"((unsigned)((l >> 2) * 64 + (((l & 3) ^ (2 * ((l >> 4) & 1))) * 16))),
                                   "s"(wphi + (hp * 2 + hs) * 1024), "s"(lds0 + L_WPHI + hs * 2048) : "memory");
                }
                fill(N);
            });
            flag(lds0 + L_FLD + hs * 4);
            RS(1);
            waitv<28>();
            static_assert(FBA + 3 * FBS + 4 < NP, "");
            fpin();
        } else {
            const int row0 = (mt * 16 + hp) * 8 + 4 * hs;
            u64 T;
            if (SPRIO) __builtin_amdgcn_s_setprio(SPRIO);
            u32x4 rv[2], gv[2], xv[4][2];
            u64 ev;
            if (RT0) lds_wait8(W4 + L_PROG + RT0 * 16, 1);
            asm volatile("global_load_dwordx2 %4, %5, %9 sc1\n\t"
                         "global_store_dwordx4 %10, %11, %9 sc1\n\t"
                         "global_store_dwordx2 %12, %13, %9 sc1\n\t"
                         "global_load_dwordx4 %0, %6, %7\n\tglobal_load_dwordx4 %1, %6, %7 offset:1024\n\t"
                         "global_load_dwordx4 %2, %6, %8\n\tglobal_load_dwordx4 %3, %6, %8 offset:1024"
                         : "=&v"(rv[0]), "=&v"(rv[1]), "=&v"(gv[0]), "=&v"(gv[1]), "=&v"(ev)
                         : "v"(0u), "v"(l16), "s"(wr), "s"(gw), "s"(ws),
                           "v"(l < 32 ? WS_Y + cs * 4096 + kq * 1024 + hs * 512 + l16 : WS_GU + cs * 4096 + kq * 512 + hs * 256 + l15 * 16), "v"(z4),
                           "v"(hs ? kf_z + sch * 8 : kf_kv + l * 8), "v"(0ull)
                         : "memory");
            RS(1);
#pragma unroll
            for (int r = 0; r < 2; ++r)
                asm volatile("global_load_dwordx4 %0, %4, %5\n\tglobal_load_dwordx4 %1, %4, %5 offset:1024\n\t"
                             "global_load_dwordx4 %2, %4, %5 offset:2048\n\tglobal_load_dwordx4 %3, %4, %5 offset:3072"
                             : "=&v"(xv[2 * r][0]), "=&v"(xv[2 * r][1]), "=&v"(xv[2 * r + 1][0]), "=&v"(xv[2 * r + 1][1])
                             : "v"(l16), "s"(x + (size_t)(row0 + 2 * r) * DM) : "memory");
            SB0();
            waitv<0>();
            asm volatile("" : "+v"(rv[0]), "+v"(rv[1]), "+v"(gv[0]), "+v"(gv[1]), "+v"(ev));
#pragma unroll
            for (int r = 0; r < 4; ++r) asm volatile("" : "+v"(xv[r][0]), "+v"(xv[r][1]));
            RS(2);
            T = ((u64)__builtin_amdgcn_readfirstlane((unsigned)(ev >> 32)) << 32 | __builtin_amdgcn_readfirstlane((unsigned)ev)) + 1;
            float ra_[4] = {0.f, 0.f, 0.f, 0.f}, ga_[4] = {0.f, 0.f, 0.f, 0.f};
#pragma unroll
            for (int r = 0; r < 4; ++r)
#pragma unroll
                for (int i = 0; i < 2; ++i)
#pragma unroll
                    for (int j = 0; j < 4; ++j) {
                        ra_[r] = dot2(xv[r][i][j], rv[i][j], ra_[r]);
                        ga_[r] = dot2(xv[r][i][j], gv[i][j], ga_[r]);
                    }
            const int rr = (g >> 1) | ((g & 1) << 1);
            unsigned k = f2bf(sum4(ra_));
            k ^= (k & 0x8000u) ? 0xFFFFu : 0x8000u;
            k = (k << 3) | (7 - 4 * hs - rr);
            auto r1 = __builtin_amdgcn_permlane16_swap(k, k, false, false);
            k = max(r1[0], r1[1]);
            auto r2 = __builtin_amdgcn_permlane32_swap(k, k, false, false);
            k = max(r2[0], r2[1]);
            if (hs) {
                lds_write<unsigned>(lds0 + L_BEST, k | 0x80000000u);
            } else {
                lds_wait(lds0 + L_BEST, 1);
                const u64 rq = (T << 19) | max(k, lds_read<unsigned>(lds0 + L_BEST) & 0x7FFFFu);
                ST_MASK("global_store_dwordx2", 1ull, 0u, rq, ws + WS_RG + (mt * 16 + hp) * 8);
            }
            const u64 gq = (T << 16) | f2bf(1.f / (1.f + __expf(-rbf(sum4(ga_)))));
            ST_MASK("global_store_dwordx2", 0x0001000100010001ull, (unsigned)(rr * 8), gq, ws + WS_GG + row0 * 8);
            RS(3);
            __builtin_amdgcn_s_setprio(SPR2);
            fdef();
            if (GQ0 || WPF == 3) {
                lds_wait8(W4 + L_PROG + (FBA + FBS * turn) * 16, 1);
                __builtin_amdgcn_s_sleep(FBD);
                gu_w(0, GQ0);
                if (WPF == 3) {
                    unsigned p0, p1;
                    asm volatile("global_load_dword %0, %2, %3\n\tglobal_load_dword %1, %2, %4\n\ts_waitcnt vmcnt(0)" : "=&v"(p0), "=&v"(p1)
                                 : "v"((unsigned)(((rt & 3) * 256 + hs * 128 + l) * 2048)), "s"((const char*)wo + cb * 128), "s"((const char*)wo + cb * 128 + 64 * 2048) : "memory");
                }
            }
            lds_wait8(W4 + L_PROG + (CLR0 + CLS * ((ph4 + CLP) & 3)) * 16, 1);
            if (!CLO) o_clear();
#pragma unroll
            for (int n = 0; n < 4; ++n) st_wt(rsrc, WS_KV + ((hp * 8 + hs * 4 + n) * 16 + mt) * 1024 + l16, z4);
            ST_MASK("global_store_dwordx4", 0xFFFFull, WS_Z + ((hp * 4 + hs * 2 + (srow & 1)) * 16 + mt) * 128 + sch * 16, z4, ws);
            waitv<0>();
            ST_MASK("global_store_dwordx2", 1ull, 0u, T, ws + WS_CL + ((mt * 2 + hs) * 16 + hp) * 8);
            if (hs) {
                u32x4 sv[2], xs;
                lds_wait8(W4 + L_PROG + (SEL0) * 16, 1);
                const unsigned tl = (unsigned)T & 0x1FFFu, thi = (unsigned)(T >> 13);
#pragma unroll 1
                for (unsigned s = 0; s < SPIN_CAP; ++s) {
                    if (s) __builtin_amdgcn_s_sleep(16);
                    asm volatile("global_load_dwordx4 %0, %2, %3 sc1\n\tglobal_load_dwordx4 %1, %2, %3 offset:16 sc1\n\ts_waitcnt vmcnt(0)"
                                 : "=&v"(sv[0]), "=&v"(sv[1]) : "v"(l * 32), "s"(ws + WS_RG) : "memory");
                    const unsigned bad = ((sv[0][0] >> 19) ^ tl) | (sv[0][1] ^ thi) | ((sv[0][2] >> 19) ^ tl) | (sv[0][3] ^ thi) |
                                         ((sv[1][0] >> 19) ^ tl) | (sv[1][1] ^ thi) | ((sv[1][2] >> 19) ^ tl) | (sv[1][3] ^ thi);
                    if (__all(bad == 0u)) break;
                }
                RS(4);
                unsigned k2 = 0;
#pragma unroll
                for (int j = 0; j < 4; ++j) {
                    const unsigned v = sv[j >> 1][(j & 1) * 2] & 0x7FFFFu;
                    k2 = max(k2, ((v >> 3) << 11) | ((255 - (4 * l + j)) << 3) | (v & 7));
                }
                const unsigned key = __builtin_amdgcn_readfirstlane(wave_umax(k2));
                asm volatile("global_load_dwordx4 %0, %1, %2" : "=&v"(xs) : "v"((unsigned)(512 * kq + (l & 31) * 16)), "s"(x + (size_t)(2047 - (int)(key & 2047u)) * DM) : "memory");
                unsigned rb = key >> 11;
                rb ^= (rb & 0x8000u) ? 0x8000u : 0xFFFFu;
                const float rsel = rbf(1.f / (1.f + __expf(-bf2f((u16)rb))));
                lds_write<u32x4>(lds0 + L_SEL, (u32x4){(unsigned)T, (unsigned)(T >> 32), 2047u - (key & 2047u), __float_as_uint(rsel)});
                if (bid == 0) ST_MASK("global_store_dwordx2", 1ull, 0u, T, ws + WS_E);
                waitv<0>();
                pin(xs);
                lds_write<u32x4>(lds0 + L_XS + (l & 31) * 16, xs);
                RS(5);
            }
            if (GQ0) {
                waitv<0>();
                fpin();
            }
            if (FBL) lds_wait8(lds0 + L_FLD + (l & 1) * 4, 1);
            else lds_wait8(W4 + L_PROG + (FBB) * 16, 1);
        }
        const char* cla = ws + WS_CL + ((l15 * 2 + (mt >> 3)) * 16 + hp) * 8;
        u64 cl;
        unsigned pf;
        auto wo_warm = [&]() AI { asm volatile("global_load_dword %0, %1, %2" NTO : "=&v"(pf) : "v"((unsigned)(((rt & 3) * 256 + rg * 64 + l) * 2048)), "s"((const char*)wo + cb * 128) : "memory"); };
        RS(6);
        gu_w(GQ0, GQ1);
        if (WPF == 1) wo_warm();
        RS(7);
        if (HPRIO) __builtin_amdgcn_s_setprio(HPRIO);
        {
            bf16x8 a0[2], a1[2], kb[2];
            f32x4 pk[2][2][2];
            unsigned pw[4];
            phi_w(a0, 0);
            phi_w(a1, 1);
            phi_adr(pw, lds0 + P_U[0] * 8192);
            asm volatile("" : "+v"(pw[0]), "+v"(pw[1]), "+v"(pw[2]), "+v"(pw[3]));
            auto logits = [&](int h) AI {
#pragma unroll
                for (int i = 0; i < (SH && h ? 1 : 2); ++i) kb[i] = lds_read128(AX + (2 * h + i) * 1024);
#pragma unroll
                for (int i = 0; i < (SH && h ? 1 : 2); ++i)
#pragma unroll
                    for (int fb = 0; fb < 2; ++fb) pk[h][i][fb] = mfma16(h ? a1[fb] : a0[fb], kb[i], zero);
            };
            auto round = [&](int h) AI {
#pragma unroll
                for (int i = 0; i < (SH && h ? 2 : 4); ++i) pk[h][i >> 1][i & 1] = rbf4(pk[h][i >> 1][i & 1]);
            };
            lds_wait(WR + L_FK0, 1);
            RS(8);
            logits(0);
            if (KRND) round(0);
            SB0();
            RS(9);
            phi_k<KPK & 1>(pk[0], pw, 0, [&]() AI {
                lds_wait(W4 + L_FK, 1);
                logits(1);
                SB0();
                flag(WR + L_FKT);
                if (!LF && loader) sfor<NF1, NF>(fill);
                gu_w(GQ1, GQ2);
            });
            SB0();
            if (KRND) round(1);
#pragma unroll
            for (int i = 0; i < 4; ++i) pw[i] += (P_U[2] - P_U[0]) * 8192;
            gu_w(GQ2, 8);
            if (WPF == 2) wo_warm();
            asm volatile("global_load_dwordx2 %0, %1, off sc1" : "=&v"(cl) : "v"(cla) : "memory");
            RS(10);
            if (SH) phi_k1(pk[1][0], pw);
            else phi_k<(KPK >> 1)>(pk[1], pw, 0, []() {});
            SB0();
            flag(WR + L_FP);
        }
        RS(11);
        {
            bf16x8 ones;
#pragma unroll
            for (int j = 0; j < 8; ++j) ones[j] = (short)0x3f80;
            const unsigned ra = lds0 + (hs ? P_U[2] : P_U[0]) * 8192 + (4 * g + q) * 128 + (((8 * (rg & 1) + p) ^ fsw(4 * g + q)) << 3);
            const unsigned va = lds0 + L_A + hs * 2048 + l16;
            bf16x8 b0[4], b1[4], af[2][4];
            lds_wait(lds0 + L_FV + sch * 4, 1);
            RS(12);
#pragma unroll
            for (int i = 0; i < 4; ++i) {
                af[0][i] = cat8(tr_read(ra + i * 4096), tr_read(ra + i * 4096 + 2048));
                b0[i] = lds_read128(va + i * 4096);
                b1[i] = lds_read128(va + i * 4096 + 1024);
            }
            sq = lds_read<u32x4>(lds0 + L_SEL);
#pragma unroll
            for (int i = 0; i < 4; ++i) af[1][i] = cat8(tr_read((ra ^ 32) + i * 4096), tr_read((ra ^ 32) + i * 4096 + 2048));
            SB0();
            f32x4 k0[2], k1[2], zt[2];
#pragma unroll
            for (int e = 0; e < 2; ++e) {
                k0[e] = k1[e] = zt[e] = zero;
#pragma unroll
                for (int i = 0; i < 4; ++i) {
                    k0[e] = mfma16(af[e][i], b0[i], k0[e]);
                    k1[e] = mfma16(af[e][i], b1[i], k1[e]);
                    zt[e] = mfma16(af[e][i], ones, zt[e]);
                }
            }
            const unsigned kv_st = WS_KV + ((hp * 8 + rg * 2) * 16 + mt) * 1024 + l16;
            st_wt(rsrc, kv_st, nz4(__builtin_bit_cast(u32x4, pack44(k0[0], k0[1]))));
            st_wt(rsrc, kv_st + 16384, nz4(__builtin_bit_cast(u32x4, pack44(k1[0], k1[1]))));
            const u32x4 zo = nz4((l & 1) ? zt[1] : zt[0]);
            ST_MASK("global_store_dwordx4", 0x0003000300030003ull, WS_Z + ((hp * 4 + rg) * 16 + mt) * 128 + (l & 1) * 64 + g * 16, zo, ws);
            SB0();
            flag(WR + L_FKS);
        }
        RS(13);
        auto gate_up = [&]() AI {
            sel_init();
            u32x4 xs[4];
#pragma unroll
            for (int i = 0; i < 4; ++i) xs[i] = lds_read<u32x4>(lds0 + L_XS + (i >> 1) * 256 + srow * 32 + (i & 1) * 16);
            f32x4 a = zero;
#pragma unroll
            for (int i = 0; i < 32; ++i) a = mfma16(spread(xs[i >> 3], i & 7), __builtin_bit_cast(bf16x8, fw[i]), a);
            fold_store(a, WS_GU + cs * 4096 + kq * 512 + hs * 256);
            SB0();
        };
        waitv<3>();
        asm volatile("" : "+v"(cl));
        if (WPF == 1 || WPF == 2) asm volatile("" : "+v"(pf));
        if (!loader) {
            __builtin_amdgcn_s_setprio(0);
            fpin();
            if (GUF) gate_up();
            if (WOE) {
                lds_wait(lds0 + L_FKS + (l & 8 ? L_FGI - L_FKS + (l & 1) * 4 : p * 4), 1);
                if (LF && NF1 + LC < NF) {
                    vw_init();
                    sfor<NF1 + LC, NF>(fill);
                }
                if (CLO == 1) o_clear();
                wo_load(0, WOE, WOE == WON);
                if (CLO == 3) o_clear();
            }
            if (CLO) {
                if (CLO == 1) waitv<4 * WOE>();
                else waitv<0>();
                const u32x2 tv = {sq[0], sq[1]};
                ST_MASK("global_store_dwordx2", 1ull, 0u, tv, ws + WS_CL + 4096 + ((mt * 2 + hs) * 16 + hp) * 8);
                flag(lds0 + L_FKF + 8 + hs * 4);
            }
            RS(14);
            if (!GUF) gate_up();
            RS(15);
            if (WON > WOE) {
                lds_wait(lds0 + L_FKS + (l15 < (WG2 ? 8 : 10) ? l15 : 0) * 4, 1);
                wo_load(WOE, WON, true);
            }
        } else {
            {
                u32x4 kp_[6];
                const char* kb = ws + WS_KV + (hp * 8 + (mt >> 1)) * 16384 + (mt & 1) * 512 + hs * 256;
                const bool zw = hs && !(mt & 3);
                const char* zb = zw ? ws + WS_Z + (hp * 4 + (mt >> 2)) * 2048 : kb;
                const unsigned kvo = g * 1024 + l15 * 16, zo = zw ? l15 * 128 + g * 16 : 0u;
                auto ko = [&](int i) { return i < 4 ? kvo : zo; };
                auto ka = [&](int i) { return i < 4 ? kb + i * 4096 : zb + (i - 4) * 64; };
                const u64 T = (u64)sq[1] << 32 | sq[0];
#pragma unroll 1
                for (unsigned s = 0; s < SPIN_CAP && __ballot(cl != T); ++s)
                    asm volatile("global_load_dwordx2 %0, %1, off sc1\n\ts_waitcnt vmcnt(0)" : "=&v"(cl) : "v"(cla) : "memory");
                if (GW) lds_wait(W4 + L_FKS, 1);
#pragma unroll
                for (int i = 0; i < 6; ++i) ld_async(kp_[i], ko(i), ka(i));
                flag(lds0 + L_FGI + hs * 4);
                SB0();
                RS(14);
                waitv<0>();
                RS(15);
                settle(kp_, ko, ka);
                RS(24);
                float v[8], u[4];
#pragma unroll
                for (int e = 0; e < 4; ++e) {
                    v[2 * e] = (lo16(kp_[0][e]) + lo16(kp_[1][e])) + (lo16(kp_[2][e]) + lo16(kp_[3][e]));
                    v[2 * e + 1] = (hi16(kp_[0][e]) + hi16(kp_[1][e])) + (hi16(kp_[2][e]) + hi16(kp_[3][e]));
                }
#pragma unroll
                for (int e = 0; e < 4; ++e) u[e] = swap32_add(v[e], v[e + 4]);
                const unsigned o = nz1(pack2(swap16_add(u[0], u[2]), swap16_add(u[1], u[3])));
                ST_MASK("global_store_dword", ~0ull, kf_kv + hs * 256 + l15 * 16 + g * 4, o, ws);
                if (zw) {
                    f32x4 z[2];
#pragma unroll
                    for (int e = 0; e < 2; ++e)
#pragma unroll
                        for (int i = 0; i < 4; ++i) z[e][i] = dpp_add<0x140>(dpp_add<0x141>(dpp_add<0x4E>(dpp_add<0xB1>(__uint_as_float(kp_[4 + e][i])))));
                    const u32x4 oz = nz4(__builtin_bit_cast(u32x4, pack44(z[0], z[1])));
                    ST_MASK("global_store_dwordx4", 0x0001000100010001ull, kf_z + g * 16, oz, ws);
                }
                flag(lds0 + L_FG + hs * 4);
            }
            RS(25);
            u32x4 kf[6];
            const char* kfb = ws + WS_KF + (hp * 2 + hs) * 8192;
            auto fo = [&](int i) { return i < 4 ? l16 : (unsigned)g * 16; };
            auto fb = [&](int i) { return i < 4 ? kfb + i * 1024 : kfb + 4096 + (i - 4) * 64; };
            if (KFD) __builtin_amdgcn_s_sleep(KFD);
#pragma unroll
            for (int i = 0; i < 6; ++i) ld_async(kf[i], fo(i), fb(i));
            waitv<0>();
            RS(29);
            settle(kf, fo, fb);
#pragma unroll
            for (int i = 0; i < 4; ++i) lds_write<u32x4>(lds0 + L_KF + hs * 4096 + i * 1024 + l16, kf[i]);
#pragma unroll
            for (int i = 0; i < 2; ++i) lds_write<u32x4>(lds0 + L_KZ + hs * 128 + i * 64 + g * 16, kf[4 + i]);
            SB0();
            flag(lds0 + L_FKF + hs * 4);
        }
        RS(26);
        lds_wait(WR + L_FQ, 1);
#pragma unroll
        for (int i = 0; i < 4; ++i) ph[i >> 1][i & 1] = lds_read<u32x4>(AX + i * 1024);
    } else {
        if (CPRIO) __builtin_amdgcn_s_setprio(CPRIO);
        unsigned ra[4], xa[2];
#pragma unroll
        for (int u = 0; u < 4; ++u) ra[u] = lds0 + (J_U[0] >> 3) * 65536 + (8 * g + q) * 128 + ((u ^ (q >> 1) ^ ((g & 1) << 1)) * 32) + 8 * p;
#pragma unroll
        for (int ks = 0; ks < 2; ++ks) xa[ks] = lds0 + (32 * (w & 1) + l15) * 128 + (((4 * ks + g) ^ (l15 >> 1)) * 16);
        const unsigned fa = lds0 + L_FULL + (l & 1) * 4;
        f32x4 acc[3][4][2];
#pragma unroll
        for (int m = 0; m < 3; ++m)
#pragma unroll
            for (int u = 0; u < 4; ++u) acc[m][u][0] = acc[m][u][1] = zero;
        bf16x8 xf[ND][2][2];
        s16x4 wh[PD + 1][2];
        unsigned fv = 0, xt;
        bf16x8 kown;
        auto chk = [&](unsigned a, int o) AI {
            SB0();
            if (__builtin_expect(__builtin_amdgcn_ballot_w64(fv == 0u) != 0ull, 0)) {
                unsigned c_;
                asm volatile("s_mov_b32 %1, 0x400000\n"
                             ".Lp%=:\n\tds_read_b32 %0, %2 offset:%3\n\ts_waitcnt lgkmcnt(0)\n\tv_cmp_eq_u32 vcc, 0, %0\n\ts_cbranch_vccz .Lf%=\n\t"
                             "s_sub_u32 %1, %1, 1\n\ts_cbranch_scc0 .Lp%=\n.Lf%=:"
                             : "=&v"(fv), "=&s"(c_) : "v"(a), "n"(o) : "memory", "vcc", "scc");
            }
            SB0();
        };
        auto rd1 = [&](auto F, auto H) AI {
            constexpr int f = F, h = H, n = f >> 2, e = f & 3, j = Q_J[n], ks = f_ks(f), u = f_u(f);
            if constexpr (h == 0 && e == 0 && Q_C[n] >= 0) chk(fa, Q_C[n] * 8);
            constexpr int st = f_step(f);
            if constexpr (h == 0 && st != 0) {
                ra[u] += st * 65536;
                asm volatile("" : "+v"(ra[u]));
            }
            wh[f % (PD + 1)][h] = tr_read(ra[u] + (J_U[j] & 7) * 8192 + ks * 4096 + h * 512);
            if constexpr (Q_X[n] >= 0 && (e & 1) == 0) {
                constexpr int s = Q_X[n] & 15;
                if constexpr (h == 0) {
                    asm volatile("v_add_u32 %0, %1, %2" : "=v"(xt) : "s"((hs ? X_U1[s] : X_U0[s]) * 8192), "v"(xa[e >> 1]));
                }
                xf[s % ND][h][e >> 1] = lds_read128(xt + h * 2048);
            }
            if constexpr (h == 1 && e == 0 && n < 95 && Q_C[n < 95 ? n + 1 : 0] >= 0) fv = *(volatile AS3 unsigned*)(size_t)(fa + Q_C[n < 95 ? n + 1 : 0] * 8);
        };
        RS(1);
        sfor<0, PD>([&](auto F) { rd1(F, IC<0>{}); rd1(F, IC<1>{}); });
        RS(2);
        constexpr int KD = 2 * (3 * NA + ND), VD = KD + 2 * ND;
        constexpr int K0F = SPLIT ? 4 * (KD - SPLIT) + 1 : 4 * KD, K1F = 4 * KD + 1;
        sfor<0, 96>([&](auto N) {
            sfor<0, 4>([&](auto E) {
                constexpr int n = N, e = E, f = 4 * n + e, j = Q_J[n], ks = f_ks(f), u = f_u(f), s = J_S[j], m = J_M[j];
                const bf16x8 wv = cat8(wh[f % (PD + 1)][0], wh[f % (PD + 1)][1]);
                sfor<0, 2>([&](auto RB) {
                    constexpr int rb = RB;
                    const bf16x8 xv = xf[s % ND][rb][ks];
                    if (AMF) {
                        if (f < 24 && ks == 0) asm volatile("v_mfma_f32_16x16x32_bf16 %0, %1, %2, 0" : "=&v"(acc[m][u][rb]) : "v"(m == 1 ? xv : wv), "v"(m == 1 ? wv : xv));
                        else asm volatile("v_mfma_f32_16x16x32_bf16 %0, %1, %2, %0" : "+v"(acc[m][u][rb]) : "v"(m == 1 ? xv : wv), "v"(m == 1 ? wv : xv));
                    } else if (m == 1) acc[m][u][rb] = mfma16(xv, wv, acc[m][u][rb]);
                    else acc[m][u][rb] = mfma16(wv, xv, acc[m][u][rb]);
                    SB0();
                    if constexpr (f + PD < 384 && (f + PD < 4 * VD || f >= 4 * VD)) rd1(IC<f + PD>{}, RB);
                    if constexpr (AMF && rb == 0 && (f == K0F || f == K1F)) asm volatile("s_nop 7");
                    SB0();
                    if constexpr (f == K0F) lds_write<bf16x8>(AX + rb * 1024, pack44(acc[0][0][rb], acc[0][1][rb]));
                    if constexpr (f == K1F) {
                        kown = pack44(acc[0][2][rb], acc[0][3][rb]);
                        if (!SH || rb == 0) lds_write<bf16x8>(AX + (2 + rb) * 1024, kown);
                        else asm volatile("" : "+v"(kown));
                    }
                    SB0();
                });
                if constexpr (f == K0F) { flag(WR + L_FK0); RS(12); }
                if constexpr (f == K1F) { flag(WR + L_FK); RS(4); }
                if constexpr (f == 4 * VD - 1) {
                    bf16x8 ak[2];
                    if (SH) {
                        __builtin_amdgcn_s_setprio(HPRIO);
                        phi_w(ak, 1);
                    }
                    lds_wait(WR + L_FKT, 1);
                    asm volatile("s_nop 3");
                    SB0();
#pragma unroll
                    for (int i = 0; i < 4; ++i) lds_write<bf16x8>(AX + i * 1024, pack44(acc[1][i][0], acc[1][i][1]));
                    SB0();
                    RS(5);
                    if (SH) {
                        f32x4 pk[2];
                        unsigned pw[4];
#pragma unroll
                        for (int fb = 0; fb < 2; ++fb) pk[fb] = rbf4(mfma16(ak[fb], kown, zero));
                        phi_adr(pw, lds0 + P_U[2] * 8192 + 2048);
                        phi_k1(pk, pw);
                        SB0();
                    }
                    flag(WR + L_FV);
                    RS(13);
                    if (LC) {
                        vw_init();
                        lds_wait(lds0 + L_FV + sch * 4, 1);
                        if (!(w & 1)) sfor<NF1, NF1 + LC>(fill);
                    }
                    if (QW) lds_wait(WR + (QW == 1 ? L_FP : L_FKS), 1);
                    __builtin_amdgcn_s_setprio(CPRIO);
                    fv = 0;
                    sfor<4 * VD, 4 * VD + PD>([&](auto F) { rd1(F, IC<0>{}); rd1(F, IC<1>{}); });
                    RS(10);
                }
                if constexpr (e == 3) {
                    if constexpr (Q_P[n] > 0) {
                        constexpr int r = Q_P[n];
                        if constexpr (r >= PWL) flag(WR + L_PROG + r * 16);
                        else if constexpr (PWN == 0 || r <= FBA || r > FBA + 3 * FBS + PWN) mark(lds0 + L_PROG + r * 16 + w * 4);
                        else if (__builtin_expect((unsigned)(r - FBA - 1 - FBS * turn) < PWN, 0)) flag(WR + L_PROG + r * 16);
                        else mark(lds0 + L_PROG + r * 16 + w * 4);
                    }
                    SB0();
                    if constexpr (n % 6 == 5) RSC(32 + n / 6);
                    if constexpr (n == 6 * NA - 1) RS(3);
                }
            });
        });
        asm volatile("s_nop 7");
        SB0();
        RS(6);
        if (CPRIO) __builtin_amdgcn_s_setprio(0);
        flag(WR + L_FQD);
        bf16x8 a0[2], a1[2];
        f32x4 pq[2][2][2];
        phi_w(a1, 1);
        phi_w(a0, 0);
#pragma unroll
        for (int h = 1; h >= 0; --h)
#pragma unroll
            for (int rb = 0; rb < 2; ++rb) {
                const bf16x8 b = pack44(acc[2][2 * h][rb], acc[2][2 * h + 1][rb]);
#pragma unroll
                for (int fb = 0; fb < 2; ++fb) pq[h][rb][fb] = rbf4(mfma16(h ? a1[fb] : a0[fb], b, zero));
            }
        softmax2<true>(pq[1], ph, []() {});
        RS(7);
        lds_wait(W4 + L_FKS, 1);
#pragma unroll
        for (int i = 0; i < 4; ++i) lds_write<u32x4>(AX + i * 1024, ph[i >> 1][i & 1]);
        SB0();
        flag(WR + L_FQ);
        sq = lds_read<u32x4>(lds0 + L_SEL);
        RS(8);
        softmax2<true>(pq[0], ph, []() {});
        asm volatile("" : "+v"(ph[0][0]), "+v"(ph[0][1]), "+v"(ph[1][0]), "+v"(ph[1][1]));
        RS(9);
        RS(11);
    }
    const char* cla = ws + WS_CL + (CLO ? 4096 : 0) + ((rt * 2 + kp) * 16 + l15) * 8;
    const u64 T = (u64)sq[1] << 32 | sq[0];
    const int row0 = rt * 128 + rg * 32 + kp * 16;
    u32x4 xr[2];
    u64 gg[2];
    const unsigned xo = srow * 2048 + sch * 16;
    auto g_load = [&]() AI {
        asm volatile("global_load_dwordx2 %0, %2, %3 sc1\n\tglobal_load_dwordx2 %1, %2, %3 offset:64 sc1"
                     : "=&v"(gg[0]), "=&v"(gg[1]) : "v"((unsigned)srow * 8), "s"(ws + WS_GG + row0 * 8) : "memory");
    };
    u64 cl;
    if (OTL)
        asm volatile("s_mov_b32 m0, %2\n\ts_mov_b64 exec, 1\n\tglobal_load_lds_dword %0, %1\n\ts_mov_b64 exec, -1"
                     ::"v"(v0), "s"(out + (size_t)row0 * DM + cb * 64), "s"(lds0 + L_SM + 3584) : "memory");
#pragma unroll
    for (int j = 0; j < 2; ++j) asm volatile("global_load_dwordx4 %0, %1, %2" : "=&v"(xr[j]) : "v"(xo), "s"(x + (size_t)(row0 + 8 * j) * DM + cb * 64) : "memory");
    g_load();
    {
        u32x4 kf[6];
        lds_wait(lds0 + L_FKF + kp * 4 + (CLO ? (l & 1) * 8 : 0), 1);
#pragma unroll
        for (int i = 0; i < 4; ++i) kf[i] = lds_read<u32x4>(lds0 + L_KF + kp * 4096 + i * 1024 + l16);
#pragma unroll
        for (int i = 0; i < 2; ++i) kf[4 + i] = lds_read<u32x4>(lds0 + L_KZ + kp * 128 + i * 64 + g * 16);
        asm volatile("global_load_dwordx2 %0, %1, off sc1" : "=&v"(cl) : "v"(cla) : "memory");
        RS(27);
        o_store(kf, ph, rsrc, o_st + kp * 4096);
        RS(28);
    }
    if (WON < 16) flag(lds0 + L_FOS + w * 4);
    waitv<3>();
    flag(lds0 + L_FWO + w * 4);
    __builtin_amdgcn_s_setprio(0);
    if ((w & 5) == 4) gu_load();
    if (WON < 16) {
        if ((w & 5) == 5) {
            lds_wait(lds0 + L_FOS + sch * 4, 1);
            wo_load(WON, 16, true);
        }
    }
    const u32x2 selv = {sq[2], sq[3]};

    RS(30);
    const char* ob = ws + WS_O + (rt * 2 + (rg >> 1)) * 131072 + (rg & 1) * 2048;
    const unsigned ov0 = l16 + kp * 1024, ov1 = l16 + (kp ^ 1) * 1024;
    auto oa = [&](int i) { return ob + ((2 * i + kp + rot) & 31) * 4096; };
    unsigned pv = 0, rdy = 0;
    const unsigned pof = ((2 * l15 + kp + rot) & 31) * 4096 + 2044;
    auto poll = [&]() {
        u64 keep;
        asm volatile("s_mov_b64 %1, exec\n\ts_mov_b64 exec, %4\n\tglobal_load_dword %0, %2, %3 sc1\n\ts_mov_b64 exec, %1\n\ts_waitcnt vmcnt(0)"
                     : "+&v"(pv), "=&s"(keep) : "v"(pof), "s"(ob), "s"((u64)(~rdy & 0xFFFFu)) : "memory");
        rdy = (unsigned)__ballot(pv != 0u);
    };
    constexpr int OW = OWIN;
    u32x4 of[OW][4];
    auto fetch = [&](int k) {
#pragma unroll 1
        for (unsigned s = 0; k < NPL && s < SPIN_CAP && (~rdy & (3u << (2 * k))); ++s) poll();
#pragma unroll
        for (int j = 0; j < 4; ++j) ld_async(of[k % OW][j], (j & 1) ? ov1 : ov0, oa(2 * k + (j >> 1)));
    };
    const bool own = (__builtin_amdgcn_readfirstlane(selv[0]) >> 4) == (unsigned)(rt * 8 + rg * 2 + kp);
    float gv_[2];
    f32x2 gb[2][4];
    auto g_mix = [&]() AI {
#pragma unroll
        for (int j = 0; j < 2; ++j) {
            gv_[j] = bf2f((u16)gg[j]);
            const float og = rbf(1.f - gv_[j]);
#pragma unroll
            for (int i = 0; i < 4; ++i) {
                const unsigned t = pack2(og * lo16(xr[j][i]), og * hi16(xr[j][i]));
                gb[j][i] = (f32x2){lo16(t), hi16(t)};
                asm volatile("" : "+v"(gb[j][i]));
            }
        }
    };
    asm volatile("" : "+v"(xr[0]), "+v"(xr[1]), "+v"(gg[0]), "+v"(gg[1]));
#pragma unroll 1
    for (unsigned s = 0; s < SPIN_CAP && __ballot((gg[0] >> 16) != (T & 0xFFFFFFFFFFFFull) || (gg[1] >> 16) != (T & 0xFFFFFFFFFFFFull)); ++s) {
        g_load();
        waitv<0>();
        asm volatile("" : "+v"(gg[0]), "+v"(gg[1]));
    }
    g_mix();
    if (PDL && (w & 5) != 5) __builtin_amdgcn_s_sleep(PDL);
    if (!kp) waitv<2>();
    else if (!(w & 1)) waitv<4>();
    else waitv<(WON < 16 ? 3 + 4 * (16 - WON) : 2)>();
    asm volatile("" : "+v"(cl));
#pragma unroll 1
    for (unsigned s = 0; s < SPIN_CAP && __ballot(cl != T); ++s)
        asm volatile("global_load_dwordx2 %0, %1, off sc1\n\ts_waitcnt vmcnt(0)" : "=&v"(cl) : "v"(cla) : "memory");
    if (NPL) poll();
    RS(16);
#pragma unroll
    for (int k = 0; k < OW - 1; ++k) fetch(k);
    RS(17);
    f32x4 acc[2][4];
#pragma unroll
    for (int i = 0; i < 4; ++i) acc[0][i] = acc[1][i] = zero;
    const unsigned wa = lds0 + 4096 * kp + (4 * g + q) * 128 + ((((g & 1) << 1) | (q >> 1)) * 32) + 8 * p;
    bf16x8 wf[2][2][4];
    auto wrd = [&](int k) {
#pragma unroll
        for (int hh = 0; hh < 2; ++hh)
#pragma unroll
            for (int nb = 0; nb < 4; ++nb) {
                const unsigned a = (wa ^ (nb << 5)) + wo_unit(2 * k + hh) * 8192;
                wf[k & 1][hh][nb] = cat8(tr_read(a), tr_read(a + 2048));
            }
    };
    u32x4 yp[14];
    const unsigned yo = srow * 4096 + cb * 256 + sch * 32;
    auto ya = [&](int i) { return ws + WS_Y + (i >> 1) * 32768 + (i & 1) * 16; };
    lds_wait(lds0 + L_FWD + ((l & 8) ? 16 + sch * 4 : (l & 1) * 4), 1);
    RS(18);
    if ((w & 5) == 4) {
        waitv<4 * (OW - 1)>();
        down();
    }
    RS(31);
    if (BPRIO && !kp) __builtin_amdgcn_s_setprio(BPRIO);
    wrd(0);
#pragma unroll
    for (int k = 0; k < 8; ++k) {
        if (k + OW - 1 < 8) fetch(k + OW - 1);
        waitvn(4 * (k + OW - 1 < 8 ? OW - 1 : 7 - k));
        settle(of[k % OW], [&](int j) { return (j & 1) ? ov1 : ov0; }, [&](int j) { return oa(2 * k + (j >> 1)); });
        if (k == YK && own) {
#pragma unroll
            for (int i = 0; i < 14; ++i) ld_async(yp[i], yo, ya(i));
        }
        SB0();
        if (WON < 16 && 2 * (k + 1) == WON) lds_wait(lds0 + L_FW2 + (l & 1) * 4, 1);
        if (k < 7) wrd(k + 1);
#pragma unroll
        for (int j = 0; j < 4; ++j)
#pragma unroll
            for (int nb = 0; nb < 4; ++nb) acc[j & 1][nb] = mfma16(wf[k & 1][j >> 1][nb], __builtin_bit_cast(bf16x8, of[k % OW][j]), acc[j & 1][nb]);
        if (k < 7) {
#pragma unroll
            for (int i = 0; i < 16; ++i) {
                __builtin_amdgcn_sched_group_barrier(0x008, 1, 0);
                __builtin_amdgcn_sched_group_barrier(0x100, 1, 0);
            }
        }
        SB0();
        if (k == 0) RS(19);
        if (k == 3) RS(23);
    }
    RS(20);
    const unsigned ea = lds0 + L_A + l16 + 4096 * w, eb = lds0 + L_A + 4096 * (w ^ 4);
    lds_write<u32x4>(ea, nz4(acc[1][0]));
#pragma unroll
    for (int nb = 1; nb < 4; ++nb) lds_write<f32x4>(ea + nb * 1024, acc[1][nb]);
    if (own) {
        waitv<0>();
        settle(yp, [&](int) { return yo; }, ya);
        f32x4 yv[2];
#pragma unroll
        for (int h = 0; h < 2; ++h) {
            yv[h] = __builtin_bit_cast(f32x4, yp[h]);
#pragma unroll
            for (int i = 1; i < 7; ++i) yv[h] += __builtin_bit_cast(f32x4, yp[2 * i + h]);
#pragma unroll
            for (int i = 0; i < 4; ++i) {
                float t = dpp_add<0x128>(yv[h][i]);
                t = swap16_add(t, t);
                yv[h][i] = rbf(rbf(swap32_add(t, t)) * __uint_as_float(selv[1]));
            }
        }
#pragma unroll
        for (int j = 0; j < 2; ++j) {
            const bool hit = row0 + 8 * j + srow == (int)selv[0];
#pragma unroll
            for (int i = 0; i < 4; ++i) {
                const unsigned t = pack2(lo16(xr[j][i]) + yv[i >> 1][2 * (i & 1)], hi16(xr[j][i]) + yv[i >> 1][2 * (i & 1) + 1]);
                xr[j][i] = hit ? t : xr[j][i];
            }
        }
        g_mix();
    }
    BAR();
    RS(21);
#pragma unroll
    for (int nb = 0; nb < 4; ++nb) acc[0][nb] += lds_read<f32x4>(eb + l16 + nb * 1024);
#pragma unroll
    for (int nb = 0; nb < 4; ++nb) asm volatile("" : "+v"(acc[0][nb]));
#pragma unroll
    for (int nb = 0; nb < 4; ++nb) {
        const u32x2 a = {pack2(acc[0][nb][0], acc[0][nb][1]), pack2(acc[0][nb][2], acc[0][nb][3])};
        lds_write<u32x2>(eb + l15 * 128 + (((2 * nb + (g >> 1)) ^ (l & 7)) * 16) + (g & 1) * 8, a);
    }
#pragma unroll
    for (int j = 0; j < 2; ++j) {
        const u32x4 av = lds_read<u32x4>(eb + j * 1024 + srow * 128 + ((sch ^ srow) * 16));
        u32x4 o;
#pragma unroll
        for (int i = 0; i < 4; ++i) {
            const unsigned ga = pack2(gv_[j] * lo16(av[i]), gv_[j] * hi16(av[i]));
            o[i] = pack2(lo16(ga) + gb[j][i][0], hi16(ga) + gb[j][i][1]);
        }
        asm volatile("global_store_dwordx4 %0, %1, %2" OST "\n\ts_nop 1" ::"v"(xo), "v"(o), "s"(out + (size_t)(row0 + 8 * j) * DM + cb * 64) : "memory");
    }
    RS(22);
    RSDUMP();
}

extern "C" void kernel_launch(void* const* d_in, const int* in_sizes, int n_in, void* d_out, int out_size,
                              void* d_ws, size_t ws_size, hipStream_t stream) {
    fused_mix<<<256, 512, 0, stream>>>((const u16*)d_in[0], (const u16*)d_in[1], (const u16*)d_in[2], (const u16*)d_in[3],
                                       (const u16*)d_in[5], (const u16*)d_in[6], (const u16*)d_in[7], (const u16*)d_in[8],
                                       (const u16*)d_in[9], (char*)d_ws, (u16*)d_out, d_ws);
}
```

```cpp
#include <hip/hip_runtime.h>
#include <hip/hip_bf16.h>
#include <stdint.h>
#include <type_traits>

typedef __attribute__((ext_vector_type(8))) short bf16x8;
typedef __attribute__((ext_vector_type(4))) short s16x4;
typedef __attribute__((ext_vector_type(4))) float f32x4;
typedef __attribute__((ext_vector_type(2))) float f32x2;
typedef __attribute__((ext_vector_type(2))) unsigned u32x2;
typedef __attribute__((ext_vector_type(4))) unsigned u32x4;
typedef __attribute__((ext_vector_type(2))) __bf16 bf16x2_t;
typedef unsigned short u16;
typedef unsigned long long u64;

#define DI __device__ __forceinline__
#define AS3 __attribute__((address_space(3)))
#define AS1 __attribute__((address_space(1)))
#define RLX_AGENT __ATOMIC_RELAXED, __HIP_MEMORY_SCOPE_AGENT

constexpr int SEQ = 2048, DM = 1024, NH = 32, FF = 3584;

DI float bf2f(u16 v) { return __uint_as_float(((unsigned)v) << 16); }
DI u16 f2bf(float f) { __bf16 b = (__bf16)f; return __builtin_bit_cast(u16, b); }
DI float rbf(float f) { return bf2f(f2bf(f)); }
DI unsigned pack2(float lo, float hi) {
    const f32x2 v = {lo, hi};
    return __builtin_bit_cast(unsigned, __builtin_convertvector(v, bf16x2_t));
}
DI float lo16(unsigned u) { return __uint_as_float(u << 16); }
DI float hi16(unsigned u) { return __uint_as_float(u & 0xffff0000u); }

DI s16x4 tr_read(unsigned a) { return __builtin_amdgcn_ds_read_tr16_b64_v4i16((AS3 s16x4*)(size_t)a); }
DI bf16x8 lds_read128(unsigned a) { return *(AS3 const bf16x8*)(size_t)a; }
template <class T> DI void lds_write(unsigned a, T v) { *(AS3 T*)(size_t)a = v; }
template <class T> DI T lds_read(unsigned a) { return *(AS3 const T*)(size_t)a; }
DI bf16x8 cat8(s16x4 a, s16x4 b) { return __builtin_shufflevector(a, b, 0, 1, 2, 3, 4, 5, 6, 7); }
DI f32x4 mfma16(bf16x8 a, bf16x8 b, f32x4 c) { return __builtin_amdgcn_mfma_f32_16x16x32_bf16(a, b, c, 0, 0, 0); }
DI bf16x8 pack44(const f32x4& a, const f32x4& b) {
    u32x4 r;
    r[0] = pack2(a[0], a[1]);
    r[1] = pack2(a[2], a[3]);
    r[2] = pack2(b[0], b[1]);
    r[3] = pack2(b[2], b[3]);
    return __builtin_bit_cast(bf16x8, r);
}
DI unsigned lds_of(const void* p) { return (unsigned)(size_t)(AS3 const char*)p; }

template <int CTRL>
DI unsigned dpp(unsigned x) { return __builtin_amdgcn_update_dpp(0u, x, CTRL, 0xf, 0xf, true); }
template <int CTRL>
DI float dpp_add(float x) { return x + __uint_as_float(dpp<CTRL>(__float_as_uint(x))); }
DI float swap32_add(float a, float b) {
    auto r = __builtin_amdgcn_permlane32_swap(__float_as_uint(a), __float_as_uint(b), false, false);
    return __uint_as_float(r[0]) + __uint_as_float(r[1]);
}
DI float swap16_add(float a, float b) {
    auto r = __builtin_amdgcn_permlane16_swap(__float_as_uint(a), __float_as_uint(b), false, false);
    return __uint_as_float(r[0]) + __uint_as_float(r[1]);
}
DI float wave_sum(float x) {
    x = dpp_add<0xB1>(x);
    x = dpp_add<0x4E>(x);
    x = dpp_add<0x141>(x);
    x = dpp_add<0x140>(x);
    x = swap16_add(x, x);
    return swap32_add(x, x);
}
DI unsigned wave_umax(unsigned x) {
    x = max(x, dpp<0xB1>(x));
    x = max(x, dpp<0x4E>(x));
    x = max(x, dpp<0x141>(x));
    x = max(x, dpp<0x140>(x));
    auto r = __builtin_amdgcn_permlane16_swap(x, x, false, false);
    x = max(r[0], r[1]);
    auto s = __builtin_amdgcn_permlane32_swap(x, x, false, false);
    return max(s[0], s[1]);
}
template <class OP, class POST>
DI void red2(float& a, float& b, OP op, POST post) {
    auto x = __builtin_amdgcn_permlane32_swap(__float_as_uint(a), __float_as_uint(b), false, false);
    const float m = op(__uint_as_float(x[0]), __uint_as_float(x[1]));
    auto y = __builtin_amdgcn_permlane16_swap(__float_as_uint(m), __float_as_uint(m), false, false);
    const float f = post(op(__uint_as_float(y[0]), __uint_as_float(y[1])));
    auto z = __builtin_amdgcn_permlane32_swap(__float_as_uint(f), __float_as_uint(f), false, false);
    a = __uint_as_float(z[0]);
    b = __uint_as_float(z[1]);
}

#define RS(i) do {} while (0)
#define RSD(i) do {} while (0)
#define RSDUMP() do {} while (0)

constexpr unsigned WS_E = 0;
constexpr unsigned WS_RG = 256;
constexpr unsigned WS_GG = WS_RG + 2048;
constexpr unsigned WS_CL = 32768;
constexpr unsigned WS_Z = 65536;
constexpr unsigned WS_KF = WS_Z + 131072;
constexpr unsigned WS_GU = WS_KF + 262144;
constexpr unsigned WS_Y = WS_GU + 262144;
constexpr unsigned WS_KV = WS_Y + 262144;
constexpr unsigned WS_O = WS_KV + (2u << 20);
constexpr unsigned WS_END = WS_O + (4u << 20);

constexpr int WE = 0;
constexpr int NQ = 10, ND = 6, NX = 16, KD = 78, VD = 84, NF = 64, NF1 = 59, NF2 = 59, NP1 = 12, NP = 14, NM1 = 21, NM2 = 21, NM = 22, KFN = 100;
constexpr int J_S[48] = {0, 0, 0, 1, 1, 1, 2, 2, 2, 3, 3, 3, 4, 4, 4, 5, 5, 5, 6, 6, 6, 7, 7, 7, 8, 8, 8, 9, 9, 9, 10, 10, 11, 11, 12, 12, 13, 14, 15, 13, 14, 15, 10, 11, 12, 13, 14, 15};
constexpr int J_M[48] = {0, 1, 2, 0, 1, 2, 0, 1, 2, 0, 1, 2, 0, 1, 2, 0, 1, 2, 0, 1, 2, 0, 1, 2, 0, 1, 2, 0, 1, 2, 0, 1, 0, 1, 0, 1, 0, 0, 0, 1, 1, 1, 2, 2, 2, 2, 2, 2};
constexpr int J_X[48] = {0, 0, 0, 1, 1, 1, 2, 2, 2, 3, 3, 3, 4, 4, 4, 5, 5, 5, 6, 6, 6, 7, 7, 7, 8, 8, 8, 9, 9, 9, 10, 10, 11, 11, 12, 12, 13, 14, 15, 13, 14, 15, 10, 11, 12, 13, 14, 15};
constexpr int J_U[48] = {2, 4, 8, 18, 11, 12, 17, 15, 13, 0, 1, 6, 4, 8, 9, 11, 12, 14, 15, 13, 7, 1, 6, 5, 8, 9, 10, 12, 14, 16, 13, 7, 1, 6, 4, 8, 18, 14, 15, 13, 7, 3, 0, 1, 6, 5, 2, 4};
constexpr int J_B[48] = {2, 2, 2, 11, 11, 11, 11, 11, 11, 0, 0, 0, 0, 8, 8, 8, 8, 8, 8, 8, 1, 1, 1, 1, 1, 9, 9, 9, 9, 9, 9, 1, 1, 1, 1, 1, 13, 13, 13, 13, 0, 0, 0, 0, 0, 0, 0, 0};
constexpr int Q_C[96] = {1, 0, 2, 0, 3, 0, 4, 0, 5, 0, 6, 0, 7, 0, 0, 0, 0, 0, 8, 0, 0, 0, 0, 0, 9, 0, 0, 0, 0, 0, 10, 0, 0, 0, 0, 0, 11, 0, 0, 0, 0, 0, 12, 0, 0, 0, 0, 0, 13, 0, 0, 0, 0, 0, 14, 0, 0, 0, 0, 0, 15, 0, 0, 0, 0, 0, 16, 0, 0, 0, 0, 0, 17, 0, 0, 0, 0, 0, 18, 0, 19, 0, 20, 0, 21, 0, 22, 0, 0, 0, 0, 0, 0, 0, 0, 0};
constexpr int X_U0[16] = {6, 9, 14, 7, 5, 10, 16, 3, 2, 18, 17, 3, 5, 9, 11, 16};
constexpr int X_U1[16] = {5, 10, 16, 3, 2, 18, 17, 0, 4, 11, 15, 0, 2, 10, 12, 17};
constexpr int P_U[4] = {11, 12, 9, 10};
constexpr int W_U[16] = {8, 14, 15, 13, 7, 3, 9, 10, 11, 12, 0, 1, 6, 5, 2, 4};
constexpr int W_N[16] = {100, 100, 100, 100, 100, 100, 101, 101, 101, 101, 13, 13, 14, 14, 99, 99};
constexpr int F_J[64] = {-1, 0, 1, 2, -2, 3, 4, 5, -3, 6, 7, 8, -4, 9, 10, 11, -5, 12, 13, 14, -6, 15, 16, 17, -7, 18, 19, 20, -8, 21, 22, 23, -9, 24, 25, 26, -10, 27, 28, 29, -11, 30, 31, -12, 32, 33, -13, 34, 35, -14, 36, -15, 37, -16, 38, 39, 40, 41, 42, 43, 44, 45, 46, 47};
constexpr int F_N[64] = {0, 0, 0, 0, 0, 0, 0, 0, 0, 0, 0, 0, 0, 0, 0, 1, 1, 1, 1, 1, 2, 2, 2, 2, 3, 3, 3, 3, 4, 4, 4, 4, 5, 5, 5, 5, 6, 6, 6, 6, 7, 7, 7, 7, 8, 8, 8, 8, 9, 9, 9, 10, 10, 10, 10, 11, 11, 11, 11, 12, 12, 12, 12, 12};
constexpr int F_K[64] = {0, 1, 2, 3, 0, 4, 5, 6, 0, 0, 0, 7, 0, 0, 0, 8, 0, 0, 0, 9, 0, 0, 0, 10, 0, 0, 0, 11, 0, 0, 0, 12, 0, 0, 0, 13, 0, 0, 0, 14, 0, 0, 0, 0, 15, 0, 0, 0, 16, 0, 0, 0, 0, 0, 17, 18, 19, 20, 21, 0, 0, 0, 0, 22};
constexpr int E_A[384] = {300, 325, 350, 375, 0, 0, 0, 502, 0, 0, 0, 403, 0, 0, 0, 503, 201, 251, 0, 404, 0, 100, 101, 504, 301, 326, 351, 376, 0, 0, 0, 505, 601, 0, 0, 406, 0, 0, 0, 506, 202, 252, 0, 407, 0, 0, 0, 507, 302, 327, 352, 377, 0, 0, 0, 602, 0, 0, 0, 0, 0, 0, 0, 203, 253, 0, 0, 408, 0, 100, 101, 508, 303, 328, 353, 378, 0, 0, 0, 603, 0, 0, 0, 0, 0, 0, 0, 204, 254, 0, 0, 409, 0, 0, 0, 509, 304, 329, 354, 379, 0, 100, 101, 604, 102, 103, 0, 0, 0, 0, 0, 205, 255, 0, 0, 410, 0, 0, 0, 510, 305, 330, 355, 380, 0, 0, 0, 605, 0, 0, 0, 0, 0, 0, 0, 206, 256, 0, 0, 411, 0, 0, 0, 511, 306, 331, 356, 381, 0, 0, 0, 606, 0, 0, 0, 0, 0, 100, 101, 102, 103, 207, 257, 412, 0, 0, 0, 512, 307, 332, 357, 382, 0, 0, 0, 607, 0, 0, 0, 0, 0, 0, 0, 208, 258, 0, 0, 413, 0, 0, 0, 513, 308, 333, 358, 383, 0, 100, 101, 608, 102, 103, 0, 0, 0, 0, 0, 209, 259, 0, 0, 414, 0, 0, 0, 514, 309, 334, 359, 384, 0, 0, 0, 609, 0, 0, 0, 0, 0, 0, 0, 210, 260, 0, 0, 415, 0, 0, 0, 515, 310, 335, 360, 385, 0, 100, 101, 610, 102, 103, 211, 261, 0, 0, 0, 0, 311, 336, 361, 386, 0, 0, 0, 516, 611, 212, 262, 0, 0, 0, 0, 0, 312, 337, 362, 387, 0, 0, 0, 213, 263, 0, 0, 417, 0, 100, 101, 517, 313, 338, 363, 388, 214, 264, 0, 612, 314, 339, 364, 389, 215, 265, 0, 0, 315, 340, 365, 390, 0, 0, 0, 518, 0, 0, 0, 419, 0, 100, 101, 519, 102, 103, 0, 420, 0, 0, 0, 520, 0, 0, 0, 0, 0, 0, 0, 0, 0, 0, 0, 422, 0, 0, 0, 522, 0, 0, 0, 0, 0, 0, 0, 0, 0, 0, 0, 613, 0, 0, 0, 0, 0, 0, 0, 0, 0, 0, 0, 0, 0, 0, 0, 614, 0, 0, 0, 0, 0, 0, 0, 0, 0, 0, 0, 0};
constexpr int E_B[384] = {0, 0, 402, 0, 0, 0, 0, 0, 0, 0, 0, 0, 0, 0, 0, 0, 0, 0, 0, 0, 0, 0, 0, 102, 103, 0, 405, 0, 0, 0, 0, 0, 0, 0, 0, 0, 0, 0, 0, 0, 0, 0, 0, 0, 0, 0, 0, 0, 0, 0, 0, 0, 0, 0, 0, 0, 0, 0, 0, 0, 0, 0, 0, 0, 0, 0, 0, 0, 0, 0, 0, 102, 103, 0, 0, 0, 0, 0, 0, 0, 0, 0, 0, 0, 0, 0, 0, 0, 0, 0, 0, 0, 0, 0, 0, 0, 0, 0, 0, 0, 0, 0, 0, 0, 0, 0, 0, 0, 0, 0, 0, 0, 0, 0, 0, 0, 0, 0, 0, 0, 0, 0, 0, 0, 0, 0, 0, 0, 0, 0, 0, 0, 0, 0, 0, 0, 0, 0, 0, 0, 0, 0, 0, 0, 0, 0, 0, 0, 0, 0, 0, 0, 0, 0, 0, 0, 0, 0, 0, 0, 0, 0, 0, 0, 0, 0, 0, 0, 0, 0, 0, 0, 0, 0, 0, 0, 0, 0, 0, 0, 0, 0, 0, 0, 0, 0, 0, 0, 0, 0, 0, 0, 0, 0, 0, 0, 0, 0, 0, 0, 0, 0, 0, 0, 0, 0, 0, 0, 0, 0, 0, 0, 0, 0, 0, 0, 0, 0, 0, 0, 0, 0, 0, 0, 0, 0, 0, 0, 0, 0, 0, 0, 0, 0, 0, 0, 0, 0, 0, 0, 0, 0, 0, 0, 0, 0, 0, 0, 0, 0, 0, 0, 0, 0, 0, 0, 0, 0, 416, 0, 0, 0, 0, 0, 0, 0, 0, 0, 0, 0, 0, 0, 0, 0, 0, 0, 0, 0, 0, 0, 0, 0, 0, 0, 0, 0, 0, 102, 103, 0, 0, 0, 0, 0, 0, 0, 0, 0, 0, 0, 0, 0, 0, 0, 0, 0, 418, 0, 0, 0, 0, 0, 0, 0, 0, 0, 0, 0, 0, 0, 0, 0, 0, 0, 0, 0, 0, 0, 0, 0, 0, 0, 0, 0, 0, 0, 0, 0, 0, 0, 0, 0, 0, 0, 0, 0, 0, 0, 0, 0, 0, 0, 0, 0, 0, 0, 0, 0, 0, 0, 0, 0, 0, 0, 0, 0, 0, 0, 0, 0, 0, 0, 0, 0, 0, 0, 0, 0, 0, 0, 0, 0, 0, 0};
#define LH 0
constexpr unsigned L_A = 16 * 8192;
constexpr unsigned L_KF = 18 * 8192;
constexpr unsigned L_SM = 19 * 8192;
constexpr unsigned L_FULL = L_SM + 256;
constexpr unsigned L_FVR = L_SM + 320, L_PRL = L_SM + 336;
constexpr unsigned L_PROG = L_SM + 896;
constexpr unsigned L_FW2 = L_SM + 512, L_FOS = L_SM + 480, L_FGI = L_SM + 520, L_FK0 = L_SM + 752, L_FBI = L_SM + 744;
constexpr unsigned L_FK = L_SM + 528, L_FKT = L_SM + 544, L_FV = L_SM + 560, L_FP = L_SM + 576, L_FKS = L_SM + 592;
constexpr unsigned L_FQD = L_SM + 608, L_FG = L_SM + 624, L_FKF = L_SM + 736, L_FQ = L_SM + 720, L_FWD = L_SM + 640, L_FWO = L_SM + 656, L_FLD = L_SM + 688, L_BEST = L_SM + 696;
constexpr unsigned L_SEL = L_SM + 704;
constexpr unsigned L_XS = L_SM + 2048, L_KZ = L_SM + 3072, L_WPHI = L_SM + 4096;
static_assert(NP <= 31 && NM <= 63, "");
static_assert(P_U[1] == P_U[0] + 1 && P_U[3] == P_U[2] + 1, "feature maps of a head lie side by side");
constexpr int wo_unit(int i) { return W_U[i]; }
constexpr int f_u(int f) { return f & 3; }
constexpr bool j_first(int j) {
    for (int i = 0; i < j; ++i)
        if (J_M[i] == J_M[j]) return false;
    return true;
}
constexpr int younger(int p) {
    int c = 0;
    for (int n = 0; n < NF1 - LH; ++n)
        if (F_N[n] >= p) c += F_J[n] < 0 ? 8 : 4;
    return c;
}
constexpr int nfv() {
    for (int n = 0; n < NF1; ++n)
        if (F_J[n] == VD / 2 - 1) return n + 1;
    return NF1;
}
constexpr int next_need(int n) {
    for (int i = n; i < NF; ++i)
        if (F_N[i] != F_N[n]) return F_N[i];
    return 99;
}
constexpr int x_stage(int i) { return i < 16 ? i : i - 16 + NQ; }
constexpr unsigned SPIN_CAP = 1u << 16;
#define N8(a) a, a + 1, a + 2, a + 3, a + 4, a + 5, a + 6, a + 7
__device__ const unsigned k_num[64] = {N8(0), N8(8), N8(16), N8(24), N8(32), N8(40), N8(48), N8(56)};

#define RK(i) do {} while (0)
#define RSL(i) do {} while (0)
#define RSC(i) do {} while (0)
#define CSN 6
#define CFP 0
#define QWM 0
#define RLD 11
#define CLF 3
#define CLT 1
#define FLV 1
#define SP1 0
#define SP2 0
#define SP3 0
#define SP4 0
#define SPW 0
#define SPI "s_load_dwordx2"
#define DNP 1
#define WOT 1
#define YT 1
#define GDL 0
#define SSM 0
#define CLE 1
#define GUO 0
#define TL0 0
#define TL0W ""
#define TLD 0
#define LPRIO 0
#define LPRW 3
#define LTH 63
#define LT0 14
#define PEEK 1
#define CSP 1
#define GUA 12
#define OPL 8
#define OPM ""
#define PD 4
#define NPH 4
#define KSK 3
#define ROTK 8
#define OWIN 2
#define CLR0 4
#define CLS 1
#define CLP 2
#define SLK 2
#define CPH 3
#define NPL 0
#define CPRIO 1
#define HPRIO 2
#define KPK 3
#define KRND 1
#define QW 1
#define WOG 1
#define WON 12
#define KFD 0
#define SEL0 9
#define SPRIO 2
#define FBA 3
#define FBB 10
#define FBS 1
#define FBL 1
#define WOP 0
#define GTL 3
#define WPF 0
#define WOE 6
#define GW 0
#define GUF 0
#define FBH 0
#define WG2 0
#define NTX 0
#define NTW 0
#define NTS 16
#define NTO ""
#define GQ1 4
#define GQ2 8
#define CLO 0
#define PDL 6
#define XK 3
#define YK 7
#define BPRIO 0
#define AMF 1
#define LF 1
#define SH 2
#define LC 0
#define SPR2 0
#define GQ0 0
#define CPF 53248
#define NF0 4
#define OTL 0
#define OST " sc1"
#define TLW 3
#define THR -1
#define THQ -1
#define FDH 0
#define WLK -1
#define XGE 1
#define KXO 3
#define KXM 3
#define KX8 0
#define KXB 0xFEDBA9C768534210ull
#define BCK 10
#define GPR 0
#define GHL 0
#define TGT 1
#define PORD 1
#define TRM 3
#define LFE 1
#define PWN 0
#define PWL 99
#define TRN 0
#define RT0 0
#define BSP 0
#define WOV 0
#define GUP 1
#define NOF 0
#define FBD 8
#define BAR() asm volatile("s_waitcnt lgkmcnt(0)\n\ts_barrier" ::: "memory")
template <int N>
DI void waitv() { asm volatile("s_waitcnt vmcnt(%0)" ::"n"(N) : "memory"); }

DI u64 ld_agent(const char* p) { return __hip_atomic_load((const AS1 u64*)p, RLX_AGENT); }
DI void st_agent(char* p, u64 v) { __hip_atomic_store((AS1 u64*)p, v, RLX_AGENT); }
DI unsigned nz1(unsigned v) { return max(v, 1u); }
DI unsigned umin(unsigned a, unsigned b) { return min(a, b); }
DI u32x4 nz4(u32x4 v) { return (u32x4){max(v[0], 1u), max(v[1], 1u), max(v[2], 1u), max(v[3], 1u)}; }
DI u32x4 nz4(f32x4 v) { return nz4(__builtin_bit_cast(u32x4, v)); }
DI unsigned min4(u32x4 v) { return min(min(v[0], v[1]), min(v[2], v[3])); }
DI u32x4 ld_wt(__amdgpu_buffer_rsrc_t r, unsigned off) { return __builtin_amdgcn_raw_buffer_load_b128(r, off, 0, 16); }
DI void st_wt(__amdgpu_buffer_rsrc_t r, unsigned off, u32x4 v) { __builtin_amdgcn_raw_buffer_store_b128(v, r, off, 0, 16); }
DI void ld_async(u32x4& v, unsigned off, const char* base) {
    asm volatile("global_load_dwordx4 %0, %1, %2 sc1" : "=&v"(v) : "v"(off), "s"(base) : "memory");
}
#define ST_MASK(op, m, off, v, base) asm volatile("s_mov_b64 exec, %3\n\t" op " %0, %1, %2 sc1\n\ts_mov_b64 exec, -1\n\ts_nop 0" ::"v"(off), "v"(v), "s"(base), "s"((u64)(m)) : "memory")
DI void pin(u32x4& v) { asm volatile("" : "+v"(v)); }
template <int N, class OFF, class BASE, class OK>
DI void settle(u32x4 (&v)[N], OFF off, BASE base, OK ok) {
#pragma unroll
    for (int i = 0; i < N; ++i) pin(v[i]);
#pragma unroll 1
    for (unsigned s = 0; s < SPIN_CAP; ++s) {
        unsigned m = ok(v[0]);
#pragma unroll
        for (int i = 1; i < N; ++i) m = min(m, ok(v[i]));
        if (__ballot(m == 0u) == 0ull) break;
#pragma unroll
        for (int i = 0; i < N; ++i) {
            u64 keep;
            asm volatile("s_mov_b64 %1, exec\n\ts_mov_b64 exec, %4\n\tglobal_load_dwordx4 %0, %2, %3 sc1\n\ts_mov_b64 exec, %1"
                         : "+&v"(v[i]), "=&s"(keep) : "v"(off(i)), "s"(base(i)), "s"((u64)__ballot(ok(v[i]) == 0u)) : "memory");
        }
        waitv<0>();
#pragma unroll
        for (int i = 0; i < N; ++i) pin(v[i]);
    }
}
template <int N, class OFF, class BASE>
DI void settle(u32x4 (&v)[N], OFF off, BASE base) { settle(v, off, base, [](const u32x4& a) { return min4(a); }); }
DI void lds_wait(unsigned a, unsigned n) {
    unsigned v, c;
    asm volatile("s_mov_b32 %1, 0x400000\n"
                 ".Lp%=:\n\tds_read_b32 %0, %2\n\ts_waitcnt lgkmcnt(0)\n\tv_cmp_gt_u32 vcc, %3, %0\n\ts_cbranch_vccz .Lf%=\n\t"
                 "s_sleep 1\n\ts_sub_u32 %1, %1, 1\n\ts_cbranch_scc0 .Lp%=\n.Lf%=:"
                 : "=&v"(v), "=&s"(c) : "v"(a), "s"(n) : "memory", "vcc", "scc");
}
DI void lds_wait0(unsigned a) {
    if (!TGT) { lds_wait(a, 1); return; }
    unsigned v, c;
    asm volatile("s_mov_b32 %1, 0x1000000\n"
                 ".Lp%=:\n\tds_read_b32 %0, %2\n\ts_sub_u32 %1, %1, 1\n\ts_cbranch_scc1 .Lf%=\n\ts_waitcnt lgkmcnt(0)\n\tv_cmp_eq_u32 vcc, 0, %0\n\ts_cbranch_vccnz .Lp%=\n"
                 ".Lf%=:\n\ts_waitcnt lgkmcnt(0)"
                 : "=&v"(v), "=&s"(c) : "v"(a) : "memory", "vcc", "scc");
}
DI void lds_wait8(unsigned a, unsigned n) {
    unsigned v, c;
    asm volatile("s_mov_b32 %1, 0x100000\n"
                 ".Lp%=:\n\tds_read_b32 %0, %2\n\ts_waitcnt lgkmcnt(0)\n\tv_cmp_gt_u32 vcc, %3, %0\n\ts_cbranch_vccz .Lf%=\n\t"
                 "s_sleep 4\n\ts_sub_u32 %1, %1, 1\n\ts_cbranch_scc0 .Lp%=\n.Lf%=:"
                 : "=&v"(v), "=&s"(c) : "v"(a), "s"(n) : "memory", "vcc", "scc");
}
DI void lds_peek(unsigned& v, unsigned a) { asm volatile("ds_read_b32 %0, %1" : "=v"(v) : "v"(a) : "memory"); }
DI void lds_wait(unsigned& v, unsigned a, unsigned n) {
    unsigned c;
    asm volatile("s_mov_b32 %1, 0x400000\n"
                 ".Lp%=:\n\ts_waitcnt lgkmcnt(0)\n\tv_cmp_gt_u32 vcc, %3, %0\n\ts_cbranch_vccz .Lf%=\n\t"
                 "ds_read_b32 %0, %2\n\ts_sub_u32 %1, %1, 1\n\ts_cbranch_scc0 .Lp%=\n.Lf%=:"
                 : "+v"(v), "=&s"(c) : "v"(a), "s"(n) : "memory", "vcc", "scc");
}
DI void lds_set(unsigned a) {
    asm volatile("s_waitcnt lgkmcnt(0)\n\tds_write_b32 %0, %1" ::"v"(a), "v"(1u) : "memory");
}
DI float dot2(unsigned a, unsigned b, float c) {
    return __builtin_amdgcn_fdot2_f32_bf16(__builtin_bit_cast(bf16x2_t, a), __builtin_bit_cast(bf16x2_t, b), c, false);
}
DI float sum4(const float (&a)[4]) {
    float c = swap16_add(swap32_add(a[0], a[1]), swap32_add(a[2], a[3]));
    c = dpp_add<0xB1>(c);
    c = dpp_add<0x4E>(c);
    c = dpp_add<0x141>(c);
    return dpp_add<0x140>(c);
}

template <class OP, class POST>
DI void red4(float& a, float& b, float& c, float& d, OP op, POST post) {
    auto x = __builtin_amdgcn_permlane32_swap(__float_as_uint(a), __float_as_uint(b), false, false);
    auto y = __builtin_amdgcn_permlane32_swap(__float_as_uint(c), __float_as_uint(d), false, false);
    const float m = op(__uint_as_float(x[0]), __uint_as_float(x[1])), n = op(__uint_as_float(y[0]), __uint_as_float(y[1]));
    auto z = __builtin_amdgcn_permlane16_swap(__float_as_uint(m), __float_as_uint(n), false, false);
    const unsigned f = __float_as_uint(post(op(__uint_as_float(z[0]), __uint_as_float(z[1]))));
    auto u = __builtin_amdgcn_permlane16_swap(f, f, false, false);
    auto v = __builtin_amdgcn_permlane32_swap(u[0], u[0], false, false);
    auto t = __builtin_amdgcn_permlane32_swap(u[1], u[1], false, false);
    a = __uint_as_float(v[0]);
    b = __uint_as_float(v[1]);
    c = __uint_as_float(t[0]);
    d = __uint_as_float(t[1]);
}
DI float mx2(float a, float b) { return __builtin_elementwise_maximum(a, b); }
DI float opq(float a) { asm("" : "+v"(a)); return a; }
template <bool PK, class HOOK>
DI void softmax2(const f32x4 (&v)[2][2], u32x4 (&ph)[2][2], HOOK hook) {
    constexpr float L2E = 1.4426950408889634f;
    float mx[2], mn[2], sp_[2], sn_[2];
#pragma unroll
    for (int b = 0; b < 2; ++b) {
        const f32x4 s = v[b][0], t = v[b][1];
        mx[b] = mx2(mx2(mx2(mx2(s[0], s[1]), s[2]), mx2(mx2(s[3], t[0]), t[1])), mx2(t[2], t[3]));
        mn[b] = mx2(mx2(mx2(mx2(-s[0], -s[1]), -s[2]), mx2(mx2(-s[3], -t[0]), -t[1])), mx2(-t[2], -t[3]));
    }
    red4(mx[0], mx[1], mn[0], mn[1], [](float a, float b) { return mx2(a, b); }, [](float a) { return a * -L2E; });
    f32x2 ep[2][4], en[2][4];
#pragma unroll
    for (int b = 0; b < 2; ++b) {
#pragma unroll
        for (int e = 0; e < 4; ++e) {
            const f32x2 u = {v[b][e >> 1][2 * (e & 1)], v[b][e >> 1][2 * (e & 1) + 1]};
            f32x2 a, c;
            if (PK) {
                a = __builtin_elementwise_fma(u, (f32x2){L2E, L2E}, (f32x2){mx[b], mx[b]});
                c = __builtin_elementwise_fma(u, (f32x2){-L2E, -L2E}, (f32x2){mn[b], mn[b]});
            } else {
                a = (f32x2){__builtin_fmaf(u[0], L2E, mx[b]), __builtin_fmaf(u[1], L2E, mx[b])};
                c = (f32x2){__builtin_fmaf(u[0], -L2E, mn[b]), __builtin_fmaf(u[1], -L2E, mn[b])};
            }
            ep[b][e] = (f32x2){__builtin_amdgcn_exp2f(a[0]), __builtin_amdgcn_exp2f(a[1])};
            en[b][e] = (f32x2){__builtin_amdgcn_exp2f(c[0]), __builtin_amdgcn_exp2f(c[1])};
        }
        if (PK) {
            const f32x2 s = (ep[b][0] + ep[b][1]) + (ep[b][2] + ep[b][3]), t = (en[b][0] + en[b][1]) + (en[b][2] + en[b][3]);
            sp_[b] = s[0] + s[1];
            sn_[b] = t[0] + t[1];
        } else {
            sp_[b] = ep[b][0][0];
            sn_[b] = en[b][0][0];
#pragma unroll
            for (int e = 1; e < 8; ++e) sp_[b] += ep[b][e >> 1][e & 1], sn_[b] += en[b][e >> 1][e & 1];
        }
    }
    asm volatile("" : "+v"(sp_[0]), "+v"(sn_[0]), "+v"(sp_[1]), "+v"(sn_[1]));
    hook();
    red4(sp_[0], sp_[1], sn_[0], sn_[1], [](float a, float b) { return a + b; }, [](float a) { return __builtin_amdgcn_rcpf(a); });
#pragma unroll
    for (int b = 0; b < 2; ++b)
#pragma unroll
        for (int e = 0; e < 4; ++e) {
            if (PK) {
                const f32x2 a = ep[b][e] * (f32x2){sp_[b], sp_[b]}, c = en[b][e] * (f32x2){sn_[b], sn_[b]};
                ph[b][0][e] = pack2(a[0], a[1]);
                ph[b][1][e] = pack2(c[0], c[1]);
            } else {
                ph[b][0][e] = pack2(opq(ep[b][e][0] * sp_[b]), ep[b][e][1] * sp_[b]);
                ph[b][1][e] = pack2(opq(en[b][e][0] * sn_[b]), en[b][e][1] * sn_[b]);
            }
        }
}
DI f32x4 rbf4(const f32x4& a) {
    const unsigned u = pack2(a[0], a[1]), v = pack2(a[2], a[3]);
    return (f32x4){lo16(u), hi16(u), lo16(v), hi16(v)};
}
DI int fsw(int row) { return (((row >> 1) & 3) << 2) | (((row >> 3) & 1) << 1) | (row & 1); }
template <bool PK, class HOOK>
DI void phi_k(const f32x4 (&pp)[2][2], const unsigned (&pw)[4], unsigned off, HOOK hook) {
    u32x4 ph[2][2];
    softmax2<PK>(pp, ph, hook);
#pragma unroll
    for (int b = 0; b < 2; ++b)
#pragma unroll
        for (int kf = 0; kf < 2; ++kf)
#pragma unroll
            for (int fb = 0; fb < 2; ++fb) {
                const u32x2 a = {ph[b][kf][2 * fb], ph[b][kf][2 * fb + 1]};
                lds_write<u32x2>(pw[2 * kf + fb] + off + b * 2048, a);
            }
}

DI void phi_k1(const f32x4 (&v)[2], const unsigned (&pw)[4]) {
    constexpr float L2E = 1.4426950408889634f;
    const f32x4 s = v[0], t = v[1];
    float mx = mx2(mx2(mx2(mx2(s[0], s[1]), s[2]), mx2(mx2(s[3], t[0]), t[1])), mx2(t[2], t[3]));
    float mn = mx2(mx2(mx2(mx2(-s[0], -s[1]), -s[2]), mx2(mx2(-s[3], -t[0]), -t[1])), mx2(-t[2], -t[3]));
    red2(mx, mn, [](float a, float b) { return mx2(a, b); }, [](float a) { return a * -L2E; });
    f32x2 ep[4], en[4];
#pragma unroll
    for (int e = 0; e < 4; ++e) {
        const f32x2 u = {v[e >> 1][2 * (e & 1)], v[e >> 1][2 * (e & 1) + 1]};
        const f32x2 a = __builtin_elementwise_fma(u, (f32x2){L2E, L2E}, (f32x2){mx, mx});
        const f32x2 c = __builtin_elementwise_fma(u, (f32x2){-L2E, -L2E}, (f32x2){mn, mn});
        ep[e] = (f32x2){__builtin_amdgcn_exp2f(a[0]), __builtin_amdgcn_exp2f(a[1])};
        en[e] = (f32x2){__builtin_amdgcn_exp2f(c[0]), __builtin_amdgcn_exp2f(c[1])};
    }
    const f32x2 a = (ep[0] + ep[1]) + (ep[2] + ep[3]), c = (en[0] + en[1]) + (en[2] + en[3]);
    float sp = a[0] + a[1], sn = c[0] + c[1];
    red2(sp, sn, [](float a, float b) { return a + b; }, [](float a) { return __builtin_amdgcn_rcpf(a); });
#pragma unroll
    for (int i = 0; i < 4; ++i) {
        const float r = i < 2 ? sp : sn;
        const f32x2 lo = (i < 2 ? ep[2 * i] : en[2 * i - 4]) * (f32x2){r, r}, hi = (i < 2 ? ep[2 * i + 1] : en[2 * i - 3]) * (f32x2){r, r};
        lds_write<u32x2>(pw[i], (u32x2){pack2(lo[0], lo[1]), pack2(hi[0], hi[1])});
    }
}

DI void waitvn(int n) {
#define WV(k) case k: waitv<k>(); break;
#define WV8(k) WV(k) WV(k + 1) WV(k + 2) WV(k + 3) WV(k + 4) WV(k + 5) WV(k + 6) WV(k + 7)
    switch (n < 63 ? n : 63) { WV8(0) WV8(8) WV8(16) WV8(24) WV8(32) WV8(40) WV8(48) WV8(56) }
}
#define SB0() __builtin_amdgcn_sched_barrier(0)
template <int A, int B, class F>
DI void sfor(F f) {
    if constexpr (A < B) {
        f(std::integral_constant<int, A>{});
        sfor<A + 1, B>(f);
    }
}
template <int N> using IC = std::integral_constant<int, N>;
#define AI __attribute__((always_inline))
#define RSK(b, k) do { switch (k) { case 0: RS(b); break; case 1: RS(b + 1); break; case 2: RS(b + 2); break; case 3: RS(b + 3); break; \
    case 4: RS(b + 4); break; case 5: RS(b + 5); break; case 6: RS(b + 6); break; case 7: RS(b + 7); break; } } while (0)

DI void o_store(const u32x4 (&kf)[6], const u32x4 (&ph)[2][2], __amdgpu_buffer_rsrc_t rsrc, unsigned o_st) {
    const f32x4 zero = {0.f, 0.f, 0.f, 0.f};
#pragma unroll
    for (int i = 0; i < 2; ++i) {
        const bf16x8 p0 = __builtin_bit_cast(bf16x8, ph[i][0]), p1 = __builtin_bit_cast(bf16x8, ph[i][1]);
        f32x4 dn = mfma16(__builtin_bit_cast(bf16x8, kf[4]), p0, zero);
        f32x4 n0 = mfma16(__builtin_bit_cast(bf16x8, kf[0]), p0, zero);
        f32x4 n1 = mfma16(__builtin_bit_cast(bf16x8, kf[1]), p0, zero);
        dn = mfma16(__builtin_bit_cast(bf16x8, kf[5]), p1, dn);
        n0 = mfma16(__builtin_bit_cast(bf16x8, kf[2]), p1, n0);
        n1 = mfma16(__builtin_bit_cast(bf16x8, kf[3]), p1, n1);
        const float rd_ = __builtin_amdgcn_rcpf(rbf(rbf(dn[0]) + 1e-6f));
        const u32x4 nb_ = __builtin_bit_cast(u32x4, pack44(n0, n1));
        u32x4 o;
#pragma unroll
        for (int j = 0; j < 4; ++j) o[j] = pack2(lo16(nb_[j]) * rd_, hi16(nb_[j]) * rd_);
        st_wt(rsrc, o_st + i * 1024, nz4(o));
    }
}

typedef __attribute__((ext_vector_type(16))) unsigned u32x16;
__global__ __launch_bounds__(512, 2) void fused_mix(const u16*, const u16*, const u16*, const u16*, const u16*, const u16*, const u16*, const u16*,
                                                    const u16*, char*, u16*) {
    __shared__ __attribute__((aligned(16))) char smem[163840];
    u32x4 ka;
    u32x16 kb_;
    u64 kz;
    asm volatile("s_load_dwordx4 %0, %3, 0x0\n\ts_load_dwordx16 %1, %3, 0x10\n\ts_load_dwordx2 %2, %3, 0x50"
                 : "=&s"(ka), "=&s"(kb_), "=&s"(kz) : "s"(__builtin_amdgcn_kernarg_segment_ptr()));
    SB0();
    const int tid = threadIdx.x;
    int l = tid & 63;
    const int w = __builtin_amdgcn_readfirstlane(tid >> 6) ^ 4;
    int bid = blockIdx.x;
    const unsigned lds0 = lds_of(smem);
    int g = l >> 4, l15 = l & 15, q = l15 >> 2, p = l & 3;
    int srow = l >> 3, sch = l & 7;
    unsigned l16 = l * 16;
    unsigned vst = 0;
    const int rg = w & 3, kp = w >> 2;
    const int hs = rg >> 1;
    RS(0);
    if (CPF) {
        u64 pc;
        asm volatile("s_getpc_b64 %0\n\ts_mov_b32 m0, %2\n\ts_mov_b64 exec, 3\n\tglobal_load_lds_dword %1, %0\n\ts_mov_b64 exec, -1"
                     : "=&s"(pc) : "v"(min((unsigned)(((bid & ~7) + w) * 2 + l) * 128u, (unsigned)CPF)), "s"(lds0 + L_SM + 3584) : "memory");
    }
    lds_write<unsigned>(lds0 + L_SM + tid * 4, 0u);
    const int slot = bid >> 3;
    const int mt = (bid & 1) * 8 + (slot & 7);
    const int hp = ((bid >> 1) & 3) * 4 + (slot >> 3);
    unsigned vx[4], vw[4];
    auto vw_init = [&]() AI {
        int t = l;
        asm volatile("" : "+v"(t));
#pragma unroll
        for (int i = 0; i < 4; ++i) vw[i] = (unsigned)((8 * i + (t >> 3)) * 6144 + ((t & 7) ^ (((t >> 4) & 1) << 1) ^ ((i & 1) << 2)) * 16 + 4096 - i * 1024);
    };
    auto vx_init = [&]() AI {
        int t = l;
        asm volatile("" : "+v"(t));
#pragma unroll
        for (int i = 0; i < 4; ++i) vx[i] = (unsigned)((8 * i + (t >> 3)) * 2048 + (((t & 7) ^ ((4 * (i & 1) + (t >> 4)) & 7)) * 16) + 4096 - i * 1024);
    };
    vx_init();
    vw_init();
    asm volatile("" : "+v"(vx[0]), "+v"(vx[1]), "+v"(vx[2]), "+v"(vx[3]), "+v"(vw[0]), "+v"(vw[1]), "+v"(vw[2]), "+v"(vw[3]));
    const int ksa = SLK * (slot & 7), ksb = KSK * ((slot + (slot >> 3)) & (NPH - 1));
    unsigned xo0 = (mt * 128 + hs * 64) * 2048, wo0 = hp * 128 + hs * (32 * 6144);
    asm volatile("s_waitcnt lgkmcnt(0)" : "+s"(ka), "+s"(kb_), "+s"(kz), "+s"(xo0), "+s"(wo0) :: "memory");
    RS(48);
    const u16* x = (const u16*)((u64)ka[1] << 32 | ka[0]);
    const u16* wqkv = (const u16*)((u64)ka[3] << 32 | ka[2]);
    auto arg = [&](int i) { return (char*)((u64)kb_[2 * i + 1] << 32 | kb_[2 * i]); };
    const u16 *wphi = (const u16*)arg(0), *wo = (const u16*)arg(1), *wr = (const u16*)arg(2), *wg = (const u16*)arg(3);
    const u16 *wu = (const u16*)arg(4), *wd = (const u16*)arg(5), *gw = (const u16*)arg(6);
    char* ws = arg(7);
    u16* out = (u16*)kz;
    const int sc = (3 - (int)((unsigned)(size_t)wqkv >> 7)) & 7;
    const unsigned* one = k_num;
    unsigned v0 = 0;
    asm volatile("" : "+s"(one), "+v"(v0));
    auto mark = [&](unsigned dst, int n = 1, int lanes = 1) AI {
        asm volatile("s_mov_b32 m0, %2\n\ts_mov_b64 exec, %4\n\tglobal_load_lds_dword %0, %1 offset:%3\n\ts_mov_b64 exec, -1"
                     ::"v"(v0), "s"(one), "s"(dst - 4 * n), "n"(4 * n), "n"(lanes) : "memory");
    };
    auto four = [&](const unsigned (&v)[4], const char* src, unsigned dst, int nt = 0) AI {
#define FOUR(P) asm volatile("s_mov_b32 m0, %5\n\ts_nop 0\n\t" \
                     "global_load_lds_dwordx4 %0, %4" P "\n\tglobal_load_lds_dwordx4 %1, %4 offset:1024" P "\n\t" \
                     "global_load_lds_dwordx4 %2, %4 offset:2048" P "\n\tglobal_load_lds_dwordx4 %3, %4 offset:3072" P \
                     ::"v"(v[0]), "v"(v[1]), "v"(v[2]), "v"(v[3]), "s"(src), "s"(dst) : "memory")
        if (nt == 1) FOUR(" nt");
        else if (nt == 2) FOUR(" sc1");
        else if (nt == 3) FOUR(" sc0 sc1");
        else FOUR("");
    };
    const int ks0 = ((slot + (slot >> 3)) & KXM) | (KXO == 2 || (KXO == 3 && (hp & 7) == sc) ? slot & 4 : 0);
    const char* xb0 = (const char*)x + xo0 - 4096;
    const char* wb0 = (const char*)wqkv + wo0 - 4096;
    auto fill = [&](auto N) AI {
        constexpr int n = N, j = F_J[n] < 0 ? 0 : F_J[n], xi = F_J[n] < 0 ? -1 - F_J[n] : 0, s = F_J[n] < 0 ? x_stage(xi) : J_S[j], m = J_M[j];
        const int ks = KXO ? ks0 ^ (int)((KXB >> (4 * s)) & 15) : (ks0 + s) & 15;
        if constexpr (n >= NF1 && n < NF2) {
            if constexpr (F_J[n] >= 0) {
                asm volatile("s_mov_b32 m0, %3\n\ts_nop 0\n\tglobal_load_lds_dwordx4 %0, %2\n\tglobal_load_lds_dwordx4 %1, %2 offset:1024"
                             ::"v"(vw[0]), "v"(vw[1]), "s"(wb0 + (ks * 64 + (w & 1) * 16) * 6144), "s"(lds0 + J_U[j] * 8192 + w * 2048) : "memory");
            } else four(vx, xb0 + ks * 128 + (w & 1) * (32 * 2048), lds0 + (hs ? X_U1[xi] : X_U0[xi]) * 8192 + (w & 1) * 4096);
            if constexpr (F_K[n] > 1) mark(lds0 + L_FULL + 16 + w * 4, F_K[n]);
            return;
        }
        if constexpr (F_J[n] >= 0) {
            four(vw, wb0 + ks * (64 * 6144) + (m == 2 ? 0 : 2048 * (m + 1)), lds0 + J_U[j] * 8192 + hs * 4096, s < NTS ? NTW : 0);
        } else {
            const char* xs_ = xb0 + ks * 128;
            const unsigned d = lds0 + (hs ? X_U1[xi] : X_U0[xi]) * 8192;
            four(vx, xs_, d, s < NTS ? NTX : 0);
            four(vx, xs_ + 32 * 2048, d + 4096, s < NTS ? NTX : 0);
        }
        if constexpr (F_K[n] > 1) mark(lds0 + L_FULL + (n < NF1 ? 0 : 32) + hs * 8, F_K[n], 3);
    };
    static_assert(F_K[1] == 1, "");
    if (__builtin_expect((w & 5) == 4, 1)) sfor<0, 2>(fill);
    RS(49);
    unsigned sd = 0, st;
    asm volatile("s_cmp_lg_u32 %2, 5\n\ts_cbranch_scc1 .La%=\n\t"
                 "s_min_u32 %1, %3, 23\n\ts_lshl_b32 %1, %1, 18\n\ts_load_dword %0, %5, %1\n\ts_load_dword %0, %6, 0x0\n\t"
                 "s_and_b32 %1, %1, 0x3c0000\n\ts_load_dword %0, %4, %1\n\ts_load_dword %0, %10, %1\n\t"
                 "s_load_dword %0, %7, 0x0\n\ts_load_dword %0, %8, 0x0\n\ts_load_dword %0, %9, 0x0\n.La%=:"
                 : "+s"(sd), "=&s"(st) : "s"(w), "s"(slot), "s"(x), "s"(wqkv), "s"(one), "s"(wr), "s"(gw), "s"(wphi), "s"(out) : "memory", "scc");
    asm volatile("s_cmp_lg_u32 %2, 7\n\ts_cbranch_scc1 .La%=\n\t"
                 "s_min_u32 %1, %3, 27\n\ts_lshl_b32 %1, %1, 18\n\ts_load_dword %0, %4, 0x0\n\ts_load_dword %0, %4, %1\n\t"
                 "s_load_dword %0, %5, %1\n\ts_load_dword %0, %6, %1\n\ts_load_dword %0, %7, %1\n\t"
                 "s_and_b32 %1, %1, 0x1c0000\n\ts_load_dword %0, %8, %1\n.La%=:\n\ts_barrier"
                 : "+s"(sd), "=&s"(st) : "s"(w), "s"(slot), "s"(ws), "s"(wg), "s"(wu), "s"(wd), "s"(wo) : "memory", "scc");
    RS(50);
    if (__builtin_expect((w & 5) == 4, 1)) {
        mark(lds0 + L_FULL + hs * 8, 1, 3);
        sfor<2, NF0>(fill);
    }
    RS(51);
    asm volatile("" : "+s"(bid));
    const int xcd = bid & 7;
    const int rt = (xcd >> 1) * 4 + ((bid >> 3) & 3), cb = (xcd & 1) * 8 + (bid >> 5), rot = 4 * cb + ROTK * rt;
    const __amdgpu_buffer_rsrc_t rsrc = __builtin_amdgcn_make_buffer_rsrc(ws, 0, WS_END, 0x00020000);
    const u32x4 z4 = {0u, 0u, 0u, 0u};
    const f32x4 zero = {0.f, 0.f, 0.f, 0.f};
    unsigned AX = lds0 + L_A + rg * 4096 + l16;
    unsigned W4 = lds0 + p * 4;
    unsigned PG = lds0 + L_PROG + p * 256;
    const unsigned WR = lds0 + rg * 4;
    auto flag = [&](unsigned a) AI { asm volatile("ds_write_b32 %0, %0" ::"v"(a) : "memory"); };
    auto phi_w = [&](bf16x8 (&a)[2], int h) AI {
        const unsigned ad = lds0 + L_WPHI + h * 2048 + (4 * g + q) * 64 + (((p >> 1) ^ (2 * (g & 1))) * 16) + 8 * (p & 1);
        a[0] = cat8(tr_read(ad), tr_read(ad + 1024));
        a[1] = cat8(tr_read(ad ^ 32), tr_read((ad ^ 32) + 1024));
    };
    u32x4 fw[32], fd[16], gu[2];
    unsigned sel_[4][2];
    u32x4 ph[2][2], sq;
    const int hp2 = ((bid >> 1) & 3) * 4 + (bid >> 6);
    const bool ffn = (hp2 & 7) != sc && !(NOF & 1);
    const int ph4 = ((bid >> 3) + (bid >> 6)) & 3;
    const int turn = (ph4 + TRN) & TRM;
    const int kq = mt >> 2;
    const int cs = (ffn ? hp2 - (hp2 > sc) - (hp2 > sc + 8) : 14 + (hp2 >> 3)) * 4 + ((ph4 + CPH - (int)((unsigned)(size_t)wg >> 7)) & 3);
    auto spread = [&](const u32x4& v, int i) -> bf16x8 {
        const unsigned s = v[i >> 1];
        u32x4 a;
#pragma unroll
        for (int d = 0; d < 4; ++d) a[d] = __builtin_amdgcn_perm(s, s, sel_[d][i & 1]);
        return __builtin_bit_cast(bf16x8, a);
    };
    auto fold_store = [&](const f32x4& a, unsigned off) {
        f32x4 v;
#pragma unroll
        for (int i = 0; i < 4; ++i) {
            const unsigned u = __float_as_uint(a[i]);
            const float r = __uint_as_float(__builtin_amdgcn_update_dpp(u, u, 0x128, 0xc, 0xf, false));
            v[i] = swap32_add(r, r);
        }
        const u32x4 o = nz4(v);
        ST_MASK("global_store_dwordx4", 0x00FF00FFull, off + l15 * 32 + g * 16, o, ws);
    };
    auto sel_init = [&]() AI {
#pragma unroll
        for (int d = 0; d < 4; ++d)
#pragma unroll
            for (int hf = 0; hf < 2; ++hf) {
                const unsigned b = hf ? 0x0302u : 0x0100u;
                sel_[d][hf] = (sch >> 1) != d ? 0x0c0c0c0cu : (l & 1) ? (b << 16) | 0x0c0cu : 0x0c0c0000u | b;
            }
    };
    auto gua = [&](int i) { return ws + WS_GU + cs * 4096 + i * 1024; };
    auto gu_load = [&]() AI {
#pragma unroll
        for (int i = 0; i < 2; ++i) ld_async(gu[i], l16, gua(i));
    };
    auto down = [&]() AI {
#pragma unroll
        for (int i = 0; i < 16; ++i) pin(fd[i]);
        settle(gu, [&](int) { return l16; }, gua);
        const f32x4 v = __builtin_bit_cast(f32x4, gu[0]) + __builtin_bit_cast(f32x4, gu[1]);
        auto r = __builtin_amdgcn_permlane16_swap(__float_as_uint(swap32_add(v[0], v[2])), __float_as_uint(swap32_add(v[1], v[3])), false, false);
        const float gt = rbf(__uint_as_float(r[0])), up = rbf(__uint_as_float(r[1]));
        const unsigned ha = lds0 + L_XS + hs * 512;
        lds_write<u16>(ha + l15 * 8 + g * 2, f2bf(rbf(gt / (1.f + __expf(-gt))) * up));
        const u32x4 hv = lds_read<u32x4>(ha + srow * 16);
        f32x4 y0 = zero, y1 = zero;
#pragma unroll
        for (int i = 0; i < 8; ++i) {
            const bf16x8 a = spread(hv, i);
            y0 = mfma16(a, __builtin_bit_cast(bf16x8, fd[i]), y0);
            y1 = mfma16(a, __builtin_bit_cast(bf16x8, fd[8 + i]), y1);
        }
        fold_store(y0, WS_Y + cs * 4096 + kq * 1024 + hs * 512);
        fold_store(y1, WS_Y + cs * 4096 + kq * 1024 + hs * 512 + 256);
    };
    auto need = [&](int c) AI {
        if (c == 101) lds_wait(W4 + L_FKS, 1);
        else if (c == 100) lds_wait(W4 + L_FV, 1);
        else if (c == 99) lds_wait(W4 + L_FQD, 1);
        else if (c > 0) lds_wait(c > RLD ? W4 + L_PRL : PG, c);
    };
    auto wo_load = [&](int n0, int n1, bool mk) AI {
        unsigned vo[4];
#pragma unroll
        for (int i = 0; i < 4; ++i) vo[i] = (unsigned)((8 * i + srow) * 2048 + ((sch ^ ((g & 3) << 1)) * 16) + 4096 - i * 1024);
        const char* wob = (const char*)wo + cb * 128 - 4096;
#pragma unroll
        for (int n = n0; n < ((NOF & 4) ? n0 : n1); ++n) {
            if (n < WON && (n == 0 || W_N[n] != W_N[n - 1])) need(W_N[n]);
            four(vo, wob + ((2 * n + hs + rot) & 31) * 65536, lds0 + wo_unit(n) * 8192 + hs * 4096);
        }
        if (mk) mark(lds0 + (n0 >= WON && WON ? L_FW2 : L_FWD) + hs * 4);
    };
    auto phi_adr = [&](unsigned (&pw)[4], unsigned base) AI {
#pragma unroll
        for (int i = 0; i < 4; ++i) pw[i] = (base + rg * 4096 + l15 * 128 + ((g ^ fsw(l15)) << 3)) ^ (i << 5);
    };
    const int row0 = rt * 128 + rg * 32 + kp * 16;
    u32x4 xr[2];
    u64 gg[2];
    unsigned xo = srow * 2048 + sch * 16;
    auto g_load = [&]() AI {
        asm volatile("global_load_dwordx2 %0, %2, %3 sc1\n\tglobal_load_dwordx2 %1, %2, %3 offset:64 sc1"
                     : "=&v"(gg[0]), "=&v"(gg[1]) : "v"((unsigned)srow * 8), "s"(ws + WS_GG + row0 * 8) : "memory");
    };
    auto xg_load = [&](int skip) AI {
        asm volatile("s_cmp_eq_u32 %4, 4\n\ts_cbranch_scc1 .Lk%=\n\t"
                     "global_load_dwordx4 %0, %5, %6\n\tglobal_load_dwordx4 %1, %5, %7\n\t"
                     "global_load_dwordx2 %2, %8, %9 sc1\n\tglobal_load_dwordx2 %3, %8, %9 offset:64 sc1\n.Lk%=:"
                     : "+v"(xr[0]), "+v"(xr[1]), "+v"(gg[0]), "+v"(gg[1])
                     : "s"(skip), "v"(xo), "s"(x + (size_t)row0 * DM + cb * 64), "s"(x + (size_t)(row0 + 8) * DM + cb * 64),
                       "v"((unsigned)srow * 8), "s"(ws + WS_GG + row0 * 8) : "memory", "scc");
    };
    auto xg_def = [&]() AI { asm volatile("" : "=v"(xr[0]), "=v"(xr[1]), "=v"(gg[0]), "=v"(gg[1])); };
    auto relane = [&]() AI {
        unsigned z = 0;
        asm volatile("" : "+v"(z));
        l = __builtin_amdgcn_mbcnt_hi(~0u, __builtin_amdgcn_mbcnt_lo(~0u, z));
        g = l >> 4, l15 = l & 15, q = l15 >> 2, p = l & 3, srow = l >> 3, sch = l & 7, l16 = l * 16;
        AX = lds0 + L_A + rg * 4096 + l16, W4 = lds0 + p * 4, PG = lds0 + L_PROG + p * 256, xo = srow * 2048 + sch * 16;
    };
    u32x4 kf[6];
    auto kf_def = [&]() AI { asm volatile("" : "=v"(kf[0]), "=v"(kf[1]), "=v"(kf[2]), "=v"(kf[3]), "=v"(kf[4]), "=v"(kf[5])); };
    const int oh = hs ^ kp ^ 1;

    if (__builtin_expect(w >= 4, 1)) {
        const bool loader = !(w & 1);
        const unsigned kf_kv = WS_KF + (hp * 2 + (mt >> 3)) * 8192 + (mt & 7) * 512;
        const unsigned kf_z = WS_KF + (hp * 2 + (mt >> 3)) * 8192 + 4096 + ((mt >> 2) & 1) * 64;
        const unsigned fgo = ffn ? (unsigned)((256 * kq + 16 * srow) * 7168 + cs * 128 + sch * 16) : 0u;
        const char* fsrc = (const char*)(hs ? wu : wg);
        auto gu_w = [&](int q0, int q1) AI {
#pragma unroll
            for (int i = q0; i < q1; ++i) {
                unsigned t1, t2, t3;
                if (NOF & 8) fw[4 * i] = fw[4 * i + 1] = fw[4 * i + 2] = fw[4 * i + 3] = z4; else
                asm volatile("v_add_u32 %4, 0x1c00, %7\n\tv_add_u32 %5, 0x3800, %7\n\tv_add_u32 %6, 0x5400, %7\n\t"
                             "global_load_dwordx4 %0, %7, %8 nt\n\tglobal_load_dwordx4 %1, %4, %8 nt\n\t"
                             "global_load_dwordx4 %2, %5, %8 nt\n\tglobal_load_dwordx4 %3, %6, %8 nt"
                             : "=&v"(fw[4 * i]), "=&v"(fw[4 * i + 1]), "=&v"(fw[4 * i + 2]), "=&v"(fw[4 * i + 3]), "=&v"(t1), "=&v"(t2), "=&v"(t3)
                             : "v"(fgo), "s"(fsrc + (128 * (i >> 2) + 4 * (i & 3)) * 7168) : "memory");
            }
        };
        const unsigned fdo = ffn ? (unsigned)((64 * cs + 8 * srow) * 2048 + 512 * kq + 256 * hs + sch * 16) : 0u;
        auto dn_w = [&](int q0, int q1) AI {
#pragma unroll
            for (int i = q0; i < q1; ++i)
                if (NOF & 8) fd[4 * i] = fd[4 * i + 1] = fd[4 * i + 2] = fd[4 * i + 3] = z4; else
                asm volatile("global_load_dwordx4 %0, %4, %5 offset:-4096 nt\n\tglobal_load_dwordx4 %1, %4, %5 offset:-2048 nt\n\t"
                             "global_load_dwordx4 %2, %4, %5 nt\n\tglobal_load_dwordx4 %3, %4, %5 offset:2048 nt"
                             : "=&v"(fd[4 * i]), "=&v"(fd[4 * i + 1]), "=&v"(fd[4 * i + 2]), "=&v"(fd[4 * i + 3])
                             : "v"(fdo), "s"((const char*)wd + 4096 + (i & 1) * 8192 + (i >> 1) * 128) : "memory");
        };
        auto gate_up = [&]() AI {
            sel_init();
#pragma unroll
            for (int i = 0; i < 32; ++i) pin(fw[i]);
            u32x4 xs[4];
#pragma unroll
            for (int i = 0; i < 4; ++i) xs[i] = lds_read<u32x4>(lds0 + L_XS + hs * 512 + (i >> 1) * 256 + srow * 32 + (i & 1) * 16);
            f32x4 a = zero;
#pragma unroll
            for (int i = 0; i < 32; ++i) a = mfma16(spread(xs[i >> 3], i & 7), __builtin_bit_cast(bf16x8, fw[i]), a);
            fold_store(a, WS_GU + cs * 4096 + kq * 512 + hs * 256);
            SB0();
        };
        auto o_clear = [&]() AI {
#pragma unroll
            for (int n = 0; n < ((NOF & 2) ? 1 : 8); ++n) st_wt(rsrc, WS_O + ((mt * 2 + (n >> 2)) * 32 + 2 * hp + hs) * 4096 + (n & 3) * 1024 + l16, z4);
        };
        u64 T;
        u32x4 sv[2];
        auto clears = [&]() AI {
            o_clear();
#pragma unroll
            for (int n = 0; n < ((NOF & 16) ? 0 : 4); ++n) st_wt(rsrc, WS_KV + ((hp * 8 + hs * 4 + n) * 16 + mt) * 1024 + l16, z4);
            ST_MASK("global_store_dwordx4", 0xFFFFull, WS_Z + ((hp * 4 + hs * 2 + (srow & 1)) * 16 + mt) * 128 + sch * 16, z4, ws);
        };
        auto cleared = [&]() AI { ST_MASK("global_store_dwordx2", 1ull, 0u, T, ws + WS_CL + ((mt * 2 + hs) * 16 + hp) * 8); };
        auto rg_load = [&]() AI {
            asm volatile("global_load_dwordx4 %0, %2, %3 sc1\n\tglobal_load_dwordx4 %1, %2, %3 offset:16 sc1"
                         : "=&v"(sv[0]), "=&v"(sv[1]) : "v"(l * 32), "s"(ws + WS_RG) : "memory");
        };
        auto select = [&]() AI {
            const unsigned tl = (unsigned)T & 0x1FFFu, thi = (unsigned)(T >> 13);
#pragma unroll 1
            for (unsigned s = 0; s < SPIN_CAP; ++s) {
                pin(sv[0]);
                pin(sv[1]);
                const unsigned bad = ((sv[0][0] >> 19) ^ tl) | (sv[0][1] ^ thi) | ((sv[0][2] >> 19) ^ tl) | (sv[0][3] ^ thi) |
                                     ((sv[1][0] >> 19) ^ tl) | (sv[1][1] ^ thi) | ((sv[1][2] >> 19) ^ tl) | (sv[1][3] ^ thi);
                if (__all(bad == 0u)) break;
                __builtin_amdgcn_s_sleep(16);
                rg_load();
                waitv<0>();
            }
            pin(sv[0]);
            pin(sv[1]);
            RS(4);
            unsigned k2 = 0;
#pragma unroll
            for (int j = 0; j < 4; ++j) {
                const unsigned v = sv[j >> 1][(j & 1) * 2] & 0x7FFFFu;
                k2 = max(k2, ((v >> 3) << 11) | ((255 - (4 * l + j)) << 3) | (v & 7));
            }
            const unsigned key = __builtin_amdgcn_readfirstlane(wave_umax(k2));
            asm volatile("s_mov_b32 m0, %2\n\ts_mov_b64 exec, %3\n\tglobal_load_lds_dwordx4 %0, %1\n\ts_mov_b64 exec, -1"
                         ::"v"((unsigned)(512 * kq) + l16), "s"(x + (size_t)(2047 - (int)(key & 2047u)) * DM), "s"(lds0 + L_XS + hs * 512), "s"(0xFFFFFFFFull) : "memory");
            unsigned rb = key >> 11;
            rb ^= (rb & 0x8000u) ? 0x8000u : 0xFFFFu;
            const float rsel = rbf(1.f / (1.f + __expf(-bf2f((u16)rb))));
            lds_write<u32x4>(lds0 + L_SEL, (u32x4){(unsigned)T, (unsigned)(T >> 32), 2047u - (key & 2047u), __float_as_uint(rsel)});
            if (bid == 0 && hs) ST_MASK("global_store_dwordx2", 1ull, 0u, T, ws + WS_E);
            RS(5);
        };
        auto k_soft = [&](int np) AI {
            bf16x8 a0[2], kb[2];
            f32x4 pk[2][2][2];
            unsigned pw[4];
            phi_w(a0, 0);
            phi_adr(pw, lds0 + P_U[0] * 8192);
            asm volatile("" : "+v"(pw[0]), "+v"(pw[1]), "+v"(pw[2]), "+v"(pw[3]));
            auto logits = [&](int h) AI {
                lds_wait(WR + L_FK0 + 4 * h, 1);
#pragma unroll
                for (int i = 0; i < 2; ++i) kb[i] = lds_read128(AX + (P_U[0] * 8192 - L_A) + h * 4096 + i * 1024);
#pragma unroll
                for (int i = 0; i < 4; ++i) pk[h][i >> 1][i & 1] = mfma16(a0[i & 1], kb[i >> 1], zero);
            };
            auto round = [&](int h) AI {
#pragma unroll
                for (int i = 0; i < 4; ++i) pk[h][i >> 1][i & 1] = rbf4(pk[h][i >> 1][i & 1]);
            };
            logits(0);
            RS(12);
            round(0);
            SB0();
            phi_k<KPK & 1>(pk[0], pw, 0, [&]() AI {
                if (np > 1) logits(1);
                SB0();
            });
            SB0();
            if (np > 1) {
                round(1);
                phi_k<(KPK >> 1)>(pk[1], pw, 4096, []() {});
                SB0();
            }
            flag(WR + L_FP);
        };
        if (__builtin_expect(loader, 1)) {
            vx_init();
            vw_init();
            if (LPRIO) __builtin_amdgcn_s_setprio(LPRIO);
            unsigned pv = 0;
            sfor<NF0, NF1>([&](auto N) {
                constexpr int n = N;
                if constexpr (n > 0 && F_N[n] > F_N[n > 0 ? n - 1 : 0]) {
                    RSL(32 + F_N[n]);
                    if (LPRW != LPRIO) __builtin_amdgcn_s_setprio(LPRW);
                    if (NOF & 256) {
                    } else if (F_N[n] > RLD) {
                        lds_wait(W4 + L_PRL, F_N[n]);
                    } else if (PEEK) {
                        lds_wait(pv, PG, F_N[n]);
                        if (F_N[n] < F_N[NF1 - 1] && next_need(n) <= RLD) lds_peek(pv, PG);
                    } else lds_wait(PG, F_N[n]);
                    if (LPRW != LPRIO) __builtin_amdgcn_s_setprio(LPRIO);
                    RSL(48 + F_N[n]);
                }
                if constexpr (n == 12) {
                    asm volatile("s_mov_b32 m0, %2\n\ts_nop 0\n\tglobal_load_lds_dwordx4 %0, %1\n\tglobal_load_lds_dwordx4 %0, %1 offset:1024"
                                 ::"v"((unsigned)((l >> 2) * 64 + (((l & 3) ^ (2 * ((l >> 4) & 1))) * 16))),
                                   "s"(wphi + (hp * 2 + hs) * 1024), "s"(lds0 + L_WPHI + hs * 2048) : "memory");
                }
                if constexpr (LTH < 63 && n >= LT0) waitv<LTH>();
                fill(N);
            });
            flag(lds0 + L_FLD + hs * 4);
            RS(1);
            if (HPRIO) __builtin_amdgcn_s_setprio(HPRIO);
            const char* cla = ws + WS_CL + ((l15 * 2 + (mt >> 3)) * 16 + hp) * 8;
            u64 cl;
            if (CLE) asm volatile("global_load_dwordx2 %0, %1, off sc1" : "=&v"(cl) : "v"(cla) : "memory");
            k_soft(SSM ? 1 : 2);
            if (!CLE) asm volatile("global_load_dwordx2 %0, %1, off sc1" : "=&v"(cl) : "v"(cla) : "memory");
            RS(11);
            lds_wait(W4 + L_FP, 1);
            sq = lds_read<u32x4>(lds0 + L_SEL);
            waitv<0>();
            asm volatile("" : "+v"(cl));
            RS(13);
            {
                u32x4 kp_[5];
                const char* kb = ws + WS_KV + (hp * 8 + (mt >> 1)) * 16384 + (mt & 1) * 512 + hs * 256;
                const bool zw = !(mt & 3);
                const char* zb = zw ? ws + WS_Z + (hp * 4 + (mt >> 2)) * 2048 + hs * 64 : kb;
                const unsigned kvo = g * 1024 + l15 * 16, zo = zw ? l15 * 128 + g * 16 : 0u;
                auto ko = [&](int i) { return i < 4 ? kvo : zo; };
                auto ka = [&](int i) { return i < 4 ? kb + i * 4096 : zb; };
                const u64 T = (u64)sq[1] << 32 | sq[0];
#pragma unroll 1
                for (unsigned s = 0; s < SPIN_CAP && __ballot(cl != T); ++s)
                    asm volatile("global_load_dwordx2 %0, %1, off sc1\n\ts_waitcnt vmcnt(0)" : "=&v"(cl) : "v"(cla) : "memory");
                lds_wait0(W4 + L_FKS);
                if (GDL) __builtin_amdgcn_s_sleep(GDL);
#pragma unroll
                for (int i = 0; i < 5; ++i) ld_async(kp_[i], ko(i), ka(i));
                if (XGE) {
                    xg_def();
                    xg_load(w + 1);
                }
                flag(lds0 + L_FGI + hs * 4);
                SB0();
                RS(14);
                waitv<0>();
                RS(15);
                settle(kp_, ko, ka);
                RS(24);
                float v[8], u[4];
#pragma unroll
                for (int e = 0; e < 4; ++e) {
                    v[2 * e] = (lo16(kp_[0][e]) + lo16(kp_[1][e])) + (lo16(kp_[2][e]) + lo16(kp_[3][e]));
                    v[2 * e + 1] = (hi16(kp_[0][e]) + hi16(kp_[1][e])) + (hi16(kp_[2][e]) + hi16(kp_[3][e]));
                }
#pragma unroll
                for (int e = 0; e < 4; ++e) u[e] = swap32_add(v[e], v[e + 4]);
                const unsigned o = nz1(pack2(swap16_add(u[0], u[2]), swap16_add(u[1], u[3])));
                ST_MASK("global_store_dword", ~0ull, kf_kv + hs * 256 + l15 * 16 + g * 4, o, ws);
                if (zw) {
                    f32x4 z;
#pragma unroll
                    for (int i = 0; i < 4; ++i) z[i] = dpp_add<0x140>(dpp_add<0x141>(dpp_add<0x4E>(dpp_add<0xB1>(__uint_as_float(kp_[4][i])))));
                    const u32x2 oz = {nz1(pack2(z[0], z[1])), nz1(pack2(z[2], z[3]))};
                    ST_MASK("global_store_dwordx2", 0x0001000100010001ull, kf_z + g * 16 + hs * 8, oz, ws);
                }
                flag(lds0 + L_FG + hs * 4);
            }
            RS(25);
            const char* kfb = ws + WS_KF + (hp * 2 + hs) * 8192;
            auto fo = [&](int i) { return i < 4 ? l16 : (unsigned)g * 16; };
            auto fb = [&](int i) { return i < 4 ? kfb + i * 1024 : kfb + 4096 + (i - 4) * 64; };
            if (KFD) __builtin_amdgcn_s_sleep(KFD);
#pragma unroll
            for (int i = 0; i < 6; ++i) ld_async(kf[i], fo(i), fb(i));
            if (!XGE) {
                xg_def();
                xg_load(w + 1);
            }
            waitv<0>();
            asm volatile("" : "+v"(xr[0]), "+v"(xr[1]), "+v"(gg[0]), "+v"(gg[1]));
            RS(29);
            settle(kf, fo, fb);
            need(KFN);
#pragma unroll
            for (int i = 0; i < 4; ++i) lds_write<u32x4>(lds0 + L_KF + hs * 4096 + i * 1024 + l16, kf[i]);
#pragma unroll
            for (int i = 0; i < 2; ++i) lds_write<u32x4>(lds0 + L_KZ + hs * 128 + i * 64 + g * 16, kf[4 + i]);
            SB0();
            flag(lds0 + L_FKF + hs * 4);
        } else {
            const int row0 = (mt * 16 + hp) * 8 + 4 * hs;
            if (SPRIO) __builtin_amdgcn_s_setprio(SPRIO);
            asm volatile("s_waitcnt lgkmcnt(0)" : "+s"(sd));
            if (TLD) __builtin_amdgcn_s_sleep(TLD);
            u32x4 rv[2], gv[2], xv[4][2];
            u64 ev;
            if (RT0) lds_wait8(PG, RT0);
            asm volatile("global_load_dwordx2 %4, %5, %9 sc1\n\t"
                         "global_store_dwordx4 %10, %11, %9 sc1\n\t"
                         "global_store_dwordx2 %12, %13, %9 sc1\n\t"
                         "global_load_dwordx4 %0, %6, %7\n\tglobal_load_dwordx4 %1, %6, %7 offset:1024\n\t"
                         "global_load_dwordx4 %2, %6, %8\n\tglobal_load_dwordx4 %3, %6, %8 offset:1024"
                         : "=&v"(rv[0]), "=&v"(rv[1]), "=&v"(gv[0]), "=&v"(gv[1]), "=&v"(ev)
                         : "v"(0u), "v"(l16), "s"(wr), "s"(gw), "s"(ws),
                           "v"(l < 32 ? WS_Y + cs * 4096 + kq * 1024 + hs * 512 + l16 : WS_GU + cs * 4096 + kq * 512 + hs * 256 + l15 * 16), "v"(z4),
                           "v"(hs ? kf_z + sch * 8 : kf_kv + l * 8), "v"(0ull)
                         : "memory");
            RS(1);
#pragma unroll
            for (int r = 0; r < 2; ++r)
                if (NOF & 64) xv[2 * r][0] = xv[2 * r][1] = xv[2 * r + 1][0] = xv[2 * r + 1][1] = z4; else
                asm volatile("global_load_dwordx4 %0, %4, %5\n\tglobal_load_dwordx4 %1, %4, %5 offset:1024\n\t"
                             "global_load_dwordx4 %2, %4, %5 offset:2048\n\tglobal_load_dwordx4 %3, %4, %5 offset:3072"
                             : "=&v"(xv[2 * r][0]), "=&v"(xv[2 * r][1]), "=&v"(xv[2 * r + 1][0]), "=&v"(xv[2 * r + 1][1])
                             : "v"(l16), "s"(x + (size_t)(row0 + 2 * r) * DM) : "memory");
            SB0();
            waitv<0>();
            asm volatile("" : "+v"(rv[0]), "+v"(rv[1]), "+v"(gv[0]), "+v"(gv[1]), "+v"(ev));
#pragma unroll
            for (int r = 0; r < 4; ++r) asm volatile("" : "+v"(xv[r][0]), "+v"(xv[r][1]));
            RS(2);
            T = ((u64)__builtin_amdgcn_readfirstlane((unsigned)(ev >> 32)) << 32 | __builtin_amdgcn_readfirstlane((unsigned)ev)) + 1;
            float ra_[4] = {0.f, 0.f, 0.f, 0.f}, ga_[4] = {0.f, 0.f, 0.f, 0.f};
#pragma unroll
            for (int r = 0; r < 4; ++r)
#pragma unroll
                for (int i = 0; i < 2; ++i)
#pragma unroll
                    for (int j = 0; j < 4; ++j) {
                        ra_[r] = dot2(xv[r][i][j], rv[i][j], ra_[r]);
                        ga_[r] = dot2(xv[r][i][j], gv[i][j], ga_[r]);
                    }
            const int rr = (g >> 1) | ((g & 1) << 1);
            unsigned k = f2bf(sum4(ra_));
            k ^= (k & 0x8000u) ? 0xFFFFu : 0x8000u;
            k = (k << 3) | (7 - 4 * hs - rr);
            auto r1 = __builtin_amdgcn_permlane16_swap(k, k, false, false);
            k = max(r1[0], r1[1]);
            auto r2 = __builtin_amdgcn_permlane32_swap(k, k, false, false);
            k = max(r2[0], r2[1]);
            if (hs) {
                lds_write<unsigned>(lds0 + L_BEST, k | 0x80000000u);
            } else {
                lds_wait(lds0 + L_BEST, 1);
                const u64 rq = (T << 19) | max(k, lds_read<unsigned>(lds0 + L_BEST) & 0x7FFFFu);
                ST_MASK("global_store_dwordx2", 1ull, 0u, rq, ws + WS_RG + (mt * 16 + hp) * 8);
            }
            const u64 gq = (T << 16) | f2bf(1.f / (1.f + __expf(-rbf(sum4(ga_)))));
            ST_MASK("global_store_dwordx2", 0x0001000100010001ull, (unsigned)(rr * 8), gq, ws + WS_GG + row0 * 8);
            RS(3);
            __builtin_amdgcn_s_setprio(SPR2);
            auto q_fill = [&]() AI {
                vx_init();
                vw_init();
                sfor<NF2, NF>([&](auto N) {
                    constexpr int n = N;
                    if constexpr (n == NF2 || F_N[n] != F_N[n - 1]) need(F_N[n]);
                    fill(N);
                });
            };
            auto wt = [&](unsigned f) AI { lds_wait(lds0 + f + (l & 1) * 4, 1); };
            unsigned pf[2];
            auto wo_warm = [&]() AI {
                asm volatile("global_load_dword %0, %2, %3\n\tglobal_load_dword %1, %2, %4" : "=&v"(pf[0]), "=&v"(pf[1])
                             : "v"((unsigned)(((rt & 3) * 256 + hs * 128 + l) * 2048)), "s"((const char*)wo + cb * 128), "s"((const char*)wo + cb * 128 + 64 * 2048) : "memory");
            };
            if (DNP == 0) {
                lds_wait8(PG, FBA + FBS * turn);
                dn_w(0, 4);
            }
            if (WPF == 2) {
                lds_wait8(PG, FBA + FBS * turn);
                wo_warm();
            }
            lds_wait8(PG, CLR0 + CLS * ((ph4 + CLP) & 3));
            clears();
            waitv<0>();
            if (WPF == 2) asm volatile("" : "+v"(pf[0]), "+v"(pf[1]));
            cleared();
            lds_wait8(PG, SEL0);
            rg_load();
            if (WPF == 3) wo_warm();
            waitv<0>();
            if (WPF == 3) asm volatile("" : "+v"(pf[0]), "+v"(pf[1]));
            select();
            if (!SSM) flag(WR + L_FP);
            lds_wait8(lds0 + L_FLD + (l & 1) * 4, 1);
            RS(6);
            if (WPF == 4) wo_warm();
            gu_w(0, GQ1);
            q_fill();
            RS(7);
            gu_w(GQ1, GQ2);
            if (SSM) {
                __builtin_amdgcn_s_setprio(HPRIO);
                k_soft(1);
                __builtin_amdgcn_s_setprio(SPR2);
                RS(11);
            }
            gu_w(GQ2, 8);
            if (WPF == 1) wo_warm();
            if (DNP == 1) dn_w(0, 4);
            RS(8);
            waitv<(DNP == 1 ? 16 : 0)>();
            if ((WPF == 1 && DNP != 1) || WPF == 4) asm volatile("" : "+v"(pf[0]), "+v"(pf[1]));
            RS(9);
            if (GUO == 0) gate_up();
            RS(10);
            if (WOT == 1) lds_wait(W4 + L_FKS, 1);
            if (WOT == 2) wt(L_FGI);
            if (DNP == 2) dn_w(0, 4);
            wo_load(0, WOE, WOE == WON);
            RS(14);
            if (GUO == 1) {
                __builtin_amdgcn_s_setprio(GPR);
                gate_up();
                __builtin_amdgcn_s_setprio(SPR2);
            }
            RS(13);
            if (YT == 1) wt(L_FG);
            gu_load();
            if (DNP == 3) dn_w(0, 4);
            if (WOE < WON) wo_load(WOE, WON, true);
            waitv<(WOE < WON ? 4 * (WON - WOE) + 1 : 0)>();
            if (WPF == 1 && DNP == 1) asm volatile("" : "+v"(pf[0]), "+v"(pf[1]));
            RS(15);
            down();
            RS(24);
            sq = lds_read<u32x4>(lds0 + L_SEL);
            xg_def();
            kf_def();
        }
        RS(26);
        lds_wait0(WR + L_FQ);
#pragma unroll
        for (int i = 0; i < 4; ++i) ph[i >> 1][i & 1] = lds_read<u32x4>(AX + i * 1024);
    } else {
        if (CPRIO) __builtin_amdgcn_s_setprio(CPRIO);
        unsigned ra[4], xt[2];
#pragma unroll
        for (int u = 0; u < 4; ++u) ra[u] = lds0 + J_B[0] * 8192 + (8 * g + q) * 128 + ((u ^ (q >> 1) ^ ((g & 1) << 1)) * 32) + 8 * p;
        unsigned xa = lds0 + (32 * (w & 1) + l15) * 128 + ((g ^ (l15 >> 1)) * 16), fa = lds0 + L_FULL + (l & 3) * 4;
        xt[0] = xa + (hs ? X_U1[0] : X_U0[0]) * 8192;
        xt[1] = xt[0] ^ 64;
        asm volatile("s_mov_b32 m0, %4" : "+v"(xa), "+v"(fa), "+v"(xt[0]), "+v"(xt[1]) : "s"(lds0 + L_PROG + w * 256 - 124));
        f32x4 acc[3][4][2];
        bf16x8 xf[ND][2][2];
        s16x4 wh[PD + 1][2];
        unsigned fv = 0;
        bf16x8 kown[2];
        u64 bad = ~0ull;
        auto chk = [&](int c) AI {
            SB0();
            if (__builtin_expect(bad != 0ull, 0)) {
                unsigned c_;
                asm volatile("s_mov_b32 %1, 0x400000\n"
                             ".Lp%=:\n\tds_read_b32 %0, %2 offset:%3\n\ts_waitcnt lgkmcnt(0)\n\tv_cmp_gt_u32 vcc, %4, %0\n\ts_cbranch_vccz .Lf%=\n\t"
                             "s_sub_u32 %1, %1, 1\n\ts_cbranch_scc0 .Lp%=\n.Lf%=:"
                             : "=&v"(fv), "=&s"(c_) : "v"(fa), "n"(c > NM2 ? 32 : c > NM1 ? 16 : 0), "n"(c) : "memory", "vcc", "scc");
            }
            SB0();
        };
        auto kv_part = [&]() AI
        {
            bf16x8 ones;
#pragma unroll
            for (int j = 0; j < 8; ++j) ones[j] = (short)0x3f80;
            const unsigned pa = lds0 + (hs ? P_U[2] : P_U[0]) * 8192 + (4 * g + q) * 128 + (((8 * (rg & 1) + p) ^ fsw(4 * g + q)) << 3);
            const unsigned va = lds0 + L_A + hs * 2048 + l16;
            bf16x8 b0[4], b1[4], af[2][4];
            lds_wait0(lds0 + L_FV + sch * 4);
            RS(15);
#pragma unroll
            for (int i = 0; i < 4; ++i) {
                af[0][i] = cat8(tr_read(pa + i * 4096), tr_read(pa + i * 4096 + 2048));
                b0[i] = lds_read128(va + i * 4096);
                af[1][i] = cat8(tr_read((pa ^ 32) + i * 4096), tr_read((pa ^ 32) + i * 4096 + 2048));
            }
#pragma unroll
            for (int i = 0; i < 4; ++i) b1[i] = lds_read128(va + i * 4096 + 1024);
            SB0();
            f32x4 k0[2] = {zero, zero}, k1[2] = {zero, zero}, zt[2] = {zero, zero};
            const unsigned kv_st = WS_KV + ((hp * 8 + rg * 2) * 16 + mt) * 1024 + l16;
#pragma unroll
            for (int i = 0; i < 4; ++i)
#pragma unroll
                for (int e = 0; e < 2; ++e) {
                    k0[e] = mfma16(af[e][i], b0[i], k0[e]);
                    if (PORD) zt[e] = mfma16(af[e][i], ones, zt[e]);
                }
            SB0();
#pragma unroll
            for (int i = 0; i < 4; ++i)
#pragma unroll
                for (int e = 0; e < 2; ++e) k1[e] = mfma16(af[e][i], b1[i], k1[e]);
            st_wt(rsrc, kv_st, nz4(__builtin_bit_cast(u32x4, pack44(k0[0], k0[1]))));
            if (!PORD) {
                SB0();
#pragma unroll
                for (int i = 0; i < 4; ++i)
#pragma unroll
                    for (int e = 0; e < 2; ++e) zt[e] = mfma16(af[e][i], ones, zt[e]);
                st_wt(rsrc, kv_st + 16384, nz4(__builtin_bit_cast(u32x4, pack44(k1[0], k1[1]))));
            }
            const u32x4 zo = nz4((l & 1) ? zt[1] : zt[0]);
            ST_MASK("global_store_dwordx4", 0x0003000300030003ull, WS_Z + ((hp * 4 + rg) * 16 + mt) * 128 + (l & 1) * 64 + g * 16, zo, ws);
            if (PORD) st_wt(rsrc, kv_st + 16384, nz4(__builtin_bit_cast(u32x4, pack44(k1[0], k1[1]))));
            SB0();
            flag(WR + L_FKS);
        };
        auto report = [&](int r) AI {
            if (r > RLD) {
                unsigned t;
                unsigned a;
                asm volatile("v_mov_b32 %0, %3\n\tv_mov_b32 %1, %2\n\tds_write_b32 %1, %0" : "=&v"(t), "=&v"(a) : "s"(WR + L_PRL), "n"(r) : "memory");
            } else asm volatile("global_load_lds_dword %0, %1 offset:%2" ::"v"(v0), "s"(one), "n"(4 * r) : "memory"); };
        auto rd1 = [&](auto F, auto H) AI {
            constexpr int f = F, h = H, n = f >> 2, j = f >> 3, c = h ? E_B[f] : E_A[f], a = c % 100;
            if constexpr (h == 0 && (f & 3) == 0 && Q_C[n] > 0 && !((NOF & 512) && n >= 12 && n < KD)) chk(Q_C[n]);
            wh[f % (PD + 1)][h] = tr_read(ra[f & 3] + (J_U[j] - J_B[j]) * 8192 + (n & 1) * 4096 + h * 512);
            SB0();
            if constexpr (c / 100 == 1) {
                constexpr int jn = (f + 3 - a) >> 3;
                asm volatile("v_add_u32 %0, %1, %0" : "+v"(ra[a]) : "n"((J_B[jn] - J_B[jn - 1]) * 8192));
            } else if constexpr (c / 50 == 4) {
                asm volatile("v_add_u32 %0, %1, %2" : "=v"(xt[0]) : "s"((hs ? X_U1[a] : X_U0[a]) * 8192), "v"(xa));
            } else if constexpr (c / 50 == 5) {
                asm volatile("v_xor_b32 %0, 64, %1" : "=v"(xt[1]) : "v"(xt[0]));
            } else if constexpr (c / 100 == 3) {
                xf[a % 25 % ND][a / 25 & 1][a / 50] = lds_read128(xt[a / 50] + (a / 25 & 1) * 2048);
            } else if constexpr (c / 100 == 4) {
                fv = *(volatile AS3 unsigned*)(size_t)(fa + (a > NM2 ? 32 : a > NM1 ? 16 : 0));
            } else if constexpr (c / 100 == 5) {
                bad = __builtin_amdgcn_ballot_w64(fv < (unsigned)a);
                asm volatile("" : "+s"(bad));
            } else if constexpr (c / 100 == 6 && !((NOF & 1024) && a < NP1)) {
                report(a);
            }
        };
        RS(1);
        sfor<0, PD>([&](auto F) { rd1(F, IC<0>{}); rd1(F, IC<1>{}); });
        RS(2);
        constexpr int K0F = 4 * KD, K1F = 4 * KD + 1;
        sfor<0, 96>([&](auto N) {
            sfor<0, 4>([&](auto E) {
                constexpr int n = N, e = E, f = 4 * n + e, j = n >> 1, ks = n & 1, u = f_u(f), s = J_X[j], m = J_M[j];
                const bf16x8 wv = cat8(wh[f % (PD + 1)][0], wh[f % (PD + 1)][1]);
                sfor<0, 2>([&](auto RB) {
                    constexpr int rb = RB;
                    const bf16x8 xv = xf[s % ND][rb][ks];
                    if (AMF) {
                        if (j_first(j) && ks == 0) asm volatile("v_mfma_f32_16x16x32_bf16 %0, %1, %2, 0" : "=&v"(acc[m][u][rb]) : "v"(m == 1 ? xv : wv), "v"(m == 1 ? wv : xv));
                        else asm volatile("v_mfma_f32_16x16x32_bf16 %0, %1, %2, %0" : "+v"(acc[m][u][rb]) : "v"(m == 1 ? xv : wv), "v"(m == 1 ? wv : xv));
                    } else if (m == 1) acc[m][u][rb] = mfma16(xv, wv, acc[m][u][rb]);
                    else acc[m][u][rb] = mfma16(wv, xv, acc[m][u][rb]);
                    SB0();
                    if constexpr (f + PD < 384 && (f + PD < 4 * VD || f >= 4 * VD)) rd1(IC<f + PD>{}, RB);
                    if constexpr (rb == 0 && f == K0F) relane();
                    if constexpr (AMF && rb == 0 && (f == K0F || f == K1F)) asm volatile("s_nop 7");
                    SB0();
                    if constexpr (f == K0F) lds_write<bf16x8>(AX + (P_U[0] * 8192 - L_A) + rb * 1024, pack44(acc[0][0][rb], acc[0][1][rb]));
                    if constexpr (f == K1F) {
                        kown[rb] = pack44(acc[0][2][rb], acc[0][3][rb]);
                        if (rb < 2 - SH) lds_write<bf16x8>(AX + (P_U[2] * 8192 - L_A) + rb * 1024, kown[rb]);
                        else asm volatile("" : "+v"(kown[rb]));
                    }
                    SB0();
                });
                if constexpr (f == K0F) { flag(WR + L_FK0); RS(12); }
                if constexpr (f == K1F) { if (SH < 2) flag(WR + L_FK); RS(4); }
                if constexpr (f == 4 * VD - 1) {
                    bf16x8 ak[2];
                    if (NF2 > NF1) flag(WR + L_FVR);
                    auto own = [&]() AI {
                        if (NF2 == NF1) return;
                        vx_init();
                        vw_init();
                        lds_wait0(W4 + L_FVR);
                        sfor<NF1, NF2>(fill);
                        asm volatile("s_mov_b32 m0, %0" ::"s"(lds0 + L_PROG + w * 256 - 124));
                        RS(14);
                    };
                    if (SH) {
                        __builtin_amdgcn_s_setprio(HPRIO);
                        phi_w(ak, 1);
                    }
                    asm volatile("s_nop 7");
                    SB0();
#pragma unroll
                    for (int i = 0; i < 4; ++i) lds_write<bf16x8>(AX + i * 1024, pack44(acc[1][i][0], acc[1][i][1]));
                    SB0();
                    RS(5);
                    if (CFP == 1 && SH) own();
                    if (SH) {
                        f32x4 pk[2][2];
                        unsigned pw[4];
#pragma unroll
                        for (int i = 4 - 2 * SH; i < 4; ++i) pk[i >> 1][i & 1] = rbf4(mfma16(ak[i & 1], kown[i >> 1], zero));
                        phi_adr(pw, lds0 + P_U[2] * 8192 + (SH == 1 ? 2048 : 0));
                        if (SH == 1) phi_k1(pk[1], pw);
                        else phi_k<true>(pk, pw, 0, []() {});
                        SB0();
                    }
                    flag(WR + L_FV);
                    RS(13);
                    if (CFP == 0 || !SH) own();
                    kv_part();
                    __builtin_amdgcn_s_setprio(CPRIO);
                    fv = 0;
                    bad = ~0ull;
                    sfor<4 * VD, 4 * VD + PD>([&](auto F) { rd1(F, IC<0>{}); rd1(F, IC<1>{}); });
                    RS(10);
                }
                if constexpr (e == 3) {
                    SB0();
                    if constexpr (n % CSN == CSN - 1) { RSC(32 + n / CSN); RK(48 + n / CSN); }
                    if constexpr (n == 31) RS(3);
                }
            });
        });
        asm volatile("s_nop 7");
        SB0();
        RS(6);
        if (CPRIO) __builtin_amdgcn_s_setprio(0);
        flag(WR + L_FQD);
        bf16x8 a0[2], a1[2];
        f32x4 pq[2][2][2];
        phi_w(a1, hs);
        phi_w(a0, hs ^ 1);
#pragma unroll
        for (int rb = 0; rb < 2; ++rb) {
            const bf16x8 b0 = pack44(acc[2][0][rb], acc[2][1][rb]), b1 = pack44(acc[2][2][rb], acc[2][3][rb]);
#pragma unroll
            for (int fb = 0; fb < 2; ++fb) {
                pq[1][rb][fb] = rbf4(mfma16(a1[fb], hs ? b1 : b0, zero));
                pq[0][rb][fb] = rbf4(mfma16(a0[fb], hs ? b0 : b1, zero));
            }
        }
        softmax2<true>(pq[1], ph, []() {});
        RS(7);
        lds_wait(W4 + L_FKS, 1);
#pragma unroll
        for (int i = 0; i < 4; ++i) lds_write<u32x4>(AX + i * 1024, ph[i >> 1][i & 1]);
        SB0();
        flag(WR + L_FQ);
        sq = lds_read<u32x4>(lds0 + L_SEL);
        RS(8);
        softmax2<true>(pq[0], ph, []() {});
        asm volatile("" : "+v"(ph[0][0]), "+v"(ph[0][1]), "+v"(ph[1][0]), "+v"(ph[1][1]));
        RS(9);
        RS(11);
        xg_def();
        kf_def();
    }
    relane();
    const char* cla = ws + WS_CL + (CLO ? 4096 : 0) + ((rt * 2 + kp) * 16 + l15) * 8;
    const u64 T = (u64)sq[1] << 32 | sq[0];
    u64 cl;
    xg_load(w & 5);
    {
        if ((w & 5) != 4) {
            lds_wait0(lds0 + L_FKF + oh * 4);
#pragma unroll
            for (int i = 0; i < 4; ++i) kf[i] = lds_read<u32x4>(lds0 + L_KF + oh * 4096 + i * 1024 + l16);
#pragma unroll
            for (int i = 0; i < 2; ++i) kf[4 + i] = lds_read<u32x4>(lds0 + L_KZ + oh * 128 + i * 64 + g * 16);
        }
        asm volatile("global_load_dwordx2 %0, %1, off sc1" : "=&v"(cl) : "v"(cla) : "memory");
        RS(27);
        o_store(kf, ph, rsrc, WS_O + ((mt * 2 + hs) * 32 + 2 * hp + oh) * 4096 + (rg & 1) * 2048 + l16);
        RS(28);
    }
    if (WON < 16 && WLK < 0) flag(lds0 + L_FOS + w * 4);
    waitv<3>();
    flag(lds0 + L_FWO + w * 4);
    __builtin_amdgcn_s_setprio(0);
    if (WON < 16 && WLK < 0) {
        if ((w & 5) == 5) {
            lds_wait(lds0 + L_FOS + sch * 4, 1);
            wo_load(WON, 16, true);
        }
    }
    const u32x2 selv = {sq[2], sq[3]};

    RS(30);
    const char* ob = ws + WS_O + (rt * 2 + (rg >> 1)) * 131072 + (rg & 1) * 2048;
    const unsigned ov0 = l16 + kp * 1024, ov1 = l16 + (kp ^ 1) * 1024;
    auto oa = [&](int i) { return ob + ((2 * i + kp + rot) & 31) * 4096; };
    u32x4 of[3][4];
    auto fetch1 = [&](int k, int j) AI {
        if (k < OPL) ld_async(of[k % 3][j], (j & 1) ? ov1 : ov0, oa(2 * k + (j >> 1)));
        else asm volatile("global_load_dwordx4 %0, %1, %2" OPM : "=&v"(of[k % 3][j]) : "v"((j & 1) ? ov1 : ov0), "s"(oa(2 * k + (j >> 1))) : "memory");
    };
    auto fix = [&](int k) AI { if (NOF & 128) { for (int j = 0; j < 4; ++j) pin(of[k % 3][j]); return; } settle(of[k % 3], [&](int j) { return (j & 1) ? ov1 : ov0; }, [&](int j) { return oa(2 * k + (j >> 1)); }); };
    const bool own = (__builtin_amdgcn_readfirstlane(selv[0]) >> 4) == (unsigned)(rt * 8 + rg * 2 + kp);
    float gv_[2];
    f32x2 gb[2][4];
    auto g_mix = [&]() AI {
#pragma unroll
        for (int j = 0; j < 2; ++j) {
            gv_[j] = bf2f((u16)gg[j]);
            const float og = rbf(1.f - gv_[j]);
#pragma unroll
            for (int i = 0; i < 4; ++i) {
                const unsigned t = pack2(og * lo16(xr[j][i]), og * hi16(xr[j][i]));
                gb[j][i] = (f32x2){lo16(t), hi16(t)};
                asm volatile("" : "+v"(gb[j][i]));
            }
        }
    };
    asm volatile("" : "+v"(xr[0]), "+v"(xr[1]), "+v"(gg[0]), "+v"(gg[1]));
#pragma unroll 1
    for (unsigned s = 0; s < SPIN_CAP && __ballot((gg[0] >> 16) != (T & 0xFFFFFFFFFFFFull) || (gg[1] >> 16) != (T & 0xFFFFFFFFFFFFull)); ++s) {
        g_load();
        waitv<0>();
        asm volatile("" : "+v"(gg[0]), "+v"(gg[1]));
    }
    g_mix();
    if (PDL && ((w & 5) != 5 || WLK >= 0)) __builtin_amdgcn_s_sleep(PDL);
    if ((w & 5) != 5) waitv<2>();
    else waitv<(WON < 16 && WLK < 0 ? 3 + 4 * (16 - WON) : 2)>();
    asm volatile("" : "+v"(cl));
#pragma unroll 1
    for (unsigned s = 0; s < SPIN_CAP && __ballot(cl != T); ++s)
        asm volatile("global_load_dwordx2 %0, %1, off sc1\n\ts_waitcnt vmcnt(0)" : "=&v"(cl) : "v"(cla) : "memory");
    RS(16);
#pragma unroll
    for (int j = 0; j < 4; ++j) fetch1(0, j);
    RS(17);
    f32x4 acc[2][4];
    unsigned wa[2][4];
#pragma unroll
    for (int i = 0; i < 8; ++i) {
        wa[i >> 2][i & 3] = ((lds0 + 4096 * kp + (4 * g + q) * 128 + ((((g & 1) << 1) | (q >> 1)) * 32) + 8 * p) ^ ((i & 3) << 5)) + (i >> 2) * 65536;
        asm volatile("" : "+v"(wa[i >> 2][i & 3]));
    }
    s16x4 wf[2][8][2];
    auto wrd = [&](int k, int i) AI {
        const int u = wo_unit(2 * k + (i >> 3));
        wf[k & 1][i >> 1][i & 1] = tr_read(wa[u >> 3][(i >> 1) & 3] + (u & 7) * 8192 + (i & 1) * 2048);
    };
    u32x4 yp[14];
    const unsigned yo = srow * 4096 + cb * 256 + sch * 32;
    auto ya = [&](int i) { return ws + WS_Y + (i >> 1) * 32768 + (i & 1) * 16; };
    lds_wait(lds0 + L_FWD + ((l & 8) ? 16 + sch * 4 : (l & 1) * 4), 1);
    RS(18);
    RS(31);
    if (BPRIO && !kp) __builtin_amdgcn_s_setprio(BPRIO);
#pragma unroll
    for (int i = 0; i < 16; ++i) wrd(0, i);
#pragma unroll
    for (int j = 0; j < 4; ++j) fetch1(1, j);
    waitv<4>();
    fix(0);
    sfor<0, 8>([&](auto K) {
        constexpr int k = K;
        unsigned m[4];
        if constexpr (WON < 16 && 2 * (k + 1) == WON) lds_wait(lds0 + L_FW2 + (l & 1) * 4, 1);
        if constexpr (k == YK) {
            if (own) {
#pragma unroll
                for (int i = 0; i < 14; ++i) ld_async(yp[i], yo, ya(i));
            }
        }
        SB0();
        sfor<0, 16>([&](auto I) {
            constexpr int i = I, j = i >> 2, nb = i & 3;
            const bf16x8 a = cat8(wf[k & 1][(j >> 1) * 4 + nb][0], wf[k & 1][(j >> 1) * 4 + nb][1]);
            if (k == 0 && j < 2) asm volatile("v_mfma_f32_16x16x32_bf16 %0, %1, %2, 0" : "=&v"(acc[j & 1][nb]) : "v"(a), "v"(of[k % 3][j]));
            else asm volatile("v_mfma_f32_16x16x32_bf16 %0, %1, %2, %0" : "+v"(acc[j & 1][nb]) : "v"(a), "v"(of[k % 3][j]));
            SB0();
            if constexpr (k < 7) wrd(k + 1, i);
            if constexpr (k < 6 && i < 4) fetch1(k + 2, i);
            if constexpr (k < 7 && i == BCK) {
                waitv<(k < 6 ? 4 : 0)>();
#pragma unroll
                for (int n = 0; n < 4; ++n) pin(of[(k + 1) % 3][n]);
            }
            if constexpr (k < 7 && i > BCK && i <= BCK + 4) {
                m[i - BCK - 1] = min4(of[(k + 1) % 3][i - BCK - 1]);
                asm volatile("" : "+v"(m[i - BCK - 1]));
            }
            SB0();
        });
        if constexpr (k < 7) {
            if (__builtin_expect(__builtin_amdgcn_ballot_w64(min(min(m[0], m[1]), min(m[2], m[3])) == 0u) != 0ull, 0)) fix(k + 1);
        }
        SB0();
        if (k == 0) RS(19);
        if (k == 3) RS(23);
    });
    asm volatile("s_nop 7");
    SB0();
    RS(20);
    const unsigned ea = lds0 + L_A + l16 + 4096 * w, eb = lds0 + L_A + 4096 * (w ^ 4);
    lds_write<u32x4>(ea, nz4(acc[1][0]));
#pragma unroll
    for (int nb = 1; nb < 4; ++nb) lds_write<f32x4>(ea + nb * 1024, acc[1][nb]);
    if (own) {
        waitv<0>();
        settle(yp, [&](int) { return yo; }, ya);
        f32x4 yv[2];
#pragma unroll
        for (int h = 0; h < 2; ++h) {
            yv[h] = __builtin_bit_cast(f32x4, yp[h]);
#pragma unroll
            for (int i = 1; i < 7; ++i) yv[h] += __builtin_bit_cast(f32x4, yp[2 * i + h]);
#pragma unroll
            for (int i = 0; i < 4; ++i) {
                float t = dpp_add<0x128>(yv[h][i]);
                t = swap16_add(t, t);
                yv[h][i] = rbf(rbf(swap32_add(t, t)) * __uint_as_float(selv[1]));
            }
        }
#pragma unroll
        for (int j = 0; j < 2; ++j) {
            const bool hit = row0 + 8 * j + srow == (int)selv[0];
#pragma unroll
            for (int i = 0; i < 4; ++i) {
                const unsigned t = pack2(lo16(xr[j][i]) + yv[i >> 1][2 * (i & 1)], hi16(xr[j][i]) + yv[i >> 1][2 * (i & 1) + 1]);
                xr[j][i] = hit ? t : xr[j][i];
            }
        }
        g_mix();
    }
    BAR();
    RS(21);
#pragma unroll
    for (int nb = 0; nb < 4; ++nb) acc[0][nb] += lds_read<f32x4>(eb + l16 + nb * 1024);
#pragma unroll
    for (int nb = 0; nb < 4; ++nb) asm volatile("" : "+v"(acc[0][nb]));
#pragma unroll
    for (int nb = 0; nb < 4; ++nb) {
        const u32x2 a = {pack2(acc[0][nb][0], acc[0][nb][1]), pack2(acc[0][nb][2], acc[0][nb][3])};
        lds_write<u32x2>(eb + l15 * 128 + (((2 * nb + (g >> 1)) ^ (l & 7)) * 16) + (g & 1) * 8, a);
    }
#pragma unroll
    for (int j = 0; j < 2; ++j) {
        const u32x4 av = lds_read<u32x4>(eb + j * 1024 + srow * 128 + ((sch ^ srow) * 16));
        u32x4 o;
#pragma unroll
        for (int i = 0; i < 4; ++i) {
            const unsigned ga = pack2(gv_[j] * lo16(av[i]), gv_[j] * hi16(av[i]));
            o[i] = pack2(lo16(ga) + gb[j][i][0], hi16(ga) + gb[j][i][1]);
        }
        asm volatile("global_store_dwordx4 %0, %1, %2" OST "\n\ts_nop 1" ::"v"(xo), "v"(o), "s"(out + (size_t)(row0 + 8 * j) * DM + cb * 64) : "memory");
    }
    RS(22);
    RSDUMP();
}

extern "C" void kernel_launch(void* const* d_in, const int* in_sizes, int n_in, void* d_out, int out_size,
                              void* d_ws, size_t ws_size, hipStream_t stream) {
    fused_mix<<<256, 512, 0, stream>>>((const u16*)d_in[0], (const u16*)d_in[1], (const u16*)d_in[2], (const u16*)d_in[3],
                                       (const u16*)d_in[5], (const u16*)d_in[6], (const u16*)d_in[7], (const u16*)d_in[8],
                                       (const u16*)d_in[9], (char*)d_ws, (u16*)d_out);
}
```

```cpp
#include <hip/hip_runtime.h>
#include <hip/hip_bf16.h>
#include <stdint.h>
#include <type_traits>
#define GDL 16
#define KFD 24

typedef __attribute__((ext_vector_type(8))) short bf16x8;
typedef __attribute__((ext_vector_type(4))) short s16x4;
typedef __attribute__((ext_vector_type(4))) float f32x4;
typedef __attribute__((ext_vector_type(2))) float f32x2;
typedef __attribute__((ext_vector_type(2))) unsigned u32x2;
typedef __attribute__((ext_vector_type(4))) unsigned u32x4;
typedef __attribute__((ext_vector_type(2))) __bf16 bf16x2_t;
typedef unsigned short u16;
typedef unsigned long long u64;

#define DI __device__ __forceinline__
#define AS3 __attribute__((address_space(3)))
#define AS1 __attribute__((address_space(1)))
#define RLX_AGENT __ATOMIC_RELAXED, __HIP_MEMORY_SCOPE_AGENT

constexpr int SEQ = 2048, DM = 1024, NH = 32, FF = 3584;

DI float bf2f(u16 v) { return __uint_as_float(((unsigned)v) << 16); }
DI u16 f2bf(float f) { __bf16 b = (__bf16)f; return __builtin_bit_cast(u16, b); }
DI float rbf(float f) { return bf2f(f2bf(f)); }
DI unsigned pack2(float lo, float hi) {
    const f32x2 v = {lo, hi};
    return __builtin_bit_cast(unsigned, __builtin_convertvector(v, bf16x2_t));
}
DI float lo16(unsigned u) { return __uint_as_float(u << 16); }
DI float hi16(unsigned u) { return __uint_as_float(u & 0xffff0000u); }

DI s16x4 tr_read(unsigned a) { return __builtin_amdgcn_ds_read_tr16_b64_v4i16((AS3 s16x4*)(size_t)a); }
DI bf16x8 lds_read128(unsigned a) { return *(AS3 const bf16x8*)(size_t)a; }
template <class T> DI void lds_write(unsigned a, T v) { *(AS3 T*)(size_t)a = v; }
template <class T> DI T lds_read(unsigned a) { return *(AS3 const T*)(size_t)a; }
DI bf16x8 cat8(s16x4 a, s16x4 b) { return __builtin_shufflevector(a, b, 0, 1, 2, 3, 4, 5, 6, 7); }
DI f32x4 mfma16(bf16x8 a, bf16x8 b, f32x4 c) { return __builtin_amdgcn_mfma_f32_16x16x32_bf16(a, b, c, 0, 0, 0); }
DI bf16x8 pack44(const f32x4& a, const f32x4& b) {
    u32x4 r;
    r[0] = pack2(a[0], a[1]);
    r[1] = pack2(a[2], a[3]);
    r[2] = pack2(b[0], b[1]);
    r[3] = pack2(b[2], b[3]);
    return __builtin_bit_cast(bf16x8, r);
}
DI unsigned lds_of(const void* p) { return (unsigned)(size_t)(AS3 const char*)p; }

template <int CTRL>
DI unsigned dpp(unsigned x) { return __builtin_amdgcn_update_dpp(0u, x, CTRL, 0xf, 0xf, true); }
template <int CTRL>
DI float dpp_add(float x) { return x + __uint_as_float(dpp<CTRL>(__float_as_uint(x))); }
DI float swap32_add(float a, float b) {
    auto r = __builtin_amdgcn_permlane32_swap(__float_as_uint(a), __float_as_uint(b), false, false);
    return __uint_as_float(r[0]) + __uint_as_float(r[1]);
}
DI float swap16_add(float a, float b) {
    auto r = __builtin_amdgcn_permlane16_swap(__float_as_uint(a), __float_as_uint(b), false, false);
    return __uint_as_float(r[0]) + __uint_as_float(r[1]);
}
DI float wave_sum(float x) {
    x = dpp_add<0xB1>(x);
    x = dpp_add<0x4E>(x);
    x = dpp_add<0x141>(x);
    x = dpp_add<0x140>(x);
    x = swap16_add(x, x);
    return swap32_add(x, x);
}
DI unsigned wave_umax(unsigned x) {
    x = max(x, dpp<0xB1>(x));
    x = max(x, dpp<0x4E>(x));
    x = max(x, dpp<0x141>(x));
    x = max(x, dpp<0x140>(x));
    auto r = __builtin_amdgcn_permlane16_swap(x, x, false, false);
    x = max(r[0], r[1]);
    auto s = __builtin_amdgcn_permlane32_swap(x, x, false, false);
    return max(s[0], s[1]);
}
template <class OP, class POST>
DI void red2(float& a, float& b, OP op, POST post) {
    auto x = __builtin_amdgcn_permlane32_swap(__float_as_uint(a), __float_as_uint(b), false, false);
    const float m = op(__uint_as_float(x[0]), __uint_as_float(x[1]));
    auto y = __builtin_amdgcn_permlane16_swap(__float_as_uint(m), __float_as_uint(m), false, false);
    const float f = post(op(__uint_as_float(y[0]), __uint_as_float(y[1])));
    auto z = __builtin_amdgcn_permlane32_swap(__float_as_uint(f), __float_as_uint(f), false, false);
    a = __uint_as_float(z[0]);
    b = __uint_as_float(z[1]);
}

#define RS(i) do {} while (0)
#define RSD(i) do {} while (0)
#define RSDUMP() do {} while (0)

constexpr unsigned WS_E = 0;
constexpr unsigned WS_RG = 256;
constexpr unsigned WS_GG = WS_RG + 2048;
constexpr unsigned WS_CL = 32768;
constexpr unsigned WS_Z = 65536;
constexpr unsigned WS_KF = WS_Z + 131072;
constexpr unsigned WS_GU = WS_KF + 262144;
constexpr unsigned WS_Y = WS_GU + 262144;
constexpr unsigned WS_KV = WS_Y + 262144;
constexpr unsigned WS_O = WS_KV + (2u << 20);
constexpr unsigned WS_END = WS_O + (4u << 20);

constexpr int WE = 0;
constexpr int NQ = 10, ND = 6, NX = 16, KD = 78, VD = 84, NF = 64, NF1 = 59, NF2 = 59, NP1 = 12, NP = 14, NM1 = 21, NM2 = 21, NM = 22, KFN = 100;
constexpr int J_S[48] = {0, 0, 0, 1, 1, 1, 2, 2, 2, 3, 3, 3, 4, 4, 4, 5, 5, 5, 6, 6, 6, 7, 7, 7, 8, 8, 8, 9, 9, 9, 10, 10, 11, 11, 12, 12, 13, 14, 15, 13, 14, 15, 10, 11, 12, 13, 14, 15};
constexpr int J_M[48] = {0, 1, 2, 0, 1, 2, 0, 1, 2, 0, 1, 2, 0, 1, 2, 0, 1, 2, 0, 1, 2, 0, 1, 2, 0, 1, 2, 0, 1, 2, 0, 1, 0, 1, 0, 1, 0, 0, 0, 1, 1, 1, 2, 2, 2, 2, 2, 2};
constexpr int J_X[48] = {0, 0, 0, 1, 1, 1, 2, 2, 2, 3, 3, 3, 4, 4, 4, 5, 5, 5, 6, 6, 6, 7, 7, 7, 8, 8, 8, 9, 9, 9, 10, 10, 11, 11, 12, 12, 13, 14, 15, 13, 14, 15, 10, 11, 12, 13, 14, 15};
constexpr int J_U[48] = {2, 4, 8, 18, 11, 12, 17, 15, 13, 0, 1, 6, 4, 8, 9, 11, 12, 14, 15, 13, 7, 1, 6, 5, 8, 9, 10, 12, 14, 16, 13, 7, 1, 6, 4, 8, 18, 14, 15, 13, 7, 3, 0, 1, 6, 5, 2, 4};
constexpr int J_B[48] = {2, 2, 2, 11, 11, 11, 11, 11, 11, 0, 0, 0, 0, 8, 8, 8, 8, 8, 8, 8, 1, 1, 1, 1, 1, 9, 9, 9, 9, 9, 9, 1, 1, 1, 1, 1, 13, 13, 13, 13, 0, 0, 0, 0, 0, 0, 0, 0};
constexpr int Q_C[96] = {1, 0, 2, 0, 3, 0, 4, 0, 5, 0, 6, 0, 7, 0, 0, 0, 0, 0, 8, 0, 0, 0, 0, 0, 9, 0, 0, 0, 0, 0, 10, 0, 0, 0, 0, 0, 11, 0, 0, 0, 0, 0, 12, 0, 0, 0, 0, 0, 13, 0, 0, 0, 0, 0, 14, 0, 0, 0, 0, 0, 15, 0, 0, 0, 0, 0, 16, 0, 0, 0, 0, 0, 17, 0, 0, 0, 0, 0, 18, 0, 19, 0, 20, 0, 21, 0, 22, 0, 0, 0, 0, 0, 0, 0, 0, 0};
constexpr int X_U0[16] = {6, 9, 14, 7, 5, 10, 16, 3, 2, 18, 17, 3, 5, 9, 11, 16};
constexpr int X_U1[16] = {5, 10, 16, 3, 2, 18, 17, 0, 4, 11, 15, 0, 2, 10, 12, 17};
constexpr int P_U[4] = {11, 12, 9, 10};
constexpr int W_U[16] = {8, 14, 15, 13, 7, 3, 9, 10, 11, 12, 0, 1, 6, 5, 2, 4};
constexpr int W_N[16] = {100, 100, 100, 100, 100, 100, 101, 101, 101, 101, 13, 13, 14, 14, 99, 99};
constexpr int F_J[64] = {-1, 0, 1, 2, -2, 3, 4, 5, -3, 6, 7, 8, -4, 9, 10, 11, -5, 12, 13, 14, -6, 15, 16, 17, -7, 18, 19, 20, -8, 21, 22, 23, -9, 24, 25, 26, -10, 27, 28, 29, -11, 30, 31, -12, 32, 33, -13, 34, 35, -14, 36, -15, 37, -16, 38, 39, 40, 41, 42, 43, 44, 45, 46, 47};
constexpr int F_N[64] = {0, 0, 0, 0, 0, 0, 0, 0, 0, 0, 0, 0, 0, 0, 0, 1, 1, 1, 1, 1, 2, 2, 2, 2, 3, 3, 3, 3, 4, 4, 4, 4, 5, 5, 5, 5, 6, 6, 6, 6, 7, 7, 7, 7, 8, 8, 8, 8, 9, 9, 9, 10, 10, 10, 10, 11, 11, 11, 11, 12, 12, 12, 12, 12};
constexpr int F_K[64] = {0, 1, 2, 3, 0, 4, 5, 6, 0, 0, 0, 7, 0, 0, 0, 8, 0, 0, 0, 9, 0, 0, 0, 10, 0, 0, 0, 11, 0, 0, 0, 12, 0, 0, 0, 13, 0, 0, 0, 14, 0, 0, 0, 0, 15, 0, 0, 0, 16, 0, 0, 0, 0, 0, 17, 18, 19, 20, 21, 0, 0, 0, 0, 22};
constexpr int E_A[384] = {300, 325, 350, 375, 0, 0, 0, 502, 0, 0, 0, 403, 0, 0, 0, 503, 201, 251, 0, 404, 0, 100, 101, 504, 301, 326, 351, 376, 0, 0, 0, 505, 601, 0, 0, 406, 0, 0, 0, 506, 202, 252, 0, 407, 0, 0, 0, 507, 302, 327, 352, 377, 0, 0, 0, 602, 0, 0, 0, 0, 0, 0, 0, 203, 253, 0, 0, 408, 0, 100, 101, 508, 303, 328, 353, 378, 0, 0, 0, 603, 0, 0, 0, 0, 0, 0, 0, 204, 254, 0, 0, 409, 0, 0, 0, 509, 304, 329, 354, 379, 0, 100, 101, 604, 102, 103, 0, 0, 0, 0, 0, 205, 255, 0, 0, 410, 0, 0, 0, 510, 305, 330, 355, 380, 0, 0, 0, 605, 0, 0, 0, 0, 0, 0, 0, 206, 256, 0, 0, 411, 0, 0, 0, 511, 306, 331, 356, 381, 0, 0, 0, 606, 0, 0, 0, 0, 0, 100, 101, 102, 103, 207, 257, 412, 0, 0, 0, 512, 307, 332, 357, 382, 0, 0, 0, 607, 0, 0, 0, 0, 0, 0, 0, 208, 258, 0, 0, 413, 0, 0, 0, 513, 308, 333, 358, 383, 0, 100, 101, 608, 102, 103, 0, 0, 0, 0, 0, 209, 259, 0, 0, 414, 0, 0, 0, 514, 309, 334, 359, 384, 0, 0, 0, 609, 0, 0, 0, 0, 0, 0, 0, 210, 260, 0, 0, 415, 0, 0, 0, 515, 310, 335, 360, 385, 0, 100, 101, 610, 102, 103, 211, 261, 0, 0, 0, 0, 311, 336, 361, 386, 0, 0, 0, 516, 611, 212, 262, 0, 0, 0, 0, 0, 312, 337, 362, 387, 0, 0, 0, 213, 263, 0, 0, 417, 0, 100, 101, 517, 313, 338, 363, 388, 214, 264, 0, 612, 314, 339, 364, 389, 215, 265, 0, 0, 315, 340, 365, 390, 0, 0, 0, 518, 0, 0, 0, 419, 0, 100, 101, 519, 102, 103, 0, 420, 0, 0, 0, 520, 0, 0, 0, 0, 0, 0, 0, 0, 0, 0, 0, 422, 0, 0, 0, 522, 0, 0, 0, 0, 0, 0, 0, 0, 0, 0, 0, 613, 0, 0, 0, 0, 0, 0, 0, 0, 0, 0, 0, 0, 0, 0, 0, 614, 0, 0, 0, 0, 0, 0, 0, 0, 0, 0, 0, 0};
constexpr int E_B[384] = {0, 0, 402, 0, 0, 0, 0, 0, 0, 0, 0, 0, 0, 0, 0, 0, 0, 0, 0, 0, 0, 0, 0, 102, 103, 0, 405, 0, 0, 0, 0, 0, 0, 0, 0, 0, 0, 0, 0, 0, 0, 0, 0, 0, 0, 0, 0, 0, 0, 0, 0, 0, 0, 0, 0, 0, 0, 0, 0, 0, 0, 0, 0, 0, 0, 0, 0, 0, 0, 0, 0, 102, 103, 0, 0, 0, 0, 0, 0, 0, 0, 0, 0, 0, 0, 0, 0, 0, 0, 0, 0, 0, 0, 0, 0, 0, 0, 0, 0, 0, 0, 0, 0, 0, 0, 0, 0, 0, 0, 0, 0, 0, 0, 0, 0, 0, 0, 0, 0, 0, 0, 0, 0, 0, 0, 0, 0, 0, 0, 0, 0, 0, 0, 0, 0, 0, 0, 0, 0, 0, 0, 0, 0, 0, 0, 0, 0, 0, 0, 0, 0, 0, 0, 0, 0, 0, 0, 0, 0, 0, 0, 0, 0, 0, 0, 0, 0, 0, 0, 0, 0, 0, 0, 0, 0, 0, 0, 0, 0, 0, 0, 0, 0, 0, 0, 0, 0, 0, 0, 0, 0, 0, 0, 0, 0, 0, 0, 0, 0, 0, 0, 0, 0, 0, 0, 0, 0, 0, 0, 0, 0, 0, 0, 0, 0, 0, 0, 0, 0, 0, 0, 0, 0, 0, 0, 0, 0, 0, 0, 0, 0, 0, 0, 0, 0, 0, 0, 0, 0, 0, 0, 0, 0, 0, 0, 0, 0, 0, 0, 0, 0, 0, 0, 0, 0, 0, 0, 0, 416, 0, 0, 0, 0, 0, 0, 0, 0, 0, 0, 0, 0, 0, 0, 0, 0, 0, 0, 0, 0, 0, 0, 0, 0, 0, 0, 0, 0, 102, 103, 0, 0, 0, 0, 0, 0, 0, 0, 0, 0, 0, 0, 0, 0, 0, 0, 0, 418, 0, 0, 0, 0, 0, 0, 0, 0, 0, 0, 0, 0, 0, 0, 0, 0, 0, 0, 0, 0, 0, 0, 0, 0, 0, 0, 0, 0, 0, 0, 0, 0, 0, 0, 0, 0, 0, 0, 0, 0, 0, 0, 0, 0, 0, 0, 0, 0, 0, 0, 0, 0, 0, 0, 0, 0, 0, 0, 0, 0, 0, 0, 0, 0, 0, 0, 0, 0, 0, 0, 0, 0, 0, 0, 0, 0, 0};
#define LH 0
constexpr unsigned L_A = 16 * 8192;
constexpr unsigned L_KF = 18 * 8192;
constexpr unsigned L_SM = 19 * 8192;
constexpr unsigned L_FULL = L_SM + 256;
constexpr unsigned L_FVR = L_SM + 320, L_PRL = L_SM + 336;
constexpr unsigned L_PROG = L_SM + 896;
constexpr unsigned L_FW2 = L_SM + 512, L_FOS = L_SM + 480, L_FGI = L_SM + 520, L_FK0 = L_SM + 752, L_FBI = L_SM + 744;
constexpr unsigned L_FK = L_SM + 528, L_FKT = L_SM + 544, L_FV = L_SM + 560, L_FP = L_SM + 576, L_FKS = L_SM + 592;
constexpr unsigned L_FQD = L_SM + 608, L_FG = L_SM + 624, L_FKF = L_SM + 736, L_FQ = L_SM + 720, L_FWD = L_SM + 640, L_FWO = L_SM + 656, L_FLD = L_SM + 688, L_BEST = L_SM + 696;
constexpr unsigned L_SEL = L_SM + 704;
constexpr unsigned L_XS = L_SM + 2048, L_KZ = L_SM + 3072, L_WPHI = L_SM + 4096;
static_assert(NP <= 31 && NM <= 63, "");
static_assert(P_U[1] == P_U[0] + 1 && P_U[3] == P_U[2] + 1, "feature maps of a head lie side by side");
constexpr int wo_unit(int i) { return W_U[i]; }
constexpr int f_u(int f) { return f & 3; }
constexpr bool j_first(int j) {
    for (int i = 0; i < j; ++i)
        if (J_M[i] == J_M[j]) return false;
    return true;
}
constexpr int younger(int p) {
    int c = 0;
    for (int n = 0; n < NF1 - LH; ++n)
        if (F_N[n] >= p) c += F_J[n] < 0 ? 8 : 4;
    return c;
}
constexpr int nfv() {
    for (int n = 0; n < NF1; ++n)
        if (F_J[n] == VD / 2 - 1) return n + 1;
    return NF1;
}
constexpr int next_need(int n) {
    for (int i = n; i < NF; ++i)
        if (F_N[i] != F_N[n]) return F_N[i];
    return 99;
}
constexpr int x_stage(int i) { return i < 16 ? i : i - 16 + NQ; }
constexpr unsigned SPIN_CAP = 1u << 16;
#define N8(a) a, a + 1, a + 2, a + 3, a + 4, a + 5, a + 6, a + 7
__device__ const unsigned k_num[64] = {N8(0), N8(8), N8(16), N8(24), N8(32), N8(40), N8(48), N8(56)};

#define RK(i) do {} while (0)
#define RSL(i) do {} while (0)
#define RSC(i) do {} while (0)
#define CSN 6
#define CFP 0
#define QWM 0
#define RLD 11
#define CLF 3
#define CLT 1
#define FLV 1
#define SP1 0
#define SP2 0
#define SP3 0
#define SP4 0
#define SPW 0
#define SPI "s_load_dwordx2"
#define DNP 1
#define WOT 1
#define YT 1
#define SSM 0
#define CLE 1
#define GUO 0
#define TL0 0
#define TL0W ""
#define TLD 0
#define SPA 0
#define SPS 7
#define MRG 0
#define PP1 0
#define PD1 0
#define PP2 0
#define PD2 0
#define PP3 0
#define PD3 0
#define PDI 0
#define HLD " sc1"
#define HST " sc1"
#define HSA 16
#define LPRIO 0
#define LPRW 3
#define LTH 63
#define LT0 14
#define PEEK 1
#define CSP 1
#define GUA 12
#define OPL 8
#define OPM ""
#define PD 4
#define NPH 4
#define KSK 3
#define ROTK 8
#define OWIN 2
#define CLR0 4
#define CLS 1
#define CLP 2
#define SLK 2
#define CPH 3
#define NPL 0
#define CPRIO 1
#define HPRIO 2
#define KPK 3
#define KRND 1
#define QW 1
#define WOG 1
#define WON 12
#define SEL0 9
#define SPRIO 2
#define FBA 3
#define FBB 10
#define FBS 1
#define FBL 1
#define WOP 0
#define GTL 3
#define WPF 0
#define WOE 6
#define GW 0
#define GUF 0
#define FBH 0
#define WG2 0
#define NTX 0
#define NTW 0
#define NTS 16
#define NTO ""
#define GQ1 4
#define GQ2 8
#define CLO 0
#define PDL 6
#define XK 3
#define YK 7
#define BPRIO 0
#define AMF 1
#define LF 1
#define SH 2
#define LC 0
#define SPR2 0
#define GQ0 0
#define CPF 53248
#define NF0 4
#define OTL 0
#define OST " sc1"
#define TLW 3
#define THR -1
#define THQ -1
#define FDH 0
#define WLK -1
#define XGE 1
#define KXO 3
#define KXM 3
#define KX8 0
#define KXB 0xFEDBA9C768534210ull
#define BCK 10
#define GPR 0
#define GHL 0
#define TGT 1
#define PORD 1
#define TRM 3
#define LFE 1
#define PWN 0
#define PWL 99
#define TRN 0
#define RT0 0
#define BSP 0
#define WOV 0
#define GUP 1
#define NOF 0
#define FBD 8
#define BAR() asm volatile("s_waitcnt lgkmcnt(0)\n\ts_barrier" ::: "memory")
template <int N>
DI void waitv() { asm volatile("s_waitcnt vmcnt(%0)" ::"n"(N) : "memory"); }

DI u64 ld_agent(const char* p) { return __hip_atomic_load((const AS1 u64*)p, RLX_AGENT); }
DI void st_agent(char* p, u64 v) { __hip_atomic_store((AS1 u64*)p, v, RLX_AGENT); }
DI unsigned nz1(unsigned v) { return max(v, 1u); }
DI unsigned umin(unsigned a, unsigned b) { return min(a, b); }
DI u32x4 nz4(u32x4 v) { return (u32x4){max(v[0], 1u), max(v[1], 1u), max(v[2], 1u), max(v[3], 1u)}; }
DI u32x4 nz4(f32x4 v) { return nz4(__builtin_bit_cast(u32x4, v)); }
DI unsigned min4(u32x4 v) { return min(min(v[0], v[1]), min(v[2], v[3])); }
DI u32x4 ld_wt(__amdgpu_buffer_rsrc_t r, unsigned off) { return __builtin_amdgcn_raw_buffer_load_b128(r, off, 0, 16); }
DI void st_wt(__amdgpu_buffer_rsrc_t r, unsigned off, u32x4 v) { __builtin_amdgcn_raw_buffer_store_b128(v, r, off, 0, HSA); }
DI void ld_async(u32x4& v, unsigned off, const char* base) {
    asm volatile("global_load_dwordx4 %0, %1, %2 sc1" : "=&v"(v) : "v"(off), "s"(base) : "memory");
}
#define ST_MASK(op, m, off, v, base) asm volatile("s_mov_b64 exec, %3\n\t" op " %0, %1, %2" HST "\n\ts_mov_b64 exec, -1\n\ts_nop 0" ::"v"(off), "v"(v), "s"(base), "s"((u64)(m)) : "memory")
DI void pin(u32x4& v) { asm volatile("" : "+v"(v)); }
template <int N, class OFF, class BASE, class OK>
DI void settle(u32x4 (&v)[N], OFF off, BASE base, OK ok) {
#pragma unroll
    for (int i = 0; i < N; ++i) pin(v[i]);
#pragma unroll 1
    for (unsigned s = 0; s < SPIN_CAP; ++s) {
        unsigned m = ok(v[0]);
#pragma unroll
        for (int i = 1; i < N; ++i) m = min(m, ok(v[i]));
        if (__ballot(m == 0u) == 0ull) break;
#pragma unroll
        for (int i = 0; i < N; ++i) {
            u64 keep;
            asm volatile("s_mov_b64 %1, exec\n\ts_mov_b64 exec, %4\n\tglobal_load_dwordx4 %0, %2, %3 sc1\n\ts_mov_b64 exec, %1"
                         : "+&v"(v[i]), "=&s"(keep) : "v"(off(i)), "s"(base(i)), "s"((u64)__ballot(ok(v[i]) == 0u)) : "memory");
        }
        waitv<0>();
#pragma unroll
        for (int i = 0; i < N; ++i) pin(v[i]);
    }
}
template <int N, class OFF, class BASE>
DI void settle(u32x4 (&v)[N], OFF off, BASE base) { settle(v, off, base, [](const u32x4& a) { return min4(a); }); }
template <int P, int D, int N, class OFF, class BASE>
DI void settle_p(u32x4 (&v)[N], OFF off, BASE base) {
#pragma unroll 1
    for (unsigned s = 0;; ++s) {
        u64 miss[N], any = 0;
#pragma unroll
        for (int i = 0; i < N; ++i) {
            pin(v[i]);
            miss[i] = __ballot(min4(v[i]) == 0u);
            any |= miss[i];
        }
        if (s >= SPIN_CAP) any = 0;
        asm volatile("" : "+s"(any));
        if (any == 0ull) break;
#pragma unroll
        for (int i = 0; i < N; ++i)
            asm volatile("s_mov_b64 exec, %3\n\tglobal_load_dwordx4 %0, %1, %2" HLD "\n\ts_mov_b64 exec, -1"
                         ::"v"(v[i]), "v"(off(i)), "s"(base(i)), "s"(miss[i] | 1ull) : "memory");
        if (D) __builtin_amdgcn_s_sleep(D);
        waitv<N * (P - 1)>();
    }
}
DI void lds_wait(unsigned a, unsigned n) {
    unsigned v, c;
    asm volatile("s_mov_b32 %1, 0x400000\n"
                 ".Lp%=:\n\tds_read_b32 %0, %2\n\ts_waitcnt lgkmcnt(0)\n\tv_cmp_gt_u32 vcc, %3, %0\n\ts_cbranch_vccz .Lf%=\n\t"
                 "s_sleep 1\n\ts_sub_u32 %1, %1, 1\n\ts_cbranch_scc0 .Lp%=\n.Lf%=:"
                 : "=&v"(v), "=&s"(c) : "v"(a), "s"(n) : "memory", "vcc", "scc");
}
DI void lds_wait0(unsigned a) {
    if (!TGT) { lds_wait(a, 1); return; }
    unsigned v, c;
    asm volatile("s_mov_b32 %1, 0x1000000\n"
                 ".Lp%=:\n\tds_read_b32 %0, %2\n\ts_sub_u32 %1, %1, 1\n\ts_cbranch_scc1 .Lf%=\n\ts_waitcnt lgkmcnt(0)\n\tv_cmp_eq_u32 vcc, 0, %0\n\ts_cbranch_vccnz .Lp%=\n"
                 ".Lf%=:\n\ts_waitcnt lgkmcnt(0)"
                 : "=&v"(v), "=&s"(c) : "v"(a) : "memory", "vcc", "scc");
}
DI void lds_wait8(unsigned a, unsigned n) {
    unsigned v, c;
    asm volatile("s_mov_b32 %1, 0x100000\n"
                 ".Lp%=:\n\tds_read_b32 %0, %2\n\ts_waitcnt lgkmcnt(0)\n\tv_cmp_gt_u32 vcc, %3, %0\n\ts_cbranch_vccz .Lf%=\n\t"
                 "s_sleep 4\n\ts_sub_u32 %1, %1, 1\n\ts_cbranch_scc0 .Lp%=\n.Lf%=:"
                 : "=&v"(v), "=&s"(c) : "v"(a), "s"(n) : "memory", "vcc", "scc");
}
DI void lds_peek(unsigned& v, unsigned a) { asm volatile("ds_read_b32 %0, %1" : "=v"(v) : "v"(a) : "memory"); }
DI void lds_wait(unsigned& v, unsigned a, unsigned n) {
    unsigned c;
    asm volatile("s_mov_b32 %1, 0x400000\n"
                 ".Lp%=:\n\ts_waitcnt lgkmcnt(0)\n\tv_cmp_gt_u32 vcc, %3, %0\n\ts_cbranch_vccz .Lf%=\n\t"
                 "ds_read_b32 %0, %2\n\ts_sub_u32 %1, %1, 1\n\ts_cbranch_scc0 .Lp%=\n.Lf%=:"
                 : "+v"(v), "=&s"(c) : "v"(a), "s"(n) : "memory", "vcc", "scc");
}
DI void lds_set(unsigned a) {
    asm volatile("s_waitcnt lgkmcnt(0)\n\tds_write_b32 %0, %1" ::"v"(a), "v"(1u) : "memory");
}
DI float dot2(unsigned a, unsigned b, float c) {
    return __builtin_amdgcn_fdot2_f32_bf16(__builtin_bit_cast(bf16x2_t, a), __builtin_bit_cast(bf16x2_t, b), c, false);
}
DI float sum4(const float (&a)[4]) {
    float c = swap16_add(swap32_add(a[0], a[1]), swap32_add(a[2], a[3]));
    c = dpp_add<0xB1>(c);
    c = dpp_add<0x4E>(c);
    c = dpp_add<0x141>(c);
    return dpp_add<0x140>(c);
}

template <class OP, class POST>
DI void red4(float& a, float& b, float& c, float& d, OP op, POST post) {
    auto x = __builtin_amdgcn_permlane32_swap(__float_as_uint(a), __float_as_uint(b), false, false);
    auto y = __builtin_amdgcn_permlane32_swap(__float_as_uint(c), __float_as_uint(d), false, false);
    const float m = op(__uint_as_float(x[0]), __uint_as_float(x[1])), n = op(__uint_as_float(y[0]), __uint_as_float(y[1]));
    auto z = __builtin_amdgcn_permlane16_swap(__float_as_uint(m), __float_as_uint(n), false, false);
    const unsigned f = __float_as_uint(post(op(__uint_as_float(z[0]), __uint_as_float(z[1]))));
    auto u = __builtin_amdgcn_permlane16_swap(f, f, false, false);
    auto v = __builtin_amdgcn_permlane32_swap(u[0], u[0], false, false);
    auto t = __builtin_amdgcn_permlane32_swap(u[1], u[1], false, false);
    a = __uint_as_float(v[0]);
    b = __uint_as_float(v[1]);
    c = __uint_as_float(t[0]);
    d = __uint_as_float(t[1]);
}
DI float mx2(float a, float b) { return __builtin_elementwise_maximum(a, b); }
DI float opq(float a) { asm("" : "+v"(a)); return a; }
template <bool PK, class HOOK>
DI void softmax2(const f32x4 (&v)[2][2], u32x4 (&ph)[2][2], HOOK hook) {
    constexpr float L2E = 1.4426950408889634f;
    float mx[2], mn[2], sp_[2], sn_[2];
#pragma unroll
    for (int b = 0; b < 2; ++b) {
        const f32x4 s = v[b][0], t = v[b][1];
        mx[b] = mx2(mx2(mx2(mx2(s[0], s[1]), s[2]), mx2(mx2(s[3], t[0]), t[1])), mx2(t[2], t[3]));
        mn[b] = mx2(mx2(mx2(mx2(-s[0], -s[1]), -s[2]), mx2(mx2(-s[3], -t[0]), -t[1])), mx2(-t[2], -t[3]));
    }
    red4(mx[0], mx[1], mn[0], mn[1], [](float a, float b) { return mx2(a, b); }, [](float a) { return a * -L2E; });
    f32x2 ep[2][4], en[2][4];
#pragma unroll
    for (int b = 0; b < 2; ++b) {
#pragma unroll
        for (int e = 0; e < 4; ++e) {
            const f32x2 u = {v[b][e >> 1][2 * (e & 1)], v[b][e >> 1][2 * (e & 1) + 1]};
            f32x2 a, c;
            if (PK) {
                a = __builtin_elementwise_fma(u, (f32x2){L2E, L2E}, (f32x2){mx[b], mx[b]});
                c = __builtin_elementwise_fma(u, (f32x2){-L2E, -L2E}, (f32x2){mn[b], mn[b]});
            } else {
                a = (f32x2){__builtin_fmaf(u[0], L2E, mx[b]), __builtin_fmaf(u[1], L2E, mx[b])};
                c = (f32x2){__builtin_fmaf(u[0], -L2E, mn[b]), __builtin_fmaf(u[1], -L2E, mn[b])};
            }
            ep[b][e] = (f32x2){__builtin_amdgcn_exp2f(a[0]), __builtin_amdgcn_exp2f(a[1])};
            en[b][e] = (f32x2){__builtin_amdgcn_exp2f(c[0]), __builtin_amdgcn_exp2f(c[1])};
        }
        if (PK) {
            const f32x2 s = (ep[b][0] + ep[b][1]) + (ep[b][2] + ep[b][3]), t = (en[b][0] + en[b][1]) + (en[b][2] + en[b][3]);
            sp_[b] = s[0] + s[1];
            sn_[b] = t[0] + t[1];
        } else {
            sp_[b] = ep[b][0][0];
            sn_[b] = en[b][0][0];
#pragma unroll
            for (int e = 1; e < 8; ++e) sp_[b] += ep[b][e >> 1][e & 1], sn_[b] += en[b][e >> 1][e & 1];
        }
    }
    asm volatile("" : "+v"(sp_[0]), "+v"(sn_[0]), "+v"(sp_[1]), "+v"(sn_[1]));
    hook();
    red4(sp_[0], sp_[1], sn_[0], sn_[1], [](float a, float b) { return a + b; }, [](float a) { return __builtin_amdgcn_rcpf(a); });
#pragma unroll
    for (int b = 0; b < 2; ++b)
#pragma unroll
        for (int e = 0; e < 4; ++e) {
            if (PK) {
                const f32x2 a = ep[b][e] * (f32x2){sp_[b], sp_[b]}, c = en[b][e] * (f32x2){sn_[b], sn_[b]};
                ph[b][0][e] = pack2(a[0], a[1]);
                ph[b][1][e] = pack2(c[0], c[1]);
            } else {
                ph[b][0][e] = pack2(opq(ep[b][e][0] * sp_[b]), ep[b][e][1] * sp_[b]);
                ph[b][1][e] = pack2(opq(en[b][e][0] * sn_[b]), en[b][e][1] * sn_[b]);
            }
        }
}
DI f32x4 rbf4(const f32x4& a) {
    const unsigned u = pack2(a[0], a[1]), v = pack2(a[2], a[3]);
    return (f32x4){lo16(u), hi16(u), lo16(v), hi16(v)};
}
DI int fsw(int row) { return (((row >> 1) & 3) << 2) | (((row >> 3) & 1) << 1) | (row & 1); }
template <bool PK, class HOOK>
DI void phi_k(const f32x4 (&pp)[2][2], const unsigned (&pw)[4], unsigned off, HOOK hook) {
    u32x4 ph[2][2];
    softmax2<PK>(pp, ph, hook);
#pragma unroll
    for (int b = 0; b < 2; ++b)
#pragma unroll
        for (int kf = 0; kf < 2; ++kf)
#pragma unroll
            for (int fb = 0; fb < 2; ++fb) {
                const u32x2 a = {ph[b][kf][2 * fb], ph[b][kf][2 * fb + 1]};
                lds_write<u32x2>(pw[2 * kf + fb] + off + b * 2048, a);
            }
}

DI void phi_k1(const f32x4 (&v)[2], const unsigned (&pw)[4]) {
    constexpr float L2E = 1.4426950408889634f;
    const f32x4 s = v[0], t = v[1];
    float mx = mx2(mx2(mx2(mx2(s[0], s[1]), s[2]), mx2(mx2(s[3], t[0]), t[1])), mx2(t[2], t[3]));
    float mn = mx2(mx2(mx2(mx2(-s[0], -s[1]), -s[2]), mx2(mx2(-s[3], -t[0]), -t[1])), mx2(-t[2], -t[3]));
    red2(mx, mn, [](float a, float b) { return mx2(a, b); }, [](float a) { return a * -L2E; });
    f32x2 ep[4], en[4];
#pragma unroll
    for (int e = 0; e < 4; ++e) {
        const f32x2 u = {v[e >> 1][2 * (e & 1)], v[e >> 1][2 * (e & 1) + 1]};
        const f32x2 a = __builtin_elementwise_fma(u, (f32x2){L2E, L2E}, (f32x2){mx, mx});
        const f32x2 c = __builtin_elementwise_fma(u, (f32x2){-L2E, -L2E}, (f32x2){mn, mn});
        ep[e] = (f32x2){__builtin_amdgcn_exp2f(a[0]), __builtin_amdgcn_exp2f(a[1])};
        en[e] = (f32x2){__builtin_amdgcn_exp2f(c[0]), __builtin_amdgcn_exp2f(c[1])};
    }
    const f32x2 a = (ep[0] + ep[1]) + (ep[2] + ep[3]), c = (en[0] + en[1]) + (en[2] + en[3]);
    float sp = a[0] + a[1], sn = c[0] + c[1];
    red2(sp, sn, [](float a, float b) { return a + b; }, [](float a) { return __builtin_amdgcn_rcpf(a); });
#pragma unroll
    for (int i = 0; i < 4; ++i) {
        const float r = i < 2 ? sp : sn;
        const f32x2 lo = (i < 2 ? ep[2 * i] : en[2 * i - 4]) * (f32x2){r, r}, hi = (i < 2 ? ep[2 * i + 1] : en[2 * i - 3]) * (f32x2){r, r};
        lds_write<u32x2>(pw[i], (u32x2){pack2(lo[0], lo[1]), pack2(hi[0], hi[1])});
    }
}

DI void waitvn(int n) {
#define WV(k) case k: waitv<k>(); break;
#define WV8(k) WV(k) WV(k + 1) WV(k + 2) WV(k + 3) WV(k + 4) WV(k + 5) WV(k + 6) WV(k + 7)
    switch (n < 63 ? n : 63) { WV8(0) WV8(8) WV8(16) WV8(24) WV8(32) WV8(40) WV8(48) WV8(56) }
}
#define SB0() __builtin_amdgcn_sched_barrier(0)
template <int A, int B, class F>
DI void sfor(F f) {
    if constexpr (A < B) {
        f(std::integral_constant<int, A>{});
        sfor<A + 1, B>(f);
    }
}
template <int N> using IC = std::integral_constant<int, N>;
#define AI __attribute__((always_inline))
#define RSK(b, k) do { switch (k) { case 0: RS(b); break; case 1: RS(b + 1); break; case 2: RS(b + 2); break; case 3: RS(b + 3); break; \
    case 4: RS(b + 4); break; case 5: RS(b + 5); break; case 6: RS(b + 6); break; case 7: RS(b + 7); break; } } while (0)

DI void o_store(const u32x4 (&kf)[6], const u32x4 (&ph)[2][2], __amdgpu_buffer_rsrc_t rsrc, unsigned o_st) {
    const f32x4 zero = {0.f, 0.f, 0.f, 0.f};
#pragma unroll
    for (int i = 0; i < 2; ++i) {
        const bf16x8 p0 = __builtin_bit_cast(bf16x8, ph[i][0]), p1 = __builtin_bit_cast(bf16x8, ph[i][1]);
        f32x4 dn = mfma16(__builtin_bit_cast(bf16x8, kf[4]), p0, zero);
        f32x4 n0 = mfma16(__builtin_bit_cast(bf16x8, kf[0]), p0, zero);
        f32x4 n1 = mfma16(__builtin_bit_cast(bf16x8, kf[1]), p0, zero);
        dn = mfma16(__builtin_bit_cast(bf16x8, kf[5]), p1, dn);
        n0 = mfma16(__builtin_bit_cast(bf16x8, kf[2]), p1, n0);
        n1 = mfma16(__builtin_bit_cast(bf16x8, kf[3]), p1, n1);
        const float rd_ = __builtin_amdgcn_rcpf(rbf(rbf(dn[0]) + 1e-6f));
        const u32x4 nb_ = __builtin_bit_cast(u32x4, pack44(n0, n1));
        u32x4 o;
#pragma unroll
        for (int j = 0; j < 4; ++j) o[j] = pack2(lo16(nb_[j]) * rd_, hi16(nb_[j]) * rd_);
        st_wt(rsrc, o_st + i * 1024, nz4(o));
    }
}

typedef __attribute__((ext_vector_type(16))) unsigned u32x16;
__global__ __launch_bounds__(512, 2) void fused_mix(const u16*, const u16*, const u16*, const u16*, const u16*, const u16*, const u16*, const u16*,
                                                    const u16*, char*, u16*) {
    __shared__ __attribute__((aligned(16))) char smem[163840];
    u32x4 ka;
    u32x16 kb_;
    u64 kz;
    asm volatile("s_load_dwordx4 %0, %3, 0x0\n\ts_load_dwordx16 %1, %3, 0x10\n\ts_load_dwordx2 %2, %3, 0x50"
                 : "=&s"(ka), "=&s"(kb_), "=&s"(kz) : "s"(__builtin_amdgcn_kernarg_segment_ptr()));
    SB0();
    const int tid = threadIdx.x;
    int l = tid & 63;
    const int w = __builtin_amdgcn_readfirstlane(tid >> 6) ^ 4;
    int bid = blockIdx.x;
    const unsigned lds0 = lds_of(smem);
    int g = l >> 4, l15 = l & 15, q = l15 >> 2, p = l & 3;
    int srow = l >> 3, sch = l & 7;
    unsigned l16 = l * 16;
    unsigned vst = 0;
    const int rg = w & 3, kp = w >> 2;
    const int hs = rg >> 1;
    RS(0);
    if (CPF) {
        u64 pc;
        asm volatile("s_getpc_b64 %0\n\ts_mov_b32 m0, %2\n\ts_mov_b64 exec, 3\n\tglobal_load_lds_dword %1, %0\n\ts_mov_b64 exec, -1"
                     : "=&s"(pc) : "v"(min((unsigned)(((bid & ~7) + w) * 2 + l) * 128u, (unsigned)CPF)), "s"(lds0 + L_SM + 3584) : "memory");
    }
    lds_write<unsigned>(lds0 + L_SM + tid * 4, 0u);
    const int slot = bid >> 3;
    const int mt = (bid & 1) * 8 + (slot & 7);
    const int hp = ((bid >> 1) & 3) * 4 + (slot >> 3);
    unsigned vx[4], vw[4];
    auto vw_init = [&]() AI {
        int t = l;
        asm volatile("" : "+v"(t));
#pragma unroll
        for (int i = 0; i < 4; ++i) vw[i] = (unsigned)((8 * i + (t >> 3)) * 6144 + ((t & 7) ^ (((t >> 4) & 1) << 1) ^ ((i & 1) << 2)) * 16 + 4096 - i * 1024);
    };
    auto vx_init = [&]() AI {
        int t = l;
        asm volatile("" : "+v"(t));
#pragma unroll
        for (int i = 0; i < 4; ++i) vx[i] = (unsigned)((8 * i + (t >> 3)) * 2048 + (((t & 7) ^ ((4 * (i & 1) + (t >> 4)) & 7)) * 16) + 4096 - i * 1024);
    };
    vx_init();
    vw_init();
    asm volatile("" : "+v"(vx[0]), "+v"(vx[1]), "+v"(vx[2]), "+v"(vx[3]), "+v"(vw[0]), "+v"(vw[1]), "+v"(vw[2]), "+v"(vw[3]));
    const int ksa = SLK * (slot & 7), ksb = KSK * ((slot + (slot >> 3)) & (NPH - 1));
    unsigned xo0 = (mt * 128 + hs * 64) * 2048, wo0 = hp * 128 + hs * (32 * 6144);
    asm volatile("s_waitcnt lgkmcnt(0)" : "+s"(ka), "+s"(kb_), "+s"(kz), "+s"(xo0), "+s"(wo0) :: "memory");
    RS(48);
    const u16* x = (const u16*)((u64)ka[1] << 32 | ka[0]);
    const u16* wqkv = (const u16*)((u64)ka[3] << 32 | ka[2]);
    auto arg = [&](int i) { return (char*)((u64)kb_[2 * i + 1] << 32 | kb_[2 * i]); };
    const u16 *wphi = (const u16*)arg(0), *wo = (const u16*)arg(1), *wr = (const u16*)arg(2), *wg = (const u16*)arg(3);
    const u16 *wu = (const u16*)arg(4), *wd = (const u16*)arg(5), *gw = (const u16*)arg(6);
    char* ws = arg(7);
    u16* out = (u16*)kz;
    const int sc = (3 - (int)((unsigned)(size_t)wqkv >> 7)) & 7;
    const unsigned* one = k_num;
    unsigned v0 = 0;
    asm volatile("" : "+s"(one), "+v"(v0));
    auto mark = [&](unsigned dst, int n = 1, int lanes = 1) AI {
        asm volatile("s_mov_b32 m0, %2\n\ts_mov_b64 exec, %4\n\tglobal_load_lds_dword %0, %1 offset:%3\n\ts_mov_b64 exec, -1"
                     ::"v"(v0), "s"(one), "s"(dst - 4 * n), "n"(4 * n), "n"(lanes) : "memory");
    };
    auto four = [&](const unsigned (&v)[4], const char* src, unsigned dst, int nt = 0) AI {
#define FOUR(P) asm volatile("s_mov_b32 m0, %5\n\ts_nop 0\n\t" \
                     "global_load_lds_dwordx4 %0, %4" P "\n\tglobal_load_lds_dwordx4 %1, %4 offset:1024" P "\n\t" \
                     "global_load_lds_dwordx4 %2, %4 offset:2048" P "\n\tglobal_load_lds_dwordx4 %3, %4 offset:3072" P \
                     ::"v"(v[0]), "v"(v[1]), "v"(v[2]), "v"(v[3]), "s"(src), "s"(dst) : "memory")
        if (nt == 1) FOUR(" nt");
        else if (nt == 2) FOUR(" sc1");
        else if (nt == 3) FOUR(" sc0 sc1");
        else FOUR("");
    };
    const int ks0 = ((slot + (slot >> 3)) & KXM) | (KXO == 2 || (KXO == 3 && (hp & 7) == sc) ? slot & 4 : 0);
    const char* xb0 = (const char*)x + xo0 - 4096;
    const char* wb0 = (const char*)wqkv + wo0 - 4096;
    auto fill = [&](auto N) AI {
        constexpr int n = N, j = F_J[n] < 0 ? 0 : F_J[n], xi = F_J[n] < 0 ? -1 - F_J[n] : 0, s = F_J[n] < 0 ? x_stage(xi) : J_S[j], m = J_M[j];
        const int ks = KXO ? ks0 ^ (int)((KXB >> (4 * s)) & 15) : (ks0 + s) & 15;
        if constexpr (n >= NF1 && n < NF2) {
            if constexpr (F_J[n] >= 0) {
                asm volatile("s_mov_b32 m0, %3\n\ts_nop 0\n\tglobal_load_lds_dwordx4 %0, %2\n\tglobal_load_lds_dwordx4 %1, %2 offset:1024"
                             ::"v"(vw[0]), "v"(vw[1]), "s"(wb0 + (ks * 64 + (w & 1) * 16) * 6144), "s"(lds0 + J_U[j] * 8192 + w * 2048) : "memory");
            } else four(vx, xb0 + ks * 128 + (w & 1) * (32 * 2048), lds0 + (hs ? X_U1[xi] : X_U0[xi]) * 8192 + (w & 1) * 4096);
            if constexpr (F_K[n] > 1) mark(lds0 + L_FULL + 16 + w * 4, F_K[n]);
            return;
        }
        if constexpr (F_J[n] >= 0) {
            four(vw, wb0 + ks * (64 * 6144) + (m == 2 ? 0 : 2048 * (m + 1)), lds0 + J_U[j] * 8192 + hs * 4096, s < NTS ? NTW : 0);
        } else {
            const char* xs_ = xb0 + ks * 128;
            const unsigned d = lds0 + (hs ? X_U1[xi] : X_U0[xi]) * 8192;
            four(vx, xs_, d, s < NTS ? NTX : 0);
            four(vx, xs_ + 32 * 2048, d + 4096, s < NTS ? NTX : 0);
        }
        if constexpr (F_K[n] > 1) mark(lds0 + L_FULL + (n < NF1 ? 0 : 32) + hs * 8, F_K[n], 3);
    };
    static_assert(F_K[1] == 1, "");
    if (__builtin_expect((w & 5) == 4, 1)) sfor<0, 2>(fill);
    RS(49);
    unsigned sd = 0, st;
    asm volatile("s_cmp_lg_u32 %2, 5\n\ts_cbranch_scc1 .La%=\n\t"
                 "s_min_u32 %1, %3, 23\n\ts_lshl_b32 %1, %1, 18\n\ts_load_dword %0, %5, %1\n\ts_load_dword %0, %6, 0x0\n\t"
                 "s_and_b32 %1, %1, 0x3c0000\n\ts_load_dword %0, %4, %1\n\ts_load_dword %0, %10, %1\n\t"
                 "s_load_dword %0, %7, 0x0\n\ts_load_dword %0, %8, 0x0\n\ts_load_dword %0, %9, 0x0\n.La%=:"
                 : "+s"(sd), "=&s"(st) : "s"(w), "s"(slot), "s"(x), "s"(wqkv), "s"(one), "s"(wr), "s"(gw), "s"(wphi), "s"(out) : "memory", "scc");
    asm volatile("s_cmp_lg_u32 %2, 7\n\ts_cbranch_scc1 .La%=\n\t"
                 "s_min_u32 %1, %3, 27\n\ts_lshl_b32 %1, %1, 18\n\ts_load_dword %0, %4, 0x0\n\ts_load_dword %0, %4, %1\n\t"
                 "s_load_dword %0, %5, %1\n\ts_load_dword %0, %6, %1\n\ts_load_dword %0, %7, %1\n\t"
                 "s_and_b32 %1, %1, 0x1c0000\n\ts_load_dword %0, %8, %1\n.La%=:\n\ts_barrier"
                 : "+s"(sd), "=&s"(st) : "s"(w), "s"(slot), "s"(ws), "s"(wg), "s"(wu), "s"(wd), "s"(wo) : "memory", "scc");
    RS(50);
    if (__builtin_expect((w & 5) == 4, 1)) {
        mark(lds0 + L_FULL + hs * 8, 1, 3);
        sfor<2, NF0>(fill);
    }
    RS(51);
    asm volatile("" : "+s"(bid));
    const int xcd = bid & 7;
    const int rt = (xcd >> 1) * 4 + ((bid >> 3) & 3), cb = (xcd & 1) * 8 + (bid >> 5), rot = 4 * cb + ROTK * rt;
    const __amdgpu_buffer_rsrc_t rsrc = __builtin_amdgcn_make_buffer_rsrc(ws, 0, WS_END, 0x00020000);
    const u32x4 z4 = {0u, 0u, 0u, 0u};
    const f32x4 zero = {0.f, 0.f, 0.f, 0.f};
    unsigned AX = lds0 + L_A + rg * 4096 + l16;
    unsigned W4 = lds0 + p * 4;
    unsigned PG = lds0 + L_PROG + p * 256;
    const unsigned WR = lds0 + rg * 4;
    auto flag = [&](unsigned a) AI { asm volatile("ds_write_b32 %0, %0" ::"v"(a) : "memory"); };
    auto phi_w = [&](bf16x8 (&a)[2], int h) AI {
        const unsigned ad = lds0 + L_WPHI + h * 2048 + (4 * g + q) * 64 + (((p >> 1) ^ (2 * (g & 1))) * 16) + 8 * (p & 1);
        a[0] = cat8(tr_read(ad), tr_read(ad + 1024));
        a[1] = cat8(tr_read(ad ^ 32), tr_read((ad ^ 32) + 1024));
    };
    u32x4 fw[32], fd[16], gu[2];
    unsigned sel_[4][2];
    u32x4 ph[2][2], sq;
    const int hp2 = ((bid >> 1) & 3) * 4 + (bid >> 6);
    const bool ffn = (hp2 & 7) != sc && !(NOF & 1);
    const int ph4 = ((bid >> 3) + (bid >> 6)) & 3;
    const int turn = (ph4 + TRN) & TRM;
    const int kq = mt >> 2;
    const int cs = (ffn ? hp2 - (hp2 > sc) - (hp2 > sc + 8) : 14 + (hp2 >> 3)) * 4 + ((ph4 + CPH - (int)((unsigned)(size_t)wg >> 7)) & 3);
    auto spread = [&](const u32x4& v, int i) -> bf16x8 {
        const unsigned s = v[i >> 1];
        u32x4 a;
#pragma unroll
        for (int d = 0; d < 4; ++d) a[d] = __builtin_amdgcn_perm(s, s, sel_[d][i & 1]);
        return __builtin_bit_cast(bf16x8, a);
    };
    auto fold_store = [&](const f32x4& a, unsigned off) {
        f32x4 v;
#pragma unroll
        for (int i = 0; i < 4; ++i) {
            const unsigned u = __float_as_uint(a[i]);
            const float r = __uint_as_float(__builtin_amdgcn_update_dpp(u, u, 0x128, 0xc, 0xf, false));
            v[i] = swap32_add(r, r);
        }
        const u32x4 o = nz4(v);
        ST_MASK("global_store_dwordx4", 0x00FF00FFull, off + l15 * 32 + g * 16, o, ws);
    };
    auto sel_init = [&]() AI {
#pragma unroll
        for (int d = 0; d < 4; ++d)
#pragma unroll
            for (int hf = 0; hf < 2; ++hf) {
                const unsigned b = hf ? 0x0302u : 0x0100u;
                sel_[d][hf] = (sch >> 1) != d ? 0x0c0c0c0cu : (l & 1) ? (b << 16) | 0x0c0cu : 0x0c0c0000u | b;
            }
    };
    auto gua = [&](int i) { return ws + WS_GU + cs * 4096 + i * 1024; };
    auto gu_load = [&]() AI {
#pragma unroll
        for (int i = 0; i < 2; ++i) ld_async(gu[i], l16, gua(i));
    };
    auto down = [&]() AI {
#pragma unroll
        for (int i = 0; i < 16; ++i) pin(fd[i]);
        settle(gu, [&](int) { return l16; }, gua);
        const f32x4 v = __builtin_bit_cast(f32x4, gu[0]) + __builtin_bit_cast(f32x4, gu[1]);
        auto r = __builtin_amdgcn_permlane16_swap(__float_as_uint(swap32_add(v[0], v[2])), __float_as_uint(swap32_add(v[1], v[3])), false, false);
        const float gt = rbf(__uint_as_float(r[0])), up = rbf(__uint_as_float(r[1]));
        const unsigned ha = lds0 + L_XS + hs * 512;
        lds_write<u16>(ha + l15 * 8 + g * 2, f2bf(rbf(gt / (1.f + __expf(-gt))) * up));
        const u32x4 hv = lds_read<u32x4>(ha + srow * 16);
        f32x4 y0 = zero, y1 = zero;
#pragma unroll
        for (int i = 0; i < 8; ++i) {
            const bf16x8 a = spread(hv, i);
            y0 = mfma16(a, __builtin_bit_cast(bf16x8, fd[i]), y0);
            y1 = mfma16(a, __builtin_bit_cast(bf16x8, fd[8 + i]), y1);
        }
        fold_store(y0, WS_Y + cs * 4096 + kq * 1024 + hs * 512);
        fold_store(y1, WS_Y + cs * 4096 + kq * 1024 + hs * 512 + 256);
    };
    auto need = [&](int c) AI {
        if (c == 101) lds_wait(W4 + L_FKS, 1);
        else if (c == 100) lds_wait(W4 + L_FV, 1);
        else if (c == 99) lds_wait(W4 + L_FQD, 1);
        else if (c > 0) lds_wait(c > RLD ? W4 + L_PRL : PG, c);
    };
    auto wo_load = [&](int n0, int n1, bool mk) AI {
        unsigned vo[4];
#pragma unroll
        for (int i = 0; i < 4; ++i) vo[i] = (unsigned)((8 * i + srow) * 2048 + ((sch ^ ((g & 3) << 1)) * 16) + 4096 - i * 1024);
        const char* wob = (const char*)wo + cb * 128 - 4096;
#pragma unroll
        for (int n = n0; n < ((NOF & 4) ? n0 : n1); ++n) {
            if (n < WON && (n == 0 || W_N[n] != W_N[n - 1])) need(W_N[n]);
            four(vo, wob + ((2 * n + hs + rot) & 31) * 65536, lds0 + wo_unit(n) * 8192 + hs * 4096);
        }
        if (mk) mark(lds0 + (n0 >= WON && WON ? L_FW2 : L_FWD) + hs * 4);
    };
    auto phi_adr = [&](unsigned (&pw)[4], unsigned base) AI {
#pragma unroll
        for (int i = 0; i < 4; ++i) pw[i] = (base + rg * 4096 + l15 * 128 + ((g ^ fsw(l15)) << 3)) ^ (i << 5);
    };
    const int row0 = rt * 128 + rg * 32 + kp * 16;
    u32x4 xr[2];
    u64 gg[2];
    unsigned xo = srow * 2048 + sch * 16;
    auto g_load = [&]() AI {
        asm volatile("global_load_dwordx2 %0, %2, %3 sc1\n\tglobal_load_dwordx2 %1, %2, %3 offset:64 sc1"
                     : "=&v"(gg[0]), "=&v"(gg[1]) : "v"((unsigned)srow * 8), "s"(ws + WS_GG + row0 * 8) : "memory");
    };
    auto xg_load = [&](int skip) AI {
        asm volatile("s_cmp_eq_u32 %4, 4\n\ts_cbranch_scc1 .Lk%=\n\t"
                     "global_load_dwordx4 %0, %5, %6\n\tglobal_load_dwordx4 %1, %5, %7\n\t"
                     "global_load_dwordx2 %2, %8, %9 sc1\n\tglobal_load_dwordx2 %3, %8, %9 offset:64 sc1\n.Lk%=:"
                     : "+v"(xr[0]), "+v"(xr[1]), "+v"(gg[0]), "+v"(gg[1])
                     : "s"(skip), "v"(xo), "s"(x + (size_t)row0 * DM + cb * 64), "s"(x + (size_t)(row0 + 8) * DM + cb * 64),
                       "v"((unsigned)srow * 8), "s"(ws + WS_GG + row0 * 8) : "memory", "scc");
    };
    auto xg_def = [&]() AI { asm volatile("" : "=v"(xr[0]), "=v"(xr[1]), "=v"(gg[0]), "=v"(gg[1])); };
    auto relane = [&]() AI {
        unsigned z = 0;
        asm volatile("" : "+v"(z));
        l = __builtin_amdgcn_mbcnt_hi(~0u, __builtin_amdgcn_mbcnt_lo(~0u, z));
        g = l >> 4, l15 = l & 15, q = l15 >> 2, p = l & 3, srow = l >> 3, sch = l & 7, l16 = l * 16;
        AX = lds0 + L_A + rg * 4096 + l16, W4 = lds0 + p * 4, PG = lds0 + L_PROG + p * 256, xo = srow * 2048 + sch * 16;
    };
    u32x4 kf[6];
    auto kf_def = [&]() AI { asm volatile("" : "=v"(kf[0]), "=v"(kf[1]), "=v"(kf[2]), "=v"(kf[3]), "=v"(kf[4]), "=v"(kf[5])); };
    const int oh = hs ^ kp ^ 1;

    if (__builtin_expect(w >= 4, 1)) {
        const bool loader = !(w & 1);
        const unsigned kf_kv = WS_KF + (hp * 2 + (mt >> 3)) * 8192 + (mt & 7) * 512;
        const unsigned kf_z = WS_KF + (hp * 2 + (mt >> 3)) * 8192 + 4096 + ((mt >> 2) & 1) * 64;
        const unsigned fgo = ffn ? (unsigned)((256 * kq + 16 * srow) * 7168 + cs * 128 + sch * 16) : 0u;
        const char* fsrc = (const char*)(hs ? wu : wg);
        auto gu_w = [&](int q0, int q1) AI {
#pragma unroll
            for (int i = q0; i < q1; ++i) {
                unsigned t1, t2, t3;
                if (NOF & 8) fw[4 * i] = fw[4 * i + 1] = fw[4 * i + 2] = fw[4 * i + 3] = z4; else
                asm volatile("v_add_u32 %4, 0x1c00, %7\n\tv_add_u32 %5, 0x3800, %7\n\tv_add_u32 %6, 0x5400, %7\n\t"
                             "global_load_dwordx4 %0, %7, %8 nt\n\tglobal_load_dwordx4 %1, %4, %8 nt\n\t"
                             "global_load_dwordx4 %2, %5, %8 nt\n\tglobal_load_dwordx4 %3, %6, %8 nt"
                             : "=&v"(fw[4 * i]), "=&v"(fw[4 * i + 1]), "=&v"(fw[4 * i + 2]), "=&v"(fw[4 * i + 3]), "=&v"(t1), "=&v"(t2), "=&v"(t3)
                             : "v"(fgo), "s"(fsrc + (128 * (i >> 2) + 4 * (i & 3)) * 7168) : "memory");
            }
        };
        const unsigned fdo = ffn ? (unsigned)((64 * cs + 8 * srow) * 2048 + 512 * kq + 256 * hs + sch * 16) : 0u;
        auto dn_w = [&](int q0, int q1) AI {
#pragma unroll
            for (int i = q0; i < q1; ++i)
                if (NOF & 8) fd[4 * i] = fd[4 * i + 1] = fd[4 * i + 2] = fd[4 * i + 3] = z4; else
                asm volatile("global_load_dwordx4 %0, %4, %5 offset:-4096 nt\n\tglobal_load_dwordx4 %1, %4, %5 offset:-2048 nt\n\t"
                             "global_load_dwordx4 %2, %4, %5 nt\n\tglobal_load_dwordx4 %3, %4, %5 offset:2048 nt"
                             : "=&v"(fd[4 * i]), "=&v"(fd[4 * i + 1]), "=&v"(fd[4 * i + 2]), "=&v"(fd[4 * i + 3])
                             : "v"(fdo), "s"((const char*)wd + 4096 + (i & 1) * 8192 + (i >> 1) * 128) : "memory");
        };
        auto gate_up = [&]() AI {
            sel_init();
#pragma unroll
            for (int i = 0; i < 32; ++i) pin(fw[i]);
            u32x4 xs[4];
#pragma unroll
            for (int i = 0; i < 4; ++i) xs[i] = lds_read<u32x4>(lds0 + L_XS + hs * 512 + (i >> 1) * 256 + srow * 32 + (i & 1) * 16);
            f32x4 a = zero;
#pragma unroll
            for (int i = 0; i < 32; ++i) a = mfma16(spread(xs[i >> 3], i & 7), __builtin_bit_cast(bf16x8, fw[i]), a);
            fold_store(a, WS_GU + cs * 4096 + kq * 512 + hs * 256);
            SB0();
        };
        auto o_clear = [&]() AI {
#pragma unroll
            for (int n = 0; n < ((NOF & 2) ? 1 : 8); ++n) st_wt(rsrc, WS_O + ((mt * 2 + (n >> 2)) * 32 + 2 * hp + hs) * 4096 + (n & 3) * 1024 + l16, z4);
        };
        u64 T;
        u32x4 sv[2];
        auto clears = [&]() AI {
            o_clear();
#pragma unroll
            for (int n = 0; n < ((NOF & 16) ? 0 : 4); ++n) st_wt(rsrc, WS_KV + ((hp * 8 + hs * 4 + n) * 16 + mt) * 1024 + l16, z4);
            ST_MASK("global_store_dwordx4", 0xFFFFull, WS_Z + ((hp * 4 + hs * 2 + (srow & 1)) * 16 + mt) * 128 + sch * 16, z4, ws);
        };
        auto cleared = [&]() AI { ST_MASK("global_store_dwordx2", 1ull, 0u, T, ws + WS_CL + ((mt * 2 + hs) * 16 + hp) * 8); };
        auto rg_load = [&]() AI {
            asm volatile("global_load_dwordx4 %0, %2, %3 sc1\n\tglobal_load_dwordx4 %1, %2, %3 offset:16 sc1"
                         : "=&v"(sv[0]), "=&v"(sv[1]) : "v"(l * 32), "s"(ws + WS_RG) : "memory");
        };
        auto select = [&]() AI {
            const unsigned tl = (unsigned)T & 0x1FFFu, thi = (unsigned)(T >> 13);
#pragma unroll 1
            for (unsigned s = 0; s < SPIN_CAP; ++s) {
                pin(sv[0]);
                pin(sv[1]);
                const unsigned bad = ((sv[0][0] >> 19) ^ tl) | (sv[0][1] ^ thi) | ((sv[0][2] >> 19) ^ tl) | (sv[0][3] ^ thi) |
                                     ((sv[1][0] >> 19) ^ tl) | (sv[1][1] ^ thi) | ((sv[1][2] >> 19) ^ tl) | (sv[1][3] ^ thi);
                if (__all(bad == 0u)) break;
                __builtin_amdgcn_s_sleep(16);
                rg_load();
                waitv<0>();
            }
            pin(sv[0]);
            pin(sv[1]);
            RS(4);
            unsigned k2 = 0;
#pragma unroll
            for (int j = 0; j < 4; ++j) {
                const unsigned v = sv[j >> 1][(j & 1) * 2] & 0x7FFFFu;
                k2 = max(k2, ((v >> 3) << 11) | ((255 - (4 * l + j)) << 3) | (v & 7));
            }
            const unsigned key = __builtin_amdgcn_readfirstlane(wave_umax(k2));
            asm volatile("s_mov_b32 m0, %2\n\ts_mov_b64 exec, %3\n\tglobal_load_lds_dwordx4 %0, %1\n\ts_mov_b64 exec, -1"
                         ::"v"((unsigned)(512 * kq) + l16), "s"(x + (size_t)(2047 - (int)(key & 2047u)) * DM), "s"(lds0 + L_XS + hs * 512), "s"(0xFFFFFFFFull) : "memory");
            unsigned rb = key >> 11;
            rb ^= (rb & 0x8000u) ? 0x8000u : 0xFFFFu;
            const float rsel = rbf(1.f / (1.f + __expf(-bf2f((u16)rb))));
            lds_write<u32x4>(lds0 + L_SEL, (u32x4){(unsigned)T, (unsigned)(T >> 32), 2047u - (key & 2047u), __float_as_uint(rsel)});
            if (bid == 0 && hs) ST_MASK("global_store_dwordx2", 1ull, 0u, T, ws + WS_E);
            RS(5);
        };
        auto k_soft = [&](int np) AI {
            bf16x8 a0[2], kb[2];
            f32x4 pk[2][2][2];
            unsigned pw[4];
            phi_w(a0, 0);
            phi_adr(pw, lds0 + P_U[0] * 8192);
            asm volatile("" : "+v"(pw[0]), "+v"(pw[1]), "+v"(pw[2]), "+v"(pw[3]));
            auto logits = [&](int h) AI {
                lds_wait(WR + L_FK0 + 4 * h, 1);
#pragma unroll
                for (int i = 0; i < 2; ++i) kb[i] = lds_read128(AX + (P_U[0] * 8192 - L_A) + h * 4096 + i * 1024);
#pragma unroll
                for (int i = 0; i < 4; ++i) pk[h][i >> 1][i & 1] = mfma16(a0[i & 1], kb[i >> 1], zero);
            };
            auto round = [&](int h) AI {
#pragma unroll
                for (int i = 0; i < 4; ++i) pk[h][i >> 1][i & 1] = rbf4(pk[h][i >> 1][i & 1]);
            };
            logits(0);
            RS(12);
            round(0);
            SB0();
            phi_k<KPK & 1>(pk[0], pw, 0, [&]() AI {
                if (np > 1) logits(1);
                SB0();
            });
            SB0();
            if (np > 1) {
                round(1);
                phi_k<(KPK >> 1)>(pk[1], pw, 4096, []() {});
                SB0();
            }
            flag(WR + L_FP);
        };
        if (__builtin_expect(loader, 1)) {
            vx_init();
            vw_init();
            if (LPRIO) __builtin_amdgcn_s_setprio(LPRIO);
            unsigned pv = 0;
            sfor<NF0, NF1>([&](auto N) {
                constexpr int n = N;
                if constexpr (n > 0 && F_N[n] > F_N[n > 0 ? n - 1 : 0]) {
                    RSL(32 + F_N[n]);
                    if (LPRW != LPRIO) __builtin_amdgcn_s_setprio(LPRW);
                    if (NOF & 256) {
                    } else if (F_N[n] > RLD) {
                        lds_wait(W4 + L_PRL, F_N[n]);
                    } else if (PEEK) {
                        lds_wait(pv, PG, F_N[n]);
                        if (F_N[n] < F_N[NF1 - 1] && next_need(n) <= RLD) lds_peek(pv, PG);
                    } else lds_wait(PG, F_N[n]);
                    if (LPRW != LPRIO) __builtin_amdgcn_s_setprio(LPRIO);
                    RSL(48 + F_N[n]);
                }
                if constexpr (n == 12) {
                    asm volatile("s_mov_b32 m0, %2\n\ts_nop 0\n\tglobal_load_lds_dwordx4 %0, %1\n\tglobal_load_lds_dwordx4 %0, %1 offset:1024"
                                 ::"v"((unsigned)((l >> 2) * 64 + (((l & 3) ^ (2 * ((l >> 4) & 1))) * 16))),
                                   "s"(wphi + (hp * 2 + hs) * 1024), "s"(lds0 + L_WPHI + hs * 2048) : "memory");
                }
                if constexpr (LTH < 63 && n >= LT0) waitv<LTH>();
                fill(N);
            });
            flag(lds0 + L_FLD + hs * 4);
            RS(1);
            if (HPRIO) __builtin_amdgcn_s_setprio(HPRIO);
            const char* cla = ws + WS_CL + ((l15 * 2 + (mt >> 3)) * 16 + hp) * 8;
            u64 cl;
            if (CLE) asm volatile("global_load_dwordx2 %0, %1, off sc1" : "=&v"(cl) : "v"(cla) : "memory");
            k_soft(SSM ? 1 : 2);
            if (!CLE) asm volatile("global_load_dwordx2 %0, %1, off sc1" : "=&v"(cl) : "v"(cla) : "memory");
            RS(11);
            lds_wait(W4 + L_FP, 1);
            sq = lds_read<u32x4>(lds0 + L_SEL);
            waitv<0>();
            asm volatile("" : "+v"(cl));
            RS(13);
            u32x4 kp_[5];
            {
                const char* kb = ws + WS_KV + (hp * 8 + (mt >> 1)) * 16384 + (mt & 1) * 512 + hs * 256;
                const bool zw = !(mt & 3);
                const char* zb = zw ? ws + WS_Z + (hp * 4 + (mt >> 2)) * 2048 + hs * 64 : kb;
                const unsigned kvo = g * 1024 + l15 * 16, zo = zw ? l15 * 128 + g * 16 : 0u;
                auto ko = [&](int i) { return i < 4 ? kvo : zo; };
                auto ka = [&](int i) { return i < 4 ? kb + i * 4096 : zb; };
                const u64 T = (u64)sq[1] << 32 | sq[0];
#pragma unroll 1
                for (unsigned s = 0; s < SPIN_CAP && __ballot(cl != T); ++s)
                    asm volatile("global_load_dwordx2 %0, %1, off sc1\n\ts_waitcnt vmcnt(0)" : "=&v"(cl) : "v"(cla) : "memory");
                lds_wait0(W4 + L_FKS);
                if (GDL) __builtin_amdgcn_s_sleep(GDL);
#pragma unroll
                for (int i = 0; i < 5; ++i) { if (PDI & 1) kp_[i] = z4; else ld_async(kp_[i], ko(i), ka(i)); }
                if (XGE) {
                    xg_def();
                    xg_load(w + 1);
                }
                flag(lds0 + L_FGI + hs * 4);
                SB0();
                RS(14);
                if (!(PDI & 1)) waitv<0>();
                RS(15);
                if (PP1) settle_p<PP1 + !PP1, PD1>(kp_, ko, ka);
                else settle(kp_, ko, ka);
                RS(24);
                float v[8], u[4];
#pragma unroll
                for (int e = 0; e < 4; ++e) {
                    v[2 * e] = (lo16(kp_[0][e]) + lo16(kp_[1][e])) + (lo16(kp_[2][e]) + lo16(kp_[3][e]));
                    v[2 * e + 1] = (hi16(kp_[0][e]) + hi16(kp_[1][e])) + (hi16(kp_[2][e]) + hi16(kp_[3][e]));
                }
#pragma unroll
                for (int e = 0; e < 4; ++e) u[e] = swap32_add(v[e], v[e + 4]);
                const unsigned o = nz1(pack2(swap16_add(u[0], u[2]), swap16_add(u[1], u[3])));
                ST_MASK("global_store_dword", ~0ull, kf_kv + hs * 256 + l15 * 16 + g * 4, o, ws);
                if (zw) {
                    f32x4 z;
#pragma unroll
                    for (int i = 0; i < 4; ++i) z[i] = dpp_add<0x140>(dpp_add<0x141>(dpp_add<0x4E>(dpp_add<0xB1>(__uint_as_float(kp_[4][i])))));
                    const u32x2 oz = {nz1(pack2(z[0], z[1])), nz1(pack2(z[2], z[3]))};
                    ST_MASK("global_store_dwordx2", 0x0001000100010001ull, kf_z + g * 16 + hs * 8, oz, ws);
                }
                flag(lds0 + L_FG + hs * 4);
            }
            RS(25);
            const char* kfb = ws + WS_KF + (hp * 2 + hs) * 8192;
            auto fo = [&](int i) { return i < 4 ? l16 : (unsigned)g * 16; };
            auto fb = [&](int i) { return i < 4 ? kfb + i * 1024 : kfb + 4096 + (i - 4) * 64; };
            if (KFD) __builtin_amdgcn_s_sleep(KFD);
#pragma unroll
            for (int i = 0; i < 6; ++i) { if (PDI & 2) kf[i] = z4; else ld_async(kf[i], fo(i), fb(i)); }
            if (!XGE) {
                xg_def();
                xg_load(w + 1);
            }
            if (!(PDI & 2)) waitv<0>();
            RS(29);
            if (PP2) settle_p<PP2 + !PP2, PD2>(kf, fo, fb);
            else settle(kf, fo, fb);
#pragma unroll
            for (int i = 0; i < 5; ++i) pin(kp_[i]);
            asm volatile("" : "+v"(xr[0]), "+v"(xr[1]), "+v"(gg[0]), "+v"(gg[1]));
            need(KFN);
#pragma unroll
            for (int i = 0; i < 4; ++i) lds_write<u32x4>(lds0 + L_KF + hs * 4096 + i * 1024 + l16, kf[i]);
#pragma unroll
            for (int i = 0; i < 2; ++i) lds_write<u32x4>(lds0 + L_KZ + hs * 128 + i * 64 + g * 16, kf[4 + i]);
            SB0();
            flag(lds0 + L_FKF + hs * 4);
        } else {
            const int row0 = (mt * 16 + hp) * 8 + 4 * hs;
            if (SPRIO) __builtin_amdgcn_s_setprio(SPRIO);
            asm volatile("s_waitcnt lgkmcnt(0)" : "+s"(sd));
            if (TLD) __builtin_amdgcn_s_sleep(TLD);
            u32x4 rv[2], gv[2], xv[4][2];
            u64 ev;
            if (RT0) lds_wait8(PG, RT0);
            asm volatile("global_load_dwordx2 %4, %5, %9 sc1\n\t"
                         "global_store_dwordx4 %10, %11, %9 sc1\n\t"
                         "global_store_dwordx2 %12, %13, %9 sc1\n\t"
                         "global_load_dwordx4 %0, %6, %7\n\tglobal_load_dwordx4 %1, %6, %7 offset:1024\n\t"
                         "global_load_dwordx4 %2, %6, %8\n\tglobal_load_dwordx4 %3, %6, %8 offset:1024"
                         : "=&v"(rv[0]), "=&v"(rv[1]), "=&v"(gv[0]), "=&v"(gv[1]), "=&v"(ev)
                         : "v"(0u), "v"(l16), "s"(wr), "s"(gw), "s"(ws),
                           "v"(l < 32 ? WS_Y + cs * 4096 + kq * 1024 + hs * 512 + l16 : WS_GU + cs * 4096 + kq * 512 + hs * 256 + l15 * 16), "v"(z4),
                           "v"(hs ? kf_z + sch * 8 : kf_kv + l * 8), "v"(0ull)
                         : "memory");
            RS(1);
#pragma unroll
            for (int r = 0; r < 2; ++r)
                if (NOF & 64) xv[2 * r][0] = xv[2 * r][1] = xv[2 * r + 1][0] = xv[2 * r + 1][1] = z4; else
                asm volatile("global_load_dwordx4 %0, %4, %5\n\tglobal_load_dwordx4 %1, %4, %5 offset:1024\n\t"
                             "global_load_dwordx4 %2, %4, %5 offset:2048\n\tglobal_load_dwordx4 %3, %4, %5 offset:3072"
                             : "=&v"(xv[2 * r][0]), "=&v"(xv[2 * r][1]), "=&v"(xv[2 * r + 1][0]), "=&v"(xv[2 * r + 1][1])
                             : "v"(l16), "s"(x + (size_t)(row0 + 2 * r) * DM) : "memory");
            SB0();
            if (MRG & 8) clears();
            waitv<0>();
            asm volatile("" : "+v"(rv[0]), "+v"(rv[1]), "+v"(gv[0]), "+v"(gv[1]), "+v"(ev));
#pragma unroll
            for (int r = 0; r < 4; ++r) asm volatile("" : "+v"(xv[r][0]), "+v"(xv[r][1]));
            RS(2);
            T = ((u64)__builtin_amdgcn_readfirstlane((unsigned)(ev >> 32)) << 32 | __builtin_amdgcn_readfirstlane((unsigned)ev)) + 1;
            float ra_[4] = {0.f, 0.f, 0.f, 0.f}, ga_[4] = {0.f, 0.f, 0.f, 0.f};
#pragma unroll
            for (int r = 0; r < 4; ++r)
#pragma unroll
                for (int i = 0; i < 2; ++i)
#pragma unroll
                    for (int j = 0; j < 4; ++j) {
                        ra_[r] = dot2(xv[r][i][j], rv[i][j], ra_[r]);
                        ga_[r] = dot2(xv[r][i][j], gv[i][j], ga_[r]);
                    }
            const int rr = (g >> 1) | ((g & 1) << 1);
            unsigned k = f2bf(sum4(ra_));
            k ^= (k & 0x8000u) ? 0xFFFFu : 0x8000u;
            k = (k << 3) | (7 - 4 * hs - rr);
            auto r1 = __builtin_amdgcn_permlane16_swap(k, k, false, false);
            k = max(r1[0], r1[1]);
            auto r2 = __builtin_amdgcn_permlane32_swap(k, k, false, false);
            k = max(r2[0], r2[1]);
            u64 gq = (T << 16) | f2bf(1.f / (1.f + __expf(-rbf(sum4(ga_)))));
            if (hs) lds_write<unsigned>(lds0 + L_BEST, k | 0x80000000u);
            asm volatile("" : "+v"(gq), "+v"(k));
            if (MRG & 1) {
                __builtin_amdgcn_s_setprio(SPR2);
                lds_wait8(PG, CLR0 + CLS * ((ph4 + CLP) & 3));
            }
            if (!hs) {
                lds_wait(lds0 + L_BEST, 1);
                const u64 rq = (T << 19) | max(k, lds_read<unsigned>(lds0 + L_BEST) & 0x7FFFFu);
                ST_MASK("global_store_dwordx2", 1ull, 0u, rq, ws + WS_RG + (mt * 16 + hp) * 8);
            }
            ST_MASK("global_store_dwordx2", 0x0001000100010001ull, (unsigned)(rr * 8), gq, ws + WS_GG + row0 * 8);
            RS(3);
            __builtin_amdgcn_s_setprio(SPR2);
            auto q_fill = [&]() AI {
                vx_init();
                vw_init();
                sfor<NF2, NF>([&](auto N) {
                    constexpr int n = N;
                    if constexpr (n == NF2 || F_N[n] != F_N[n - 1]) need(F_N[n]);
                    fill(N);
                });
            };
            auto wt = [&](unsigned f) AI { lds_wait(lds0 + f + (l & 1) * 4, 1); };
            unsigned pf[2];
            auto wo_warm = [&]() AI {
                asm volatile("global_load_dword %0, %2, %3\n\tglobal_load_dword %1, %2, %4" : "=&v"(pf[0]), "=&v"(pf[1])
                             : "v"((unsigned)(((rt & 3) * 256 + hs * 128 + l) * 2048)), "s"((const char*)wo + cb * 128), "s"((const char*)wo + cb * 128 + 64 * 2048) : "memory");
            };
            if (DNP == 0) {
                lds_wait8(PG, FBA + FBS * turn);
                dn_w(0, 4);
            }
            if (!(MRG & 8)) {
                if (!(MRG & 1)) lds_wait8(PG, CLR0 + CLS * ((ph4 + CLP) & 3));
                clears();
                waitv<0>();
            }
            if (!(MRG & 6) || (MRG & 8)) cleared();
            RS(25);
            if (!(MRG & 4)) {
                lds_wait8(PG, SEL0);
                rg_load();
                if ((MRG & 10) == 2) cleared();
                waitv<(MRG & 10) == 2>();
                if (MRG & 2) lds_wait8(lds0 + L_FLD + (l & 1) * 4, 1);
                select();
                if (!SSM) flag(WR + L_FP);
            }
            if (!(MRG & 2)) lds_wait8(lds0 + L_FLD + (l & 1) * 4, 1);
            if (MRG & 4) {
                rg_load();
                if (!(MRG & 8)) cleared();
            }
            RS(6);
            if (WPF == 4) wo_warm();
            gu_w(0, GQ1);
            if (MRG & 4) {
                waitv<4 * GQ1>();
                select();
                if (!SSM) flag(WR + L_FP);
            }
            q_fill();
            RS(7);
            gu_w(GQ1, GQ2);
            if (SSM) {
                __builtin_amdgcn_s_setprio(HPRIO);
                k_soft(1);
                __builtin_amdgcn_s_setprio(SPR2);
                RS(11);
            }
            gu_w(GQ2, 8);
            if (WPF == 1) wo_warm();
            if (DNP == 1) dn_w(0, 4);
            RS(8);
            waitv<(DNP == 1 ? 16 : 0)>();
            if ((WPF == 1 && DNP != 1) || WPF == 4) asm volatile("" : "+v"(pf[0]), "+v"(pf[1]));
            RS(9);
            if (GUO == 0) gate_up();
            RS(10);
            if (WOT == 1) lds_wait(W4 + L_FKS, 1);
            if (WOT == 2) wt(L_FGI);
            if (DNP == 2) dn_w(0, 4);
            wo_load(0, WOE, WOE == WON);
            RS(14);
            if (GUO == 1) {
                __builtin_amdgcn_s_setprio(GPR);
                gate_up();
                __builtin_amdgcn_s_setprio(SPR2);
            }
            RS(13);
            if (YT == 1) wt(L_FG);
            gu_load();
            if (DNP == 3) dn_w(0, 4);
            if (WOE < WON) wo_load(WOE, WON, true);
            waitv<(WOE < WON ? 4 * (WON - WOE) + 1 : 0)>();
            if (WPF == 1 && DNP == 1) asm volatile("" : "+v"(pf[0]), "+v"(pf[1]));
            RS(15);
            down();
            RS(24);
            sq = lds_read<u32x4>(lds0 + L_SEL);
            xg_def();
            kf_def();
        }
        RS(26);
        lds_wait0(WR + L_FQ);
#pragma unroll
        for (int i = 0; i < 4; ++i) ph[i >> 1][i & 1] = lds_read<u32x4>(AX + i * 1024);
    } else {
        if (CPRIO) __builtin_amdgcn_s_setprio(CPRIO);
        unsigned ra[4], xt[2];
#pragma unroll
        for (int u = 0; u < 4; ++u) ra[u] = lds0 + J_B[0] * 8192 + (8 * g + q) * 128 + ((u ^ (q >> 1) ^ ((g & 1) << 1)) * 32) + 8 * p;
        unsigned xa = lds0 + (32 * (w & 1) + l15) * 128 + ((g ^ (l15 >> 1)) * 16), fa = lds0 + L_FULL + (l & 3) * 4;
        xt[0] = xa + (hs ? X_U1[0] : X_U0[0]) * 8192;
        xt[1] = xt[0] ^ 64;
        asm volatile("s_mov_b32 m0, %4" : "+v"(xa), "+v"(fa), "+v"(xt[0]), "+v"(xt[1]) : "s"(lds0 + L_PROG + w * 256 - 124));
        f32x4 acc[3][4][2];
        bf16x8 xf[ND][2][2];
        s16x4 wh[PD + 1][2];
        unsigned fv = 0;
        bf16x8 kown[2];
        u64 bad = ~0ull;
        auto chk = [&](int c) AI {
            SB0();
            if (__builtin_expect(bad != 0ull, 0)) {
                unsigned c_;
                asm volatile("s_mov_b32 %1, 0x400000\n"
                             ".Lp%=:\n\tds_read_b32 %0, %2 offset:%3\n\ts_waitcnt lgkmcnt(0)\n\tv_cmp_gt_u32 vcc, %4, %0\n\ts_cbranch_vccz .Lf%=\n\t"
                             "s_sub_u32 %1, %1, 1\n\ts_cbranch_scc0 .Lp%=\n.Lf%=:"
                             : "=&v"(fv), "=&s"(c_) : "v"(fa), "n"(c > NM2 ? 32 : c > NM1 ? 16 : 0), "n"(c) : "memory", "vcc", "scc");
            }
            SB0();
        };
        auto kv_part = [&]() AI
        {
            bf16x8 ones;
#pragma unroll
            for (int j = 0; j < 8; ++j) ones[j] = (short)0x3f80;
            const unsigned pa = lds0 + (hs ? P_U[2] : P_U[0]) * 8192 + (4 * g + q) * 128 + (((8 * (rg & 1) + p) ^ fsw(4 * g + q)) << 3);
            const unsigned va = lds0 + L_A + hs * 2048 + l16;
            bf16x8 b0[4], b1[4], af[2][4];
            lds_wait0(lds0 + L_FV + sch * 4);
            RS(15);
#pragma unroll
            for (int i = 0; i < 4; ++i) {
                af[0][i] = cat8(tr_read(pa + i * 4096), tr_read(pa + i * 4096 + 2048));
                b0[i] = lds_read128(va + i * 4096);
                af[1][i] = cat8(tr_read((pa ^ 32) + i * 4096), tr_read((pa ^ 32) + i * 4096 + 2048));
            }
#pragma unroll
            for (int i = 0; i < 4; ++i) b1[i] = lds_read128(va + i * 4096 + 1024);
            SB0();
            f32x4 k0[2] = {zero, zero}, k1[2] = {zero, zero}, zt[2] = {zero, zero};
            const unsigned kv_st = WS_KV + ((hp * 8 + rg * 2) * 16 + mt) * 1024 + l16;
#pragma unroll
            for (int i = 0; i < 4; ++i)
#pragma unroll
                for (int e = 0; e < 2; ++e) {
                    k0[e] = mfma16(af[e][i], b0[i], k0[e]);
                    if (PORD) zt[e] = mfma16(af[e][i], ones, zt[e]);
                }
            SB0();
#pragma unroll
            for (int i = 0; i < 4; ++i)
#pragma unroll
                for (int e = 0; e < 2; ++e) k1[e] = mfma16(af[e][i], b1[i], k1[e]);
            st_wt(rsrc, kv_st, nz4(__builtin_bit_cast(u32x4, pack44(k0[0], k0[1]))));
            if (!PORD) {
                SB0();
#pragma unroll
                for (int i = 0; i < 4; ++i)
#pragma unroll
                    for (int e = 0; e < 2; ++e) zt[e] = mfma16(af[e][i], ones, zt[e]);
                st_wt(rsrc, kv_st + 16384, nz4(__builtin_bit_cast(u32x4, pack44(k1[0], k1[1]))));
            }
            const u32x4 zo = nz4((l & 1) ? zt[1] : zt[0]);
            ST_MASK("global_store_dwordx4", 0x0003000300030003ull, WS_Z + ((hp * 4 + rg) * 16 + mt) * 128 + (l & 1) * 64 + g * 16, zo, ws);
            if (PORD) st_wt(rsrc, kv_st + 16384, nz4(__builtin_bit_cast(u32x4, pack44(k1[0], k1[1]))));
            SB0();
            flag(WR + L_FKS);
        };
        auto report = [&](int r) AI {
            if (r > RLD) {
                unsigned t;
                unsigned a;
                asm volatile("v_mov_b32 %0, %3\n\tv_mov_b32 %1, %2\n\tds_write_b32 %1, %0" : "=&v"(t), "=&v"(a) : "s"(WR + L_PRL), "n"(r) : "memory");
            } else asm volatile("global_load_lds_dword %0, %1 offset:%2" ::"v"(v0), "s"(one), "n"(4 * r) : "memory"); };
        auto rd1 = [&](auto F, auto H) AI {
            constexpr int f = F, h = H, n = f >> 2, j = f >> 3, c = h ? E_B[f] : E_A[f], a = c % 100;
            if constexpr (h == 0 && (f & 3) == 0 && Q_C[n] > 0 && !((NOF & 512) && n >= 12 && n < KD)) chk(Q_C[n]);
            wh[f % (PD + 1)][h] = tr_read(ra[f & 3] + (J_U[j] - J_B[j]) * 8192 + (n & 1) * 4096 + h * 512);
            SB0();
            if constexpr (c / 100 == 1) {
                constexpr int jn = (f + 3 - a) >> 3;
                asm volatile("v_add_u32 %0, %1, %0" : "+v"(ra[a]) : "n"((J_B[jn] - J_B[jn - 1]) * 8192));
            } else if constexpr (c / 50 == 4) {
                asm volatile("v_add_u32 %0, %1, %2" : "=v"(xt[0]) : "s"((hs ? X_U1[a] : X_U0[a]) * 8192), "v"(xa));
            } else if constexpr (c / 50 == 5) {
                asm volatile("v_xor_b32 %0, 64, %1" : "=v"(xt[1]) : "v"(xt[0]));
            } else if constexpr (c / 100 == 3) {
                xf[a % 25 % ND][a / 25 & 1][a / 50] = lds_read128(xt[a / 50] + (a / 25 & 1) * 2048);
            } else if constexpr (c / 100 == 4) {
                fv = *(volatile AS3 unsigned*)(size_t)(fa + (a > NM2 ? 32 : a > NM1 ? 16 : 0));
            } else if constexpr (c / 100 == 5) {
                bad = __builtin_amdgcn_ballot_w64(fv < (unsigned)a);
                asm volatile("" : "+s"(bad));
            } else if constexpr (c / 100 == 6 && !((NOF & 1024) && a < NP1)) {
                report(a);
            }
        };
        RS(1);
        sfor<0, PD>([&](auto F) { rd1(F, IC<0>{}); rd1(F, IC<1>{}); });
        RS(2);
        constexpr int K0F = 4 * KD, K1F = 4 * KD + 1;
        sfor<0, 96>([&](auto N) {
            sfor<0, 4>([&](auto E) {
                constexpr int n = N, e = E, f = 4 * n + e, j = n >> 1, ks = n & 1, u = f_u(f), s = J_X[j], m = J_M[j];
                const bf16x8 wv = cat8(wh[f % (PD + 1)][0], wh[f % (PD + 1)][1]);
                sfor<0, 2>([&](auto RB) {
                    constexpr int rb = RB;
                    const bf16x8 xv = xf[s % ND][rb][ks];
                    if (AMF) {
                        if (j_first(j) && ks == 0) asm volatile("v_mfma_f32_16x16x32_bf16 %0, %1, %2, 0" : "=&v"(acc[m][u][rb]) : "v"(m == 1 ? xv : wv), "v"(m == 1 ? wv : xv));
                        else asm volatile("v_mfma_f32_16x16x32_bf16 %0, %1, %2, %0" : "+v"(acc[m][u][rb]) : "v"(m == 1 ? xv : wv), "v"(m == 1 ? wv : xv));
                    } else if (m == 1) acc[m][u][rb] = mfma16(xv, wv, acc[m][u][rb]);
                    else acc[m][u][rb] = mfma16(wv, xv, acc[m][u][rb]);
                    SB0();
                    if constexpr (f + PD < 384 && (f + PD < 4 * VD || f >= 4 * VD)) rd1(IC<f + PD>{}, RB);
                    if constexpr (rb == 0 && f == K0F) relane();
                    if constexpr (AMF && rb == 0 && (f == K0F || f == K1F)) asm volatile("s_nop 7");
                    SB0();
                    if constexpr (f == K0F) lds_write<bf16x8>(AX + (P_U[0] * 8192 - L_A) + rb * 1024, pack44(acc[0][0][rb], acc[0][1][rb]));
                    if constexpr (f == K1F) {
                        kown[rb] = pack44(acc[0][2][rb], acc[0][3][rb]);
                        if (rb < 2 - SH) lds_write<bf16x8>(AX + (P_U[2] * 8192 - L_A) + rb * 1024, kown[rb]);
                        else asm volatile("" : "+v"(kown[rb]));
                    }
                    SB0();
                });
                if constexpr (f == K0F) { flag(WR + L_FK0); RS(12); }
                if constexpr (f == K1F) { if (SH < 2) flag(WR + L_FK); RS(4); }
                if constexpr (f == 4 * VD - 1) {
                    bf16x8 ak[2];
                    if (NF2 > NF1) flag(WR + L_FVR);
                    auto own = [&]() AI {
                        if (NF2 == NF1) return;
                        vx_init();
                        vw_init();
                        lds_wait0(W4 + L_FVR);
                        sfor<NF1, NF2>(fill);
                        asm volatile("s_mov_b32 m0, %0" ::"s"(lds0 + L_PROG + w * 256 - 124));
                        RS(14);
                    };
                    if (SH) {
                        __builtin_amdgcn_s_setprio(HPRIO);
                        phi_w(ak, 1);
                    }
                    asm volatile("s_nop 7");
                    SB0();
#pragma unroll
                    for (int i = 0; i < 4; ++i) lds_write<bf16x8>(AX + i * 1024, pack44(acc[1][i][0], acc[1][i][1]));
                    SB0();
                    RS(5);
                    if (CFP == 1 && SH) own();
                    if (SH) {
                        f32x4 pk[2][2];
                        unsigned pw[4];
#pragma unroll
                        for (int i = 4 - 2 * SH; i < 4; ++i) pk[i >> 1][i & 1] = rbf4(mfma16(ak[i & 1], kown[i >> 1], zero));
                        phi_adr(pw, lds0 + P_U[2] * 8192 + (SH == 1 ? 2048 : 0));
                        if (SH == 1) phi_k1(pk[1], pw);
                        else phi_k<true>(pk, pw, 0, []() {});
                        SB0();
                    }
                    flag(WR + L_FV);
                    RS(13);
                    if (CFP == 0 || !SH) own();
                    kv_part();
                    __builtin_amdgcn_s_setprio(CPRIO);
                    fv = 0;
                    bad = ~0ull;
                    sfor<4 * VD, 4 * VD + PD>([&](auto F) { rd1(F, IC<0>{}); rd1(F, IC<1>{}); });
                    RS(10);
                }
                if constexpr (e == 3) {
                    SB0();
                    if constexpr (n % CSN == CSN - 1) { RSC(32 + n / CSN); RK(48 + n / CSN); }
                    if constexpr (n == 31) RS(3);
                }
            });
        });
        asm volatile("s_nop 7");
        SB0();
        RS(6);
        if (CPRIO) __builtin_amdgcn_s_setprio(0);
        flag(WR + L_FQD);
        bf16x8 a0[2], a1[2];
        f32x4 pq[2][2][2];
        phi_w(a1, hs);
        phi_w(a0, hs ^ 1);
#pragma unroll
        for (int rb = 0; rb < 2; ++rb) {
            const bf16x8 b0 = pack44(acc[2][0][rb], acc[2][1][rb]), b1 = pack44(acc[2][2][rb], acc[2][3][rb]);
#pragma unroll
            for (int fb = 0; fb < 2; ++fb) {
                pq[1][rb][fb] = rbf4(mfma16(a1[fb], hs ? b1 : b0, zero));
                pq[0][rb][fb] = rbf4(mfma16(a0[fb], hs ? b0 : b1, zero));
            }
        }
        softmax2<true>(pq[1], ph, []() {});
        RS(7);
        lds_wait(W4 + L_FKS, 1);
#pragma unroll
        for (int i = 0; i < 4; ++i) lds_write<u32x4>(AX + i * 1024, ph[i >> 1][i & 1]);
        SB0();
        flag(WR + L_FQ);
        sq = lds_read<u32x4>(lds0 + L_SEL);
        RS(8);
        softmax2<true>(pq[0], ph, []() {});
        asm volatile("" : "+v"(ph[0][0]), "+v"(ph[0][1]), "+v"(ph[1][0]), "+v"(ph[1][1]));
        RS(9);
        RS(11);
        xg_def();
        kf_def();
    }
    relane();
    const char* cla = ws + WS_CL + (CLO ? 4096 : 0) + ((rt * 2 + kp) * 16 + l15) * 8;
    const u64 T = (u64)sq[1] << 32 | sq[0];
    u64 cl;
    xg_load(w & 5);
    {
        if ((w & 5) != 4) {
            lds_wait0(lds0 + L_FKF + oh * 4);
#pragma unroll
            for (int i = 0; i < 4; ++i) kf[i] = lds_read<u32x4>(lds0 + L_KF + oh * 4096 + i * 1024 + l16);
#pragma unroll
            for (int i = 0; i < 2; ++i) kf[4 + i] = lds_read<u32x4>(lds0 + L_KZ + oh * 128 + i * 64 + g * 16);
        }
        asm volatile("global_load_dwordx2 %0, %1, off sc1" : "=&v"(cl) : "v"(cla) : "memory");
        RS(27);
        o_store(kf, ph, rsrc, WS_O + ((mt * 2 + hs) * 32 + 2 * hp + oh) * 4096 + (rg & 1) * 2048 + l16);
        RS(28);
    }
    if (WON < 16 && WLK < 0) flag(lds0 + L_FOS + w * 4);
    waitv<3>();
#pragma unroll
    for (int i = 0; i < 6; ++i) pin(kf[i]);
    flag(lds0 + L_FWO + w * 4);
    __builtin_amdgcn_s_setprio(0);
    if (WON < 16 && WLK < 0) {
        if ((w & 5) == 5) {
            lds_wait(lds0 + L_FOS + sch * 4, 1);
            wo_load(WON, 16, true);
        }
    }
    const u32x2 selv = {sq[2], sq[3]};

    RS(30);
    const char* ob = ws + WS_O + (rt * 2 + (rg >> 1)) * 131072 + (rg & 1) * 2048;
    const unsigned ov0 = l16 + kp * 1024, ov1 = l16 + (kp ^ 1) * 1024;
    auto oa = [&](int i) { return ob + ((2 * i + kp + rot) & 31) * 4096; };
    u32x4 of[3][4];
    auto fetch1 = [&](int k, int j) AI {
        if (k < OPL) ld_async(of[k % 3][j], (j & 1) ? ov1 : ov0, oa(2 * k + (j >> 1)));
        else asm volatile("global_load_dwordx4 %0, %1, %2" OPM : "=&v"(of[k % 3][j]) : "v"((j & 1) ? ov1 : ov0), "s"(oa(2 * k + (j >> 1))) : "memory");
    };
    auto fix = [&](int k) AI { if (NOF & 128) { for (int j = 0; j < 4; ++j) pin(of[k % 3][j]); return; }
        if (PP3) settle_p<PP3 + !PP3, PD3>(of[k % 3], [&](int j) { return (j & 1) ? ov1 : ov0; }, [&](int j) { return oa(2 * k + (j >> 1)); });
        else settle(of[k % 3], [&](int j) { return (j & 1) ? ov1 : ov0; }, [&](int j) { return oa(2 * k + (j >> 1)); }); };
    const bool own = (__builtin_amdgcn_readfirstlane(selv[0]) >> 4) == (unsigned)(rt * 8 + rg * 2 + kp);
    float gv_[2];
    f32x2 gb[2][4];
    auto g_mix = [&]() AI {
#pragma unroll
        for (int j = 0; j < 2; ++j) {
            gv_[j] = bf2f((u16)gg[j]);
            const float og = rbf(1.f - gv_[j]);
#pragma unroll
            for (int i = 0; i < 4; ++i) {
                const unsigned t = pack2(og * lo16(xr[j][i]), og * hi16(xr[j][i]));
                gb[j][i] = (f32x2){lo16(t), hi16(t)};
                asm volatile("" : "+v"(gb[j][i]));
            }
        }
    };
    asm volatile("" : "+v"(xr[0]), "+v"(xr[1]), "+v"(gg[0]), "+v"(gg[1]));
#pragma unroll 1
    for (unsigned s = 0; s < SPIN_CAP && __ballot((gg[0] >> 16) != (T & 0xFFFFFFFFFFFFull) || (gg[1] >> 16) != (T & 0xFFFFFFFFFFFFull)); ++s) {
        g_load();
        waitv<0>();
        asm volatile("" : "+v"(gg[0]), "+v"(gg[1]));
    }
    g_mix();
    if (PDL && ((w & 5) != 5 || WLK >= 0)) __builtin_amdgcn_s_sleep(PDL);
    if ((w & 5) != 5) waitv<2>();
    else waitv<(WON < 16 && WLK < 0 ? 3 + 4 * (16 - WON) : 2)>();
    asm volatile("" : "+v"(cl));
#pragma unroll 1
    for (unsigned s = 0; s < SPIN_CAP && __ballot(cl != T); ++s)
        asm volatile("global_load_dwordx2 %0, %1, off sc1\n\ts_waitcnt vmcnt(0)" : "=&v"(cl) : "v"(cla) : "memory");
    RS(16);
#pragma unroll
    for (int j = 0; j < 4; ++j) fetch1(0, j);
    RS(17);
    f32x4 acc[2][4];
    unsigned wa[2][4];
#pragma unroll
    for (int i = 0; i < 8; ++i) {
        wa[i >> 2][i & 3] = ((lds0 + 4096 * kp + (4 * g + q) * 128 + ((((g & 1) << 1) | (q >> 1)) * 32) + 8 * p) ^ ((i & 3) << 5)) + (i >> 2) * 65536;
        asm volatile("" : "+v"(wa[i >> 2][i & 3]));
    }
    s16x4 wf[2][8][2];
    auto wrd = [&](int k, int i) AI {
        const int u = wo_unit(2 * k + (i >> 3));
        wf[k & 1][i >> 1][i & 1] = tr_read(wa[u >> 3][(i >> 1) & 3] + (u & 7) * 8192 + (i & 1) * 2048);
    };
    u32x4 yp[14];
    const unsigned yo = srow * 4096 + cb * 256 + sch * 32;
    auto ya = [&](int i) { return ws + WS_Y + (i >> 1) * 32768 + (i & 1) * 16; };
    lds_wait(lds0 + L_FWD + ((l & 8) ? 16 + sch * 4 : (l & 1) * 4), 1);
    RS(18);
    RS(31);
    if (BPRIO && !kp) __builtin_amdgcn_s_setprio(BPRIO);
#pragma unroll
    for (int i = 0; i < 16; ++i) wrd(0, i);
#pragma unroll
    for (int j = 0; j < 4; ++j) fetch1(1, j);
    waitv<4>();
    fix(0);
    sfor<0, 8>([&](auto K) {
        constexpr int k = K;
        unsigned m[4];
        if constexpr (WON < 16 && 2 * (k + 1) == WON) lds_wait(lds0 + L_FW2 + (l & 1) * 4, 1);
        if constexpr (k == YK) {
            if (own) {
#pragma unroll
                for (int i = 0; i < 14; ++i) ld_async(yp[i], yo, ya(i));
            }
        }
        SB0();
        sfor<0, 16>([&](auto I) {
            constexpr int i = I, j = i >> 2, nb = i & 3;
            const bf16x8 a = cat8(wf[k & 1][(j >> 1) * 4 + nb][0], wf[k & 1][(j >> 1) * 4 + nb][1]);
            if (k == 0 && j < 2) asm volatile("v_mfma_f32_16x16x32_bf16 %0, %1, %2, 0" : "=&v"(acc[j & 1][nb]) : "v"(a), "v"(of[k % 3][j]));
            else asm volatile("v_mfma_f32_16x16x32_bf16 %0, %1, %2, %0" : "+v"(acc[j & 1][nb]) : "v"(a), "v"(of[k % 3][j]));
            SB0();
            if constexpr (k < 7) wrd(k + 1, i);
            if constexpr (k < 6 && i < 4) fetch1(k + 2, i);
            if constexpr (k < 7 && i == BCK) {
                waitv<(k < 6 ? 4 : 0)>();
#pragma unroll
                for (int n = 0; n < 4; ++n) pin(of[(k + 1) % 3][n]);
            }
            if constexpr (k < 7 && i > BCK && i <= BCK + 4) {
                m[i - BCK - 1] = min4(of[(k + 1) % 3][i - BCK - 1]);
                asm volatile("" : "+v"(m[i - BCK - 1]));
            }
            SB0();
        });
        if constexpr (k < 7) {
            if (__builtin_expect(__builtin_amdgcn_ballot_w64(min(min(m[0], m[1]), min(m[2], m[3])) == 0u) != 0ull, 0)) fix(k + 1);
        }
        SB0();
        if (k == 0) RS(19);
        if (k == 3) RS(23);
    });
    asm volatile("s_nop 7");
    SB0();
    RS(20);
    const unsigned ea = lds0 + L_A + l16 + 4096 * w, eb = lds0 + L_A + 4096 * (w ^ 4);
    lds_write<u32x4>(ea, nz4(acc[1][0]));
#pragma unroll
    for (int nb = 1; nb < 4; ++nb) lds_write<f32x4>(ea + nb * 1024, acc[1][nb]);
    if (own) {
        waitv<0>();
        settle(yp, [&](int) { return yo; }, ya);
        f32x4 yv[2];
#pragma unroll
        for (int h = 0; h < 2; ++h) {
            yv[h] = __builtin_bit_cast(f32x4, yp[h]);
#pragma unroll
            for (int i = 1; i < 7; ++i) yv[h] += __builtin_bit_cast(f32x4, yp[2 * i + h]);
#pragma unroll
            for (int i = 0; i < 4; ++i) {
                float t = dpp_add<0x128>(yv[h][i]);
                t = swap16_add(t, t);
                yv[h][i] = rbf(rbf(swap32_add(t, t)) * __uint_as_float(selv[1]));
            }
        }
#pragma unroll
        for (int j = 0; j < 2; ++j) {
            const bool hit = row0 + 8 * j + srow == (int)selv[0];
#pragma unroll
            for (int i = 0; i < 4; ++i) {
                const unsigned t = pack2(lo16(xr[j][i]) + yv[i >> 1][2 * (i & 1)], hi16(xr[j][i]) + yv[i >> 1][2 * (i & 1) + 1]);
                xr[j][i] = hit ? t : xr[j][i];
            }
        }
        g_mix();
    }
    BAR();
    RS(21);
#pragma unroll
    for (int nb = 0; nb < 4; ++nb) acc[0][nb] += lds_read<f32x4>(eb + l16 + nb * 1024);
#pragma unroll
    for (int nb = 0; nb < 4; ++nb) asm volatile("" : "+v"(acc[0][nb]));
#pragma unroll
    for (int nb = 0; nb < 4; ++nb) {
        const u32x2 a = {pack2(acc[0][nb][0], acc[0][nb][1]), pack2(acc[0][nb][2], acc[0][nb][3])};
        lds_write<u32x2>(eb + l15 * 128 + (((2 * nb + (g >> 1)) ^ (l & 7)) * 16) + (g & 1) * 8, a);
    }
#pragma unroll
    for (int j = 0; j < 2; ++j) {
        const u32x4 av = lds_read<u32x4>(eb + j * 1024 + srow * 128 + ((sch ^ srow) * 16));
        u32x4 o;
#pragma unroll
        for (int i = 0; i < 4; ++i) {
            const unsigned ga = pack2(gv_[j] * lo16(av[i]), gv_[j] * hi16(av[i]));
            o[i] = pack2(lo16(ga) + gb[j][i][0], hi16(ga) + gb[j][i][1]);
        }
        asm volatile("global_store_dwordx4 %0, %1, %2" OST "\n\ts_nop 1" ::"v"(xo), "v"(o), "s"(out + (size_t)(row0 + 8 * j) * DM + cb * 64) : "memory");
    }
    RS(22);
    RSDUMP();
}

extern "C" void kernel_launch(void* const* d_in, const int* in_sizes, int n_in, void* d_out, int out_size,
                              void* d_ws, size_t ws_size, hipStream_t stream) {
    fused_mix<<<256, 512, 0, stream>>>((const u16*)d_in[0], (const u16*)d_in[1], (const u16*)d_in[2], (const u16*)d_in[3],
                                       (const u16*)d_in[5], (const u16*)d_in[6], (const u16*)d_in[7], (const u16*)d_in[8],
                                       (const u16*)d_in[9], (char*)d_ws, (u16*)d_out);
}
```

```cpp
#include <hip/hip_runtime.h>
#include <hip/hip_bf16.h>
#include <stdint.h>
#include <type_traits>

typedef __attribute__((ext_vector_type(8))) short bf16x8;
typedef __attribute__((ext_vector_type(4))) short s16x4;
typedef __attribute__((ext_vector_type(4))) float f32x4;
typedef __attribute__((ext_vector_type(2))) float f32x2;
typedef __attribute__((ext_vector_type(2))) unsigned u32x2;
typedef __attribute__((ext_vector_type(4))) unsigned u32x4;
typedef __attribute__((ext_vector_type(2))) __bf16 bf16x2_t;
typedef unsigned short u16;
typedef unsigned long long u64;

#define DI __device__ __forceinline__
#define AS3 __attribute__((address_space(3)))
#define AS1 __attribute__((address_space(1)))
#define RLX_AGENT __ATOMIC_RELAXED, __HIP_MEMORY_SCOPE_AGENT

constexpr int SEQ = 2048, DM = 1024, NH = 32, FF = 3584;

DI float bf2f(u16 v) { return __uint_as_float(((unsigned)v) << 16); }
DI u16 f2bf(float f) { __bf16 b = (__bf16)f; return __builtin_bit_cast(u16, b); }
DI float rbf(float f) { return bf2f(f2bf(f)); }
DI unsigned pack2(float lo, float hi) {
    const f32x2 v = {lo, hi};
    return __builtin_bit_cast(unsigned, __builtin_convertvector(v, bf16x2_t));
}
DI float lo16(unsigned u) { return __uint_as_float(u << 16); }
DI float hi16(unsigned u) { return __uint_as_float(u & 0xffff0000u); }

DI s16x4 tr_read(unsigned a) { return __builtin_amdgcn_ds_read_tr16_b64_v4i16((AS3 s16x4*)(size_t)a); }
DI bf16x8 lds_read128(unsigned a) { return *(AS3 const bf16x8*)(size_t)a; }
template <class T> DI void lds_write(unsigned a, T v) { *(AS3 T*)(size_t)a = v; }
template <class T> DI T lds_read(unsigned a) { return *(AS3 const T*)(size_t)a; }
DI bf16x8 cat8(s16x4 a, s16x4 b) { return __builtin_shufflevector(a, b, 0, 1, 2, 3, 4, 5, 6, 7); }
DI f32x4 mfma16(bf16x8 a, bf16x8 b, f32x4 c) { return __builtin_amdgcn_mfma_f32_16x16x32_bf16(a, b, c, 0, 0, 0); }
DI bf16x8 pack44(const f32x4& a, const f32x4& b) {
    u32x4 r;
    r[0] = pack2(a[0], a[1]);
    r[1] = pack2(a[2], a[3]);
    r[2] = pack2(b[0], b[1]);
    r[3] = pack2(b[2], b[3]);
    return __builtin_bit_cast(bf16x8, r);
}
DI unsigned lds_of(const void* p) { return (unsigned)(size_t)(AS3 const char*)p; }

template <int CTRL>
DI unsigned dpp(unsigned x) { return __builtin_amdgcn_update_dpp(0u, x, CTRL, 0xf, 0xf, true); }
template <int CTRL>
DI float dpp_add(float x) { return x + __uint_as_float(dpp<CTRL>(__float_as_uint(x))); }
DI float swap32_add(float a, float b) {
    auto r = __builtin_amdgcn_permlane32_swap(__float_as_uint(a), __float_as_uint(b), false, false);
    return __uint_as_float(r[0]) + __uint_as_float(r[1]);
}
DI float swap16_add(float a, float b) {
    auto r = __builtin_amdgcn_permlane16_swap(__float_as_uint(a), __float_as_uint(b), false, false);
    return __uint_as_float(r[0]) + __uint_as_float(r[1]);
}
DI float wave_sum(float x) {
    x = dpp_add<0xB1>(x);
    x = dpp_add<0x4E>(x);
    x = dpp_add<0x141>(x);
    x = dpp_add<0x140>(x);
    x = swap16_add(x, x);
    return swap32_add(x, x);
}
DI unsigned wave_umax(unsigned x) {
    x = max(x, dpp<0xB1>(x));
    x = max(x, dpp<0x4E>(x));
    x = max(x, dpp<0x141>(x));
    x = max(x, dpp<0x140>(x));
    auto r = __builtin_amdgcn_permlane16_swap(x, x, false, false);
    x = max(r[0], r[1]);
    auto s = __builtin_amdgcn_permlane32_swap(x, x, false, false);
    return max(s[0], s[1]);
}
template <class OP, class POST>
DI void red2(float& a, float& b, OP op, POST post) {
    auto x = __builtin_amdgcn_permlane32_swap(__float_as_uint(a), __float_as_uint(b), false, false);
    const float m = op(__uint_as_float(x[0]), __uint_as_float(x[1]));
    auto y = __builtin_amdgcn_permlane16_swap(__float_as_uint(m), __float_as_uint(m), false, false);
    const float f = post(op(__uint_as_float(y[0]), __uint_as_float(y[1])));
    auto z = __builtin_amdgcn_permlane32_swap(__float_as_uint(f), __float_as_uint(f), false, false);
    a = __uint_as_float(z[0]);
    b = __uint_as_float(z[1]);
}

#define RS(i) do {} while (0)
#define RSD(i) do {} while (0)
#define RSDUMP() do {} while (0)

constexpr unsigned WS_E = 0;
constexpr unsigned WS_RG = 256;
constexpr unsigned WS_GG = WS_RG + 2048;
constexpr unsigned WS_CL = 32768;
constexpr unsigned WS_Z = 65536;
constexpr unsigned WS_KF = WS_Z + 131072;
constexpr unsigned WS_GU = WS_KF + 262144;
constexpr unsigned WS_Y = WS_GU + 262144;
constexpr unsigned WS_KV = WS_Y + 262144;
constexpr unsigned WS_O = WS_KV + (2u << 20);
constexpr unsigned WS_END = WS_O + (4u << 20);

constexpr int WE = 0;
constexpr int NQ = 10, ND = 6, NX = 16, KD = 78, VD = 84, NF = 64, NF1 = 59, NF2 = 59, NP1 = 12, NP = 14, NM1 = 21, NM2 = 21, NM = 22, KFN = 100;
constexpr int J_S[48] = {0, 0, 0, 1, 1, 1, 2, 2, 2, 3, 3, 3, 4, 4, 4, 5, 5, 5, 6, 6, 6, 7, 7, 7, 8, 8, 8, 9, 9, 9, 10, 10, 11, 11, 12, 12, 13, 14, 15, 13, 14, 15, 10, 11, 12, 13, 14, 15};
constexpr int J_M[48] = {0, 1, 2, 0, 1, 2, 0, 1, 2, 0, 1, 2, 0, 1, 2, 0, 1, 2, 0, 1, 2, 0, 1, 2, 0, 1, 2, 0, 1, 2, 0, 1, 0, 1, 0, 1, 0, 0, 0, 1, 1, 1, 2, 2, 2, 2, 2, 2};
constexpr int J_X[48] = {0, 0, 0, 1, 1, 1, 2, 2, 2, 3, 3, 3, 4, 4, 4, 5, 5, 5, 6, 6, 6, 7, 7, 7, 8, 8, 8, 9, 9, 9, 10, 10, 11, 11, 12, 12, 13, 14, 15, 13, 14, 15, 10, 11, 12, 13, 14, 15};
constexpr int J_U[48] = {2, 4, 8, 18, 11, 12, 17, 15, 13, 0, 1, 6, 4, 8, 9, 11, 12, 14, 15, 13, 7, 1, 6, 5, 8, 9, 10, 12, 14, 16, 13, 7, 1, 6, 4, 8, 18, 14, 15, 13, 7, 3, 0, 1, 6, 5, 2, 4};
constexpr int J_B[48] = {2, 2, 2, 11, 11, 11, 11, 11, 11, 0, 0, 0, 0, 8, 8, 8, 8, 8, 8, 8, 1, 1, 1, 1, 1, 9, 9, 9, 9, 9, 9, 1, 1, 1, 1, 1, 13, 13, 13, 13, 0, 0, 0, 0, 0, 0, 0, 0};
constexpr int Q_C[96] = {1, 0, 2, 0, 3, 0, 4, 0, 5, 0, 6, 0, 7, 0, 0, 0, 0, 0, 8, 0, 0, 0, 0, 0, 9, 0, 0, 0, 0, 0, 10, 0, 0, 0, 0, 0, 11, 0, 0, 0, 0, 0, 12, 0, 0, 0, 0, 0, 13, 0, 0, 0, 0, 0, 14, 0, 0, 0, 0, 0, 15, 0, 0, 0, 0, 0, 16, 0, 0, 0, 0, 0, 17, 0, 0, 0, 0, 0, 18, 0, 19, 0, 20, 0, 21, 0, 22, 0, 0, 0, 0, 0, 0, 0, 0, 0};
constexpr int X_U0[16] = {6, 9, 14, 7, 5, 10, 16, 3, 2, 18, 17, 3, 5, 9, 11, 16};
constexpr int X_U1[16] = {5, 10, 16, 3, 2, 18, 17, 0, 4, 11, 15, 0, 2, 10, 12, 17};
constexpr int P_U[4] = {11, 12, 9, 10};
constexpr int W_U[16] = {8, 14, 15, 13, 7, 3, 9, 10, 11, 12, 0, 1, 6, 5, 2, 4};
constexpr int W_N[16] = {100, 100, 100, 100, 100, 100, 101, 101, 101, 101, 13, 13, 14, 14, 99, 99};
constexpr int F_J[64] = {-1, 0, 1, 2, -2, 3, 4, 5, -3, 6, 7, 8, -4, 9, 10, 11, -5, 12, 13, 14, -6, 15, 16, 17, -7, 18, 19, 20, -8, 21, 22, 23, -9, 24, 25, 26, -10, 27, 28, 29, -11, 30, 31, -12, 32, 33, -13, 34, 35, -14, 36, -15, 37, -16, 38, 39, 40, 41, 42, 43, 44, 45, 46, 47};
constexpr int F_N[64] = {0, 0, 0, 0, 0, 0, 0, 0, 0, 0, 0, 0, 0, 0, 0, 1, 1, 1, 1, 1, 2, 2, 2, 2, 3, 3, 3, 3, 4, 4, 4, 4, 5, 5, 5, 5, 6, 6, 6, 6, 7, 7, 7, 7, 8, 8, 8, 8, 9, 9, 9, 10, 10, 10, 10, 11, 11, 11, 11, 12, 12, 12, 12, 12};
constexpr int F_K[64] = {0, 1, 2, 3, 0, 4, 5, 6, 0, 0, 0, 7, 0, 0, 0, 8, 0, 0, 0, 9, 0, 0, 0, 10, 0, 0, 0, 11, 0, 0, 0, 12, 0, 0, 0, 13, 0, 0, 0, 14, 0, 0, 0, 0, 15, 0, 0, 0, 16, 0, 0, 0, 0, 0, 17, 18, 19, 20, 21, 0, 0, 0, 0, 22};
constexpr int E_A[384] = {300, 325, 350, 375, 0, 0, 0, 502, 0, 0, 0, 403, 0, 0, 0, 503, 201, 251, 0, 404, 0, 100, 101, 504, 301, 326, 351, 376, 0, 0, 0, 505, 601, 0, 0, 406, 0, 0, 0, 506, 202, 252, 0, 407, 0, 0, 0, 507, 302, 327, 352, 377, 0, 0, 0, 602, 0, 0, 0, 0, 0, 0, 0, 203, 253, 0, 0, 408, 0, 100, 101, 508, 303, 328, 353, 378, 0, 0, 0, 603, 0, 0, 0, 0, 0, 0, 0, 204, 254, 0, 0, 409, 0, 0, 0, 509, 304, 329, 354, 379, 0, 100, 101, 604, 102, 103, 0, 0, 0, 0, 0, 205, 255, 0, 0, 410, 0, 0, 0, 510, 305, 330, 355, 380, 0, 0, 0, 605, 0, 0, 0, 0, 0, 0, 0, 206, 256, 0, 0, 411, 0, 0, 0, 511, 306, 331, 356, 381, 0, 0, 0, 606, 0, 0, 0, 0, 0, 100, 101, 102, 103, 207, 257, 412, 0, 0, 0, 512, 307, 332, 357, 382, 0, 0, 0, 607, 0, 0, 0, 0, 0, 0, 0, 208, 258, 0, 0, 413, 0, 0, 0, 513, 308, 333, 358, 383, 0, 100, 101, 608, 102, 103, 0, 0, 0, 0, 0, 209, 259, 0, 0, 414, 0, 0, 0, 514, 309, 334, 359, 384, 0, 0, 0, 609, 0, 0, 0, 0, 0, 0, 0, 210, 260, 0, 0, 415, 0, 0, 0, 515, 310, 335, 360, 385, 0, 100, 101, 610, 102, 103, 211, 261, 0, 0, 0, 0, 311, 336, 361, 386, 0, 0, 0, 516, 611, 212, 262, 0, 0, 0, 0, 0, 312, 337, 362, 387, 0, 0, 0, 213, 263, 0, 0, 417, 0, 100, 101, 517, 313, 338, 363, 388, 214, 264, 0, 612, 314, 339, 364, 389, 215, 265, 0, 0, 315, 340, 365, 390, 0, 0, 0, 518, 0, 0, 0, 419, 0, 100, 101, 519, 102, 103, 0, 420, 0, 0, 0, 520, 0, 0, 0, 0, 0, 0, 0, 0, 0, 0, 0, 422, 0, 0, 0, 522, 0, 0, 0, 0, 0, 0, 0, 0, 0, 0, 0, 613, 0, 0, 0, 0, 0, 0, 0, 0, 0, 0, 0, 0, 0, 0, 0, 614, 0, 0, 0, 0, 0, 0, 0, 0, 0, 0, 0, 0};
constexpr int E_B[384] = {0, 0, 402, 0, 0, 0, 0, 0, 0, 0, 0, 0, 0, 0, 0, 0, 0, 0, 0, 0, 0, 0, 0, 102, 103, 0, 405, 0, 0, 0, 0, 0, 0, 0, 0, 0, 0, 0, 0, 0, 0, 0, 0, 0, 0, 0, 0, 0, 0, 0, 0, 0, 0, 0, 0, 0, 0, 0, 0, 0, 0, 0, 0, 0, 0, 0, 0, 0, 0, 0, 0, 102, 103, 0, 0, 0, 0, 0, 0, 0, 0, 0, 0, 0, 0, 0, 0, 0, 0, 0, 0, 0, 0, 0, 0, 0, 0, 0, 0, 0, 0, 0, 0, 0, 0, 0, 0, 0, 0, 0, 0, 0, 0, 0, 0, 0, 0, 0, 0, 0, 0, 0, 0, 0, 0, 0, 0, 0, 0, 0, 0, 0, 0, 0, 0, 0, 0, 0, 0, 0, 0, 0, 0, 0, 0, 0, 0, 0, 0, 0, 0, 0, 0, 0, 0, 0, 0, 0, 0, 0, 0, 0, 0, 0, 0, 0, 0, 0, 0, 0, 0, 0, 0, 0, 0, 0, 0, 0, 0, 0, 0, 0, 0, 0, 0, 0, 0, 0, 0, 0, 0, 0, 0, 0, 0, 0, 0, 0, 0, 0, 0, 0, 0, 0, 0, 0, 0, 0, 0, 0, 0, 0, 0, 0, 0, 0, 0, 0, 0, 0, 0, 0, 0, 0, 0, 0, 0, 0, 0, 0, 0, 0, 0, 0, 0, 0, 0, 0, 0, 0, 0, 0, 0, 0, 0, 0, 0, 0, 0, 0, 0, 0, 0, 0, 0, 0, 0, 0, 416, 0, 0, 0, 0, 0, 0, 0, 0, 0, 0, 0, 0, 0, 0, 0, 0, 0, 0, 0, 0, 0, 0, 0, 0, 0, 0, 0, 0, 102, 103, 0, 0, 0, 0, 0, 0, 0, 0, 0, 0, 0, 0, 0, 0, 0, 0, 0, 418, 0, 0, 0, 0, 0, 0, 0, 0, 0, 0, 0, 0, 0, 0, 0, 0, 0, 0, 0, 0, 0, 0, 0, 0, 0, 0, 0, 0, 0, 0, 0, 0, 0, 0, 0, 0, 0, 0, 0, 0, 0, 0, 0, 0, 0, 0, 0, 0, 0, 0, 0, 0, 0, 0, 0, 0, 0, 0, 0, 0, 0, 0, 0, 0, 0, 0, 0, 0, 0, 0, 0, 0, 0, 0, 0, 0, 0};
#define LH 0
constexpr unsigned L_A = 16 * 8192;
constexpr unsigned L_KF = 18 * 8192;
constexpr unsigned L_SM = 19 * 8192;
constexpr unsigned L_FULL = L_SM + 256;
constexpr unsigned L_FVR = L_SM + 320, L_PRL = L_SM + 336;
constexpr unsigned L_PROG = L_SM + 896;
constexpr unsigned L_FW2 = L_SM + 512, L_FOS = L_SM + 480, L_FGI = L_SM + 520, L_FK0 = L_SM + 752, L_FBI = L_SM + 744;
constexpr unsigned L_FK = L_SM + 528, L_FKT = L_SM + 544, L_FV = L_SM + 560, L_FP = L_SM + 576, L_FKS = L_SM + 592;
constexpr unsigned L_FQD = L_SM + 608, L_FG = L_SM + 624, L_FKF = L_SM + 736, L_FQ = L_SM + 720, L_FWD = L_SM + 640, L_FWO = L_SM + 656, L_FLD = L_SM + 688, L_BEST = L_SM + 696;
constexpr unsigned L_SEL = L_SM + 704;
constexpr unsigned L_XS = L_SM + 2048, L_KZ = L_SM + 3072, L_WPHI = L_SM + 4096;
static_assert(NP <= 31 && NM <= 63, "");
static_assert(P_U[1] == P_U[0] + 1 && P_U[3] == P_U[2] + 1, "feature maps of a head lie side by side");
constexpr int wo_unit(int i) { return W_U[i]; }
constexpr int f_u(int f) { return f & 3; }
constexpr bool j_first(int j) {
    for (int i = 0; i < j; ++i)
        if (J_M[i] == J_M[j]) return false;
    return true;
}
constexpr int younger(int p) {
    int c = 0;
    for (int n = 0; n < NF1 - LH; ++n)
        if (F_N[n] >= p) c += F_J[n] < 0 ? 8 : 4;
    return c;
}
constexpr int nfv() {
    for (int n = 0; n < NF1; ++n)
        if (F_J[n] == VD / 2 - 1) return n + 1;
    return NF1;
}
constexpr int next_need(int n) {
    for (int i = n; i < NF; ++i)
        if (F_N[i] != F_N[n]) return F_N[i];
    return 99;
}
constexpr int x_stage(int i) { return i < 16 ? i : i - 16 + NQ; }
constexpr unsigned SPIN_CAP = 1u << 16;
#define N8(a) a, a + 1, a + 2, a + 3, a + 4, a + 5, a + 6, a + 7
__device__ const unsigned k_num[64] = {N8(0), N8(8), N8(16), N8(24), N8(32), N8(40), N8(48), N8(56)};

#define RK(i) do {} while (0)
#define RSL(i) do {} while (0)
#define RSC(i) do {} while (0)
#define CSN 6
#define CFP 0
#define QWM 0
#define RLD 11
#define CLF 3
#define CLT 1
#define FLV 1
#define SP1 0
#define SP2 0
#define SP3 0
#define SP4 0
#define SPW 0
#define SPI "s_load_dwordx2"
#define DNP 1
#define WOT 1
#define YT 1
#define GDL 16
#define SSM 0
#define CLE 1
#define GUO 0
#define TL0 0
#define TL0W ""
#define TLD 0
#define SPA 0
#define SPS 7
#define MRG 0
#define PP1 0
#define PD1 0
#define PP2 0
#define PD2 0
#define PP3 0
#define PD3 0
#define PDI 0
#define HLD " sc1"
#define HST " sc1"
#define HSA 16
#define BMAP 0
#define ROTC 0
#define ROT0 0
#define LPRIO 0
#define LPRW 3
#define LTH 63
#define LT0 14
#define PEEK 1
#define CSP 1
#define GUA 12
#define OPL 8
#define OPM ""
#define PD 4
#define NPH 4
#define KSK 3
#define ROTK 8
#define OWIN 2
#define CLR0 4
#define CLS 1
#define CLP 2
#define SLK 2
#define CPH 3
#define NPL 0
#define CPRIO 1
#define HPRIO 2
#define KPK 3
#define KRND 1
#define QW 1
#define WOG 1
#define WON 12
#define KFD 24
#define SEL0 9
#define SPRIO 2
#define FBA 3
#define FBB 10
#define FBS 1
#define FBL 1
#define WOP 0
#define GTL 3
#define WPF 0
#define WOE 6
#define GW 0
#define GUF 0
#define FBH 0
#define WG2 0
#define NTX 0
#define NTW 0
#define NTS 16
#define NTO ""
#define GQ1 4
#define GQ2 8
#define CLO 0
#define PDL 6
#define XK 3
#define YK 7
#define BPRIO 0
#define AMF 1
#define LF 1
#define SH 2
#define LC 0
#define SPR2 0
#define GQ0 0
#define CPF 53248
#define NF0 4
#define OTL 0
#define OST " sc1"
#define TLW 3
#define THR -1
#define THQ -1
#define FDH 0
#define WLK -1
#define XGE 1
#define KXO 3
#define KXM 3
#define KX8 0
#define KXB 0xFEDBA9C768534210ull
#define BCK 10
#define GPR 0
#define GHL 0
#define TGT 1
#define PORD 1
#define TRM 3
#define LFE 1
#define PWN 0
#define PWL 99
#define TRN 0
#define RT0 0
#define BSP 0
#define WOV 0
#define GUP 1
#define NOF 0
#define FBD 8
#define BAR() asm volatile("s_waitcnt lgkmcnt(0)\n\ts_barrier" ::: "memory")
template <int N>
DI void waitv() { asm volatile("s_waitcnt vmcnt(%0)" ::"n"(N) : "memory"); }

DI u64 ld_agent(const char* p) { return __hip_atomic_load((const AS1 u64*)p, RLX_AGENT); }
DI void st_agent(char* p, u64 v) { __hip_atomic_store((AS1 u64*)p, v, RLX_AGENT); }
DI unsigned nz1(unsigned v) { return max(v, 1u); }
DI unsigned umin(unsigned a, unsigned b) { return min(a, b); }
DI u32x4 nz4(u32x4 v) { return (u32x4){max(v[0], 1u), max(v[1], 1u), max(v[2], 1u), max(v[3], 1u)}; }
DI u32x4 nz4(f32x4 v) { return nz4(__builtin_bit_cast(u32x4, v)); }
DI unsigned min4(u32x4 v) { return min(min(v[0], v[1]), min(v[2], v[3])); }
DI u32x4 ld_wt(__amdgpu_buffer_rsrc_t r, unsigned off) { return __builtin_amdgcn_raw_buffer_load_b128(r, off, 0, 16); }
DI void st_wt(__amdgpu_buffer_rsrc_t r, unsigned off, u32x4 v) { __builtin_amdgcn_raw_buffer_store_b128(v, r, off, 0, HSA); }
DI void ld_async(u32x4& v, unsigned off, const char* base) {
    asm volatile("global_load_dwordx4 %0, %1, %2 sc1" : "=&v"(v) : "v"(off), "s"(base) : "memory");
}
#define ST_MASK(op, m, off, v, base) asm volatile("s_mov_b64 exec, %3\n\t" op " %0, %1, %2" HST "\n\ts_mov_b64 exec, -1\n\ts_nop 0" ::"v"(off), "v"(v), "s"(base), "s"((u64)(m)) : "memory")
DI void pin(u32x4& v) { asm volatile("" : "+v"(v)); }
template <int N, class OFF, class BASE, class OK>
DI void settle(u32x4 (&v)[N], OFF off, BASE base, OK ok) {
#pragma unroll
    for (int i = 0; i < N; ++i) pin(v[i]);
#pragma unroll 1
    for (unsigned s = 0; s < SPIN_CAP; ++s) {
        unsigned m = ok(v[0]);
#pragma unroll
        for (int i = 1; i < N; ++i) m = min(m, ok(v[i]));
        if (__ballot(m == 0u) == 0ull) break;
#pragma unroll
        for (int i = 0; i < N; ++i) {
            u64 keep;
            asm volatile("s_mov_b64 %1, exec\n\ts_mov_b64 exec, %4\n\tglobal_load_dwordx4 %0, %2, %3 sc1\n\ts_mov_b64 exec, %1"
                         : "+&v"(v[i]), "=&s"(keep) : "v"(off(i)), "s"(base(i)), "s"((u64)__ballot(ok(v[i]) == 0u)) : "memory");
        }
        waitv<0>();
#pragma unroll
        for (int i = 0; i < N; ++i) pin(v[i]);
    }
}
template <int N, class OFF, class BASE>
DI void settle(u32x4 (&v)[N], OFF off, BASE base) { settle(v, off, base, [](const u32x4& a) { return min4(a); }); }
template <int P, int D, int N, class OFF, class BASE>
DI void settle_p(u32x4 (&v)[N], OFF off, BASE base) {
#pragma unroll 1
    for (unsigned s = 0;; ++s) {
        u64 miss[N], any = 0;
#pragma unroll
        for (int i = 0; i < N; ++i) {
            pin(v[i]);
            miss[i] = __ballot(min4(v[i]) == 0u);
            any |= miss[i];
        }
        if (s >= SPIN_CAP) any = 0;
        asm volatile("" : "+s"(any));
        if (any == 0ull) break;
#pragma unroll
        for (int i = 0; i < N; ++i)
            asm volatile("s_mov_b64 exec, %3\n\tglobal_load_dwordx4 %0, %1, %2" HLD "\n\ts_mov_b64 exec, -1"
                         ::"v"(v[i]), "v"(off(i)), "s"(base(i)), "s"(miss[i] | 1ull) : "memory");
        if (D) __builtin_amdgcn_s_sleep(D);
        waitv<N * (P - 1)>();
    }
}
DI void lds_wait(unsigned a, unsigned n) {
    unsigned v, c;
    asm volatile("s_mov_b32 %1, 0x400000\n"
                 ".Lp%=:\n\tds_read_b32 %0, %2\n\ts_waitcnt lgkmcnt(0)\n\tv_cmp_gt_u32 vcc, %3, %0\n\ts_cbranch_vccz .Lf%=\n\t"
                 "s_sleep 1\n\ts_sub_u32 %1, %1, 1\n\ts_cbranch_scc0 .Lp%=\n.Lf%=:"
                 : "=&v"(v), "=&s"(c) : "v"(a), "s"(n) : "memory", "vcc", "scc");
}
DI void lds_wait0(unsigned a) {
    if (!TGT) { lds_wait(a, 1); return; }
    unsigned v, c;
    asm volatile("s_mov_b32 %1, 0x1000000\n"
                 ".Lp%=:\n\tds_read_b32 %0, %2\n\ts_sub_u32 %1, %1, 1\n\ts_cbranch_scc1 .Lf%=\n\ts_waitcnt lgkmcnt(0)\n\tv_cmp_eq_u32 vcc, 0, %0\n\ts_cbranch_vccnz .Lp%=\n"
                 ".Lf%=:\n\ts_waitcnt lgkmcnt(0)"
                 : "=&v"(v), "=&s"(c) : "v"(a) : "memory", "vcc", "scc");
}
DI void lds_wait8(unsigned a, unsigned n) {
    unsigned v, c;
    asm volatile("s_mov_b32 %1, 0x100000\n"
                 ".Lp%=:\n\tds_read_b32 %0, %2\n\ts_waitcnt lgkmcnt(0)\n\tv_cmp_gt_u32 vcc, %3, %0\n\ts_cbranch_vccz .Lf%=\n\t"
                 "s_sleep 4\n\ts_sub_u32 %1, %1, 1\n\ts_cbranch_scc0 .Lp%=\n.Lf%=:"
                 : "=&v"(v), "=&s"(c) : "v"(a), "s"(n) : "memory", "vcc", "scc");
}
DI void lds_peek(unsigned& v, unsigned a) { asm volatile("ds_read_b32 %0, %1" : "=v"(v) : "v"(a) : "memory"); }
DI void lds_wait(unsigned& v, unsigned a, unsigned n) {
    unsigned c;
    asm volatile("s_mov_b32 %1, 0x400000\n"
                 ".Lp%=:\n\ts_waitcnt lgkmcnt(0)\n\tv_cmp_gt_u32 vcc, %3, %0\n\ts_cbranch_vccz .Lf%=\n\t"
                 "ds_read_b32 %0, %2\n\ts_sub_u32 %1, %1, 1\n\ts_cbranch_scc0 .Lp%=\n.Lf%=:"
                 : "+v"(v), "=&s"(c) : "v"(a), "s"(n) : "memory", "vcc", "scc");
}
DI void lds_set(unsigned a) {
    asm volatile("s_waitcnt lgkmcnt(0)\n\tds_write_b32 %0, %1" ::"v"(a), "v"(1u) : "memory");
}
DI float dot2(unsigned a, unsigned b, float c) {
    return __builtin_amdgcn_fdot2_f32_bf16(__builtin_bit_cast(bf16x2_t, a), __builtin_bit_cast(bf16x2_t, b), c, false);
}
DI float sum4(const float (&a)[4]) {
    float c = swap16_add(swap32_add(a[0], a[1]), swap32_add(a[2], a[3]));
    c = dpp_add<0xB1>(c);
    c = dpp_add<0x4E>(c);
    c = dpp_add<0x141>(c);
    return dpp_add<0x140>(c);
}

template <class OP, class POST>
DI void red4(float& a, float& b, float& c, float& d, OP op, POST post) {
    auto x = __builtin_amdgcn_permlane32_swap(__float_as_uint(a), __float_as_uint(b), false, false);
    auto y = __builtin_amdgcn_permlane32_swap(__float_as_uint(c), __float_as_uint(d), false, false);
    const float m = op(__uint_as_float(x[0]), __uint_as_float(x[1])), n = op(__uint_as_float(y[0]), __uint_as_float(y[1]));
    auto z = __builtin_amdgcn_permlane16_swap(__float_as_uint(m), __float_as_uint(n), false, false);
    const unsigned f = __float_as_uint(post(op(__uint_as_float(z[0]), __uint_as_float(z[1]))));
    auto u = __builtin_amdgcn_permlane16_swap(f, f, false, false);
    auto v = __builtin_amdgcn_permlane32_swap(u[0], u[0], false, false);
    auto t = __builtin_amdgcn_permlane32_swap(u[1], u[1], false, false);
    a = __uint_as_float(v[0]);
    b = __uint_as_float(v[1]);
    c = __uint_as_float(t[0]);
    d = __uint_as_float(t[1]);
}
DI float mx2(float a, float b) { return __builtin_elementwise_maximum(a, b); }
DI float opq(float a) { asm("" : "+v"(a)); return a; }
template <bool PK, class HOOK>
DI void softmax2(const f32x4 (&v)[2][2], u32x4 (&ph)[2][2], HOOK hook) {
    constexpr float L2E = 1.4426950408889634f;
    float mx[2], mn[2], sp_[2], sn_[2];
#pragma unroll
    for (int b = 0; b < 2; ++b) {
        const f32x4 s = v[b][0], t = v[b][1];
        mx[b] = mx2(mx2(mx2(mx2(s[0], s[1]), s[2]), mx2(mx2(s[3], t[0]), t[1])), mx2(t[2], t[3]));
        mn[b] = mx2(mx2(mx2(mx2(-s[0], -s[1]), -s[2]), mx2(mx2(-s[3], -t[0]), -t[1])), mx2(-t[2], -t[3]));
    }
    red4(mx[0], mx[1], mn[0], mn[1], [](float a, float b) { return mx2(a, b); }, [](float a) { return a * -L2E; });
    f32x2 ep[2][4], en[2][4];
#pragma unroll
    for (int b = 0; b < 2; ++b) {
#pragma unroll
        for (int e = 0; e < 4; ++e) {
            const f32x2 u = {v[b][e >> 1][2 * (e & 1)], v[b][e >> 1][2 * (e & 1) + 1]};
            f32x2 a, c;
            if (PK) {
                a = __builtin_elementwise_fma(u, (f32x2){L2E, L2E}, (f32x2){mx[b], mx[b]});
                c = __builtin_elementwise_fma(u, (f32x2){-L2E, -L2E}, (f32x2){mn[b], mn[b]});
            } else {
                a = (f32x2){__builtin_fmaf(u[0], L2E, mx[b]), __builtin_fmaf(u[1], L2E, mx[b])};
                c = (f32x2){__builtin_fmaf(u[0], -L2E, mn[b]), __builtin_fmaf(u[1], -L2E, mn[b])};
            }
            ep[b][e] = (f32x2){__builtin_amdgcn_exp2f(a[0]), __builtin_amdgcn_exp2f(a[1])};
            en[b][e] = (f32x2){__builtin_amdgcn_exp2f(c[0]), __builtin_amdgcn_exp2f(c[1])};
        }
        if (PK) {
            const f32x2 s = (ep[b][0] + ep[b][1]) + (ep[b][2] + ep[b][3]), t = (en[b][0] + en[b][1]) + (en[b][2] + en[b][3]);
            sp_[b] = s[0] + s[1];
            sn_[b] = t[0] + t[1];
        } else {
            sp_[b] = ep[b][0][0];
            sn_[b] = en[b][0][0];
#pragma unroll
            for (int e = 1; e < 8; ++e) sp_[b] += ep[b][e >> 1][e & 1], sn_[b] += en[b][e >> 1][e & 1];
        }
    }
    asm volatile("" : "+v"(sp_[0]), "+v"(sn_[0]), "+v"(sp_[1]), "+v"(sn_[1]));
    hook();
    red4(sp_[0], sp_[1], sn_[0], sn_[1], [](float a, float b) { return a + b; }, [](float a) { return __builtin_amdgcn_rcpf(a); });
#pragma unroll
    for (int b = 0; b < 2; ++b)
#pragma unroll
        for (int e = 0; e < 4; ++e) {
            if (PK) {
                const f32x2 a = ep[b][e] * (f32x2){sp_[b], sp_[b]}, c = en[b][e] * (f32x2){sn_[b], sn_[b]};
                ph[b][0][e] = pack2(a[0], a[1]);
                ph[b][1][e] = pack2(c[0], c[1]);
            } else {
                ph[b][0][e] = pack2(opq(ep[b][e][0] * sp_[b]), ep[b][e][1] * sp_[b]);
                ph[b][1][e] = pack2(opq(en[b][e][0] * sn_[b]), en[b][e][1] * sn_[b]);
            }
        }
}
DI f32x4 rbf4(const f32x4& a) {
    const unsigned u = pack2(a[0], a[1]), v = pack2(a[2], a[3]);
    return (f32x4){lo16(u), hi16(u), lo16(v), hi16(v)};
}
DI int fsw(int row) { return (((row >> 1) & 3) << 2) | (((row >> 3) & 1) << 1) | (row & 1); }
template <bool PK, class HOOK>
DI void phi_k(const f32x4 (&pp)[2][2], const unsigned (&pw)[4], unsigned off, HOOK hook) {
    u32x4 ph[2][2];
    softmax2<PK>(pp, ph, hook);
#pragma unroll
    for (int b = 0; b < 2; ++b)
#pragma unroll
        for (int kf = 0; kf < 2; ++kf)
#pragma unroll
            for (int fb = 0; fb < 2; ++fb) {
                const u32x2 a = {ph[b][kf][2 * fb], ph[b][kf][2 * fb + 1]};
                lds_write<u32x2>(pw[2 * kf + fb] + off + b * 2048, a);
            }
}

DI void phi_k1(const f32x4 (&v)[2], const unsigned (&pw)[4]) {
    constexpr float L2E = 1.4426950408889634f;
    const f32x4 s = v[0], t = v[1];
    float mx = mx2(mx2(mx2(mx2(s[0], s[1]), s[2]), mx2(mx2(s[3], t[0]), t[1])), mx2(t[2], t[3]));
    float mn = mx2(mx2(mx2(mx2(-s[0], -s[1]), -s[2]), mx2(mx2(-s[3], -t[0]), -t[1])), mx2(-t[2], -t[3]));
    red2(mx, mn, [](float a, float b) { return mx2(a, b); }, [](float a) { return a * -L2E; });
    f32x2 ep[4], en[4];
#pragma unroll
    for (int e = 0; e < 4; ++e) {
        const f32x2 u = {v[e >> 1][2 * (e & 1)], v[e >> 1][2 * (e & 1) + 1]};
        const f32x2 a = __builtin_elementwise_fma(u, (f32x2){L2E, L2E}, (f32x2){mx, mx});
        const f32x2 c = __builtin_elementwise_fma(u, (f32x2){-L2E, -L2E}, (f32x2){mn, mn});
        ep[e] = (f32x2){__builtin_amdgcn_exp2f(a[0]), __builtin_amdgcn_exp2f(a[1])};
        en[e] = (f32x2){__builtin_amdgcn_exp2f(c[0]), __builtin_amdgcn_exp2f(c[1])};
    }
    const f32x2 a = (ep[0] + ep[1]) + (ep[2] + ep[3]), c = (en[0] + en[1]) + (en[2] + en[3]);
    float sp = a[0] + a[1], sn = c[0] + c[1];
    red2(sp, sn, [](float a, float b) { return a + b; }, [](float a) { return __builtin_amdgcn_rcpf(a); });
#pragma unroll
    for (int i = 0; i < 4; ++i) {
        const float r = i < 2 ? sp : sn;
        const f32x2 lo = (i < 2 ? ep[2 * i] : en[2 * i - 4]) * (f32x2){r, r}, hi = (i < 2 ? ep[2 * i + 1] : en[2 * i - 3]) * (f32x2){r, r};
        lds_write<u32x2>(pw[i], (u32x2){pack2(lo[0], lo[1]), pack2(hi[0], hi[1])});
    }
}

DI void waitvn(int n) {
#define WV(k) case k: waitv<k>(); break;
#define WV8(k) WV(k) WV(k + 1) WV(k + 2) WV(k + 3) WV(k + 4) WV(k + 5) WV(k + 6) WV(k + 7)
    switch (n < 63 ? n : 63) { WV8(0) WV8(8) WV8(16) WV8(24) WV8(32) WV8(40) WV8(48) WV8(56) }
}
#define SB0() __builtin_amdgcn_sched_barrier(0)
template <int A, int B, class F>
DI void sfor(F f) {
    if constexpr (A < B) {
        f(std::integral_constant<int, A>{});
        sfor<A + 1, B>(f);
    }
}
template <int N> using IC = std::integral_constant<int, N>;
#define AI __attribute__((always_inline))
#define RSK(b, k) do { switch (k) { case 0: RS(b); break; case 1: RS(b + 1); break; case 2: RS(b + 2); break; case 3: RS(b + 3); break; \
    case 4: RS(b + 4); break; case 5: RS(b + 5); break; case 6: RS(b + 6); break; case 7: RS(b + 7); break; } } while (0)

DI void o_store(const u32x4 (&kf)[6], const u32x4 (&ph)[2][2], __amdgpu_buffer_rsrc_t rsrc, unsigned o_st) {
    const f32x4 zero = {0.f, 0.f, 0.f, 0.f};
#pragma unroll
    for (int i = 0; i < 2; ++i) {
        const bf16x8 p0 = __builtin_bit_cast(bf16x8, ph[i][0]), p1 = __builtin_bit_cast(bf16x8, ph[i][1]);
        f32x4 dn = mfma16(__builtin_bit_cast(bf16x8, kf[4]), p0, zero);
        f32x4 n0 = mfma16(__builtin_bit_cast(bf16x8, kf[0]), p0, zero);
        f32x4 n1 = mfma16(__builtin_bit_cast(bf16x8, kf[1]), p0, zero);
        dn = mfma16(__builtin_bit_cast(bf16x8, kf[5]), p1, dn);
        n0 = mfma16(__builtin_bit_cast(bf16x8, kf[2]), p1, n0);
        n1 = mfma16(__builtin_bit_cast(bf16x8, kf[3]), p1, n1);
        const float rd_ = __builtin_amdgcn_rcpf(rbf(rbf(dn[0]) + 1e-6f));
        const u32x4 nb_ = __builtin_bit_cast(u32x4, pack44(n0, n1));
        u32x4 o;
#pragma unroll
        for (int j = 0; j < 4; ++j) o[j] = pack2(lo16(nb_[j]) * rd_, hi16(nb_[j]) * rd_);
        st_wt(rsrc, o_st + i * 1024, nz4(o));
    }
}

typedef __attribute__((ext_vector_type(16))) unsigned u32x16;
__global__ __launch_bounds__(512, 2) void fused_mix(const u16*, const u16*, const u16*, const u16*, const u16*, const u16*, const u16*, const u16*,
                                                    const u16*, char*, u16*) {
    __shared__ __attribute__((aligned(16))) char smem[163840];
    u32x4 ka;
    u32x16 kb_;
    u64 kz;
    asm volatile("s_load_dwordx4 %0, %3, 0x0\n\ts_load_dwordx16 %1, %3, 0x10\n\ts_load_dwordx2 %2, %3, 0x50"
                 : "=&s"(ka), "=&s"(kb_), "=&s"(kz) : "s"(__builtin_amdgcn_kernarg_segment_ptr()));
    SB0();
    const int tid = threadIdx.x;
    int l = tid & 63;
    const int w = __builtin_amdgcn_readfirstlane(tid >> 6) ^ 4;
    int bid = blockIdx.x;
    const unsigned lds0 = lds_of(smem);
    int g = l >> 4, l15 = l & 15, q = l15 >> 2, p = l & 3;
    int srow = l >> 3, sch = l & 7;
    unsigned l16 = l * 16;
    unsigned vst = 0;
    const int rg = w & 3, kp = w >> 2;
    const int hs = rg >> 1;
    RS(0);
    if (CPF) {
        u64 pc;
        asm volatile("s_getpc_b64 %0\n\ts_mov_b32 m0, %2\n\ts_mov_b64 exec, 3\n\tglobal_load_lds_dword %1, %0\n\ts_mov_b64 exec, -1"
                     : "=&s"(pc) : "v"(min((unsigned)(((bid & ~7) + w) * 2 + l) * 128u, (unsigned)CPF)), "s"(lds0 + L_SM + 3584) : "memory");
    }
    lds_write<unsigned>(lds0 + L_SM + tid * 4, 0u);
    const int slot = bid >> 3;
    const int mt = (bid & 1) * 8 + (slot & 7);
    const int hp = ((bid >> 1) & 3) * 4 + (slot >> 3);
    unsigned vx[4], vw[4];
    auto vw_init = [&]() AI {
        int t = l;
        asm volatile("" : "+v"(t));
#pragma unroll
        for (int i = 0; i < 4; ++i) vw[i] = (unsigned)((8 * i + (t >> 3)) * 6144 + ((t & 7) ^ (((t >> 4) & 1) << 1) ^ ((i & 1) << 2)) * 16 + 4096 - i * 1024);
    };
    auto vx_init = [&]() AI {
        int t = l;
        asm volatile("" : "+v"(t));
#pragma unroll
        for (int i = 0; i < 4; ++i) vx[i] = (unsigned)((8 * i + (t >> 3)) * 2048 + (((t & 7) ^ ((4 * (i & 1) + (t >> 4)) & 7)) * 16) + 4096 - i * 1024);
    };
    vx_init();
    vw_init();
    asm volatile("" : "+v"(vx[0]), "+v"(vx[1]), "+v"(vx[2]), "+v"(vx[3]), "+v"(vw[0]), "+v"(vw[1]), "+v"(vw[2]), "+v"(vw[3]));
    const int ksa = SLK * (slot & 7), ksb = KSK * ((slot + (slot >> 3)) & (NPH - 1));
    unsigned xo0 = (mt * 128 + hs * 64) * 2048, wo0 = hp * 128 + hs * (32 * 6144);
    asm volatile("s_waitcnt lgkmcnt(0)" : "+s"(ka), "+s"(kb_), "+s"(kz), "+s"(xo0), "+s"(wo0) :: "memory");
    RS(48);
    const u16* x = (const u16*)((u64)ka[1] << 32 | ka[0]);
    const u16* wqkv = (const u16*)((u64)ka[3] << 32 | ka[2]);
    auto arg = [&](int i) { return (char*)((u64)kb_[2 * i + 1] << 32 | kb_[2 * i]); };
    const u16 *wphi = (const u16*)arg(0), *wo = (const u16*)arg(1), *wr = (const u16*)arg(2), *wg = (const u16*)arg(3);
    const u16 *wu = (const u16*)arg(4), *wd = (const u16*)arg(5), *gw = (const u16*)arg(6);
    char* ws = arg(7);
    u16* out = (u16*)kz;
    const int sc = (3 - (int)((unsigned)(size_t)wqkv >> 7)) & 7;
    const unsigned* one = k_num;
    unsigned v0 = 0;
    asm volatile("" : "+s"(one), "+v"(v0));
    auto mark = [&](unsigned dst, int n = 1, int lanes = 1) AI {
        asm volatile("s_mov_b32 m0, %2\n\ts_mov_b64 exec, %4\n\tglobal_load_lds_dword %0, %1 offset:%3\n\ts_mov_b64 exec, -1"
                     ::"v"(v0), "s"(one), "s"(dst - 4 * n), "n"(4 * n), "n"(lanes) : "memory");
    };
    auto four = [&](const unsigned (&v)[4], const char* src, unsigned dst, int nt = 0) AI {
#define FOUR(P) asm volatile("s_mov_b32 m0, %5\n\ts_nop 0\n\t" \
                     "global_load_lds_dwordx4 %0, %4" P "\n\tglobal_load_lds_dwordx4 %1, %4 offset:1024" P "\n\t" \
                     "global_load_lds_dwordx4 %2, %4 offset:2048" P "\n\tglobal_load_lds_dwordx4 %3, %4 offset:3072" P \
                     ::"v"(v[0]), "v"(v[1]), "v"(v[2]), "v"(v[3]), "s"(src), "s"(dst) : "memory")
        if (nt == 1) FOUR(" nt");
        else if (nt == 2) FOUR(" sc1");
        else if (nt == 3) FOUR(" sc0 sc1");
        else FOUR("");
    };
    const int ks0 = ((slot + (slot >> 3)) & KXM) | (KXO == 2 || (KXO == 3 && (hp & 7) == sc) ? slot & 4 : 0);
    const char* xb0 = (const char*)x + xo0 - 4096;
    const char* wb0 = (const char*)wqkv + wo0 - 4096;
    auto fill = [&](auto N) AI {
        constexpr int n = N, j = F_J[n] < 0 ? 0 : F_J[n], xi = F_J[n] < 0 ? -1 - F_J[n] : 0, s = F_J[n] < 0 ? x_stage(xi) : J_S[j], m = J_M[j];
        const int ks = KXO ? ks0 ^ (int)((KXB >> (4 * s)) & 15) : (ks0 + s) & 15;
        if constexpr (n >= NF1 && n < NF2) {
            if constexpr (F_J[n] >= 0) {
                asm volatile("s_mov_b32 m0, %3\n\ts_nop 0\n\tglobal_load_lds_dwordx4 %0, %2\n\tglobal_load_lds_dwordx4 %1, %2 offset:1024"
                             ::"v"(vw[0]), "v"(vw[1]), "s"(wb0 + (ks * 64 + (w & 1) * 16) * 6144), "s"(lds0 + J_U[j] * 8192 + w * 2048) : "memory");
            } else four(vx, xb0 + ks * 128 + (w & 1) * (32 * 2048), lds0 + (hs ? X_U1[xi] : X_U0[xi]) * 8192 + (w & 1) * 4096);
            if constexpr (F_K[n] > 1) mark(lds0 + L_FULL + 16 + w * 4, F_K[n]);
            return;
        }
        if constexpr (F_J[n] >= 0) {
            four(vw, wb0 + ks * (64 * 6144) + (m == 2 ? 0 : 2048 * (m + 1)), lds0 + J_U[j] * 8192 + hs * 4096, s < NTS ? NTW : 0);
        } else {
            const char* xs_ = xb0 + ks * 128;
            const unsigned d = lds0 + (hs ? X_U1[xi] : X_U0[xi]) * 8192;
            four(vx, xs_, d, s < NTS ? NTX : 0);
            four(vx, xs_ + 32 * 2048, d + 4096, s < NTS ? NTX : 0);
        }
        if constexpr (F_K[n] > 1) mark(lds0 + L_FULL + (n < NF1 ? 0 : 32) + hs * 8, F_K[n], 3);
    };
    static_assert(F_K[1] == 1, "");
    if (__builtin_expect((w & 5) == 4, 1)) sfor<0, 2>(fill);
    RS(49);
    unsigned sd = 0, st;
    asm volatile("s_cmp_lg_u32 %2, 5\n\ts_cbranch_scc1 .La%=\n\t"
                 "s_min_u32 %1, %3, 23\n\ts_lshl_b32 %1, %1, 18\n\ts_load_dword %0, %5, %1\n\ts_load_dword %0, %6, 0x0\n\t"
                 "s_and_b32 %1, %1, 0x3c0000\n\ts_load_dword %0, %4, %1\n\ts_load_dword %0, %10, %1\n\t"
                 "s_load_dword %0, %7, 0x0\n\ts_load_dword %0, %8, 0x0\n\ts_load_dword %0, %9, 0x0\n.La%=:"
                 : "+s"(sd), "=&s"(st) : "s"(w), "s"(slot), "s"(x), "s"(wqkv), "s"(one), "s"(wr), "s"(gw), "s"(wphi), "s"(out) : "memory", "scc");
    asm volatile("s_cmp_lg_u32 %2, 7\n\ts_cbranch_scc1 .La%=\n\t"
                 "s_min_u32 %1, %3, 27\n\ts_lshl_b32 %1, %1, 18\n\ts_load_dword %0, %4, 0x0\n\ts_load_dword %0, %4, %1\n\t"
                 "s_load_dword %0, %5, %1\n\ts_load_dword %0, %6, %1\n\ts_load_dword %0, %7, %1\n\t"
                 "s_and_b32 %1, %1, 0x1c0000\n\ts_load_dword %0, %8, %1\n.La%=:\n\ts_barrier"
                 : "+s"(sd), "=&s"(st) : "s"(w), "s"(slot), "s"(ws), "s"(wg), "s"(wu), "s"(wd), "s"(wo) : "memory", "scc");
    RS(50);
    if (__builtin_expect((w & 5) == 4, 1)) {
        mark(lds0 + L_FULL + hs * 8, 1, 3);
        sfor<2, NF0>(fill);
    }
    RS(51);
    asm volatile("" : "+s"(bid));
    const int xcd = bid & 7;
    const int rt = BMAP ? (xcd & 1) * 8 + ((bid >> 3) & 7) : (xcd >> 1) * 4 + ((bid >> 3) & 3);
    const int cb = BMAP ? (((xcd >> 1) + BMAP - 1) & 3) * 4 + (bid >> 6) : (xcd & 1) * 8 + (bid >> 5), rot = ROTC * cb + ROTK * rt + ROT0 * (xcd >> 1);
    const __amdgpu_buffer_rsrc_t rsrc = __builtin_amdgcn_make_buffer_rsrc(ws, 0, WS_END, 0x00020000);
    const u32x4 z4 = {0u, 0u, 0u, 0u};
    const f32x4 zero = {0.f, 0.f, 0.f, 0.f};
    unsigned AX = lds0 + L_A + rg * 4096 + l16;
    unsigned W4 = lds0 + p * 4;
    unsigned PG = lds0 + L_PROG + p * 256;
    const unsigned WR = lds0 + rg * 4;
    auto flag = [&](unsigned a) AI { asm volatile("ds_write_b32 %0, %0" ::"v"(a) : "memory"); };
    auto phi_w = [&](bf16x8 (&a)[2], int h) AI {
        const unsigned ad = lds0 + L_WPHI + h * 2048 + (4 * g + q) * 64 + (((p >> 1) ^ (2 * (g & 1))) * 16) + 8 * (p & 1);
        a[0] = cat8(tr_read(ad), tr_read(ad + 1024));
        a[1] = cat8(tr_read(ad ^ 32), tr_read((ad ^ 32) + 1024));
    };
    u32x4 fw[32], fd[16], gu[2];
    unsigned sel_[4][2];
    u32x4 ph[2][2], sq;
    const int hp2 = ((bid >> 1) & 3) * 4 + (bid >> 6);
    const bool ffn = (hp2 & 7) != sc && !(NOF & 1);
    const int ph4 = ((bid >> 3) + (bid >> 6)) & 3;
    const int turn = (ph4 + TRN) & TRM;
    const int kq = mt >> 2;
    const int cs = (ffn ? hp2 - (hp2 > sc) - (hp2 > sc + 8) : 14 + (hp2 >> 3)) * 4 + ((ph4 + CPH - (int)((unsigned)(size_t)wg >> 7)) & 3);
    auto spread = [&](const u32x4& v, int i) -> bf16x8 {
        const unsigned s = v[i >> 1];
        u32x4 a;
#pragma unroll
        for (int d = 0; d < 4; ++d) a[d] = __builtin_amdgcn_perm(s, s, sel_[d][i & 1]);
        return __builtin_bit_cast(bf16x8, a);
    };
    auto fold_store = [&](const f32x4& a, unsigned off) {
        f32x4 v;
#pragma unroll
        for (int i = 0; i < 4; ++i) {
            const unsigned u = __float_as_uint(a[i]);
            const float r = __uint_as_float(__builtin_amdgcn_update_dpp(u, u, 0x128, 0xc, 0xf, false));
            v[i] = swap32_add(r, r);
        }
        const u32x4 o = nz4(v);
        ST_MASK("global_store_dwordx4", 0x00FF00FFull, off + l15 * 32 + g * 16, o, ws);
    };
    auto sel_init = [&]() AI {
#pragma unroll
        for (int d = 0; d < 4; ++d)
#pragma unroll
            for (int hf = 0; hf < 2; ++hf) {
                const unsigned b = hf ? 0x0302u : 0x0100u;
                sel_[d][hf] = (sch >> 1) != d ? 0x0c0c0c0cu : (l & 1) ? (b << 16) | 0x0c0cu : 0x0c0c0000u | b;
            }
    };
    auto gua = [&](int i) { return ws + WS_GU + cs * 4096 + i * 1024; };
    auto gu_load = [&]() AI {
#pragma unroll
        for (int i = 0; i < 2; ++i) ld_async(gu[i], l16, gua(i));
    };
    auto down = [&]() AI {
#pragma unroll
        for (int i = 0; i < 16; ++i) pin(fd[i]);
        settle(gu, [&](int) { return l16; }, gua);
        const f32x4 v = __builtin_bit_cast(f32x4, gu[0]) + __builtin_bit_cast(f32x4, gu[1]);
        auto r = __builtin_amdgcn_permlane16_swap(__float_as_uint(swap32_add(v[0], v[2])), __float_as_uint(swap32_add(v[1], v[3])), false, false);
        const float gt = rbf(__uint_as_float(r[0])), up = rbf(__uint_as_float(r[1]));
        const unsigned ha = lds0 + L_XS + hs * 512;
        lds_write<u16>(ha + l15 * 8 + g * 2, f2bf(rbf(gt / (1.f + __expf(-gt))) * up));
        const u32x4 hv = lds_read<u32x4>(ha + srow * 16);
        f32x4 y0 = zero, y1 = zero;
#pragma unroll
        for (int i = 0; i < 8; ++i) {
            const bf16x8 a = spread(hv, i);
            y0 = mfma16(a, __builtin_bit_cast(bf16x8, fd[i]), y0);
            y1 = mfma16(a, __builtin_bit_cast(bf16x8, fd[8 + i]), y1);
        }
        fold_store(y0, WS_Y + cs * 4096 + kq * 1024 + hs * 512);
        fold_store(y1, WS_Y + cs * 4096 + kq * 1024 + hs * 512 + 256);
    };
    auto need = [&](int c) AI {
        if (c == 101) lds_wait(W4 + L_FKS, 1);
        else if (c == 100) lds_wait(W4 + L_FV, 1);
        else if (c == 99) lds_wait(W4 + L_FQD, 1);
        else if (c > 0) lds_wait(c > RLD ? W4 + L_PRL : PG, c);
    };
    auto wo_load = [&](int n0, int n1, bool mk) AI {
        unsigned vo[4];
#pragma unroll
        for (int i = 0; i < 4; ++i) vo[i] = (unsigned)((8 * i + srow) * 2048 + ((sch ^ ((g & 3) << 1)) * 16) + 4096 - i * 1024);
        const char* wob = (const char*)wo + cb * 128 - 4096;
#pragma unroll
        for (int n = n0; n < ((NOF & 4) ? n0 : n1); ++n) {
            if (n < WON && (n == 0 || W_N[n] != W_N[n - 1])) need(W_N[n]);
            four(vo, wob + ((2 * n + hs + rot) & 31) * 65536, lds0 + wo_unit(n) * 8192 + hs * 4096);
        }
        if (mk) mark(lds0 + (n0 >= WON && WON ? L_FW2 : L_FWD) + hs * 4);
    };
    auto phi_adr = [&](unsigned (&pw)[4], unsigned base) AI {
#pragma unroll
        for (int i = 0; i < 4; ++i) pw[i] = (base + rg * 4096 + l15 * 128 + ((g ^ fsw(l15)) << 3)) ^ (i << 5);
    };
    const int row0 = rt * 128 + rg * 32 + kp * 16;
    u32x4 xr[2];
    u64 gg[2];
    unsigned xo = srow * 2048 + sch * 16;
    auto g_load = [&]() AI {
        asm volatile("global_load_dwordx2 %0, %2, %3 sc1\n\tglobal_load_dwordx2 %1, %2, %3 offset:64 sc1"
                     : "=&v"(gg[0]), "=&v"(gg[1]) : "v"((unsigned)srow * 8), "s"(ws + WS_GG + row0 * 8) : "memory");
    };
    auto xg_load = [&](int skip) AI {
        asm volatile("s_cmp_eq_u32 %4, 4\n\ts_cbranch_scc1 .Lk%=\n\t"
                     "global_load_dwordx4 %0, %5, %6\n\tglobal_load_dwordx4 %1, %5, %7\n\t"
                     "global_load_dwordx2 %2, %8, %9 sc1\n\tglobal_load_dwordx2 %3, %8, %9 offset:64 sc1\n.Lk%=:"
                     : "+v"(xr[0]), "+v"(xr[1]), "+v"(gg[0]), "+v"(gg[1])
                     : "s"(skip), "v"(xo), "s"(x + (size_t)row0 * DM + cb * 64), "s"(x + (size_t)(row0 + 8) * DM + cb * 64),
                       "v"((unsigned)srow * 8), "s"(ws + WS_GG + row0 * 8) : "memory", "scc");
    };
    auto xg_def = [&]() AI { asm volatile("" : "=v"(xr[0]), "=v"(xr[1]), "=v"(gg[0]), "=v"(gg[1])); };
    auto relane = [&]() AI {
        unsigned z = 0;
        asm volatile("" : "+v"(z));
        l = __builtin_amdgcn_mbcnt_hi(~0u, __builtin_amdgcn_mbcnt_lo(~0u, z));
        g = l >> 4, l15 = l & 15, q = l15 >> 2, p = l & 3, srow = l >> 3, sch = l & 7, l16 = l * 16;
        AX = lds0 + L_A + rg * 4096 + l16, W4 = lds0 + p * 4, PG = lds0 + L_PROG + p * 256, xo = srow * 2048 + sch * 16;
    };
    u32x4 kf[6];
    auto kf_def = [&]() AI { asm volatile("" : "=v"(kf[0]), "=v"(kf[1]), "=v"(kf[2]), "=v"(kf[3]), "=v"(kf[4]), "=v"(kf[5])); };
    const int oh = hs ^ kp ^ 1;

    if (__builtin_expect(w >= 4, 1)) {
        const bool loader = !(w & 1);
        const unsigned kf_kv = WS_KF + (hp * 2 + (mt >> 3)) * 8192 + (mt & 7) * 512;
        const unsigned kf_z = WS_KF + (hp * 2 + (mt >> 3)) * 8192 + 4096 + ((mt >> 2) & 1) * 64;
        const unsigned fgo = ffn ? (unsigned)((256 * kq + 16 * srow) * 7168 + cs * 128 + sch * 16) : 0u;
        const char* fsrc = (const char*)(hs ? wu : wg);
        auto gu_w = [&](int q0, int q1) AI {
#pragma unroll
            for (int i = q0; i < q1; ++i) {
                unsigned t1, t2, t3;
                if (NOF & 8) fw[4 * i] = fw[4 * i + 1] = fw[4 * i + 2] = fw[4 * i + 3] = z4; else
                asm volatile("v_add_u32 %4, 0x1c00, %7\n\tv_add_u32 %5, 0x3800, %7\n\tv_add_u32 %6, 0x5400, %7\n\t"
                             "global_load_dwordx4 %0, %7, %8 nt\n\tglobal_load_dwordx4 %1, %4, %8 nt\n\t"
                             "global_load_dwordx4 %2, %5, %8 nt\n\tglobal_load_dwordx4 %3, %6, %8 nt"
                             : "=&v"(fw[4 * i]), "=&v"(fw[4 * i + 1]), "=&v"(fw[4 * i + 2]), "=&v"(fw[4 * i + 3]), "=&v"(t1), "=&v"(t2), "=&v"(t3)
                             : "v"(fgo), "s"(fsrc + (128 * (i >> 2) + 4 * (i & 3)) * 7168) : "memory");
            }
        };
        const unsigned fdo = ffn ? (unsigned)((64 * cs + 8 * srow) * 2048 + 512 * kq + 256 * hs + sch * 16) : 0u;
        auto dn_w = [&](int q0, int q1) AI {
#pragma unroll
            for (int i = q0; i < q1; ++i)
                if (NOF & 8) fd[4 * i] = fd[4 * i + 1] = fd[4 * i + 2] = fd[4 * i + 3] = z4; else
                asm volatile("global_load_dwordx4 %0, %4, %5 offset:-4096 nt\n\tglobal_load_dwordx4 %1, %4, %5 offset:-2048 nt\n\t"
                             "global_load_dwordx4 %2, %4, %5 nt\n\tglobal_load_dwordx4 %3, %4, %5 offset:2048 nt"
                             : "=&v"(fd[4 * i]), "=&v"(fd[4 * i + 1]), "=&v"(fd[4 * i + 2]), "=&v"(fd[4 * i + 3])
                             : "v"(fdo), "s"((const char*)wd + 4096 + (i & 1) * 8192 + (i >> 1) * 128) : "memory");
        };
        auto gate_up = [&]() AI {
            sel_init();
#pragma unroll
            for (int i = 0; i < 32; ++i) pin(fw[i]);
            u32x4 xs[4];
#pragma unroll
            for (int i = 0; i < 4; ++i) xs[i] = lds_read<u32x4>(lds0 + L_XS + hs * 512 + (i >> 1) * 256 + srow * 32 + (i & 1) * 16);
            f32x4 a = zero;
#pragma unroll
            for (int i = 0; i < 32; ++i) a = mfma16(spread(xs[i >> 3], i & 7), __builtin_bit_cast(bf16x8, fw[i]), a);
            fold_store(a, WS_GU + cs * 4096 + kq * 512 + hs * 256);
            SB0();
        };
        auto o_clear = [&]() AI {
#pragma unroll
            for (int n = 0; n < ((NOF & 2) ? 1 : 8); ++n) st_wt(rsrc, WS_O + ((mt * 2 + (n >> 2)) * 32 + 2 * hp + hs) * 4096 + (n & 3) * 1024 + l16, z4);
        };
        u64 T;
        u32x4 sv[2];
        auto clears = [&]() AI {
            o_clear();
#pragma unroll
            for (int n = 0; n < ((NOF & 16) ? 0 : 4); ++n) st_wt(rsrc, WS_KV + ((hp * 8 + hs * 4 + n) * 16 + mt) * 1024 + l16, z4);
            ST_MASK("global_store_dwordx4", 0xFFFFull, WS_Z + ((hp * 4 + hs * 2 + (srow & 1)) * 16 + mt) * 128 + sch * 16, z4, ws);
        };
        auto cleared = [&]() AI { ST_MASK("global_store_dwordx2", 1ull, 0u, T, ws + WS_CL + ((mt * 2 + hs) * 16 + hp) * 8); };
        auto rg_load = [&]() AI {
            asm volatile("global_load_dwordx4 %0, %2, %3 sc1\n\tglobal_load_dwordx4 %1, %2, %3 offset:16 sc1"
                         : "=&v"(sv[0]), "=&v"(sv[1]) : "v"(l * 32), "s"(ws + WS_RG) : "memory");
        };
        auto select = [&]() AI {
            const unsigned tl = (unsigned)T & 0x1FFFu, thi = (unsigned)(T >> 13);
#pragma unroll 1
            for (unsigned s = 0; s < SPIN_CAP; ++s) {
                pin(sv[0]);
                pin(sv[1]);
                const unsigned bad = ((sv[0][0] >> 19) ^ tl) | (sv[0][1] ^ thi) | ((sv[0][2] >> 19) ^ tl) | (sv[0][3] ^ thi) |
                                     ((sv[1][0] >> 19) ^ tl) | (sv[1][1] ^ thi) | ((sv[1][2] >> 19) ^ tl) | (sv[1][3] ^ thi);
                if (__all(bad == 0u)) break;
                __builtin_amdgcn_s_sleep(16);
                rg_load();
                waitv<0>();
            }
            pin(sv[0]);
            pin(sv[1]);
            RS(4);
            unsigned k2 = 0;
#pragma unroll
            for (int j = 0; j < 4; ++j) {
                const unsigned v = sv[j >> 1][(j & 1) * 2] & 0x7FFFFu;
                k2 = max(k2, ((v >> 3) << 11) | ((255 - (4 * l + j)) << 3) | (v & 7));
            }
            const unsigned key = __builtin_amdgcn_readfirstlane(wave_umax(k2));
            asm volatile("s_mov_b32 m0, %2\n\ts_mov_b64 exec, %3\n\tglobal_load_lds_dwordx4 %0, %1\n\ts_mov_b64 exec, -1"
                         ::"v"((unsigned)(512 * kq) + l16), "s"(x + (size_t)(2047 - (int)(key & 2047u)) * DM), "s"(lds0 + L_XS + hs * 512), "s"(0xFFFFFFFFull) : "memory");
            unsigned rb = key >> 11;
            rb ^= (rb & 0x8000u) ? 0x8000u : 0xFFFFu;
            const float rsel = rbf(1.f / (1.f + __expf(-bf2f((u16)rb))));
            lds_write<u32x4>(lds0 + L_SEL, (u32x4){(unsigned)T, (unsigned)(T >> 32), 2047u - (key & 2047u), __float_as_uint(rsel)});
            if (bid == 0 && hs) ST_MASK("global_store_dwordx2", 1ull, 0u, T, ws + WS_E);
            RS(5);
        };
        auto k_soft = [&](int np) AI {
            bf16x8 a0[2], kb[2];
            f32x4 pk[2][2][2];
            unsigned pw[4];
            phi_w(a0, 0);
            phi_adr(pw, lds0 + P_U[0] * 8192);
            asm volatile("" : "+v"(pw[0]), "+v"(pw[1]), "+v"(pw[2]), "+v"(pw[3]));
            auto logits = [&](int h) AI {
                lds_wait(WR + L_FK0 + 4 * h, 1);
#pragma unroll
                for (int i = 0; i < 2; ++i) kb[i] = lds_read128(AX + (P_U[0] * 8192 - L_A) + h * 4096 + i * 1024);
#pragma unroll
                for (int i = 0; i < 4; ++i) pk[h][i >> 1][i & 1] = mfma16(a0[i & 1], kb[i >> 1], zero);
            };
            auto round = [&](int h) AI {
#pragma unroll
                for (int i = 0; i < 4; ++i) pk[h][i >> 1][i & 1] = rbf4(pk[h][i >> 1][i & 1]);
            };
            logits(0);
            RS(12);
            round(0);
            SB0();
            phi_k<KPK & 1>(pk[0], pw, 0, [&]() AI {
                if (np > 1) logits(1);
                SB0();
            });
            SB0();
            if (np > 1) {
                round(1);
                phi_k<(KPK >> 1)>(pk[1], pw, 4096, []() {});
                SB0();
            }
            flag(WR + L_FP);
        };
        if (__builtin_expect(loader, 1)) {
            vx_init();
            vw_init();
            if (LPRIO) __builtin_amdgcn_s_setprio(LPRIO);
            unsigned pv = 0;
            sfor<NF0, NF1>([&](auto N) {
                constexpr int n = N;
                if constexpr (n > 0 && F_N[n] > F_N[n > 0 ? n - 1 : 0]) {
                    RSL(32 + F_N[n]);
                    if (LPRW != LPRIO) __builtin_amdgcn_s_setprio(LPRW);
                    if (NOF & 256) {
                    } else if (F_N[n] > RLD) {
                        lds_wait(W4 + L_PRL, F_N[n]);
                    } else if (PEEK) {
                        lds_wait(pv, PG, F_N[n]);
                        if (F_N[n] < F_N[NF1 - 1] && next_need(n) <= RLD) lds_peek(pv, PG);
                    } else lds_wait(PG, F_N[n]);
                    if (LPRW != LPRIO) __builtin_amdgcn_s_setprio(LPRIO);
                    RSL(48 + F_N[n]);
                }
                if constexpr (n == 12) {
                    asm volatile("s_mov_b32 m0, %2\n\ts_nop 0\n\tglobal_load_lds_dwordx4 %0, %1\n\tglobal_load_lds_dwordx4 %0, %1 offset:1024"
                                 ::"v"((unsigned)((l >> 2) * 64 + (((l & 3) ^ (2 * ((l >> 4) & 1))) * 16))),
                                   "s"(wphi + (hp * 2 + hs) * 1024), "s"(lds0 + L_WPHI + hs * 2048) : "memory");
                }
                if constexpr (LTH < 63 && n >= LT0) waitv<LTH>();
                fill(N);
            });
            flag(lds0 + L_FLD + hs * 4);
            RS(1);
            if (HPRIO) __builtin_amdgcn_s_setprio(HPRIO);
            const char* cla = ws + WS_CL + ((l15 * 2 + (mt >> 3)) * 16 + hp) * 8;
            u64 cl;
            if (CLE) asm volatile("global_load_dwordx2 %0, %1, off sc1" : "=&v"(cl) : "v"(cla) : "memory");
            k_soft(SSM ? 1 : 2);
            if (!CLE) asm volatile("global_load_dwordx2 %0, %1, off sc1" : "=&v"(cl) : "v"(cla) : "memory");
            RS(11);
            lds_wait(W4 + L_FP, 1);
            sq = lds_read<u32x4>(lds0 + L_SEL);
            waitv<0>();
            asm volatile("" : "+v"(cl));
            RS(13);
            u32x4 kp_[5];
            {
                const char* kb = ws + WS_KV + (hp * 8 + (mt >> 1)) * 16384 + (mt & 1) * 512 + hs * 256;
                const bool zw = !(mt & 3);
                const char* zb = zw ? ws + WS_Z + (hp * 4 + (mt >> 2)) * 2048 + hs * 64 : kb;
                const unsigned kvo = g * 1024 + l15 * 16, zo = zw ? l15 * 128 + g * 16 : 0u;
                auto ko = [&](int i) { return i < 4 ? kvo : zo; };
                auto ka = [&](int i) { return i < 4 ? kb + i * 4096 : zb; };
                const u64 T = (u64)sq[1] << 32 | sq[0];
#pragma unroll 1
                for (unsigned s = 0; s < SPIN_CAP && __ballot(cl != T); ++s)
                    asm volatile("global_load_dwordx2 %0, %1, off sc1\n\ts_waitcnt vmcnt(0)" : "=&v"(cl) : "v"(cla) : "memory");
                lds_wait0(W4 + L_FKS);
                if (GDL) __builtin_amdgcn_s_sleep(GDL);
#pragma unroll
                for (int i = 0; i < 5; ++i) { if (PDI & 1) kp_[i] = z4; else ld_async(kp_[i], ko(i), ka(i)); }
                if (XGE) {
                    xg_def();
                    xg_load(w + 1);
                }
                flag(lds0 + L_FGI + hs * 4);
                SB0();
                RS(14);
                if (!(PDI & 1)) waitv<0>();
                RS(15);
                if (PP1) settle_p<PP1 + !PP1, PD1>(kp_, ko, ka);
                else settle(kp_, ko, ka);
                RS(24);
                float v[8], u[4];
#pragma unroll
                for (int e = 0; e < 4; ++e) {
                    v[2 * e] = (lo16(kp_[0][e]) + lo16(kp_[1][e])) + (lo16(kp_[2][e]) + lo16(kp_[3][e]));
                    v[2 * e + 1] = (hi16(kp_[0][e]) + hi16(kp_[1][e])) + (hi16(kp_[2][e]) + hi16(kp_[3][e]));
                }
#pragma unroll
                for (int e = 0; e < 4; ++e) u[e] = swap32_add(v[e], v[e + 4]);
                const unsigned o = nz1(pack2(swap16_add(u[0], u[2]), swap16_add(u[1], u[3])));
                ST_MASK("global_store_dword", ~0ull, kf_kv + hs * 256 + l15 * 16 + g * 4, o, ws);
                if (zw) {
                    f32x4 z;
#pragma unroll
                    for (int i = 0; i < 4; ++i) z[i] = dpp_add<0x140>(dpp_add<0x141>(dpp_add<0x4E>(dpp_add<0xB1>(__uint_as_float(kp_[4][i])))));
                    const u32x2 oz = {nz1(pack2(z[0], z[1])), nz1(pack2(z[2], z[3]))};
                    ST_MASK("global_store_dwordx2", 0x0001000100010001ull, kf_z + g * 16 + hs * 8, oz, ws);
                }
                flag(lds0 + L_FG + hs * 4);
            }
            RS(25);
            const char* kfb = ws + WS_KF + (hp * 2 + hs) * 8192;
            auto fo = [&](int i) { return i < 4 ? l16 : (unsigned)g * 16; };
            auto fb = [&](int i) { return i < 4 ? kfb + i * 1024 : kfb + 4096 + (i - 4) * 64; };
            if (KFD) __builtin_amdgcn_s_sleep(KFD);
#pragma unroll
            for (int i = 0; i < 6; ++i) { if (PDI & 2) kf[i] = z4; else ld_async(kf[i], fo(i), fb(i)); }
            if (!XGE) {
                xg_def();
                xg_load(w + 1);
            }
            if (!(PDI & 2)) waitv<0>();
            RS(29);
            if (PP2) settle_p<PP2 + !PP2, PD2>(kf, fo, fb);
            else settle(kf, fo, fb);
#pragma unroll
            for (int i = 0; i < 5; ++i) pin(kp_[i]);
            asm volatile("" : "+v"(xr[0]), "+v"(xr[1]), "+v"(gg[0]), "+v"(gg[1]));
            need(KFN);
#pragma unroll
            for (int i = 0; i < 4; ++i) lds_write<u32x4>(lds0 + L_KF + hs * 4096 + i * 1024 + l16, kf[i]);
#pragma unroll
            for (int i = 0; i < 2; ++i) lds_write<u32x4>(lds0 + L_KZ + hs * 128 + i * 64 + g * 16, kf[4 + i]);
            SB0();
            flag(lds0 + L_FKF + hs * 4);
        } else {
            const int row0 = (mt * 16 + hp) * 8 + 4 * hs;
            if (SPRIO) __builtin_amdgcn_s_setprio(SPRIO);
            asm volatile("s_waitcnt lgkmcnt(0)" : "+s"(sd));
            if (TLD) __builtin_amdgcn_s_sleep(TLD);
            u32x4 rv[2], gv[2], xv[4][2];
            u64 ev;
            if (RT0) lds_wait8(PG, RT0);
            asm volatile("global_load_dwordx2 %4, %5, %9 sc1\n\t"
                         "global_store_dwordx4 %10, %11, %9 sc1\n\t"
                         "global_store_dwordx2 %12, %13, %9 sc1\n\t"
                         "global_load_dwordx4 %0, %6, %7\n\tglobal_load_dwordx4 %1, %6, %7 offset:1024\n\t"
                         "global_load_dwordx4 %2, %6, %8\n\tglobal_load_dwordx4 %3, %6, %8 offset:1024"
                         : "=&v"(rv[0]), "=&v"(rv[1]), "=&v"(gv[0]), "=&v"(gv[1]), "=&v"(ev)
                         : "v"(0u), "v"(l16), "s"(wr), "s"(gw), "s"(ws),
                           "v"(l < 32 ? WS_Y + cs * 4096 + kq * 1024 + hs * 512 + l16 : WS_GU + cs * 4096 + kq * 512 + hs * 256 + l15 * 16), "v"(z4),
                           "v"(hs ? kf_z + sch * 8 : kf_kv + l * 8), "v"(0ull)
                         : "memory");
            RS(1);
#pragma unroll
            for (int r = 0; r < 2; ++r)
                if (NOF & 64) xv[2 * r][0] = xv[2 * r][1] = xv[2 * r + 1][0] = xv[2 * r + 1][1] = z4; else
                asm volatile("global_load_dwordx4 %0, %4, %5\n\tglobal_load_dwordx4 %1, %4, %5 offset:1024\n\t"
                             "global_load_dwordx4 %2, %4, %5 offset:2048\n\tglobal_load_dwordx4 %3, %4, %5 offset:3072"
                             : "=&v"(xv[2 * r][0]), "=&v"(xv[2 * r][1]), "=&v"(xv[2 * r + 1][0]), "=&v"(xv[2 * r + 1][1])
                             : "v"(l16), "s"(x + (size_t)(row0 + 2 * r) * DM) : "memory");
            SB0();
            if (MRG & 8) clears();
            waitv<0>();
            asm volatile("" : "+v"(rv[0]), "+v"(rv[1]), "+v"(gv[0]), "+v"(gv[1]), "+v"(ev));
#pragma unroll
            for (int r = 0; r < 4; ++r) asm volatile("" : "+v"(xv[r][0]), "+v"(xv[r][1]));
            RS(2);
            T = ((u64)__builtin_amdgcn_readfirstlane((unsigned)(ev >> 32)) << 32 | __builtin_amdgcn_readfirstlane((unsigned)ev)) + 1;
            float ra_[4] = {0.f, 0.f, 0.f, 0.f}, ga_[4] = {0.f, 0.f, 0.f, 0.f};
#pragma unroll
            for (int r = 0; r < 4; ++r)
#pragma unroll
                for (int i = 0; i < 2; ++i)
#pragma unroll
                    for (int j = 0; j < 4; ++j) {
                        ra_[r] = dot2(xv[r][i][j], rv[i][j], ra_[r]);
                        ga_[r] = dot2(xv[r][i][j], gv[i][j], ga_[r]);
                    }
            const int rr = (g >> 1) | ((g & 1) << 1);
            unsigned k = f2bf(sum4(ra_));
            k ^= (k & 0x8000u) ? 0xFFFFu : 0x8000u;
            k = (k << 3) | (7 - 4 * hs - rr);
            auto r1 = __builtin_amdgcn_permlane16_swap(k, k, false, false);
            k = max(r1[0], r1[1]);
            auto r2 = __builtin_amdgcn_permlane32_swap(k, k, false, false);
            k = max(r2[0], r2[1]);
            u64 gq = (T << 16) | f2bf(1.f / (1.f + __expf(-rbf(sum4(ga_)))));
            if (hs) lds_write<unsigned>(lds0 + L_BEST, k | 0x80000000u);
            asm volatile("" : "+v"(gq), "+v"(k));
            if (MRG & 1) {
                __builtin_amdgcn_s_setprio(SPR2);
                lds_wait8(PG, CLR0 + CLS * ((ph4 + CLP) & 3));
            }
            if (!hs) {
                lds_wait(lds0 + L_BEST, 1);
                const u64 rq = (T << 19) | max(k, lds_read<unsigned>(lds0 + L_BEST) & 0x7FFFFu);
                ST_MASK("global_store_dwordx2", 1ull, 0u, rq, ws + WS_RG + (mt * 16 + hp) * 8);
            }
            ST_MASK("global_store_dwordx2", 0x0001000100010001ull, (unsigned)(rr * 8), gq, ws + WS_GG + row0 * 8);
            RS(3);
            __builtin_amdgcn_s_setprio(SPR2);
            auto q_fill = [&]() AI {
                vx_init();
                vw_init();
                sfor<NF2, NF>([&](auto N) {
                    constexpr int n = N;
                    if constexpr (n == NF2 || F_N[n] != F_N[n - 1]) need(F_N[n]);
                    fill(N);
                });
            };
            auto wt = [&](unsigned f) AI { lds_wait(lds0 + f + (l & 1) * 4, 1); };
            unsigned pf[2];
            auto wo_warm = [&]() AI {
                asm volatile("global_load_dword %0, %2, %3\n\tglobal_load_dword %1, %2, %4" : "=&v"(pf[0]), "=&v"(pf[1])
                             : "v"((unsigned)((BMAP ? (rt & 7) * 128 + hs * 64 + (l & 31) : (rt & 3) * 256 + hs * 128 + l) * 2048)), "s"((const char*)wo + cb * 128), "s"((const char*)wo + cb * 128 + (BMAP ? 32 : 64) * 2048) : "memory");
            };
            if (DNP == 0) {
                lds_wait8(PG, FBA + FBS * turn);
                dn_w(0, 4);
            }
            if (!(MRG & 8)) {
                if (!(MRG & 1)) lds_wait8(PG, CLR0 + CLS * ((ph4 + CLP) & 3));
                clears();
                waitv<0>();
            }
            if (!(MRG & 6) || (MRG & 8)) cleared();
            RS(25);
            if (!(MRG & 4)) {
                lds_wait8(PG, SEL0);
                rg_load();
                if ((MRG & 10) == 2) cleared();
                waitv<(MRG & 10) == 2>();
                if (MRG & 2) lds_wait8(lds0 + L_FLD + (l & 1) * 4, 1);
                select();
                if (!SSM) flag(WR + L_FP);
            }
            if (!(MRG & 2)) lds_wait8(lds0 + L_FLD + (l & 1) * 4, 1);
            if (MRG & 4) {
                rg_load();
                if (!(MRG & 8)) cleared();
            }
            RS(6);
            if (WPF == 4) wo_warm();
            gu_w(0, GQ1);
            if (MRG & 4) {
                waitv<4 * GQ1>();
                select();
                if (!SSM) flag(WR + L_FP);
            }
            q_fill();
            RS(7);
            gu_w(GQ1, GQ2);
            if (SSM) {
                __builtin_amdgcn_s_setprio(HPRIO);
                k_soft(1);
                __builtin_amdgcn_s_setprio(SPR2);
                RS(11);
            }
            gu_w(GQ2, 8);
            if (WPF == 1) wo_warm();
            if (DNP == 1) dn_w(0, 4);
            RS(8);
            waitv<(DNP == 1 ? 16 : 0)>();
            if ((WPF == 1 && DNP != 1) || WPF == 4) asm volatile("" : "+v"(pf[0]), "+v"(pf[1]));
            RS(9);
            if (GUO == 0) gate_up();
            RS(10);
            if (WOT == 1) lds_wait(W4 + L_FKS, 1);
            if (WOT == 2) wt(L_FGI);
            if (DNP == 2) dn_w(0, 4);
            wo_load(0, WOE, WOE == WON);
            RS(14);
            if (GUO == 1) {
                __builtin_amdgcn_s_setprio(GPR);
                gate_up();
                __builtin_amdgcn_s_setprio(SPR2);
            }
            RS(13);
            if (YT == 1) wt(L_FG);
            gu_load();
            if (DNP == 3) dn_w(0, 4);
            if (WOE < WON) wo_load(WOE, WON, true);
            waitv<(WOE < WON ? 4 * (WON - WOE) + 1 : 0)>();
            if (WPF == 1 && DNP == 1) asm volatile("" : "+v"(pf[0]), "+v"(pf[1]));
            RS(15);
            down();
            RS(24);
            sq = lds_read<u32x4>(lds0 + L_SEL);
            xg_def();
            kf_def();
        }
        RS(26);
        lds_wait0(WR + L_FQ);
#pragma unroll
        for (int i = 0; i < 4; ++i) ph[i >> 1][i & 1] = lds_read<u32x4>(AX + i * 1024);
    } else {
        if (CPRIO) __builtin_amdgcn_s_setprio(CPRIO);
        unsigned ra[4], xt[2];
#pragma unroll
        for (int u = 0; u < 4; ++u) ra[u] = lds0 + J_B[0] * 8192 + (8 * g + q) * 128 + ((u ^ (q >> 1) ^ ((g & 1) << 1)) * 32) + 8 * p;
        unsigned xa = lds0 + (32 * (w & 1) + l15) * 128 + ((g ^ (l15 >> 1)) * 16), fa = lds0 + L_FULL + (l & 3) * 4;
        xt[0] = xa + (hs ? X_U1[0] : X_U0[0]) * 8192;
        xt[1] = xt[0] ^ 64;
        asm volatile("s_mov_b32 m0, %4" : "+v"(xa), "+v"(fa), "+v"(xt[0]), "+v"(xt[1]) : "s"(lds0 + L_PROG + w * 256 - 124));
        f32x4 acc[3][4][2];
        bf16x8 xf[ND][2][2];
        s16x4 wh[PD + 1][2];
        unsigned fv = 0;
        bf16x8 kown[2];
        u64 bad = ~0ull;
        auto chk = [&](int c) AI {
            SB0();
            if (__builtin_expect(bad != 0ull, 0)) {
                unsigned c_;
                asm volatile("s_mov_b32 %1, 0x400000\n"
                             ".Lp%=:\n\tds_read_b32 %0, %2 offset:%3\n\ts_waitcnt lgkmcnt(0)\n\tv_cmp_gt_u32 vcc, %4, %0\n\ts_cbranch_vccz .Lf%=\n\t"
                             "s_sub_u32 %1, %1, 1\n\ts_cbranch_scc0 .Lp%=\n.Lf%=:"
                             : "=&v"(fv), "=&s"(c_) : "v"(fa), "n"(c > NM2 ? 32 : c > NM1 ? 16 : 0), "n"(c) : "memory", "vcc", "scc");
            }
            SB0();
        };
        auto kv_part = [&]() AI
        {
            bf16x8 ones;
#pragma unroll
            for (int j = 0; j < 8; ++j) ones[j] = (short)0x3f80;
            const unsigned pa = lds0 + (hs ? P_U[2] : P_U[0]) * 8192 + (4 * g + q) * 128 + (((8 * (rg & 1) + p) ^ fsw(4 * g + q)) << 3);
            const unsigned va = lds0 + L_A + hs * 2048 + l16;
            bf16x8 b0[4], b1[4], af[2][4];
            lds_wait0(lds0 + L_FV + sch * 4);
            RS(15);
#pragma unroll
            for (int i = 0; i < 4; ++i) {
                af[0][i] = cat8(tr_read(pa + i * 4096), tr_read(pa + i * 4096 + 2048));
                b0[i] = lds_read128(va + i * 4096);
                af[1][i] = cat8(tr_read((pa ^ 32) + i * 4096), tr_read((pa ^ 32) + i * 4096 + 2048));
            }
#pragma unroll
            for (int i = 0; i < 4; ++i) b1[i] = lds_read128(va + i * 4096 + 1024);
            SB0();
            f32x4 k0[2] = {zero, zero}, k1[2] = {zero, zero}, zt[2] = {zero, zero};
            const unsigned kv_st = WS_KV + ((hp * 8 + rg * 2) * 16 + mt) * 1024 + l16;
#pragma unroll
            for (int i = 0; i < 4; ++i)
#pragma unroll
                for (int e = 0; e < 2; ++e) {
                    k0[e] = mfma16(af[e][i], b0[i], k0[e]);
                    if (PORD) zt[e] = mfma16(af[e][i], ones, zt[e]);
                }
            SB0();
#pragma unroll
            for (int i = 0; i < 4; ++i)
#pragma unroll
                for (int e = 0; e < 2; ++e) k1[e] = mfma16(af[e][i], b1[i], k1[e]);
            st_wt(rsrc, kv_st, nz4(__builtin_bit_cast(u32x4, pack44(k0[0], k0[1]))));
            if (!PORD) {
                SB0();
#pragma unroll
                for (int i = 0; i < 4; ++i)
#pragma unroll
                    for (int e = 0; e < 2; ++e) zt[e] = mfma16(af[e][i], ones, zt[e]);
                st_wt(rsrc, kv_st + 16384, nz4(__builtin_bit_cast(u32x4, pack44(k1[0], k1[1]))));
            }
            const u32x4 zo = nz4((l & 1) ? zt[1] : zt[0]);
            ST_MASK("global_store_dwordx4", 0x0003000300030003ull, WS_Z + ((hp * 4 + rg) * 16 + mt) * 128 + (l & 1) * 64 + g * 16, zo, ws);
            if (PORD) st_wt(rsrc, kv_st + 16384, nz4(__builtin_bit_cast(u32x4, pack44(k1[0], k1[1]))));
            SB0();
            flag(WR + L_FKS);
        };
        auto report = [&](int r) AI {
            if (r > RLD) {
                unsigned t;
                unsigned a;
                asm volatile("v_mov_b32 %0, %3\n\tv_mov_b32 %1, %2\n\tds_write_b32 %1, %0" : "=&v"(t), "=&v"(a) : "s"(WR + L_PRL), "n"(r) : "memory");
            } else asm volatile("global_load_lds_dword %0, %1 offset:%2" ::"v"(v0), "s"(one), "n"(4 * r) : "memory"); };
        auto rd1 = [&](auto F, auto H) AI {
            constexpr int f = F, h = H, n = f >> 2, j = f >> 3, c = h ? E_B[f] : E_A[f], a = c % 100;
            if constexpr (h == 0 && (f & 3) == 0 && Q_C[n] > 0 && !((NOF & 512) && n >= 12 && n < KD)) chk(Q_C[n]);
            wh[f % (PD + 1)][h] = tr_read(ra[f & 3] + (J_U[j] - J_B[j]) * 8192 + (n & 1) * 4096 + h * 512);
            SB0();
            if constexpr (c / 100 == 1) {
                constexpr int jn = (f + 3 - a) >> 3;
                asm volatile("v_add_u32 %0, %1, %0" : "+v"(ra[a]) : "n"((J_B[jn] - J_B[jn - 1]) * 8192));
            } else if constexpr (c / 50 == 4) {
                asm volatile("v_add_u32 %0, %1, %2" : "=v"(xt[0]) : "s"((hs ? X_U1[a] : X_U0[a]) * 8192), "v"(xa));
            } else if constexpr (c / 50 == 5) {
                asm volatile("v_xor_b32 %0, 64, %1" : "=v"(xt[1]) : "v"(xt[0]));
            } else if constexpr (c / 100 == 3) {
                xf[a % 25 % ND][a / 25 & 1][a / 50] = lds_read128(xt[a / 50] + (a / 25 & 1) * 2048);
            } else if constexpr (c / 100 == 4) {
                fv = *(volatile AS3 unsigned*)(size_t)(fa + (a > NM2 ? 32 : a > NM1 ? 16 : 0));
            } else if constexpr (c / 100 == 5) {
                bad = __builtin_amdgcn_ballot_w64(fv < (unsigned)a);
                asm volatile("" : "+s"(bad));
            } else if constexpr (c / 100 == 6 && !((NOF & 1024) && a < NP1)) {
                report(a);
            }
        };
        RS(1);
        sfor<0, PD>([&](auto F) { rd1(F, IC<0>{}); rd1(F, IC<1>{}); });
        RS(2);
        constexpr int K0F = 4 * KD, K1F = 4 * KD + 1;
        sfor<0, 96>([&](auto N) {
            sfor<0, 4>([&](auto E) {
                constexpr int n = N, e = E, f = 4 * n + e, j = n >> 1, ks = n & 1, u = f_u(f), s = J_X[j], m = J_M[j];
                const bf16x8 wv = cat8(wh[f % (PD + 1)][0], wh[f % (PD + 1)][1]);
                sfor<0, 2>([&](auto RB) {
                    constexpr int rb = RB;
                    const bf16x8 xv = xf[s % ND][rb][ks];
                    if (AMF) {
                        if (j_first(j) && ks == 0) asm volatile("v_mfma_f32_16x16x32_bf16 %0, %1, %2, 0" : "=&v"(acc[m][u][rb]) : "v"(m == 1 ? xv : wv), "v"(m == 1 ? wv : xv));
                        else asm volatile("v_mfma_f32_16x16x32_bf16 %0, %1, %2, %0" : "+v"(acc[m][u][rb]) : "v"(m == 1 ? xv : wv), "v"(m == 1 ? wv : xv));
                    } else if (m == 1) acc[m][u][rb] = mfma16(xv, wv, acc[m][u][rb]);
                    else acc[m][u][rb] = mfma16(wv, xv, acc[m][u][rb]);
                    SB0();
                    if constexpr (f + PD < 384 && (f + PD < 4 * VD || f >= 4 * VD)) rd1(IC<f + PD>{}, RB);
                    if constexpr (rb == 0 && f == K0F) relane();
                    if constexpr (AMF && rb == 0 && (f == K0F || f == K1F)) asm volatile("s_nop 7");
                    SB0();
                    if constexpr (f == K0F) lds_write<bf16x8>(AX + (P_U[0] * 8192 - L_A) + rb * 1024, pack44(acc[0][0][rb], acc[0][1][rb]));
                    if constexpr (f == K1F) {
                        kown[rb] = pack44(acc[0][2][rb], acc[0][3][rb]);
                        if (rb < 2 - SH) lds_write<bf16x8>(AX + (P_U[2] * 8192 - L_A) + rb * 1024, kown[rb]);
                        else asm volatile("" : "+v"(kown[rb]));
                    }
                    SB0();
                });
                if constexpr (f == K0F) { flag(WR + L_FK0); RS(12); }
                if constexpr (f == K1F) { if (SH < 2) flag(WR + L_FK); RS(4); }
                if constexpr (f == 4 * VD - 1) {
                    bf16x8 ak[2];
                    if (NF2 > NF1) flag(WR + L_FVR);
                    auto own = [&]() AI {
                        if (NF2 == NF1) return;
                        vx_init();
                        vw_init();
                        lds_wait0(W4 + L_FVR);
                        sfor<NF1, NF2>(fill);
                        asm volatile("s_mov_b32 m0, %0" ::"s"(lds0 + L_PROG + w * 256 - 124));
                        RS(14);
                    };
                    if (SH) {
                        __builtin_amdgcn_s_setprio(HPRIO);
                        phi_w(ak, 1);
                    }
                    asm volatile("s_nop 7");
                    SB0();
#pragma unroll
                    for (int i = 0; i < 4; ++i) lds_write<bf16x8>(AX + i * 1024, pack44(acc[1][i][0], acc[1][i][1]));
                    SB0();
                    RS(5);
                    if (CFP == 1 && SH) own();
                    if (SH) {
                        f32x4 pk[2][2];
                        unsigned pw[4];
#pragma unroll
                        for (int i = 4 - 2 * SH; i < 4; ++i) pk[i >> 1][i & 1] = rbf4(mfma16(ak[i & 1], kown[i >> 1], zero));
                        phi_adr(pw, lds0 + P_U[2] * 8192 + (SH == 1 ? 2048 : 0));
                        if (SH == 1) phi_k1(pk[1], pw);
                        else phi_k<true>(pk, pw, 0, []() {});
                        SB0();
                    }
                    flag(WR + L_FV);
                    RS(13);
                    if (CFP == 0 || !SH) own();
                    kv_part();
                    __builtin_amdgcn_s_setprio(CPRIO);
                    fv = 0;
                    bad = ~0ull;
                    sfor<4 * VD, 4 * VD + PD>([&](auto F) { rd1(F, IC<0>{}); rd1(F, IC<1>{}); });
                    RS(10);
                }
                if constexpr (e == 3) {
                    SB0();
                    if constexpr (n % CSN == CSN - 1) { RSC(32 + n / CSN); RK(48 + n / CSN); }
                    if constexpr (n == 31) RS(3);
                }
            });
        });
        asm volatile("s_nop 7");
        SB0();
        RS(6);
        if (CPRIO) __builtin_amdgcn_s_setprio(0);
        flag(WR + L_FQD);
        bf16x8 a0[2], a1[2];
        f32x4 pq[2][2][2];
        phi_w(a1, hs);
        phi_w(a0, hs ^ 1);
#pragma unroll
        for (int rb = 0; rb < 2; ++rb) {
            const bf16x8 b0 = pack44(acc[2][0][rb], acc[2][1][rb]), b1 = pack44(acc[2][2][rb], acc[2][3][rb]);
#pragma unroll
            for (int fb = 0; fb < 2; ++fb) {
                pq[1][rb][fb] = rbf4(mfma16(a1[fb], hs ? b1 : b0, zero));
                pq[0][rb][fb] = rbf4(mfma16(a0[fb], hs ? b0 : b1, zero));
            }
        }
        softmax2<true>(pq[1], ph, []() {});
        RS(7);
        lds_wait(W4 + L_FKS, 1);
#pragma unroll
        for (int i = 0; i < 4; ++i) lds_write<u32x4>(AX + i * 1024, ph[i >> 1][i & 1]);
        SB0();
        flag(WR + L_FQ);
        sq = lds_read<u32x4>(lds0 + L_SEL);
        RS(8);
        softmax2<true>(pq[0], ph, []() {});
        asm volatile("" : "+v"(ph[0][0]), "+v"(ph[0][1]), "+v"(ph[1][0]), "+v"(ph[1][1]));
        RS(9);
        RS(11);
        xg_def();
        kf_def();
    }
    relane();
    const char* cla = ws + WS_CL + (CLO ? 4096 : 0) + ((rt * 2 + kp) * 16 + l15) * 8;
    const u64 T = (u64)sq[1] << 32 | sq[0];
    u64 cl;
    xg_load(w & 5);
    {
        if ((w & 5) != 4) {
            lds_wait0(lds0 + L_FKF + oh * 4);
#pragma unroll
            for (int i = 0; i < 4; ++i) kf[i] = lds_read<u32x4>(lds0 + L_KF + oh * 4096 + i * 1024 + l16);
#pragma unroll
            for (int i = 0; i < 2; ++i) kf[4 + i] = lds_read<u32x4>(lds0 + L_KZ + oh * 128 + i * 64 + g * 16);
        }
        asm volatile("global_load_dwordx2 %0, %1, off sc1" : "=&v"(cl) : "v"(cla) : "memory");
        RS(27);
        o_store(kf, ph, rsrc, WS_O + ((mt * 2 + hs) * 32 + 2 * hp + oh) * 4096 + (rg & 1) * 2048 + l16);
        RS(28);
    }
    if (WON < 16 && WLK < 0) flag(lds0 + L_FOS + w * 4);
    waitv<3>();
#pragma unroll
    for (int i = 0; i < 6; ++i) pin(kf[i]);
    flag(lds0 + L_FWO + w * 4);
    __builtin_amdgcn_s_setprio(0);
    if (WON < 16 && WLK < 0) {
        if ((w & 5) == 5) {
            lds_wait(lds0 + L_FOS + sch * 4, 1);
            wo_load(WON, 16, true);
        }
    }
    const u32x2 selv = {sq[2], sq[3]};

    RS(30);
    const char* ob = ws + WS_O + (rt * 2 + (rg >> 1)) * 131072 + (rg & 1) * 2048;
    const unsigned ov0 = l16 + kp * 1024, ov1 = l16 + (kp ^ 1) * 1024;
    auto oa = [&](int i) { return ob + ((2 * i + kp + rot) & 31) * 4096; };
    u32x4 of[3][4];
    auto fetch1 = [&](int k, int j) AI {
        if (k < OPL) ld_async(of[k % 3][j], (j & 1) ? ov1 : ov0, oa(2 * k + (j >> 1)));
        else asm volatile("global_load_dwordx4 %0, %1, %2" OPM : "=&v"(of[k % 3][j]) : "v"((j & 1) ? ov1 : ov0), "s"(oa(2 * k + (j >> 1))) : "memory");
    };
    auto fix = [&](int k) AI { if (NOF & 128) { for (int j = 0; j < 4; ++j) pin(of[k % 3][j]); return; }
        if (PP3) settle_p<PP3 + !PP3, PD3>(of[k % 3], [&](int j) { return (j & 1) ? ov1 : ov0; }, [&](int j) { return oa(2 * k + (j >> 1)); });
        else settle(of[k % 3], [&](int j) { return (j & 1) ? ov1 : ov0; }, [&](int j) { return oa(2 * k + (j >> 1)); }); };
    const bool own = (__builtin_amdgcn_readfirstlane(selv[0]) >> 4) == (unsigned)(rt * 8 + rg * 2 + kp);
    float gv_[2];
    f32x2 gb[2][4];
    auto g_mix = [&]() AI {
#pragma unroll
        for (int j = 0; j < 2; ++j) {
            gv_[j] = bf2f((u16)gg[j]);
            const float og = rbf(1.f - gv_[j]);
#pragma unroll
            for (int i = 0; i < 4; ++i) {
                const unsigned t = pack2(og * lo16(xr[j][i]), og * hi16(xr[j][i]));
                gb[j][i] = (f32x2){lo16(t), hi16(t)};
                asm volatile("" : "+v"(gb[j][i]));
            }
        }
    };
    asm volatile("" : "+v"(xr[0]), "+v"(xr[1]), "+v"(gg[0]), "+v"(gg[1]));
#pragma unroll 1
    for (unsigned s = 0; s < SPIN_CAP && __ballot((gg[0] >> 16) != (T & 0xFFFFFFFFFFFFull) || (gg[1] >> 16) != (T & 0xFFFFFFFFFFFFull)); ++s) {
        g_load();
        waitv<0>();
        asm volatile("" : "+v"(gg[0]), "+v"(gg[1]));
    }
    g_mix();
    if (PDL && ((w & 5) != 5 || WLK >= 0)) __builtin_amdgcn_s_sleep(PDL);
    if ((w & 5) != 5) waitv<2>();
    else waitv<(WON < 16 && WLK < 0 ? 3 + 4 * (16 - WON) : 2)>();
    asm volatile("" : "+v"(cl));
#pragma unroll 1
    for (unsigned s = 0; s < SPIN_CAP && __ballot(cl != T); ++s)
        asm volatile("global_load_dwordx2 %0, %1, off sc1\n\ts_waitcnt vmcnt(0)" : "=&v"(cl) : "v"(cla) : "memory");
    RS(16);
#pragma unroll
    for (int j = 0; j < 4; ++j) fetch1(0, j);
    RS(17);
    f32x4 acc[2][4];
    unsigned wa[2][4];
#pragma unroll
    for (int i = 0; i < 8; ++i) {
        wa[i >> 2][i & 3] = ((lds0 + 4096 * kp + (4 * g + q) * 128 + ((((g & 1) << 1) | (q >> 1)) * 32) + 8 * p) ^ ((i & 3) << 5)) + (i >> 2) * 65536;
        asm volatile("" : "+v"(wa[i >> 2][i & 3]));
    }
    s16x4 wf[2][8][2];
    auto wrd = [&](int k, int i) AI {
        const int u = wo_unit(2 * k + (i >> 3));
        wf[k & 1][i >> 1][i & 1] = tr_read(wa[u >> 3][(i >> 1) & 3] + (u & 7) * 8192 + (i & 1) * 2048);
    };
    u32x4 yp[14];
    const unsigned yo = srow * 4096 + cb * 256 + sch * 32;
    auto ya = [&](int i) { return ws + WS_Y + (i >> 1) * 32768 + (i & 1) * 16; };
    lds_wait(lds0 + L_FWD + ((l & 8) ? 16 + sch * 4 : (l & 1) * 4), 1);
    RS(18);
    RS(31);
    if (BPRIO && !kp) __builtin_amdgcn_s_setprio(BPRIO);
#pragma unroll
    for (int i = 0; i < 16; ++i) wrd(0, i);
#pragma unroll
    for (int j = 0; j < 4; ++j) fetch1(1, j);
    waitv<4>();
    fix(0);
    sfor<0, 8>([&](auto K) {
        constexpr int k = K;
        unsigned m[4];
        if constexpr (WON < 16 && 2 * (k + 1) == WON) lds_wait(lds0 + L_FW2 + (l & 1) * 4, 1);
        if constexpr (k == YK) {
            if (own) {
#pragma unroll
                for (int i = 0; i < 14; ++i) ld_async(yp[i], yo, ya(i));
            }
        }
        SB0();
        sfor<0, 16>([&](auto I) {
            constexpr int i = I, j = i >> 2, nb = i & 3;
            const bf16x8 a = cat8(wf[k & 1][(j >> 1) * 4 + nb][0], wf[k & 1][(j >> 1) * 4 + nb][1]);
            if (k == 0 && j < 2) asm volatile("v_mfma_f32_16x16x32_bf16 %0, %1, %2, 0" : "=&v"(acc[j & 1][nb]) : "v"(a), "v"(of[k % 3][j]));
            else asm volatile("v_mfma_f32_16x16x32_bf16 %0, %1, %2, %0" : "+v"(acc[j & 1][nb]) : "v"(a), "v"(of[k % 3][j]));
            SB0();
            if constexpr (k < 7) wrd(k + 1, i);
            if constexpr (k < 6 && i < 4) fetch1(k + 2, i);
            if constexpr (k < 7 && i == BCK) {
                waitv<(k < 6 ? 4 : 0)>();
#pragma unroll
                for (int n = 0; n < 4; ++n) pin(of[(k + 1) % 3][n]);
            }
            if constexpr (k < 7 && i > BCK && i <= BCK + 4) {
                m[i - BCK - 1] = min4(of[(k + 1) % 3][i - BCK - 1]);
                asm volatile("" : "+v"(m[i - BCK - 1]));
            }
            SB0();
        });
        if constexpr (k < 7) {
            if (__builtin_expect(__builtin_amdgcn_ballot_w64(min(min(m[0], m[1]), min(m[2], m[3])) == 0u) != 0ull, 0)) fix(k + 1);
        }
        SB0();
        if (k == 0) RS(19);
        if (k == 3) RS(23);
    });
    asm volatile("s_nop 7");
    SB0();
    RS(20);
    const unsigned ea = lds0 + L_A + l16 + 4096 * w, eb = lds0 + L_A + 4096 * (w ^ 4);
    lds_write<u32x4>(ea, nz4(acc[1][0]));
#pragma unroll
    for (int nb = 1; nb < 4; ++nb) lds_write<f32x4>(ea + nb * 1024, acc[1][nb]);
    if (own) {
        waitv<0>();
        settle(yp, [&](int) { return yo; }, ya);
        f32x4 yv[2];
#pragma unroll
        for (int h = 0; h < 2; ++h) {
            yv[h] = __builtin_bit_cast(f32x4, yp[h]);
#pragma unroll
            for (int i = 1; i < 7; ++i) yv[h] += __builtin_bit_cast(f32x4, yp[2 * i + h]);
#pragma unroll
            for (int i = 0; i < 4; ++i) {
                float t = dpp_add<0x128>(yv[h][i]);
                t = swap16_add(t, t);
                yv[h][i] = rbf(rbf(swap32_add(t, t)) * __uint_as_float(selv[1]));
            }
        }
#pragma unroll
        for (int j = 0; j < 2; ++j) {
            const bool hit = row0 + 8 * j + srow == (int)selv[0];
#pragma unroll
            for (int i = 0; i < 4; ++i) {
                const unsigned t = pack2(lo16(xr[j][i]) + yv[i >> 1][2 * (i & 1)], hi16(xr[j][i]) + yv[i >> 1][2 * (i & 1) + 1]);
                xr[j][i] = hit ? t : xr[j][i];
            }
        }
        g_mix();
    }
    BAR();
    RS(21);
#pragma unroll
    for (int nb = 0; nb < 4; ++nb) acc[0][nb] += lds_read<f32x4>(eb + l16 + nb * 1024);
#pragma unroll
    for (int nb = 0; nb < 4; ++nb) asm volatile("" : "+v"(acc[0][nb]));
#pragma unroll
    for (int nb = 0; nb < 4; ++nb) {
        const u32x2 a = {pack2(acc[0][nb][0], acc[0][nb][1]), pack2(acc[0][nb][2], acc[0][nb][3])};
        lds_write<u32x2>(eb + l15 * 128 + (((2 * nb + (g >> 1)) ^ (l & 7)) * 16) + (g & 1) * 8, a);
    }
#pragma unroll
    for (int j = 0; j < 2; ++j) {
        const u32x4 av = lds_read<u32x4>(eb + j * 1024 + srow * 128 + ((sch ^ srow) * 16));
        u32x4 o;
#pragma unroll
        for (int i = 0; i < 4; ++i) {
            const unsigned ga = pack2(gv_[j] * lo16(av[i]), gv_[j] * hi16(av[i]));
            o[i] = pack2(lo16(ga) + gb[j][i][0], hi16(ga) + gb[j][i][1]);
        }
        asm volatile("global_store_dwordx4 %0, %1, %2" OST "\n\ts_nop 1" ::"v"(xo), "v"(o), "s"(out + (size_t)(row0 + 8 * j) * DM + cb * 64) : "memory");
    }
    RS(22);
    RSDUMP();
}

extern "C" void kernel_launch(void* const* d_in, const int* in_sizes, int n_in, void* d_out, int out_size,
                              void* d_ws, size_t ws_size, hipStream_t stream) {
    fused_mix<<<256, 512, 0, stream>>>((const u16*)d_in[0], (const u16*)d_in[1], (const u16*)d_in[2], (const u16*)d_in[3],
                                       (const u16*)d_in[5], (const u16*)d_in[6], (const u16*)d_in[7], (const u16*)d_in[8],
                                       (const u16*)d_in[9], (char*)d_ws, (u16*)d_out);
}
```

```cpp
#include <hip/hip_runtime.h>
#include <hip/hip_bf16.h>
#include <stdint.h>
#include <type_traits>
#define MRG 36
#define CLL 1
#define CLE 1

typedef __attribute__((ext_vector_type(8))) short bf16x8;
typedef __attribute__((ext_vector_type(4))) short s16x4;
typedef __attribute__((ext_vector_type(4))) float f32x4;
typedef __attribute__((ext_vector_type(2))) float f32x2;
typedef __attribute__((ext_vector_type(2))) unsigned u32x2;
typedef __attribute__((ext_vector_type(4))) unsigned u32x4;
typedef __attribute__((ext_vector_type(2))) __bf16 bf16x2_t;
typedef unsigned short u16;
typedef unsigned long long u64;

#define DI __device__ __forceinline__
#define AS3 __attribute__((address_space(3)))
#define AS1 __attribute__((address_space(1)))
#define RLX_AGENT __ATOMIC_RELAXED, __HIP_MEMORY_SCOPE_AGENT

constexpr int SEQ = 2048, DM = 1024, NH = 32, FF = 3584;

DI float bf2f(u16 v) { return __uint_as_float(((unsigned)v) << 16); }
DI u16 f2bf(float f) { __bf16 b = (__bf16)f; return __builtin_bit_cast(u16, b); }
DI float rbf(float f) { return bf2f(f2bf(f)); }
DI unsigned pack2(float lo, float hi) {
    const f32x2 v = {lo, hi};
    return __builtin_bit_cast(unsigned, __builtin_convertvector(v, bf16x2_t));
}
DI float lo16(unsigned u) { return __uint_as_float(u << 16); }
DI float hi16(unsigned u) { return __uint_as_float(u & 0xffff0000u); }

DI s16x4 tr_read(unsigned a) { return __builtin_amdgcn_ds_read_tr16_b64_v4i16((AS3 s16x4*)(size_t)a); }
DI bf16x8 lds_read128(unsigned a) { return *(AS3 const bf16x8*)(size_t)a; }
template <class T> DI void lds_write(unsigned a, T v) { *(AS3 T*)(size_t)a = v; }
template <class T> DI T lds_read(unsigned a) { return *(AS3 const T*)(size_t)a; }
DI bf16x8 cat8(s16x4 a, s16x4 b) { return __builtin_shufflevector(a, b, 0, 1, 2, 3, 4, 5, 6, 7); }
DI f32x4 mfma16(bf16x8 a, bf16x8 b, f32x4 c) { return __builtin_amdgcn_mfma_f32_16x16x32_bf16(a, b, c, 0, 0, 0); }
DI bf16x8 pack44(const f32x4& a, const f32x4& b) {
    u32x4 r;
    r[0] = pack2(a[0], a[1]);
    r[1] = pack2(a[2], a[3]);
    r[2] = pack2(b[0], b[1]);
    r[3] = pack2(b[2], b[3]);
    return __builtin_bit_cast(bf16x8, r);
}
DI unsigned lds_of(const void* p) { return (unsigned)(size_t)(AS3 const char*)p; }

template <int CTRL>
DI unsigned dpp(unsigned x) { return __builtin_amdgcn_update_dpp(0u, x, CTRL, 0xf, 0xf, true); }
template <int CTRL>
DI float dpp_add(float x) { return x + __uint_as_float(dpp<CTRL>(__float_as_uint(x))); }
DI float swap32_add(float a, float b) {
    auto r = __builtin_amdgcn_permlane32_swap(__float_as_uint(a), __float_as_uint(b), false, false);
    return __uint_as_float(r[0]) + __uint_as_float(r[1]);
}
DI float swap16_add(float a, float b) {
    auto r = __builtin_amdgcn_permlane16_swap(__float_as_uint(a), __float_as_uint(b), false, false);
    return __uint_as_float(r[0]) + __uint_as_float(r[1]);
}
DI float wave_sum(float x) {
    x = dpp_add<0xB1>(x);
    x = dpp_add<0x4E>(x);
    x = dpp_add<0x141>(x);
    x = dpp_add<0x140>(x);
    x = swap16_add(x, x);
    return swap32_add(x, x);
}
DI unsigned wave_umax(unsigned x) {
    x = max(x, dpp<0xB1>(x));
    x = max(x, dpp<0x4E>(x));
    x = max(x, dpp<0x141>(x));
    x = max(x, dpp<0x140>(x));
    auto r = __builtin_amdgcn_permlane16_swap(x, x, false, false);
    x = max(r[0], r[1]);
    auto s = __builtin_amdgcn_permlane32_swap(x, x, false, false);
    return max(s[0], s[1]);
}
template <class OP, class POST>
DI void red2(float& a, float& b, OP op, POST post) {
    auto x = __builtin_amdgcn_permlane32_swap(__float_as_uint(a), __float_as_uint(b), false, false);
    const float m = op(__uint_as_float(x[0]), __uint_as_float(x[1]));
    auto y = __builtin_amdgcn_permlane16_swap(__float_as_uint(m), __float_as_uint(m), false, false);
    const float f = post(op(__uint_as_float(y[0]), __uint_as_float(y[1])));
    auto z = __builtin_amdgcn_permlane32_swap(__float_as_uint(f), __float_as_uint(f), false, false);
    a = __uint_as_float(z[0]);
    b = __uint_as_float(z[1]);
}

#define RS(i) do {} while (0)
#define RSD(i) do {} while (0)
#define RSDUMP() do {} while (0)

constexpr unsigned WS_E = 0;
constexpr unsigned WS_RG = 256;
constexpr unsigned WS_GG = WS_RG + 2048;
constexpr unsigned WS_CL = 32768;
constexpr unsigned WS_Z = 65536;
constexpr unsigned WS_KF = WS_Z + 131072;
constexpr unsigned WS_GU = WS_KF + 262144;
constexpr unsigned WS_Y = WS_GU + 262144;
constexpr unsigned WS_KV = WS_Y + 262144;
constexpr unsigned WS_O = WS_KV + (2u << 20);
constexpr unsigned WS_C2 = WS_O + (4u << 20);
constexpr unsigned WS_END = WS_C2 + 65536;

constexpr int WE = 0;
constexpr int NQ = 10, ND = 6, NX = 16, KD = 78, VD = 84, NF = 64, NF1 = 59, NF2 = 59, NP1 = 12, NP = 14, NM1 = 21, NM2 = 21, NM = 22, KFN = 100;
constexpr int J_S[48] = {0, 0, 0, 1, 1, 1, 2, 2, 2, 3, 3, 3, 4, 4, 4, 5, 5, 5, 6, 6, 6, 7, 7, 7, 8, 8, 8, 9, 9, 9, 10, 10, 11, 11, 12, 12, 13, 14, 15, 13, 14, 15, 10, 11, 12, 13, 14, 15};
constexpr int J_M[48] = {0, 1, 2, 0, 1, 2, 0, 1, 2, 0, 1, 2, 0, 1, 2, 0, 1, 2, 0, 1, 2, 0, 1, 2, 0, 1, 2, 0, 1, 2, 0, 1, 0, 1, 0, 1, 0, 0, 0, 1, 1, 1, 2, 2, 2, 2, 2, 2};
constexpr int J_X[48] = {0, 0, 0, 1, 1, 1, 2, 2, 2, 3, 3, 3, 4, 4, 4, 5, 5, 5, 6, 6, 6, 7, 7, 7, 8, 8, 8, 9, 9, 9, 10, 10, 11, 11, 12, 12, 13, 14, 15, 13, 14, 15, 10, 11, 12, 13, 14, 15};
constexpr int J_U[48] = {2, 4, 8, 18, 11, 12, 17, 15, 13, 0, 1, 6, 4, 8, 9, 11, 12, 14, 15, 13, 7, 1, 6, 5, 8, 9, 10, 12, 14, 16, 13, 7, 1, 6, 4, 8, 18, 14, 15, 13, 7, 3, 0, 1, 6, 5, 2, 4};
constexpr int J_B[48] = {2, 2, 2, 11, 11, 11, 11, 11, 11, 0, 0, 0, 0, 8, 8, 8, 8, 8, 8, 8, 1, 1, 1, 1, 1, 9, 9, 9, 9, 9, 9, 1, 1, 1, 1, 1, 13, 13, 13, 13, 0, 0, 0, 0, 0, 0, 0, 0};
constexpr int Q_C[96] = {1, 0, 2, 0, 3, 0, 4, 0, 5, 0, 6, 0, 7, 0, 0, 0, 0, 0, 8, 0, 0, 0, 0, 0, 9, 0, 0, 0, 0, 0, 10, 0, 0, 0, 0, 0, 11, 0, 0, 0, 0, 0, 12, 0, 0, 0, 0, 0, 13, 0, 0, 0, 0, 0, 14, 0, 0, 0, 0, 0, 15, 0, 0, 0, 0, 0, 16, 0, 0, 0, 0, 0, 17, 0, 0, 0, 0, 0, 18, 0, 19, 0, 20, 0, 21, 0, 22, 0, 0, 0, 0, 0, 0, 0, 0, 0};
constexpr int X_U0[16] = {6, 9, 14, 7, 5, 10, 16, 3, 2, 18, 17, 3, 5, 9, 11, 16};
constexpr int X_U1[16] = {5, 10, 16, 3, 2, 18, 17, 0, 4, 11, 15, 0, 2, 10, 12, 17};
constexpr int P_U[4] = {11, 12, 9, 10};
constexpr int W_U[16] = {8, 14, 15, 13, 7, 3, 9, 10, 11, 12, 0, 1, 6, 5, 2, 4};
constexpr int W_N[16] = {100, 100, 100, 100, 100, 100, 101, 101, 101, 101, 13, 13, 14, 14, 99, 99};
constexpr int F_J[64] = {-1, 0, 1, 2, -2, 3, 4, 5, -3, 6, 7, 8, -4, 9, 10, 11, -5, 12, 13, 14, -6, 15, 16, 17, -7, 18, 19, 20, -8, 21, 22, 23, -9, 24, 25, 26, -10, 27, 28, 29, -11, 30, 31, -12, 32, 33, -13, 34, 35, -14, 36, -15, 37, -16, 38, 39, 40, 41, 42, 43, 44, 45, 46, 47};
constexpr int F_N[64] = {0, 0, 0, 0, 0, 0, 0, 0, 0, 0, 0, 0, 0, 0, 0, 1, 1, 1, 1, 1, 2, 2, 2, 2, 3, 3, 3, 3, 4, 4, 4, 4, 5, 5, 5, 5, 6, 6, 6, 6, 7, 7, 7, 7, 8, 8, 8, 8, 9, 9, 9, 10, 10, 10, 10, 11, 11, 11, 11, 12, 12, 12, 12, 12};
constexpr int F_K[64] = {0, 1, 2, 3, 0, 4, 5, 6, 0, 0, 0, 7, 0, 0, 0, 8, 0, 0, 0, 9, 0, 0, 0, 10, 0, 0, 0, 11, 0, 0, 0, 12, 0, 0, 0, 13, 0, 0, 0, 14, 0, 0, 0, 0, 15, 0, 0, 0, 16, 0, 0, 0, 0, 0, 17, 18, 19, 20, 21, 0, 0, 0, 0, 22};
constexpr int E_A[384] = {300, 325, 350, 375, 0, 0, 0, 502, 0, 0, 0, 403, 0, 0, 0, 503, 201, 251, 0, 404, 0, 100, 101, 504, 301, 326, 351, 376, 0, 0, 0, 505, 601, 0, 0, 406, 0, 0, 0, 506, 202, 252, 0, 407, 0, 0, 0, 507, 302, 327, 352, 377, 0, 0, 0, 602, 0, 0, 0, 0, 0, 0, 0, 203, 253, 0, 0, 408, 0, 100, 101, 508, 303, 328, 353, 378, 0, 0, 0, 603, 0, 0, 0, 0, 0, 0, 0, 204, 254, 0, 0, 409, 0, 0, 0, 509, 304, 329, 354, 379, 0, 100, 101, 604, 102, 103, 0, 0, 0, 0, 0, 205, 255, 0, 0, 410, 0, 0, 0, 510, 305, 330, 355, 380, 0, 0, 0, 605, 0, 0, 0, 0, 0, 0, 0, 206, 256, 0, 0, 411, 0, 0, 0, 511, 306, 331, 356, 381, 0, 0, 0, 606, 0, 0, 0, 0, 0, 100, 101, 102, 103, 207, 257, 412, 0, 0, 0, 512, 307, 332, 357, 382, 0, 0, 0, 607, 0, 0, 0, 0, 0, 0, 0, 208, 258, 0, 0, 413, 0, 0, 0, 513, 308, 333, 358, 383, 0, 100, 101, 608, 102, 103, 0, 0, 0, 0, 0, 209, 259, 0, 0, 414, 0, 0, 0, 514, 309, 334, 359, 384, 0, 0, 0, 609, 0, 0, 0, 0, 0, 0, 0, 210, 260, 0, 0, 415, 0, 0, 0, 515, 310, 335, 360, 385, 0, 100, 101, 610, 102, 103, 211, 261, 0, 0, 0, 0, 311, 336, 361, 386, 0, 0, 0, 516, 611, 212, 262, 0, 0, 0, 0, 0, 312, 337, 362, 387, 0, 0, 0, 213, 263, 0, 0, 417, 0, 100, 101, 517, 313, 338, 363, 388, 214, 264, 0, 612, 314, 339, 364, 389, 215, 265, 0, 0, 315, 340, 365, 390, 0, 0, 0, 518, 0, 0, 0, 419, 0, 100, 101, 519, 102, 103, 0, 420, 0, 0, 0, 520, 0, 0, 0, 0, 0, 0, 0, 0, 0, 0, 0, 422, 0, 0, 0, 522, 0, 0, 0, 0, 0, 0, 0, 0, 0, 0, 0, 613, 0, 0, 0, 0, 0, 0, 0, 0, 0, 0, 0, 0, 0, 0, 0, 614, 0, 0, 0, 0, 0, 0, 0, 0, 0, 0, 0, 0};
constexpr int E_B[384] = {0, 0, 402, 0, 0, 0, 0, 0, 0, 0, 0, 0, 0, 0, 0, 0, 0, 0, 0, 0, 0, 0, 0, 102, 103, 0, 405, 0, 0, 0, 0, 0, 0, 0, 0, 0, 0, 0, 0, 0, 0, 0, 0, 0, 0, 0, 0, 0, 0, 0, 0, 0, 0, 0, 0, 0, 0, 0, 0, 0, 0, 0, 0, 0, 0, 0, 0, 0, 0, 0, 0, 102, 103, 0, 0, 0, 0, 0, 0, 0, 0, 0, 0, 0, 0, 0, 0, 0, 0, 0, 0, 0, 0, 0, 0, 0, 0, 0, 0, 0, 0, 0, 0, 0, 0, 0, 0, 0, 0, 0, 0, 0, 0, 0, 0, 0, 0, 0, 0, 0, 0, 0, 0, 0, 0, 0, 0, 0, 0, 0, 0, 0, 0, 0, 0, 0, 0, 0, 0, 0, 0, 0, 0, 0, 0, 0, 0, 0, 0, 0, 0, 0, 0, 0, 0, 0, 0, 0, 0, 0, 0, 0, 0, 0, 0, 0, 0, 0, 0, 0, 0, 0, 0, 0, 0, 0, 0, 0, 0, 0, 0, 0, 0, 0, 0, 0, 0, 0, 0, 0, 0, 0, 0, 0, 0, 0, 0, 0, 0, 0, 0, 0, 0, 0, 0, 0, 0, 0, 0, 0, 0, 0, 0, 0, 0, 0, 0, 0, 0, 0, 0, 0, 0, 0, 0, 0, 0, 0, 0, 0, 0, 0, 0, 0, 0, 0, 0, 0, 0, 0, 0, 0, 0, 0, 0, 0, 0, 0, 0, 0, 0, 0, 0, 0, 0, 0, 0, 0, 416, 0, 0, 0, 0, 0, 0, 0, 0, 0, 0, 0, 0, 0, 0, 0, 0, 0, 0, 0, 0, 0, 0, 0, 0, 0, 0, 0, 0, 102, 103, 0, 0, 0, 0, 0, 0, 0, 0, 0, 0, 0, 0, 0, 0, 0, 0, 0, 418, 0, 0, 0, 0, 0, 0, 0, 0, 0, 0, 0, 0, 0, 0, 0, 0, 0, 0, 0, 0, 0, 0, 0, 0, 0, 0, 0, 0, 0, 0, 0, 0, 0, 0, 0, 0, 0, 0, 0, 0, 0, 0, 0, 0, 0, 0, 0, 0, 0, 0, 0, 0, 0, 0, 0, 0, 0, 0, 0, 0, 0, 0, 0, 0, 0, 0, 0, 0, 0, 0, 0, 0, 0, 0, 0, 0, 0};
#define LH 0
constexpr unsigned L_A = 16 * 8192;
constexpr unsigned L_KF = 18 * 8192;
constexpr unsigned L_SM = 19 * 8192;
constexpr unsigned L_FULL = L_SM + 256;
constexpr unsigned L_FVR = L_SM + 320, L_PRL = L_SM + 336;
constexpr unsigned L_PROG = L_SM + 896;
constexpr unsigned L_FW2 = L_SM + 512, L_FOS = L_SM + 480, L_FGI = L_SM + 520, L_FK0 = L_SM + 752, L_FBI = L_SM + 744;
constexpr unsigned L_FK = L_SM + 528, L_FKT = L_SM + 544, L_FV = L_SM + 560, L_FP = L_SM + 576, L_FKS = L_SM + 592;
constexpr unsigned L_FQD = L_SM + 608, L_FG = L_SM + 624, L_FKF = L_SM + 736, L_FQ = L_SM + 720, L_FWD = L_SM + 640, L_FWO = L_SM + 656, L_FLD = L_SM + 688, L_BEST = L_SM + 696;
constexpr unsigned L_SEL = L_SM + 704;
constexpr unsigned L_XS = L_SM + 2048, L_KZ = L_SM + 3072, L_WPHI = L_SM + 4096;
static_assert(NP <= 31 && NM <= 63, "");
static_assert(P_U[1] == P_U[0] + 1 && P_U[3] == P_U[2] + 1, "feature maps of a head lie side by side");
constexpr int wo_unit(int i) { return W_U[i]; }
constexpr int f_u(int f) { return f & 3; }
constexpr bool j_first(int j) {
    for (int i = 0; i < j; ++i)
        if (J_M[i] == J_M[j]) return false;
    return true;
}
constexpr int younger(int p) {
    int c = 0;
    for (int n = 0; n < NF1 - LH; ++n)
        if (F_N[n] >= p) c += F_J[n] < 0 ? 8 : 4;
    return c;
}
constexpr int nfv() {
    for (int n = 0; n < NF1; ++n)
        if (F_J[n] == VD / 2 - 1) return n + 1;
    return NF1;
}
constexpr int next_need(int n) {
    for (int i = n; i < NF; ++i)
        if (F_N[i] != F_N[n]) return F_N[i];
    return 99;
}
constexpr int x_stage(int i) { return i < 16 ? i : i - 16 + NQ; }
constexpr unsigned SPIN_CAP = 1u << 16;
#define N8(a) a, a + 1, a + 2, a + 3, a + 4, a + 5, a + 6, a + 7
__device__ const unsigned k_num[64] = {N8(0), N8(8), N8(16), N8(24), N8(32), N8(40), N8(48), N8(56)};

#define RK(i) do {} while (0)
#define RSL(i) do {} while (0)
#define RSC(i) do {} while (0)
#define CSN 6
#define CFP 0
#define QWM 0
#define RLD 11
#define CLF 3
#define CLT 1
#define FLV 1
#define SP1 0
#define SP2 0
#define SP3 0
#define SP4 0
#define SPW 0
#define SPI "s_load_dwordx2"
#define DNP 1
#define WOT 1
#define YT 1
#define GDL 16
#define SSM 0
#define GUO 0
#define TL0 0
#define TL0W ""
#define TLD 0
#define SPA 0
#define SPS 7
#define PP1 0
#define PD1 0
#define PP2 0
#define PD2 0
#define PP3 0
#define PD3 0
#define PDI 0
#define HLD " sc1"
#define HST " sc1"
#define HSA 16
#define BMAP 0
#define ROTC 0
#define ROT0 0
#define OCL 0
#define KCL 0
#define ZL 1
#define RTS 0
#define LPRIO 0
#define LPRW 3
#define LTH 63
#define LT0 14
#define PEEK 1
#define CSP 1
#define GUA 12
#define OPL 8
#define OPM ""
#define PD 4
#define NPH 4
#define KSK 3
#define ROTK 8
#define OWIN 2
#define CLR0 4
#define CLS 1
#define CLP 2
#define SLK 2
#define CPH 3
#define NPL 0
#define CPRIO 1
#define HPRIO 2
#define KPK 3
#define KRND 1
#define QW 1
#define WOG 1
#define WON 12
#define KFD 24
#define SEL0 9
#define SPRIO 2
#define FBA 3
#define FBB 10
#define FBS 1
#define FBL 1
#define WOP 0
#define GTL 3
#define WPF 0
#define WOE 6
#define GW 0
#define GUF 0
#define FBH 0
#define WG2 0
#define NTX 0
#define NTW 0
#define NTS 16
#define NTO ""
#define GQ1 6
#define GQ2 8
#define CLO 0
#define PDL 6
#define XK 3
#define YK 7
#define BPRIO 0
#define AMF 1
#define LF 1
#define SH 2
#define LC 0
#define SPR2 0
#define GQ0 0
#define CPF 53248
#define NF0 4
#define OTL 0
#define OST " sc1"
#define TLW 3
#define THR -1
#define THQ -1
#define FDH 0
#define WLK -1
#define XGE 1
#define KXO 3
#define KXM 3
#define KX8 0
#define KXB 0xFEDBA9C768534210ull
#define BCK 10
#define GPR 0
#define GHL 0
#define TGT 1
#define PORD 1
#define TRM 3
#define LFE 1
#define PWN 0
#define PWL 99
#define TRN 0
#define RT0 0
#define BSP 0
#define WOV 0
#define GUP 1
#define NOF 0
#define FBD 8
#define BAR() asm volatile("s_waitcnt lgkmcnt(0)\n\ts_barrier" ::: "memory")
template <int N>
DI void waitv() { asm volatile("s_waitcnt vmcnt(%0)" ::"n"(N) : "memory"); }

DI u64 ld_agent(const char* p) { return __hip_atomic_load((const AS1 u64*)p, RLX_AGENT); }
DI void st_agent(char* p, u64 v) { __hip_atomic_store((AS1 u64*)p, v, RLX_AGENT); }
DI unsigned nz1(unsigned v) { return max(v, 1u); }
DI unsigned umin(unsigned a, unsigned b) { return min(a, b); }
DI u32x4 nz4(u32x4 v) { return (u32x4){max(v[0], 1u), max(v[1], 1u), max(v[2], 1u), max(v[3], 1u)}; }
DI u32x4 nz4(f32x4 v) { return nz4(__builtin_bit_cast(u32x4, v)); }
DI unsigned min4(u32x4 v) { return min(min(v[0], v[1]), min(v[2], v[3])); }
DI u32x4 ld_wt(__amdgpu_buffer_rsrc_t r, unsigned off) { return __builtin_amdgcn_raw_buffer_load_b128(r, off, 0, 16); }
DI void st_wt(__amdgpu_buffer_rsrc_t r, unsigned off, u32x4 v) { __builtin_amdgcn_raw_buffer_store_b128(v, r, off, 0, HSA); }
DI void ld_async(u32x4& v, unsigned off, const char* base) {
    asm volatile("global_load_dwordx4 %0, %1, %2 sc1" : "=&v"(v) : "v"(off), "s"(base) : "memory");
}
#define ST_MASK(op, m, off, v, base) asm volatile("s_mov_b64 exec, %3\n\t" op " %0, %1, %2" HST "\n\ts_mov_b64 exec, -1\n\ts_nop 0" ::"v"(off), "v"(v), "s"(base), "s"((u64)(m)) : "memory")
DI void pin(u32x4& v) { asm volatile("" : "+v"(v)); }
template <int N, class OFF, class BASE, class OK>
DI void settle(u32x4 (&v)[N], OFF off, BASE base, OK ok) {
#pragma unroll
    for (int i = 0; i < N; ++i) pin(v[i]);
#pragma unroll 1
    for (unsigned s = 0; s < SPIN_CAP; ++s) {
        unsigned m = ok(v[0]);
#pragma unroll
        for (int i = 1; i < N; ++i) m = min(m, ok(v[i]));
        if (__ballot(m == 0u) == 0ull) break;
#pragma unroll
        for (int i = 0; i < N; ++i) {
            u64 keep;
            asm volatile("s_mov_b64 %1, exec\n\ts_mov_b64 exec, %4\n\tglobal_load_dwordx4 %0, %2, %3 sc1\n\ts_mov_b64 exec, %1"
                         : "+&v"(v[i]), "=&s"(keep) : "v"(off(i)), "s"(base(i)), "s"((u64)__ballot(ok(v[i]) == 0u)) : "memory");
        }
        waitv<0>();
#pragma unroll
        for (int i = 0; i < N; ++i) pin(v[i]);
    }
}
template <int N, class OFF, class BASE>
DI void settle(u32x4 (&v)[N], OFF off, BASE base) { settle(v, off, base, [](const u32x4& a) { return min4(a); }); }
template <int P, int D, int N, class OFF, class BASE>
DI void settle_p(u32x4 (&v)[N], OFF off, BASE base) {
#pragma unroll 1
    for (unsigned s = 0;; ++s) {
        u64 miss[N], any = 0;
#pragma unroll
        for (int i = 0; i < N; ++i) {
            pin(v[i]);
            miss[i] = __ballot(min4(v[i]) == 0u);
            any |= miss[i];
        }
        if (s >= SPIN_CAP) any = 0;
        asm volatile("" : "+s"(any));
        if (any == 0ull) break;
#pragma unroll
        for (int i = 0; i < N; ++i)
            asm volatile("s_mov_b64 exec, %3\n\tglobal_load_dwordx4 %0, %1, %2" HLD "\n\ts_mov_b64 exec, -1"
                         ::"v"(v[i]), "v"(off(i)), "s"(base(i)), "s"(miss[i] | 1ull) : "memory");
        if (D) __builtin_amdgcn_s_sleep(D);
        waitv<N * (P - 1)>();
    }
}
DI void lds_wait(unsigned a, unsigned n) {
    unsigned v, c;
    asm volatile("s_mov_b32 %1, 0x400000\n"
                 ".Lp%=:\n\tds_read_b32 %0, %2\n\ts_waitcnt lgkmcnt(0)\n\tv_cmp_gt_u32 vcc, %3, %0\n\ts_cbranch_vccz .Lf%=\n\t"
                 "s_sleep 1\n\ts_sub_u32 %1, %1, 1\n\ts_cbranch_scc0 .Lp%=\n.Lf%=:"
                 : "=&v"(v), "=&s"(c) : "v"(a), "s"(n) : "memory", "vcc", "scc");
}
DI void lds_wait0(unsigned a) {
    if (!TGT) { lds_wait(a, 1); return; }
    unsigned v, c;
    asm volatile("s_mov_b32 %1, 0x1000000\n"
                 ".Lp%=:\n\tds_read_b32 %0, %2\n\ts_sub_u32 %1, %1, 1\n\ts_cbranch_scc1 .Lf%=\n\ts_waitcnt lgkmcnt(0)\n\tv_cmp_eq_u32 vcc, 0, %0\n\ts_cbranch_vccnz .Lp%=\n"
                 ".Lf%=:\n\ts_waitcnt lgkmcnt(0)"
                 : "=&v"(v), "=&s"(c) : "v"(a) : "memory", "vcc", "scc");
}
DI void lds_wait8(unsigned a, unsigned n) {
    unsigned v, c;
    asm volatile("s_mov_b32 %1, 0x100000\n"
                 ".Lp%=:\n\tds_read_b32 %0, %2\n\ts_waitcnt lgkmcnt(0)\n\tv_cmp_gt_u32 vcc, %3, %0\n\ts_cbranch_vccz .Lf%=\n\t"
                 "s_sleep 4\n\ts_sub_u32 %1, %1, 1\n\ts_cbranch_scc0 .Lp%=\n.Lf%=:"
                 : "=&v"(v), "=&s"(c) : "v"(a), "s"(n) : "memory", "vcc", "scc");
}
DI void lds_peek(unsigned& v, unsigned a) { asm volatile("ds_read_b32 %0, %1" : "=v"(v) : "v"(a) : "memory"); }
DI void lds_wait(unsigned& v, unsigned a, unsigned n) {
    unsigned c;
    asm volatile("s_mov_b32 %1, 0x400000\n"
                 ".Lp%=:\n\ts_waitcnt lgkmcnt(0)\n\tv_cmp_gt_u32 vcc, %3, %0\n\ts_cbranch_vccz .Lf%=\n\t"
                 "ds_read_b32 %0, %2\n\ts_sub_u32 %1, %1, 1\n\ts_cbranch_scc0 .Lp%=\n.Lf%=:"
                 : "+v"(v), "=&s"(c) : "v"(a), "s"(n) : "memory", "vcc", "scc");
}
DI void lds_set(unsigned a) {
    asm volatile("s_waitcnt lgkmcnt(0)\n\tds_write_b32 %0, %1" ::"v"(a), "v"(1u) : "memory");
}
DI float dot2(unsigned a, unsigned b, float c) {
    return __builtin_amdgcn_fdot2_f32_bf16(__builtin_bit_cast(bf16x2_t, a), __builtin_bit_cast(bf16x2_t, b), c, false);
}
DI float sum4(const float (&a)[4]) {
    float c = swap16_add(swap32_add(a[0], a[1]), swap32_add(a[2], a[3]));
    c = dpp_add<0xB1>(c);
    c = dpp_add<0x4E>(c);
    c = dpp_add<0x141>(c);
    return dpp_add<0x140>(c);
}

template <class OP, class POST>
DI void red4(float& a, float& b, float& c, float& d, OP op, POST post) {
    auto x = __builtin_amdgcn_permlane32_swap(__float_as_uint(a), __float_as_uint(b), false, false);
    auto y = __builtin_amdgcn_permlane32_swap(__float_as_uint(c), __float_as_uint(d), false, false);
    const float m = op(__uint_as_float(x[0]), __uint_as_float(x[1])), n = op(__uint_as_float(y[0]), __uint_as_float(y[1]));
    auto z = __builtin_amdgcn_permlane16_swap(__float_as_uint(m), __float_as_uint(n), false, false);
    const unsigned f = __float_as_uint(post(op(__uint_as_float(z[0]), __uint_as_float(z[1]))));
    auto u = __builtin_amdgcn_permlane16_swap(f, f, false, false);
    auto v = __builtin_amdgcn_permlane32_swap(u[0], u[0], false, false);
    auto t = __builtin_amdgcn_permlane32_swap(u[1], u[1], false, false);
    a = __uint_as_float(v[0]);
    b = __uint_as_float(v[1]);
    c = __uint_as_float(t[0]);
    d = __uint_as_float(t[1]);
}
DI float mx2(float a, float b) { return __builtin_elementwise_maximum(a, b); }
DI float opq(float a) { asm("" : "+v"(a)); return a; }
template <bool PK, class HOOK>
DI void softmax2(const f32x4 (&v)[2][2], u32x4 (&ph)[2][2], HOOK hook) {
    constexpr float L2E = 1.4426950408889634f;
    float mx[2], mn[2], sp_[2], sn_[2];
#pragma unroll
    for (int b = 0; b < 2; ++b) {
        const f32x4 s = v[b][0], t = v[b][1];
        mx[b] = mx2(mx2(mx2(mx2(s[0], s[1]), s[2]), mx2(mx2(s[3], t[0]), t[1])), mx2(t[2], t[3]));
        mn[b] = mx2(mx2(mx2(mx2(-s[0], -s[1]), -s[2]), mx2(mx2(-s[3], -t[0]), -t[1])), mx2(-t[2], -t[3]));
    }
    red4(mx[0], mx[1], mn[0], mn[1], [](float a, float b) { return mx2(a, b); }, [](float a) { return a * -L2E; });
    f32x2 ep[2][4], en[2][4];
#pragma unroll
    for (int b = 0; b < 2; ++b) {
#pragma unroll
        for (int e = 0; e < 4; ++e) {
            const f32x2 u = {v[b][e >> 1][2 * (e & 1)], v[b][e >> 1][2 * (e & 1) + 1]};
            f32x2 a, c;
            if (PK) {
                a = __builtin_elementwise_fma(u, (f32x2){L2E, L2E}, (f32x2){mx[b], mx[b]});
                c = __builtin_elementwise_fma(u, (f32x2){-L2E, -L2E}, (f32x2){mn[b], mn[b]});
            } else {
                a = (f32x2){__builtin_fmaf(u[0], L2E, mx[b]), __builtin_fmaf(u[1], L2E, mx[b])};
                c = (f32x2){__builtin_fmaf(u[0], -L2E, mn[b]), __builtin_fmaf(u[1], -L2E, mn[b])};
            }
            ep[b][e] = (f32x2){__builtin_amdgcn_exp2f(a[0]), __builtin_amdgcn_exp2f(a[1])};
            en[b][e] = (f32x2){__builtin_amdgcn_exp2f(c[0]), __builtin_amdgcn_exp2f(c[1])};
        }
        if (PK) {
            const f32x2 s = (ep[b][0] + ep[b][1]) + (ep[b][2] + ep[b][3]), t = (en[b][0] + en[b][1]) + (en[b][2] + en[b][3]);
            sp_[b] = s[0] + s[1];
            sn_[b] = t[0] + t[1];
        } else {
            sp_[b] = ep[b][0][0];
            sn_[b] = en[b][0][0];
#pragma unroll
            for (int e = 1; e < 8; ++e) sp_[b] += ep[b][e >> 1][e & 1], sn_[b] += en[b][e >> 1][e & 1];
        }
    }
    asm volatile("" : "+v"(sp_[0]), "+v"(sn_[0]), "+v"(sp_[1]), "+v"(sn_[1]));
    hook();
    red4(sp_[0], sp_[1], sn_[0], sn_[1], [](float a, float b) { return a + b; }, [](float a) { return __builtin_amdgcn_rcpf(a); });
#pragma unroll
    for (int b = 0; b < 2; ++b)
#pragma unroll
        for (int e = 0; e < 4; ++e) {
            if (PK) {
                const f32x2 a = ep[b][e] * (f32x2){sp_[b], sp_[b]}, c = en[b][e] * (f32x2){sn_[b], sn_[b]};
                ph[b][0][e] = pack2(a[0], a[1]);
                ph[b][1][e] = pack2(c[0], c[1]);
            } else {
                ph[b][0][e] = pack2(opq(ep[b][e][0] * sp_[b]), ep[b][e][1] * sp_[b]);
                ph[b][1][e] = pack2(opq(en[b][e][0] * sn_[b]), en[b][e][1] * sn_[b]);
            }
        }
}
DI f32x4 rbf4(const f32x4& a) {
    const unsigned u = pack2(a[0], a[1]), v = pack2(a[2], a[3]);
    return (f32x4){lo16(u), hi16(u), lo16(v), hi16(v)};
}
DI int fsw(int row) { return (((row >> 1) & 3) << 2) | (((row >> 3) & 1) << 1) | (row & 1); }
template <bool PK, class HOOK>
DI void phi_k(const f32x4 (&pp)[2][2], const unsigned (&pw)[4], unsigned off, HOOK hook) {
    u32x4 ph[2][2];
    softmax2<PK>(pp, ph, hook);
#pragma unroll
    for (int b = 0; b < 2; ++b)
#pragma unroll
        for (int kf = 0; kf < 2; ++kf)
#pragma unroll
            for (int fb = 0; fb < 2; ++fb) {
                const u32x2 a = {ph[b][kf][2 * fb], ph[b][kf][2 * fb + 1]};
                lds_write<u32x2>(pw[2 * kf + fb] + off + b * 2048, a);
            }
}

DI void phi_k1(const f32x4 (&v)[2], const unsigned (&pw)[4]) {
    constexpr float L2E = 1.4426950408889634f;
    const f32x4 s = v[0], t = v[1];
    float mx = mx2(mx2(mx2(mx2(s[0], s[1]), s[2]), mx2(mx2(s[3], t[0]), t[1])), mx2(t[2], t[3]));
    float mn = mx2(mx2(mx2(mx2(-s[0], -s[1]), -s[2]), mx2(mx2(-s[3], -t[0]), -t[1])), mx2(-t[2], -t[3]));
    red2(mx, mn, [](float a, float b) { return mx2(a, b); }, [](float a) { return a * -L2E; });
    f32x2 ep[4], en[4];
#pragma unroll
    for (int e = 0; e < 4; ++e) {
        const f32x2 u = {v[e >> 1][2 * (e & 1)], v[e >> 1][2 * (e & 1) + 1]};
        const f32x2 a = __builtin_elementwise_fma(u, (f32x2){L2E, L2E}, (f32x2){mx, mx});
        const f32x2 c = __builtin_elementwise_fma(u, (f32x2){-L2E, -L2E}, (f32x2){mn, mn});
        ep[e] = (f32x2){__builtin_amdgcn_exp2f(a[0]), __builtin_amdgcn_exp2f(a[1])};
        en[e] = (f32x2){__builtin_amdgcn_exp2f(c[0]), __builtin_amdgcn_exp2f(c[1])};
    }
    const f32x2 a = (ep[0] + ep[1]) + (ep[2] + ep[3]), c = (en[0] + en[1]) + (en[2] + en[3]);
    float sp = a[0] + a[1], sn = c[0] + c[1];
    red2(sp, sn, [](float a, float b) { return a + b; }, [](float a) { return __builtin_amdgcn_rcpf(a); });
#pragma unroll
    for (int i = 0; i < 4; ++i) {
        const float r = i < 2 ? sp : sn;
        const f32x2 lo = (i < 2 ? ep[2 * i] : en[2 * i - 4]) * (f32x2){r, r}, hi = (i < 2 ? ep[2 * i + 1] : en[2 * i - 3]) * (f32x2){r, r};
        lds_write<u32x2>(pw[i], (u32x2){pack2(lo[0], lo[1]), pack2(hi[0], hi[1])});
    }
}

DI void waitvn(int n) {
#define WV(k) case k: waitv<k>(); break;
#define WV8(k) WV(k) WV(k + 1) WV(k + 2) WV(k + 3) WV(k + 4) WV(k + 5) WV(k + 6) WV(k + 7)
    switch (n < 63 ? n : 63) { WV8(0) WV8(8) WV8(16) WV8(24) WV8(32) WV8(40) WV8(48) WV8(56) }
}
#define SB0() __builtin_amdgcn_sched_barrier(0)
template <int A, int B, class F>
DI void sfor(F f) {
    if constexpr (A < B) {
        f(std::integral_constant<int, A>{});
        sfor<A + 1, B>(f);
    }
}
template <int N> using IC = std::integral_constant<int, N>;
#define AI __attribute__((always_inline))
#define RSK(b, k) do { switch (k) { case 0: RS(b); break; case 1: RS(b + 1); break; case 2: RS(b + 2); break; case 3: RS(b + 3); break; \
    case 4: RS(b + 4); break; case 5: RS(b + 5); break; case 6: RS(b + 6); break; case 7: RS(b + 7); break; } } while (0)

DI void o_store(const u32x4 (&kf)[6], const u32x4 (&ph)[2][2], __amdgpu_buffer_rsrc_t rsrc, unsigned o_st) {
    const f32x4 zero = {0.f, 0.f, 0.f, 0.f};
#pragma unroll
    for (int i = 0; i < 2; ++i) {
        const bf16x8 p0 = __builtin_bit_cast(bf16x8, ph[i][0]), p1 = __builtin_bit_cast(bf16x8, ph[i][1]);
        f32x4 dn = mfma16(__builtin_bit_cast(bf16x8, kf[4]), p0, zero);
        f32x4 n0 = mfma16(__builtin_bit_cast(bf16x8, kf[0]), p0, zero);
        f32x4 n1 = mfma16(__builtin_bit_cast(bf16x8, kf[1]), p0, zero);
        dn = mfma16(__builtin_bit_cast(bf16x8, kf[5]), p1, dn);
        n0 = mfma16(__builtin_bit_cast(bf16x8, kf[2]), p1, n0);
        n1 = mfma16(__builtin_bit_cast(bf16x8, kf[3]), p1, n1);
        const float rd_ = __builtin_amdgcn_rcpf(rbf(rbf(dn[0]) + 1e-6f));
        const u32x4 nb_ = __builtin_bit_cast(u32x4, pack44(n0, n1));
        u32x4 o;
#pragma unroll
        for (int j = 0; j < 4; ++j) o[j] = pack2(lo16(nb_[j]) * rd_, hi16(nb_[j]) * rd_);
        st_wt(rsrc, o_st + i * 1024, nz4(o));
    }
}

typedef __attribute__((ext_vector_type(16))) unsigned u32x16;
__global__ __launch_bounds__(512, 2) void fused_mix(const u16*, const u16*, const u16*, const u16*, const u16*, const u16*, const u16*, const u16*,
                                                    const u16*, char*, u16*) {
    __shared__ __attribute__((aligned(16))) char smem[163840];
    u32x4 ka;
    u32x16 kb_;
    u64 kz;
    asm volatile("s_load_dwordx4 %0, %3, 0x0\n\ts_load_dwordx16 %1, %3, 0x10\n\ts_load_dwordx2 %2, %3, 0x50"
                 : "=&s"(ka), "=&s"(kb_), "=&s"(kz) : "s"(__builtin_amdgcn_kernarg_segment_ptr()));
    SB0();
    const int tid = threadIdx.x;
    int l = tid & 63;
    const int w = __builtin_amdgcn_readfirstlane(tid >> 6) ^ 4;
    int bid = blockIdx.x;
    const unsigned lds0 = lds_of(smem);
    int g = l >> 4, l15 = l & 15, q = l15 >> 2, p = l & 3;
    int srow = l >> 3, sch = l & 7;
    unsigned l16 = l * 16;
    unsigned vst = 0;
    const int rg = w & 3, kp = w >> 2;
    const int hs = rg >> 1;
    RS(0);
    if (CPF) {
        u64 pc;
        asm volatile("s_getpc_b64 %0\n\ts_mov_b32 m0, %2\n\ts_mov_b64 exec, 3\n\tglobal_load_lds_dword %1, %0\n\ts_mov_b64 exec, -1"
                     : "=&s"(pc) : "v"(min((unsigned)(((bid & ~7) + w) * 2 + l) * 128u, (unsigned)CPF)), "s"(lds0 + L_SM + 3584) : "memory");
    }
    lds_write<unsigned>(lds0 + L_SM + tid * 4, 0u);
    const int slot = bid >> 3;
    const int mt = (bid & 1) * 8 + (slot & 7);
    const int hp = ((bid >> 1) & 3) * 4 + (slot >> 3);
    unsigned vx[4], vw[4];
    auto vw_init = [&]() AI {
        int t = l;
        asm volatile("" : "+v"(t));
#pragma unroll
        for (int i = 0; i < 4; ++i) vw[i] = (unsigned)((8 * i + (t >> 3)) * 6144 + ((t & 7) ^ (((t >> 4) & 1) << 1) ^ ((i & 1) << 2)) * 16 + 4096 - i * 1024);
    };
    auto vx_init = [&]() AI {
        int t = l;
        asm volatile("" : "+v"(t));
#pragma unroll
        for (int i = 0; i < 4; ++i) vx[i] = (unsigned)((8 * i + (t >> 3)) * 2048 + (((t & 7) ^ ((4 * (i & 1) + (t >> 4)) & 7)) * 16) + 4096 - i * 1024);
    };
    vx_init();
    vw_init();
    asm volatile("" : "+v"(vx[0]), "+v"(vx[1]), "+v"(vx[2]), "+v"(vx[3]), "+v"(vw[0]), "+v"(vw[1]), "+v"(vw[2]), "+v"(vw[3]));
    const int ksa = SLK * (slot & 7), ksb = KSK * ((slot + (slot >> 3)) & (NPH - 1));
    unsigned xo0 = (mt * 128 + hs * 64) * 2048, wo0 = hp * 128 + hs * (32 * 6144);
    asm volatile("s_waitcnt lgkmcnt(0)" : "+s"(ka), "+s"(kb_), "+s"(kz), "+s"(xo0), "+s"(wo0) :: "memory");
    RS(48);
    const u16* x = (const u16*)((u64)ka[1] << 32 | ka[0]);
    const u16* wqkv = (const u16*)((u64)ka[3] << 32 | ka[2]);
    auto arg = [&](int i) { return (char*)((u64)kb_[2 * i + 1] << 32 | kb_[2 * i]); };
    const u16 *wphi = (const u16*)arg(0), *wo = (const u16*)arg(1), *wr = (const u16*)arg(2), *wg = (const u16*)arg(3);
    const u16 *wu = (const u16*)arg(4), *wd = (const u16*)arg(5), *gw = (const u16*)arg(6);
    char* ws = arg(7);
    u16* out = (u16*)kz;
    const int sc = (3 - (int)((unsigned)(size_t)wqkv >> 7)) & 7;
    const unsigned* one = k_num;
    unsigned v0 = 0;
    asm volatile("" : "+s"(one), "+v"(v0));
    auto mark = [&](unsigned dst, int n = 1, int lanes = 1) AI {
        asm volatile("s_mov_b32 m0, %2\n\ts_mov_b64 exec, %4\n\tglobal_load_lds_dword %0, %1 offset:%3\n\ts_mov_b64 exec, -1"
                     ::"v"(v0), "s"(one), "s"(dst - 4 * n), "n"(4 * n), "n"(lanes) : "memory");
    };
    auto four = [&](const unsigned (&v)[4], const char* src, unsigned dst, int nt = 0) AI {
#define FOUR(P) asm volatile("s_mov_b32 m0, %5\n\ts_nop 0\n\t" \
                     "global_load_lds_dwordx4 %0, %4" P "\n\tglobal_load_lds_dwordx4 %1, %4 offset:1024" P "\n\t" \
                     "global_load_lds_dwordx4 %2, %4 offset:2048" P "\n\tglobal_load_lds_dwordx4 %3, %4 offset:3072" P \
                     ::"v"(v[0]), "v"(v[1]), "v"(v[2]), "v"(v[3]), "s"(src), "s"(dst) : "memory")
        if (nt == 1) FOUR(" nt");
        else if (nt == 2) FOUR(" sc1");
        else if (nt == 3) FOUR(" sc0 sc1");
        else FOUR("");
    };
    const int ks0 = ((slot + (slot >> 3)) & KXM) | (KXO == 2 || (KXO == 3 && (hp & 7) == sc) ? slot & 4 : 0);
    const char* xb0 = (const char*)x + xo0 - 4096;
    const char* wb0 = (const char*)wqkv + wo0 - 4096;
    auto fill = [&](auto N) AI {
        constexpr int n = N, j = F_J[n] < 0 ? 0 : F_J[n], xi = F_J[n] < 0 ? -1 - F_J[n] : 0, s = F_J[n] < 0 ? x_stage(xi) : J_S[j], m = J_M[j];
        const int ks = KXO ? ks0 ^ (int)((KXB >> (4 * s)) & 15) : (ks0 + s) & 15;
        if constexpr (n >= NF1 && n < NF2) {
            if constexpr (F_J[n] >= 0) {
                asm volatile("s_mov_b32 m0, %3\n\ts_nop 0\n\tglobal_load_lds_dwordx4 %0, %2\n\tglobal_load_lds_dwordx4 %1, %2 offset:1024"
                             ::"v"(vw[0]), "v"(vw[1]), "s"(wb0 + (ks * 64 + (w & 1) * 16) * 6144), "s"(lds0 + J_U[j] * 8192 + w * 2048) : "memory");
            } else four(vx, xb0 + ks * 128 + (w & 1) * (32 * 2048), lds0 + (hs ? X_U1[xi] : X_U0[xi]) * 8192 + (w & 1) * 4096);
            if constexpr (F_K[n] > 1) mark(lds0 + L_FULL + 16 + w * 4, F_K[n]);
            return;
        }
        if constexpr (F_J[n] >= 0) {
            four(vw, wb0 + ks * (64 * 6144) + (m == 2 ? 0 : 2048 * (m + 1)), lds0 + J_U[j] * 8192 + hs * 4096, s < NTS ? NTW : 0);
        } else {
            const char* xs_ = xb0 + ks * 128;
            const unsigned d = lds0 + (hs ? X_U1[xi] : X_U0[xi]) * 8192;
            four(vx, xs_, d, s < NTS ? NTX : 0);
            four(vx, xs_ + 32 * 2048, d + 4096, s < NTS ? NTX : 0);
        }
        if constexpr (F_K[n] > 1) mark(lds0 + L_FULL + (n < NF1 ? 0 : 32) + hs * 8, F_K[n], 3);
    };
    static_assert(F_K[1] == 1, "");
    if (__builtin_expect((w & 5) == 4, 1)) sfor<0, 2>(fill);
    RS(49);
    unsigned sd = 0, st;
    asm volatile("s_cmp_lg_u32 %2, 5\n\ts_cbranch_scc1 .La%=\n\t"
                 "s_min_u32 %1, %3, 23\n\ts_lshl_b32 %1, %1, 18\n\ts_load_dword %0, %5, %1\n\ts_load_dword %0, %6, 0x0\n\t"
                 "s_and_b32 %1, %1, 0x3c0000\n\ts_load_dword %0, %4, %1\n\ts_load_dword %0, %10, %1\n\t"
                 "s_load_dword %0, %7, 0x0\n\ts_load_dword %0, %8, 0x0\n\ts_load_dword %0, %9, 0x0\n.La%=:"
                 : "+s"(sd), "=&s"(st) : "s"(w), "s"(slot), "s"(x), "s"(wqkv), "s"(one), "s"(wr), "s"(gw), "s"(wphi), "s"(out) : "memory", "scc");
    asm volatile("s_cmp_lg_u32 %2, 7\n\ts_cbranch_scc1 .La%=\n\t"
                 "s_min_u32 %1, %3, 27\n\ts_lshl_b32 %1, %1, 18\n\ts_load_dword %0, %4, 0x0\n\ts_load_dword %0, %4, %1\n\t"
                 "s_load_dword %0, %5, %1\n\ts_load_dword %0, %6, %1\n\ts_load_dword %0, %7, %1\n\t"
                 "s_and_b32 %1, %1, 0x1c0000\n\ts_load_dword %0, %8, %1\n.La%=:\n\ts_barrier"
                 : "+s"(sd), "=&s"(st) : "s"(w), "s"(slot), "s"(ws), "s"(wg), "s"(wu), "s"(wd), "s"(wo) : "memory", "scc");
    RS(50);
    if (__builtin_expect((w & 5) == 4, 1)) {
        mark(lds0 + L_FULL + hs * 8, 1, 3);
        sfor<2, NF0>(fill);
    }
    RS(51);
    asm volatile("" : "+s"(bid));
    const int xcd = bid & 7;
    const int rt = BMAP ? (xcd & 1) * 8 + ((bid >> 3) & 7) : (xcd >> 1) * 4 + ((bid >> 3) & 3);
    const int cb = BMAP ? (((xcd >> 1) + BMAP - 1) & 3) * 4 + (bid >> 6) : (xcd & 1) * 8 + (bid >> 5), rot = ROTC * cb + ROTK * rt + ROT0 * (xcd >> 1);
    const __amdgpu_buffer_rsrc_t rsrc = __builtin_amdgcn_make_buffer_rsrc(ws, 0, WS_END, 0x00020000);
    const u32x4 z4 = {0u, 0u, 0u, 0u};
    const f32x4 zero = {0.f, 0.f, 0.f, 0.f};
    unsigned AX = lds0 + L_A + rg * 4096 + l16;
    unsigned W4 = lds0 + p * 4;
    unsigned PG = lds0 + L_PROG + p * 256;
    const unsigned WR = lds0 + rg * 4;
    auto flag = [&](unsigned a) AI { asm volatile("ds_write_b32 %0, %0" ::"v"(a) : "memory"); };
    auto phi_w = [&](bf16x8 (&a)[2], int h) AI {
        const unsigned ad = lds0 + L_WPHI + h * 2048 + (4 * g + q) * 64 + (((p >> 1) ^ (2 * (g & 1))) * 16) + 8 * (p & 1);
        a[0] = cat8(tr_read(ad), tr_read(ad + 1024));
        a[1] = cat8(tr_read(ad ^ 32), tr_read((ad ^ 32) + 1024));
    };
    u32x4 fw[32], fd[16], gu[2];
    unsigned sel_[4][2];
    u32x4 ph[2][2], sq;
    const int hp2 = ((bid >> 1) & 3) * 4 + (bid >> 6);
    const bool ffn = (hp2 & 7) != sc && !(NOF & 1);
    const int ph4 = ((bid >> 3) + (bid >> 6)) & 3;
    const int turn = (ph4 + TRN) & TRM;
    const int kq = mt >> 2;
    const int cs = (ffn ? hp2 - (hp2 > sc) - (hp2 > sc + 8) : 14 + (hp2 >> 3)) * 4 + ((ph4 + CPH - (int)((unsigned)(size_t)wg >> 7)) & 3);
    auto spread = [&](const u32x4& v, int i) -> bf16x8 {
        const unsigned s = v[i >> 1];
        u32x4 a;
#pragma unroll
        for (int d = 0; d < 4; ++d) a[d] = __builtin_amdgcn_perm(s, s, sel_[d][i & 1]);
        return __builtin_bit_cast(bf16x8, a);
    };
    auto fold_store = [&](const f32x4& a, unsigned off) {
        f32x4 v;
#pragma unroll
        for (int i = 0; i < 4; ++i) {
            const unsigned u = __float_as_uint(a[i]);
            const float r = __uint_as_float(__builtin_amdgcn_update_dpp(u, u, 0x128, 0xc, 0xf, false));
            v[i] = swap32_add(r, r);
        }
        const u32x4 o = nz4(v);
        ST_MASK("global_store_dwordx4", 0x00FF00FFull, off + l15 * 32 + g * 16, o, ws);
    };
    auto sel_init = [&]() AI {
#pragma unroll
        for (int d = 0; d < 4; ++d)
#pragma unroll
            for (int hf = 0; hf < 2; ++hf) {
                const unsigned b = hf ? 0x0302u : 0x0100u;
                sel_[d][hf] = (sch >> 1) != d ? 0x0c0c0c0cu : (l & 1) ? (b << 16) | 0x0c0cu : 0x0c0c0000u | b;
            }
    };
    auto gua = [&](int i) { return ws + WS_GU + cs * 4096 + i * 1024; };
    auto gu_load = [&]() AI {
#pragma unroll
        for (int i = 0; i < 2; ++i) ld_async(gu[i], l16, gua(i));
    };
    auto down = [&]() AI {
#pragma unroll
        for (int i = 0; i < 16; ++i) pin(fd[i]);
        settle(gu, [&](int) { return l16; }, gua);
        const f32x4 v = __builtin_bit_cast(f32x4, gu[0]) + __builtin_bit_cast(f32x4, gu[1]);
        auto r = __builtin_amdgcn_permlane16_swap(__float_as_uint(swap32_add(v[0], v[2])), __float_as_uint(swap32_add(v[1], v[3])), false, false);
        const float gt = rbf(__uint_as_float(r[0])), up = rbf(__uint_as_float(r[1]));
        const unsigned ha = lds0 + L_XS + hs * 512;
        lds_write<u16>(ha + l15 * 8 + g * 2, f2bf(rbf(gt / (1.f + __expf(-gt))) * up));
        const u32x4 hv = lds_read<u32x4>(ha + srow * 16);
        f32x4 y0 = zero, y1 = zero;
#pragma unroll
        for (int i = 0; i < 8; ++i) {
            const bf16x8 a = spread(hv, i);
            y0 = mfma16(a, __builtin_bit_cast(bf16x8, fd[i]), y0);
            y1 = mfma16(a, __builtin_bit_cast(bf16x8, fd[8 + i]), y1);
        }
        fold_store(y0, WS_Y + cs * 4096 + kq * 1024 + hs * 512);
        fold_store(y1, WS_Y + cs * 4096 + kq * 1024 + hs * 512 + 256);
    };
    auto need = [&](int c) AI {
        if (c == 101) lds_wait(W4 + L_FKS, 1);
        else if (c == 100) lds_wait(W4 + L_FV, 1);
        else if (c == 99) lds_wait(W4 + L_FQD, 1);
        else if (c > 0) lds_wait(c > RLD ? W4 + L_PRL : PG, c);
    };
    auto wo_load = [&](int n0, int n1, bool mk) AI {
        unsigned vo[4];
#pragma unroll
        for (int i = 0; i < 4; ++i) vo[i] = (unsigned)((8 * i + srow) * 2048 + ((sch ^ ((g & 3) << 1)) * 16) + 4096 - i * 1024);
        const char* wob = (const char*)wo + cb * 128 - 4096;
#pragma unroll
        for (int n = n0; n < ((NOF & 4) ? n0 : n1); ++n) {
            if (n < WON && (n == 0 || W_N[n] != W_N[n - 1])) need(W_N[n]);
            four(vo, wob + ((2 * n + hs + rot) & 31) * 65536, lds0 + wo_unit(n) * 8192 + hs * 4096);
        }
        if (mk) mark(lds0 + (n0 >= WON && WON ? L_FW2 : L_FWD) + hs * 4);
    };
    auto phi_adr = [&](unsigned (&pw)[4], unsigned base) AI {
#pragma unroll
        for (int i = 0; i < 4; ++i) pw[i] = (base + rg * 4096 + l15 * 128 + ((g ^ fsw(l15)) << 3)) ^ (i << 5);
    };
    const int row0 = rt * 128 + rg * 32 + kp * 16;
    u32x4 xr[2];
    u64 gg[2];
    unsigned xo = srow * 2048 + sch * 16;
    auto g_load = [&]() AI {
        asm volatile("global_load_dwordx2 %0, %2, %3 sc1\n\tglobal_load_dwordx2 %1, %2, %3 offset:64 sc1"
                     : "=&v"(gg[0]), "=&v"(gg[1]) : "v"((unsigned)srow * 8), "s"(ws + WS_GG + row0 * 8) : "memory");
    };
    auto xg_load = [&](int skip) AI {
        asm volatile("s_cmp_eq_u32 %4, 4\n\ts_cbranch_scc1 .Lk%=\n\t"
                     "global_load_dwordx4 %0, %5, %6\n\tglobal_load_dwordx4 %1, %5, %7\n\t"
                     "global_load_dwordx2 %2, %8, %9 sc1\n\tglobal_load_dwordx2 %3, %8, %9 offset:64 sc1\n.Lk%=:"
                     : "+v"(xr[0]), "+v"(xr[1]), "+v"(gg[0]), "+v"(gg[1])
                     : "s"(skip), "v"(xo), "s"(x + (size_t)row0 * DM + cb * 64), "s"(x + (size_t)(row0 + 8) * DM + cb * 64),
                       "v"((unsigned)srow * 8), "s"(ws + WS_GG + row0 * 8) : "memory", "scc");
    };
    auto xg_def = [&]() AI { asm volatile("" : "=v"(xr[0]), "=v"(xr[1]), "=v"(gg[0]), "=v"(gg[1])); };
    auto relane = [&]() AI {
        unsigned z = 0;
        asm volatile("" : "+v"(z));
        l = __builtin_amdgcn_mbcnt_hi(~0u, __builtin_amdgcn_mbcnt_lo(~0u, z));
        g = l >> 4, l15 = l & 15, q = l15 >> 2, p = l & 3, srow = l >> 3, sch = l & 7, l16 = l * 16;
        AX = lds0 + L_A + rg * 4096 + l16, W4 = lds0 + p * 4, PG = lds0 + L_PROG + p * 256, xo = srow * 2048 + sch * 16;
    };
    u32x4 kf[6];
    auto kf_def = [&]() AI { asm volatile("" : "=v"(kf[0]), "=v"(kf[1]), "=v"(kf[2]), "=v"(kf[3]), "=v"(kf[4]), "=v"(kf[5])); };
    const int oh = hs ^ kp ^ 1;

    if (__builtin_expect(w >= 4, 1)) {
        const bool loader = !(w & 1);
        const unsigned kf_kv = WS_KF + (hp * 2 + (mt >> 3)) * 8192 + (mt & 7) * 512;
        const unsigned kf_z = WS_KF + (hp * 2 + (mt >> 3)) * 8192 + 4096 + (ZL ? (mt & 7) * 128 : ((mt >> 2) & 1) * 64);
        const unsigned fgo = ffn ? (unsigned)((256 * kq + 16 * srow) * 7168 + cs * 128 + sch * 16) : 0u;
        const char* fsrc = (const char*)(hs ? wu : wg);
        auto gu_w = [&](int q0, int q1) AI {
#pragma unroll
            for (int i = q0; i < q1; ++i) {
                unsigned t1, t2, t3;
                if (NOF & 8) fw[4 * i] = fw[4 * i + 1] = fw[4 * i + 2] = fw[4 * i + 3] = z4; else
                asm volatile("v_add_u32 %4, 0x1c00, %7\n\tv_add_u32 %5, 0x3800, %7\n\tv_add_u32 %6, 0x5400, %7\n\t"
                             "global_load_dwordx4 %0, %7, %8 nt\n\tglobal_load_dwordx4 %1, %4, %8 nt\n\t"
                             "global_load_dwordx4 %2, %5, %8 nt\n\tglobal_load_dwordx4 %3, %6, %8 nt"
                             : "=&v"(fw[4 * i]), "=&v"(fw[4 * i + 1]), "=&v"(fw[4 * i + 2]), "=&v"(fw[4 * i + 3]), "=&v"(t1), "=&v"(t2), "=&v"(t3)
                             : "v"(fgo), "s"(fsrc + (128 * (i >> 2) + 4 * (i & 3)) * 7168) : "memory");
            }
        };
        const unsigned fdo = ffn ? (unsigned)((64 * cs + 8 * srow) * 2048 + 512 * kq + 256 * hs + sch * 16) : 0u;
        auto dn_w = [&](int q0, int q1) AI {
#pragma unroll
            for (int i = q0; i < q1; ++i)
                if (NOF & 8) fd[4 * i] = fd[4 * i + 1] = fd[4 * i + 2] = fd[4 * i + 3] = z4; else
                asm volatile("global_load_dwordx4 %0, %4, %5 offset:-4096 nt\n\tglobal_load_dwordx4 %1, %4, %5 offset:-2048 nt\n\t"
                             "global_load_dwordx4 %2, %4, %5 nt\n\tglobal_load_dwordx4 %3, %4, %5 offset:2048 nt"
                             : "=&v"(fd[4 * i]), "=&v"(fd[4 * i + 1]), "=&v"(fd[4 * i + 2]), "=&v"(fd[4 * i + 3])
                             : "v"(fdo), "s"((const char*)wd + 4096 + (i & 1) * 8192 + (i >> 1) * 128) : "memory");
        };
        auto gate_up = [&]() AI {
            sel_init();
#pragma unroll
            for (int i = 0; i < 32; ++i) pin(fw[i]);
            u32x4 xs[4];
#pragma unroll
            for (int i = 0; i < 4; ++i) xs[i] = lds_read<u32x4>(lds0 + L_XS + hs * 512 + (i >> 1) * 256 + srow * 32 + (i & 1) * 16);
            f32x4 a = zero;
#pragma unroll
            for (int i = 0; i < 32; ++i) a = mfma16(spread(xs[i >> 3], i & 7), __builtin_bit_cast(bf16x8, fw[i]), a);
            fold_store(a, WS_GU + cs * 4096 + kq * 512 + hs * 256);
            SB0();
        };
        auto o_clear = [&]() AI {
#pragma unroll
            for (int n = 0; n < ((NOF & 2) ? 1 : 8); ++n) st_wt(rsrc, WS_O + ((mt * 2 + (n >> 2)) * 32 + 2 * hp + hs) * 4096 + (n & 3) * 1024 + l16, z4);
        };
        u64 T;
        u32x4 sv[2];
        auto clears = [&]() AI {
            if (!OCL) o_clear();
#pragma unroll
            for (int n = 0; n < ((NOF & 16) ? 0 : 4); ++n) st_wt(rsrc, WS_KV + ((hp * 8 + hs * 4 + n) * 16 + mt) * 1024 + l16, z4);
            ST_MASK("global_store_dwordx4", 0xFFFFull, WS_Z + ((hp * 4 + hs * 2 + (srow & 1)) * 16 + mt) * 128 + sch * 16, z4, ws);
        };
        auto cleared = [&]() AI {
            if (CLL) ST_MASK("global_store_dwordx2", 0xFFFFull, (unsigned)l * 8, T, ws + WS_C2 + ((mt * 2 + hs) * 16 + hp) * 128);
            else ST_MASK("global_store_dwordx2", 1ull, 0u, T, ws + WS_CL + ((mt * 2 + hs) * 16 + hp) * 8);
        };
        auto rg_load = [&]() AI {
            asm volatile("global_load_dwordx4 %0, %2, %3 sc1\n\tglobal_load_dwordx4 %1, %2, %3 offset:16 sc1"
                         : "=&v"(sv[0]), "=&v"(sv[1]) : "v"(l * 32), "s"(ws + WS_RG) : "memory");
        };
        auto select = [&]() AI {
            const unsigned tl = (unsigned)T & 0x1FFFu, thi = (unsigned)(T >> 13);
#pragma unroll 1
            for (unsigned s = 0; s < SPIN_CAP; ++s) {
                pin(sv[0]);
                pin(sv[1]);
                const unsigned bad = ((sv[0][0] >> 19) ^ tl) | (sv[0][1] ^ thi) | ((sv[0][2] >> 19) ^ tl) | (sv[0][3] ^ thi) |
                                     ((sv[1][0] >> 19) ^ tl) | (sv[1][1] ^ thi) | ((sv[1][2] >> 19) ^ tl) | (sv[1][3] ^ thi);
                if (__all(bad == 0u)) break;
                __builtin_amdgcn_s_sleep(16);
                rg_load();
                waitv<0>();
            }
            pin(sv[0]);
            pin(sv[1]);
            RS(4);
            unsigned k2 = 0;
#pragma unroll
            for (int j = 0; j < 4; ++j) {
                const unsigned v = sv[j >> 1][(j & 1) * 2] & 0x7FFFFu;
                k2 = max(k2, ((v >> 3) << 11) | ((255 - (4 * l + j)) << 3) | (v & 7));
            }
            const unsigned key = __builtin_amdgcn_readfirstlane(wave_umax(k2));
            asm volatile("s_mov_b32 m0, %2\n\ts_mov_b64 exec, %3\n\tglobal_load_lds_dwordx4 %0, %1\n\ts_mov_b64 exec, -1"
                         ::"v"((unsigned)(512 * kq) + l16), "s"(x + (size_t)(2047 - (int)(key & 2047u)) * DM), "s"(lds0 + L_XS + hs * 512), "s"(0xFFFFFFFFull) : "memory");
            unsigned rb = key >> 11;
            rb ^= (rb & 0x8000u) ? 0x8000u : 0xFFFFu;
            const float rsel = rbf(1.f / (1.f + __expf(-bf2f((u16)rb))));
            lds_write<u32x4>(lds0 + L_SEL, (u32x4){(unsigned)T, (unsigned)(T >> 32), 2047u - (key & 2047u), __float_as_uint(rsel)});
            if (bid == 0 && hs) ST_MASK("global_store_dwordx2", 1ull, 0u, T, ws + WS_E);
            RS(5);
        };
        auto k_soft = [&](int np) AI {
            bf16x8 a0[2], kb[2];
            f32x4 pk[2][2][2];
            unsigned pw[4];
            phi_w(a0, 0);
            phi_adr(pw, lds0 + P_U[0] * 8192);
            asm volatile("" : "+v"(pw[0]), "+v"(pw[1]), "+v"(pw[2]), "+v"(pw[3]));
            auto logits = [&](int h) AI {
                lds_wait(WR + L_FK0 + 4 * h, 1);
#pragma unroll
                for (int i = 0; i < 2; ++i) kb[i] = lds_read128(AX + (P_U[0] * 8192 - L_A) + h * 4096 + i * 1024);
#pragma unroll
                for (int i = 0; i < 4; ++i) pk[h][i >> 1][i & 1] = mfma16(a0[i & 1], kb[i >> 1], zero);
            };
            auto round = [&](int h) AI {
#pragma unroll
                for (int i = 0; i < 4; ++i) pk[h][i >> 1][i & 1] = rbf4(pk[h][i >> 1][i & 1]);
            };
            logits(0);
            RS(12);
            round(0);
            SB0();
            phi_k<KPK & 1>(pk[0], pw, 0, [&]() AI {
                if (np > 1) logits(1);
                SB0();
            });
            SB0();
            if (np > 1) {
                round(1);
                phi_k<(KPK >> 1)>(pk[1], pw, 4096, []() {});
                SB0();
            }
            flag(WR + L_FP);
        };
        if (__builtin_expect(loader, 1)) {
            vx_init();
            vw_init();
            if (LPRIO) __builtin_amdgcn_s_setprio(LPRIO);
            unsigned pv = 0;
            sfor<NF0, NF1>([&](auto N) {
                constexpr int n = N;
                if constexpr (n > 0 && F_N[n] > F_N[n > 0 ? n - 1 : 0]) {
                    RSL(32 + F_N[n]);
                    if (LPRW != LPRIO) __builtin_amdgcn_s_setprio(LPRW);
                    if (NOF & 256) {
                    } else if (F_N[n] > RLD) {
                        lds_wait(W4 + L_PRL, F_N[n]);
                    } else if (PEEK) {
                        lds_wait(pv, PG, F_N[n]);
                        if (F_N[n] < F_N[NF1 - 1] && next_need(n) <= RLD) lds_peek(pv, PG);
                    } else lds_wait(PG, F_N[n]);
                    if (LPRW != LPRIO) __builtin_amdgcn_s_setprio(LPRIO);
                    RSL(48 + F_N[n]);
                }
                if constexpr (n == 12) {
                    asm volatile("s_mov_b32 m0, %2\n\ts_nop 0\n\tglobal_load_lds_dwordx4 %0, %1\n\tglobal_load_lds_dwordx4 %0, %1 offset:1024"
                                 ::"v"((unsigned)((l >> 2) * 64 + (((l & 3) ^ (2 * ((l >> 4) & 1))) * 16))),
                                   "s"(wphi + (hp * 2 + hs) * 1024), "s"(lds0 + L_WPHI + hs * 2048) : "memory");
                }
                if constexpr (LTH < 63 && n >= LT0) waitv<LTH>();
                fill(N);
            });
            flag(lds0 + L_FLD + hs * 4);
            RS(1);
            if (HPRIO) __builtin_amdgcn_s_setprio(HPRIO);
            const char* cla = ws + (CLL ? WS_C2 : WS_CL) + ((l15 * 2 + (mt >> 3)) * 16 + hp) * (CLL ? 128 : 8);
            u64 cl;
            if (KCL) cl = 0;
            else if (CLE) asm volatile("global_load_dwordx2 %0, %1, off sc1" : "=&v"(cl) : "v"(cla) : "memory");
            k_soft(SSM ? 1 : 2);
            if (!CLE) asm volatile("global_load_dwordx2 %0, %1, off sc1" : "=&v"(cl) : "v"(cla) : "memory");
            RS(11);
            lds_wait(W4 + L_FP, 1);
            sq = lds_read<u32x4>(lds0 + L_SEL);
            waitv<0>();
            asm volatile("" : "+v"(cl));
            RS(13);
            u32x4 kp_[5 + ZL];
            {
                const char* kb = ws + WS_KV + (hp * 8 + (mt >> 1)) * 16384 + (mt & 1) * 512 + hs * 256;
                const bool zw = !((mt & 3) | (ZL & hs));
                const char* zb = zw ? ws + WS_Z + (hp * 4 + (mt >> 2)) * 2048 + (ZL ? 0 : hs * 64) : kb;
                const unsigned kvo = g * 1024 + l15 * 16, zo = zw ? (ZL ? l16 : l15 * 128 + g * 16) : 0u;
                auto ko = [&](int i) { return i < 4 ? kvo : zo; };
                auto ka = [&](int i) { return i < 4 ? kb + i * 4096 : zb + (i - 4) * 1024; };
                const u64 T = (u64)sq[1] << 32 | sq[0];
#pragma unroll 1
                for (unsigned s = 0; !KCL && s < SPIN_CAP && __ballot(cl != T); ++s)
                    asm volatile("global_load_dwordx2 %0, %1, off sc1\n\ts_waitcnt vmcnt(0)" : "=&v"(cl) : "v"(cla) : "memory");
                lds_wait0(W4 + L_FKS);
                if (GDL) __builtin_amdgcn_s_sleep(GDL);
#pragma unroll
                for (int i = 0; i < 5 + ZL; ++i) { if (PDI & 1) kp_[i] = z4; else ld_async(kp_[i], ko(i), ka(i)); }
                if (XGE) {
                    xg_def();
                    xg_load(w + 1);
                }
                flag(lds0 + L_FGI + hs * 4);
                SB0();
                RS(14);
                if (!(PDI & 1)) waitv<0>();
                RS(15);
                if (PP1) settle_p<PP1 + !PP1, PD1>(kp_, ko, ka);
                else settle(kp_, ko, ka);
                RS(24);
                float v[8], u[4];
#pragma unroll
                for (int e = 0; e < 4; ++e) {
                    v[2 * e] = (lo16(kp_[0][e]) + lo16(kp_[1][e])) + (lo16(kp_[2][e]) + lo16(kp_[3][e]));
                    v[2 * e + 1] = (hi16(kp_[0][e]) + hi16(kp_[1][e])) + (hi16(kp_[2][e]) + hi16(kp_[3][e]));
                }
#pragma unroll
                for (int e = 0; e < 4; ++e) u[e] = swap32_add(v[e], v[e + 4]);
                const unsigned o = nz1(pack2(swap16_add(u[0], u[2]), swap16_add(u[1], u[3])));
                ST_MASK("global_store_dword", ~0ull, kf_kv + hs * 256 + l15 * 16 + g * 4, o, ws);
                if (zw && ZL) {
                    f32x4 z = __builtin_bit_cast(f32x4, kp_[4]) + __builtin_bit_cast(f32x4, kp_[4 + ZL]);
#pragma unroll
                    for (int i = 0; i < 4; ++i) {
                        float t = dpp_add<0x128>(z[i]);
                        t = swap16_add(t, t);
                        z[i] = swap32_add(t, t);
                    }
                    const u32x2 oz = {nz1(pack2(z[0], z[1])), nz1(pack2(z[2], z[3]))};
                    ST_MASK("global_store_dwordx2", 0xFFFFull, kf_z + (l & 8) * 8 + p * 16 + (sch >> 2) * 8, oz, ws);
                } else if (zw) {
                    f32x4 z;
#pragma unroll
                    for (int i = 0; i < 4; ++i) z[i] = dpp_add<0x140>(dpp_add<0x141>(dpp_add<0x4E>(dpp_add<0xB1>(__uint_as_float(kp_[4][i])))));
                    const u32x2 oz = {nz1(pack2(z[0], z[1])), nz1(pack2(z[2], z[3]))};
                    ST_MASK("global_store_dwordx2", 0x0001000100010001ull, kf_z + g * 16 + hs * 8, oz, ws);
                }
                flag(lds0 + L_FG + hs * 4);
            }
            RS(25);
            const char* kfb = ws + WS_KF + (hp * 2 + hs) * 8192;
            auto fo = [&](int i) { return i < 4 ? l16 : (unsigned)g * 16; };
            auto fb = [&](int i) { return i < 4 ? kfb + i * 1024 : kfb + 4096 + (i - 4) * (ZL ? 512 : 64); };
            if (KFD) __builtin_amdgcn_s_sleep(KFD);
#pragma unroll
            for (int i = 0; i < 6; ++i) { if (PDI & 2) kf[i] = z4; else ld_async(kf[i], fo(i), fb(i)); }
            if (!XGE) {
                xg_def();
                xg_load(w + 1);
            }
            if (!(PDI & 2)) waitv<0>();
            RS(29);
            if (PP2) settle_p<PP2 + !PP2, PD2>(kf, fo, fb);
            else settle(kf, fo, fb);
#pragma unroll
            for (int i = 0; i < 5 + ZL; ++i) pin(kp_[i]);
            asm volatile("" : "+v"(xr[0]), "+v"(xr[1]), "+v"(gg[0]), "+v"(gg[1]));
            need(KFN);
#pragma unroll
            for (int i = 0; i < 4; ++i) lds_write<u32x4>(lds0 + L_KF + hs * 4096 + i * 1024 + l16, kf[i]);
#pragma unroll
            for (int i = 0; i < 2; ++i) lds_write<u32x4>(lds0 + L_KZ + hs * 128 + i * 64 + g * 16, kf[4 + i]);
            SB0();
            flag(lds0 + L_FKF + hs * 4);
        } else {
            const int row0 = (mt * 16 + hp) * 8 + 4 * hs;
            if (SPRIO) __builtin_amdgcn_s_setprio(SPRIO);
            asm volatile("s_waitcnt lgkmcnt(0)" : "+s"(sd));
            if (TLD) __builtin_amdgcn_s_sleep(TLD);
            u32x4 rv[2], gv[2], xv[4][2];
            u64 ev;
            if (RT0) lds_wait8(PG, RT0 + RTS * ((ph4 + CLP) & 3));
            asm volatile("global_load_dwordx2 %4, %5, %9 sc1\n\t"
                         "global_store_dwordx4 %10, %11, %9 sc1\n\t"
                         "global_store_dwordx2 %12, %13, %9 sc1\n\t"
                         "global_load_dwordx4 %0, %6, %7\n\tglobal_load_dwordx4 %1, %6, %7 offset:1024\n\t"
                         "global_load_dwordx4 %2, %6, %8\n\tglobal_load_dwordx4 %3, %6, %8 offset:1024"
                         : "=&v"(rv[0]), "=&v"(rv[1]), "=&v"(gv[0]), "=&v"(gv[1]), "=&v"(ev)
                         : "v"(0u), "v"(l16), "s"(wr), "s"(gw), "s"(ws),
                           "v"(l < 32 ? WS_Y + cs * 4096 + kq * 1024 + hs * 512 + l16 : WS_GU + cs * 4096 + kq * 512 + hs * 256 + l15 * 16), "v"(z4),
                           "v"(hs ? kf_z + (ZL ? l15 : sch) * 8 : kf_kv + l * 8), "v"(0ull)
                         : "memory");
            RS(1);
#pragma unroll
            for (int r = 0; r < 2; ++r)
                if (NOF & 64) xv[2 * r][0] = xv[2 * r][1] = xv[2 * r + 1][0] = xv[2 * r + 1][1] = z4; else
                asm volatile("global_load_dwordx4 %0, %4, %5\n\tglobal_load_dwordx4 %1, %4, %5 offset:1024\n\t"
                             "global_load_dwordx4 %2, %4, %5 offset:2048\n\tglobal_load_dwordx4 %3, %4, %5 offset:3072"
                             : "=&v"(xv[2 * r][0]), "=&v"(xv[2 * r][1]), "=&v"(xv[2 * r + 1][0]), "=&v"(xv[2 * r + 1][1])
                             : "v"(l16), "s"(x + (size_t)(row0 + 2 * r) * DM) : "memory");
            SB0();
            if ((MRG & 8) || KCL) clears();
            waitv<(MRG & 8) ? 13 : 0>();
            asm volatile("" : "+v"(rv[0]), "+v"(rv[1]), "+v"(gv[0]), "+v"(gv[1]), "+v"(ev));
#pragma unroll
            for (int r = 0; r < 4; ++r) asm volatile("" : "+v"(xv[r][0]), "+v"(xv[r][1]));
            RS(2);
            T = ((u64)__builtin_amdgcn_readfirstlane((unsigned)(ev >> 32)) << 32 | __builtin_amdgcn_readfirstlane((unsigned)ev)) + 1;
            float ra_[4] = {0.f, 0.f, 0.f, 0.f}, ga_[4] = {0.f, 0.f, 0.f, 0.f};
#pragma unroll
            for (int r = 0; r < 4; ++r)
#pragma unroll
                for (int i = 0; i < 2; ++i)
#pragma unroll
                    for (int j = 0; j < 4; ++j) {
                        ra_[r] = dot2(xv[r][i][j], rv[i][j], ra_[r]);
                        ga_[r] = dot2(xv[r][i][j], gv[i][j], ga_[r]);
                    }
            const int rr = (g >> 1) | ((g & 1) << 1);
            unsigned k = f2bf(sum4(ra_));
            k ^= (k & 0x8000u) ? 0xFFFFu : 0x8000u;
            k = (k << 3) | (7 - 4 * hs - rr);
            auto r1 = __builtin_amdgcn_permlane16_swap(k, k, false, false);
            k = max(r1[0], r1[1]);
            auto r2 = __builtin_amdgcn_permlane32_swap(k, k, false, false);
            k = max(r2[0], r2[1]);
            u64 gq = (T << 16) | f2bf(1.f / (1.f + __expf(-rbf(sum4(ga_)))));
            if (MRG & 8) waitv<0>();
            if (hs) lds_write<unsigned>(lds0 + L_BEST, k | 0x80000000u);
            asm volatile("" : "+v"(gq), "+v"(k));
            if (MRG & 1) {
                __builtin_amdgcn_s_setprio(SPR2);
                lds_wait8(PG, CLR0 + CLS * ((ph4 + CLP) & 3));
            }
            if (!hs) {
                lds_wait(lds0 + L_BEST, 1);
                const u64 rq = (T << 19) | max(k, lds_read<unsigned>(lds0 + L_BEST) & 0x7FFFFu);
                ST_MASK("global_store_dwordx2", 1ull, 0u, rq, ws + WS_RG + (mt * 16 + hp) * 8);
            }
            ST_MASK("global_store_dwordx2", 0x0001000100010001ull, (unsigned)(rr * 8), gq, ws + WS_GG + row0 * 8);
            RS(3);
            __builtin_amdgcn_s_setprio(SPR2);
            auto q_fill = [&]() AI {
                vx_init();
                vw_init();
                sfor<NF2, NF>([&](auto N) {
                    constexpr int n = N;
                    if constexpr (n == NF2 || F_N[n] != F_N[n - 1]) need(F_N[n]);
                    fill(N);
                });
            };
            auto wt = [&](unsigned f) AI { lds_wait(lds0 + f + (l & 1) * 4, 1); };
            unsigned pf[2];
            auto wo_warm = [&]() AI {
                asm volatile("global_load_dword %0, %2, %3\n\tglobal_load_dword %1, %2, %4" : "=&v"(pf[0]), "=&v"(pf[1])
                             : "v"((unsigned)((BMAP ? (rt & 7) * 128 + hs * 64 + (l & 31) : (rt & 3) * 256 + hs * 128 + l) * 2048)), "s"((const char*)wo + cb * 128), "s"((const char*)wo + cb * 128 + (BMAP ? 32 : 64) * 2048) : "memory");
            };
            if (DNP == 0) {
                lds_wait8(PG, FBA + FBS * turn);
                dn_w(0, 4);
            }
            if (!(MRG & 8) && !KCL) {
                if (!(MRG & 1)) lds_wait8(PG, CLR0 + CLS * ((ph4 + CLP) & 3));
                clears();
                waitv<0>();
            }
            if ((!(MRG & 6) || (MRG & 40)) && !KCL) cleared();
            RS(25);
            if (!(MRG & 4)) {
                lds_wait8(PG, SEL0);
                rg_load();
                if ((MRG & 10) == 2) cleared();
                waitv<(MRG & 10) == 2>();
                if (MRG & 2) lds_wait8(lds0 + L_FLD + (l & 1) * 4, 1);
                select();
                if (!SSM) flag(WR + L_FP);
            }
            if (!(MRG & 2)) lds_wait8(lds0 + L_FLD + (l & 1) * 4, 1);
            if (MRG & 4) {
                rg_load();
                if (!(MRG & 40) && !KCL) cleared();
            }
            RS(6);
            if (WPF == 4) wo_warm();
            gu_w(0, GQ1);
            if ((MRG & 20) == 4) {
                waitv<4 * GQ1>();
                select();
                if (!SSM) flag(WR + L_FP);
            }
            q_fill();
            if ((MRG & 20) == 20) {
                waitv<4 * GQ1 + 4 * (NF - NF2)>();
                select();
                if (!SSM) flag(WR + L_FP);
            }
            RS(7);
            gu_w(GQ1, GQ2);
            if (SSM) {
                __builtin_amdgcn_s_setprio(HPRIO);
                k_soft(1);
                __builtin_amdgcn_s_setprio(SPR2);
                RS(11);
            }
            gu_w(GQ2, 8);
            if (WPF == 1) wo_warm();
            if (DNP == 1) dn_w(0, 4);
            RS(8);
            waitv<(DNP == 1 ? 16 : 0)>();
            if ((WPF == 1 && DNP != 1) || WPF == 4) asm volatile("" : "+v"(pf[0]), "+v"(pf[1]));
            RS(9);
            if (GUO == 0) gate_up();
            RS(10);
            if (WOT == 1) lds_wait(W4 + L_FKS, 1);
            if (WOT == 2) wt(L_FGI);
            if (DNP == 2) dn_w(0, 4);
            wo_load(0, WOE, WOE == WON);
            RS(14);
            if (GUO == 1) {
                __builtin_amdgcn_s_setprio(GPR);
                gate_up();
                __builtin_amdgcn_s_setprio(SPR2);
            }
            RS(13);
            if (YT == 1) wt(L_FG);
            gu_load();
            if (DNP == 3) dn_w(0, 4);
            if (WOE < WON) wo_load(WOE, WON, true);
            waitv<(WOE < WON ? 4 * (WON - WOE) + 1 : 0)>();
            if (WPF == 1 && DNP == 1) asm volatile("" : "+v"(pf[0]), "+v"(pf[1]));
            RS(15);
            down();
            RS(24);
            sq = lds_read<u32x4>(lds0 + L_SEL);
            xg_def();
            kf_def();
        }
        RS(26);
        lds_wait0(WR + L_FQ);
#pragma unroll
        for (int i = 0; i < 4; ++i) ph[i >> 1][i & 1] = lds_read<u32x4>(AX + i * 1024);
    } else {
        if (CPRIO) __builtin_amdgcn_s_setprio(CPRIO);
        unsigned ra[4], xt[2];
#pragma unroll
        for (int u = 0; u < 4; ++u) ra[u] = lds0 + J_B[0] * 8192 + (8 * g + q) * 128 + ((u ^ (q >> 1) ^ ((g & 1) << 1)) * 32) + 8 * p;
        unsigned xa = lds0 + (32 * (w & 1) + l15) * 128 + ((g ^ (l15 >> 1)) * 16), fa = lds0 + L_FULL + (l & 3) * 4;
        xt[0] = xa + (hs ? X_U1[0] : X_U0[0]) * 8192;
        xt[1] = xt[0] ^ 64;
        asm volatile("s_mov_b32 m0, %4" : "+v"(xa), "+v"(fa), "+v"(xt[0]), "+v"(xt[1]) : "s"(lds0 + L_PROG + w * 256 - 124));
        f32x4 acc[3][4][2];
        bf16x8 xf[ND][2][2];
        s16x4 wh[PD + 1][2];
        unsigned fv = 0;
        bf16x8 kown[2];
        u64 bad = ~0ull;
        auto chk = [&](int c) AI {
            SB0();
            if (__builtin_expect(bad != 0ull, 0)) {
                unsigned c_;
                asm volatile("s_mov_b32 %1, 0x400000\n"
                             ".Lp%=:\n\tds_read_b32 %0, %2 offset:%3\n\ts_waitcnt lgkmcnt(0)\n\tv_cmp_gt_u32 vcc, %4, %0\n\ts_cbranch_vccz .Lf%=\n\t"
                             "s_sub_u32 %1, %1, 1\n\ts_cbranch_scc0 .Lp%=\n.Lf%=:"
                             : "=&v"(fv), "=&s"(c_) : "v"(fa), "n"(c > NM2 ? 32 : c > NM1 ? 16 : 0), "n"(c) : "memory", "vcc", "scc");
            }
            SB0();
        };
        auto o_clear4 = [&]() AI {
#pragma unroll
            for (int n = 0; n < 4; ++n) st_wt(rsrc, WS_O + ((mt * 2 + hs) * 32 + 2 * hp + (n >> 1)) * 4096 + (rg & 1) * 2048 + (n & 1) * 1024 + l16, z4);
        };
        auto kv_part = [&]() AI
        {
            bf16x8 ones;
#pragma unroll
            for (int j = 0; j < 8; ++j) ones[j] = (short)0x3f80;
            const unsigned pa = lds0 + (hs ? P_U[2] : P_U[0]) * 8192 + (4 * g + q) * 128 + (((8 * (rg & 1) + p) ^ fsw(4 * g + q)) << 3);
            const unsigned va = lds0 + L_A + hs * 2048 + l16;
            bf16x8 b0[4], b1[4], af[2][4];
            lds_wait0(lds0 + L_FV + sch * 4);
            RS(15);
#pragma unroll
            for (int i = 0; i < 4; ++i) {
                af[0][i] = cat8(tr_read(pa + i * 4096), tr_read(pa + i * 4096 + 2048));
                b0[i] = lds_read128(va + i * 4096);
                af[1][i] = cat8(tr_read((pa ^ 32) + i * 4096), tr_read((pa ^ 32) + i * 4096 + 2048));
            }
#pragma unroll
            for (int i = 0; i < 4; ++i) b1[i] = lds_read128(va + i * 4096 + 1024);
            SB0();
            f32x4 k0[2] = {zero, zero}, k1[2] = {zero, zero}, zt[2] = {zero, zero};
            const unsigned kv_st = WS_KV + ((hp * 8 + rg * 2) * 16 + mt) * 1024 + l16;
#pragma unroll
            for (int i = 0; i < 4; ++i)
#pragma unroll
                for (int e = 0; e < 2; ++e) {
                    k0[e] = mfma16(af[e][i], b0[i], k0[e]);
                    if (PORD) zt[e] = mfma16(af[e][i], ones, zt[e]);
                }
            SB0();
#pragma unroll
            for (int i = 0; i < 4; ++i)
#pragma unroll
                for (int e = 0; e < 2; ++e) k1[e] = mfma16(af[e][i], b1[i], k1[e]);
            st_wt(rsrc, kv_st, nz4(__builtin_bit_cast(u32x4, pack44(k0[0], k0[1]))));
            if (!PORD) {
                SB0();
#pragma unroll
                for (int i = 0; i < 4; ++i)
#pragma unroll
                    for (int e = 0; e < 2; ++e) zt[e] = mfma16(af[e][i], ones, zt[e]);
                st_wt(rsrc, kv_st + 16384, nz4(__builtin_bit_cast(u32x4, pack44(k1[0], k1[1]))));
            }
            const u32x4 zo = nz4((l & 1) ? zt[1] : zt[0]);
            ST_MASK("global_store_dwordx4", 0x0003000300030003ull, WS_Z + ((hp * 4 + rg) * 16 + mt) * 128 + (l & 1) * 64 + g * 16, zo, ws);
            if (PORD) st_wt(rsrc, kv_st + 16384, nz4(__builtin_bit_cast(u32x4, pack44(k1[0], k1[1]))));
            SB0();
            flag(WR + L_FKS);
            if (OCL == 1) o_clear4();
        };
        auto report = [&](int r) AI {
            if (r > RLD) {
                unsigned t;
                unsigned a;
                asm volatile("v_mov_b32 %0, %3\n\tv_mov_b32 %1, %2\n\tds_write_b32 %1, %0" : "=&v"(t), "=&v"(a) : "s"(WR + L_PRL), "n"(r) : "memory");
            } else asm volatile("global_load_lds_dword %0, %1 offset:%2" ::"v"(v0), "s"(one), "n"(4 * r) : "memory"); };
        auto rd1 = [&](auto F, auto H) AI {
            constexpr int f = F, h = H, n = f >> 2, j = f >> 3, c = h ? E_B[f] : E_A[f], a = c % 100;
            if constexpr (h == 0 && (f & 3) == 0 && Q_C[n] > 0 && !((NOF & 512) && n >= 12 && n < KD)) chk(Q_C[n]);
            wh[f % (PD + 1)][h] = tr_read(ra[f & 3] + (J_U[j] - J_B[j]) * 8192 + (n & 1) * 4096 + h * 512);
            SB0();
            if constexpr (c / 100 == 1) {
                constexpr int jn = (f + 3 - a) >> 3;
                asm volatile("v_add_u32 %0, %1, %0" : "+v"(ra[a]) : "n"((J_B[jn] - J_B[jn - 1]) * 8192));
            } else if constexpr (c / 50 == 4) {
                asm volatile("v_add_u32 %0, %1, %2" : "=v"(xt[0]) : "s"((hs ? X_U1[a] : X_U0[a]) * 8192), "v"(xa));
            } else if constexpr (c / 50 == 5) {
                asm volatile("v_xor_b32 %0, 64, %1" : "=v"(xt[1]) : "v"(xt[0]));
            } else if constexpr (c / 100 == 3) {
                xf[a % 25 % ND][a / 25 & 1][a / 50] = lds_read128(xt[a / 50] + (a / 25 & 1) * 2048);
            } else if constexpr (c / 100 == 4) {
                fv = *(volatile AS3 unsigned*)(size_t)(fa + (a > NM2 ? 32 : a > NM1 ? 16 : 0));
            } else if constexpr (c / 100 == 5) {
                bad = __builtin_amdgcn_ballot_w64(fv < (unsigned)a);
                asm volatile("" : "+s"(bad));
            } else if constexpr (c / 100 == 6 && !((NOF & 1024) && a < NP1)) {
                report(a);
            }
        };
        RS(1);
        sfor<0, PD>([&](auto F) { rd1(F, IC<0>{}); rd1(F, IC<1>{}); });
        RS(2);
        constexpr int K0F = 4 * KD, K1F = 4 * KD + 1;
        sfor<0, 96>([&](auto N) {
            sfor<0, 4>([&](auto E) {
                constexpr int n = N, e = E, f = 4 * n + e, j = n >> 1, ks = n & 1, u = f_u(f), s = J_X[j], m = J_M[j];
                const bf16x8 wv = cat8(wh[f % (PD + 1)][0], wh[f % (PD + 1)][1]);
                sfor<0, 2>([&](auto RB) {
                    constexpr int rb = RB;
                    const bf16x8 xv = xf[s % ND][rb][ks];
                    if (AMF) {
                        if (j_first(j) && ks == 0) asm volatile("v_mfma_f32_16x16x32_bf16 %0, %1, %2, 0" : "=&v"(acc[m][u][rb]) : "v"(m == 1 ? xv : wv), "v"(m == 1 ? wv : xv));
                        else asm volatile("v_mfma_f32_16x16x32_bf16 %0, %1, %2, %0" : "+v"(acc[m][u][rb]) : "v"(m == 1 ? xv : wv), "v"(m == 1 ? wv : xv));
                    } else if (m == 1) acc[m][u][rb] = mfma16(xv, wv, acc[m][u][rb]);
                    else acc[m][u][rb] = mfma16(wv, xv, acc[m][u][rb]);
                    SB0();
                    if constexpr (f + PD < 384 && (f + PD < 4 * VD || f >= 4 * VD)) rd1(IC<f + PD>{}, RB);
                    if constexpr (rb == 0 && f == K0F) relane();
                    if constexpr (AMF && rb == 0 && (f == K0F || f == K1F)) asm volatile("s_nop 7");
                    SB0();
                    if constexpr (f == K0F) lds_write<bf16x8>(AX + (P_U[0] * 8192 - L_A) + rb * 1024, pack44(acc[0][0][rb], acc[0][1][rb]));
                    if constexpr (f == K1F) {
                        kown[rb] = pack44(acc[0][2][rb], acc[0][3][rb]);
                        if (rb < 2 - SH) lds_write<bf16x8>(AX + (P_U[2] * 8192 - L_A) + rb * 1024, kown[rb]);
                        else asm volatile("" : "+v"(kown[rb]));
                    }
                    SB0();
                });
                if constexpr (f == K0F) { flag(WR + L_FK0); RS(12); }
                if constexpr (f == K1F) { if (SH < 2) flag(WR + L_FK); RS(4); }
                if constexpr (f == 4 * VD - 1) {
                    bf16x8 ak[2];
                    if (NF2 > NF1) flag(WR + L_FVR);
                    auto own = [&]() AI {
                        if (NF2 == NF1) return;
                        vx_init();
                        vw_init();
                        lds_wait0(W4 + L_FVR);
                        sfor<NF1, NF2>(fill);
                        asm volatile("s_mov_b32 m0, %0" ::"s"(lds0 + L_PROG + w * 256 - 124));
                        RS(14);
                    };
                    if (SH) {
                        __builtin_amdgcn_s_setprio(HPRIO);
                        phi_w(ak, 1);
                    }
                    asm volatile("s_nop 7");
                    SB0();
#pragma unroll
                    for (int i = 0; i < 4; ++i) lds_write<bf16x8>(AX + i * 1024, pack44(acc[1][i][0], acc[1][i][1]));
                    SB0();
                    RS(5);
                    if (CFP == 1 && SH) own();
                    if (SH) {
                        f32x4 pk[2][2];
                        unsigned pw[4];
#pragma unroll
                        for (int i = 4 - 2 * SH; i < 4; ++i) pk[i >> 1][i & 1] = rbf4(mfma16(ak[i & 1], kown[i >> 1], zero));
                        phi_adr(pw, lds0 + P_U[2] * 8192 + (SH == 1 ? 2048 : 0));
                        if (SH == 1) phi_k1(pk[1], pw);
                        else phi_k<true>(pk, pw, 0, []() {});
                        SB0();
                    }
                    flag(WR + L_FV);
                    if (OCL == 2) o_clear4();
                    RS(13);
                    if (CFP == 0 || !SH) own();
                    kv_part();
                    __builtin_amdgcn_s_setprio(CPRIO);
                    fv = 0;
                    bad = ~0ull;
                    sfor<4 * VD, 4 * VD + PD>([&](auto F) { rd1(F, IC<0>{}); rd1(F, IC<1>{}); });
                    RS(10);
                }
                if constexpr (e == 3) {
                    SB0();
                    if constexpr (n % CSN == CSN - 1) { RSC(32 + n / CSN); RK(48 + n / CSN); }
                    if constexpr (n == 31) RS(3);
                }
            });
        });
        asm volatile("s_nop 7");
        SB0();
        RS(6);
        if (CPRIO) __builtin_amdgcn_s_setprio(0);
        flag(WR + L_FQD);
        if (OCL) {
            const u32x2 tv = lds_read<u32x2>(lds0 + L_SEL);
            waitv<0>();
            ST_MASK("global_store_dwordx2", 1ull, 0u, tv, ws + WS_CL + 4096 + ((mt * 4 + rg) * 16 + hp) * 8);
        }
        bf16x8 a0[2], a1[2];
        f32x4 pq[2][2][2];
        phi_w(a1, hs);
        phi_w(a0, hs ^ 1);
#pragma unroll
        for (int rb = 0; rb < 2; ++rb) {
            const bf16x8 b0 = pack44(acc[2][0][rb], acc[2][1][rb]), b1 = pack44(acc[2][2][rb], acc[2][3][rb]);
#pragma unroll
            for (int fb = 0; fb < 2; ++fb) {
                pq[1][rb][fb] = rbf4(mfma16(a1[fb], hs ? b1 : b0, zero));
                pq[0][rb][fb] = rbf4(mfma16(a0[fb], hs ? b0 : b1, zero));
            }
        }
        softmax2<true>(pq[1], ph, []() {});
        RS(7);
        lds_wait(W4 + L_FKS, 1);
#pragma unroll
        for (int i = 0; i < 4; ++i) lds_write<u32x4>(AX + i * 1024, ph[i >> 1][i & 1]);
        SB0();
        flag(WR + L_FQ);
        sq = lds_read<u32x4>(lds0 + L_SEL);
        RS(8);
        softmax2<true>(pq[0], ph, []() {});
        asm volatile("" : "+v"(ph[0][0]), "+v"(ph[0][1]), "+v"(ph[1][0]), "+v"(ph[1][1]));
        RS(9);
        RS(11);
        xg_def();
        kf_def();
    }
    relane();
    const char* cla = CLL ? ws + WS_C2 + ((rt * 2 + kp) * 16 + l15) * 128 : ws + WS_CL + (OCL ? 4096 + ((rt * 4 + rg) * 16 + l15) * 8 : ((rt * 2 + kp) * 16 + l15) * 8);
    const u64 T = (u64)sq[1] << 32 | sq[0];
    u64 cl;
    xg_load(w & 5);
    {
        if ((w & 5) != 4) {
            lds_wait0(lds0 + L_FKF + oh * 4);
#pragma unroll
            for (int i = 0; i < 4; ++i) kf[i] = lds_read<u32x4>(lds0 + L_KF + oh * 4096 + i * 1024 + l16);
#pragma unroll
            for (int i = 0; i < 2; ++i) kf[4 + i] = lds_read<u32x4>(lds0 + L_KZ + oh * 128 + i * 64 + g * 16);
        }
        asm volatile("global_load_dwordx2 %0, %1, off sc1" : "=&v"(cl) : "v"(cla) : "memory");
        RS(27);
        o_store(kf, ph, rsrc, WS_O + ((mt * 2 + hs) * 32 + 2 * hp + oh) * 4096 + (rg & 1) * 2048 + l16);
        RS(28);
    }
    if (WON < 16 && WLK < 0) flag(lds0 + L_FOS + w * 4);
    waitv<3>();
#pragma unroll
    for (int i = 0; i < 6; ++i) pin(kf[i]);
    flag(lds0 + L_FWO + w * 4);
    __builtin_amdgcn_s_setprio(0);
    if (WON < 16 && WLK < 0) {
        if ((w & 5) == 5) {
            lds_wait(lds0 + L_FOS + sch * 4, 1);
            wo_load(WON, 16, true);
        }
    }
    const u32x2 selv = {sq[2], sq[3]};

    RS(30);
    const char* ob = ws + WS_O + (rt * 2 + (rg >> 1)) * 131072 + (rg & 1) * 2048;
    const unsigned ov0 = l16 + kp * 1024, ov1 = l16 + (kp ^ 1) * 1024;
    auto oa = [&](int i) { return ob + ((2 * i + kp + rot) & 31) * 4096; };
    u32x4 of[3][4];
    auto fetch1 = [&](int k, int j) AI {
        if (k < OPL) ld_async(of[k % 3][j], (j & 1) ? ov1 : ov0, oa(2 * k + (j >> 1)));
        else asm volatile("global_load_dwordx4 %0, %1, %2" OPM : "=&v"(of[k % 3][j]) : "v"((j & 1) ? ov1 : ov0), "s"(oa(2 * k + (j >> 1))) : "memory");
    };
    auto fix = [&](int k) AI { if (NOF & 128) { for (int j = 0; j < 4; ++j) pin(of[k % 3][j]); return; }
        if (PP3) settle_p<PP3 + !PP3, PD3>(of[k % 3], [&](int j) { return (j & 1) ? ov1 : ov0; }, [&](int j) { return oa(2 * k + (j >> 1)); });
        else settle(of[k % 3], [&](int j) { return (j & 1) ? ov1 : ov0; }, [&](int j) { return oa(2 * k + (j >> 1)); }); };
    const bool own = (__builtin_amdgcn_readfirstlane(selv[0]) >> 4) == (unsigned)(rt * 8 + rg * 2 + kp);
    float gv_[2];
    f32x2 gb[2][4];
    auto g_mix = [&]() AI {
#pragma unroll
        for (int j = 0; j < 2; ++j) {
            gv_[j] = bf2f((u16)gg[j]);
            const float og = rbf(1.f - gv_[j]);
#pragma unroll
            for (int i = 0; i < 4; ++i) {
                const unsigned t = pack2(og * lo16(xr[j][i]), og * hi16(xr[j][i]));
                gb[j][i] = (f32x2){lo16(t), hi16(t)};
                asm volatile("" : "+v"(gb[j][i]));
            }
        }
    };
    asm volatile("" : "+v"(xr[0]), "+v"(xr[1]), "+v"(gg[0]), "+v"(gg[1]));
#pragma unroll 1
    for (unsigned s = 0; s < SPIN_CAP && __ballot((gg[0] >> 16) != (T & 0xFFFFFFFFFFFFull) || (gg[1] >> 16) != (T & 0xFFFFFFFFFFFFull)); ++s) {
        g_load();
        waitv<0>();
        asm volatile("" : "+v"(gg[0]), "+v"(gg[1]));
    }
    g_mix();
    if (PDL && ((w & 5) != 5 || WLK >= 0)) __builtin_amdgcn_s_sleep(PDL);
    if ((w & 5) != 5) waitv<2>();
    else waitv<(WON < 16 && WLK < 0 ? 3 + 4 * (16 - WON) : 2)>();
    asm volatile("" : "+v"(cl));
#pragma unroll 1
    for (unsigned s = 0; s < SPIN_CAP && __ballot(cl != T); ++s)
        asm volatile("global_load_dwordx2 %0, %1, off sc1\n\ts_waitcnt vmcnt(0)" : "=&v"(cl) : "v"(cla) : "memory");
    RS(16);
#pragma unroll
    for (int j = 0; j < 4; ++j) fetch1(0, j);
    RS(17);
    f32x4 acc[2][4];
    unsigned wa[2][4];
#pragma unroll
    for (int i = 0; i < 8; ++i) {
        wa[i >> 2][i & 3] = ((lds0 + 4096 * kp + (4 * g + q) * 128 + ((((g & 1) << 1) | (q >> 1)) * 32) + 8 * p) ^ ((i & 3) << 5)) + (i >> 2) * 65536;
        asm volatile("" : "+v"(wa[i >> 2][i & 3]));
    }
    s16x4 wf[2][8][2];
    auto wrd = [&](int k, int i) AI {
        const int u = wo_unit(2 * k + (i >> 3));
        wf[k & 1][i >> 1][i & 1] = tr_read(wa[u >> 3][(i >> 1) & 3] + (u & 7) * 8192 + (i & 1) * 2048);
    };
    u32x4 yp[14];
    const unsigned yo = srow * 4096 + cb * 256 + sch * 32;
    auto ya = [&](int i) { return ws + WS_Y + (i >> 1) * 32768 + (i & 1) * 16; };
    lds_wait(lds0 + L_FWD + ((l & 8) ? 16 + sch * 4 : (l & 1) * 4), 1);
    RS(18);
    RS(31);
    if (BPRIO && !kp) __builtin_amdgcn_s_setprio(BPRIO);
#pragma unroll
    for (int i = 0; i < 16; ++i) wrd(0, i);
#pragma unroll
    for (int j = 0; j < 4; ++j) fetch1(1, j);
    waitv<4>();
    fix(0);
    sfor<0, 8>([&](auto K) {
        constexpr int k = K;
        unsigned m[4];
        if constexpr (WON < 16 && 2 * (k + 1) == WON) lds_wait(lds0 + L_FW2 + (l & 1) * 4, 1);
        if constexpr (k == YK) {
            if (own) {
#pragma unroll
                for (int i = 0; i < 14; ++i) ld_async(yp[i], yo, ya(i));
            }
        }
        SB0();
        sfor<0, 16>([&](auto I) {
            constexpr int i = I, j = i >> 2, nb = i & 3;
            const bf16x8 a = cat8(wf[k & 1][(j >> 1) * 4 + nb][0], wf[k & 1][(j >> 1) * 4 + nb][1]);
            if (k == 0 && j < 2) asm volatile("v_mfma_f32_16x16x32_bf16 %0, %1, %2, 0" : "=&v"(acc[j & 1][nb]) : "v"(a), "v"(of[k % 3][j]));
            else asm volatile("v_mfma_f32_16x16x32_bf16 %0, %1, %2, %0" : "+v"(acc[j & 1][nb]) : "v"(a), "v"(of[k % 3][j]));
            SB0();
            if constexpr (k < 7) wrd(k + 1, i);
            if constexpr (k < 6 && i < 4) fetch1(k + 2, i);
            if constexpr (k < 7 && i == BCK) {
                waitv<(k < 6 ? 4 : 0)>();
#pragma unroll
                for (int n = 0; n < 4; ++n) pin(of[(k + 1) % 3][n]);
            }
            if constexpr (k < 7 && i > BCK && i <= BCK + 4) {
                m[i - BCK - 1] = min4(of[(k + 1) % 3][i - BCK - 1]);
                asm volatile("" : "+v"(m[i - BCK - 1]));
            }
            SB0();
        });
        if constexpr (k < 7) {
            if (__builtin_expect(__builtin_amdgcn_ballot_w64(min(min(m[0], m[1]), min(m[2], m[3])) == 0u) != 0ull, 0)) fix(k + 1);
        }
        SB0();
        if (k == 0) RS(19);
        if (k == 3) RS(23);
    });
    asm volatile("s_nop 7");
    SB0();
    RS(20);
    const unsigned ea = lds0 + L_A + l16 + 4096 * w, eb = lds0 + L_A + 4096 * (w ^ 4);
    lds_write<u32x4>(ea, nz4(acc[1][0]));
#pragma unroll
    for (int nb = 1; nb < 4; ++nb) lds_write<f32x4>(ea + nb * 1024, acc[1][nb]);
    if (own) {
        waitv<0>();
        settle(yp, [&](int) { return yo; }, ya);
        f32x4 yv[2];
#pragma unroll
        for (int h = 0; h < 2; ++h) {
            yv[h] = __builtin_bit_cast(f32x4, yp[h]);
#pragma unroll
            for (int i = 1; i < 7; ++i) yv[h] += __builtin_bit_cast(f32x4, yp[2 * i + h]);
#pragma unroll
            for (int i = 0; i < 4; ++i) {
                float t = dpp_add<0x128>(yv[h][i]);
                t = swap16_add(t, t);
                yv[h][i] = rbf(rbf(swap32_add(t, t)) * __uint_as_float(selv[1]));
            }
        }
#pragma unroll
        for (int j = 0; j < 2; ++j) {
            const bool hit = row0 + 8 * j + srow == (int)selv[0];
#pragma unroll
            for (int i = 0; i < 4; ++i) {
                const unsigned t = pack2(lo16(xr[j][i]) + yv[i >> 1][2 * (i & 1)], hi16(xr[j][i]) + yv[i >> 1][2 * (i & 1) + 1]);
                xr[j][i] = hit ? t : xr[j][i];
            }
        }
        g_mix();
    }
    BAR();
    RS(21);
#pragma unroll
    for (int nb = 0; nb < 4; ++nb) acc[0][nb] += lds_read<f32x4>(eb + l16 + nb * 1024);
#pragma unroll
    for (int nb = 0; nb < 4; ++nb) asm volatile("" : "+v"(acc[0][nb]));
#pragma unroll
    for (int nb = 0; nb < 4; ++nb) {
        const u32x2 a = {pack2(acc[0][nb][0], acc[0][nb][1]), pack2(acc[0][nb][2], acc[0][nb][3])};
        lds_write<u32x2>(eb + l15 * 128 + (((2 * nb + (g >> 1)) ^ (l & 7)) * 16) + (g & 1) * 8, a);
    }
#pragma unroll
    for (int j = 0; j < 2; ++j) {
        const u32x4 av = lds_read<u32x4>(eb + j * 1024 + srow * 128 + ((sch ^ srow) * 16));
        u32x4 o;
#pragma unroll
        for (int i = 0; i < 4; ++i) {
            const unsigned ga = pack2(gv_[j] * lo16(av[i]), gv_[j] * hi16(av[i]));
            o[i] = pack2(lo16(ga) + gb[j][i][0], hi16(ga) + gb[j][i][1]);
        }
        asm volatile("global_store_dwordx4 %0, %1, %2" OST "\n\ts_nop 1" ::"v"(xo), "v"(o), "s"(out + (size_t)(row0 + 8 * j) * DM + cb * 64) : "memory");
    }
    RS(22);
    RSDUMP();
}

extern "C" void kernel_launch(void* const* d_in, const int* in_sizes, int n_in, void* d_out, int out_size,
                              void* d_ws, size_t ws_size, hipStream_t stream) {
    fused_mix<<<256, 512, 0, stream>>>((const u16*)d_in[0], (const u16*)d_in[1], (const u16*)d_in[2], (const u16*)d_in[3],
                                       (const u16*)d_in[5], (const u16*)d_in[6], (const u16*)d_in[7], (const u16*)d_in[8],
                                       (const u16*)d_in[9], (char*)d_ws, (u16*)d_out);
}
```

```cpp
#include <hip/hip_runtime.h>
#include <hip/hip_bf16.h>
#include <stdint.h>
#include <type_traits>

typedef __attribute__((ext_vector_type(8))) short bf16x8;
typedef __attribute__((ext_vector_type(4))) short s16x4;
typedef __attribute__((ext_vector_type(4))) float f32x4;
typedef __attribute__((ext_vector_type(2))) float f32x2;
typedef __attribute__((ext_vector_type(2))) unsigned u32x2;
typedef __attribute__((ext_vector_type(4))) unsigned u32x4;
typedef __attribute__((ext_vector_type(2))) __bf16 bf16x2_t;
typedef unsigned short u16;
typedef unsigned long long u64;

#define DI __device__ __forceinline__
#define AS3 __attribute__((address_space(3)))
#define AS1 __attribute__((address_space(1)))
#define RLX_AGENT __ATOMIC_RELAXED, __HIP_MEMORY_SCOPE_AGENT

constexpr int SEQ = 2048, DM = 1024, NH = 32, FF = 3584;

DI float bf2f(u16 v) { return __uint_as_float(((unsigned)v) << 16); }
DI u16 f2bf(float f) { __bf16 b = (__bf16)f; return __builtin_bit_cast(u16, b); }
DI float rbf(float f) { return bf2f(f2bf(f)); }
DI unsigned pack2(float lo, float hi) {
    const f32x2 v = {lo, hi};
    return __builtin_bit_cast(unsigned, __builtin_convertvector(v, bf16x2_t));
}
DI float lo16(unsigned u) { return __uint_as_float(u << 16); }
DI float hi16(unsigned u) { return __uint_as_float(u & 0xffff0000u); }

DI s16x4 tr_read(unsigned a) { return __builtin_amdgcn_ds_read_tr16_b64_v4i16((AS3 s16x4*)(size_t)a); }
DI bf16x8 lds_read128(unsigned a) { return *(AS3 const bf16x8*)(size_t)a; }
template <class T> DI void lds_write(unsigned a, T v) { *(AS3 T*)(size_t)a = v; }
template <class T> DI T lds_read(unsigned a) { return *(AS3 const T*)(size_t)a; }
DI bf16x8 cat8(s16x4 a, s16x4 b) { return __builtin_shufflevector(a, b, 0, 1, 2, 3, 4, 5, 6, 7); }
DI f32x4 mfma16(bf16x8 a, bf16x8 b, f32x4 c) { return __builtin_amdgcn_mfma_f32_16x16x32_bf16(a, b, c, 0, 0, 0); }
DI bf16x8 pack44(const f32x4& a, const f32x4& b) {
    u32x4 r;
    r[0] = pack2(a[0], a[1]);
    r[1] = pack2(a[2], a[3]);
    r[2] = pack2(b[0], b[1]);
    r[3] = pack2(b[2], b[3]);
    return __builtin_bit_cast(bf16x8, r);
}
DI unsigned lds_of(const void* p) { return (unsigned)(size_t)(AS3 const char*)p; }

template <int CTRL>
DI unsigned dpp(unsigned x) { return __builtin_amdgcn_update_dpp(0u, x, CTRL, 0xf, 0xf, true); }
template <int CTRL>
DI float dpp_add(float x) { return x + __uint_as_float(dpp<CTRL>(__float_as_uint(x))); }
DI float swap32_add(float a, float b) {
    auto r = __builtin_amdgcn_permlane32_swap(__float_as_uint(a), __float_as_uint(b), false, false);
    return __uint_as_float(r[0]) + __uint_as_float(r[1]);
}
DI float swap16_add(float a, float b) {
    auto r = __builtin_amdgcn_permlane16_swap(__float_as_uint(a), __float_as_uint(b), false, false);
    return __uint_as_float(r[0]) + __uint_as_float(r[1]);
}
DI float wave_sum(float x) {
    x = dpp_add<0xB1>(x);
    x = dpp_add<0x4E>(x);
    x = dpp_add<0x141>(x);
    x = dpp_add<0x140>(x);
    x = swap16_add(x, x);
    return swap32_add(x, x);
}
DI unsigned wave_umax(unsigned x) {
    x = max(x, dpp<0xB1>(x));
    x = max(x, dpp<0x4E>(x));
    x = max(x, dpp<0x141>(x));
    x = max(x, dpp<0x140>(x));
    auto r = __builtin_amdgcn_permlane16_swap(x, x, false, false);
    x = max(r[0], r[1]);
    auto s = __builtin_amdgcn_permlane32_swap(x, x, false, false);
    return max(s[0], s[1]);
}
template <class OP, class POST>
DI void red2(float& a, float& b, OP op, POST post) {
    auto x = __builtin_amdgcn_permlane32_swap(__float_as_uint(a), __float_as_uint(b), false, false);
    const float m = op(__uint_as_float(x[0]), __uint_as_float(x[1]));
    auto y = __builtin_amdgcn_permlane16_swap(__float_as_uint(m), __float_as_uint(m), false, false);
    const float f = post(op(__uint_as_float(y[0]), __uint_as_float(y[1])));
    auto z = __builtin_amdgcn_permlane32_swap(__float_as_uint(f), __float_as_uint(f), false, false);
    a = __uint_as_float(z[0]);
    b = __uint_as_float(z[1]);
}

#define RS(i) do {} while (0)
#define RSD(i) do {} while (0)
#define RSDUMP() do {} while (0)

constexpr unsigned WS_E = 0;
constexpr unsigned WS_RG = 256;
constexpr unsigned WS_GG = WS_RG + 2048;
constexpr unsigned WS_CL = 32768;
constexpr unsigned WS_Z = 65536;
constexpr unsigned WS_KF = WS_Z + 131072;
constexpr unsigned WS_GU = WS_KF + 262144;
constexpr unsigned WS_Y = WS_GU + 262144;
constexpr unsigned WS_KV = WS_Y + 262144;
constexpr unsigned WS_O = WS_KV + (2u << 20);
constexpr unsigned WS_C2 = WS_O + (4u << 20);
constexpr unsigned WS_END = WS_C2 + 65536;

constexpr int WE = 0;
constexpr int NQ = 10, ND = 6, NX = 16, KD = 78, VD = 84, NF = 64, NF1 = 59, NF2 = 59, NP1 = 12, NP = 14, NM1 = 21, NM2 = 21, NM = 22, KFN = 100;
constexpr int J_S[48] = {0, 0, 0, 1, 1, 1, 2, 2, 2, 3, 3, 3, 4, 4, 4, 5, 5, 5, 6, 6, 6, 7, 7, 7, 8, 8, 8, 9, 9, 9, 10, 10, 11, 11, 12, 12, 13, 14, 15, 13, 14, 15, 10, 11, 12, 13, 14, 15};
constexpr int J_M[48] = {0, 1, 2, 0, 1, 2, 0, 1, 2, 0, 1, 2, 0, 1, 2, 0, 1, 2, 0, 1, 2, 0, 1, 2, 0, 1, 2, 0, 1, 2, 0, 1, 0, 1, 0, 1, 0, 0, 0, 1, 1, 1, 2, 2, 2, 2, 2, 2};
constexpr int J_X[48] = {0, 0, 0, 1, 1, 1, 2, 2, 2, 3, 3, 3, 4, 4, 4, 5, 5, 5, 6, 6, 6, 7, 7, 7, 8, 8, 8, 9, 9, 9, 10, 10, 11, 11, 12, 12, 13, 14, 15, 13, 14, 15, 10, 11, 12, 13, 14, 15};
constexpr int J_U[48] = {2, 4, 8, 18, 11, 12, 17, 15, 13, 0, 1, 6, 4, 8, 9, 11, 12, 14, 15, 13, 7, 1, 6, 5, 8, 9, 10, 12, 14, 16, 13, 7, 1, 6, 4, 8, 18, 14, 15, 13, 7, 3, 0, 1, 6, 5, 2, 4};
constexpr int J_B[48] = {2, 2, 2, 11, 11, 11, 11, 11, 11, 0, 0, 0, 0, 8, 8, 8, 8, 8, 8, 8, 1, 1, 1, 1, 1, 9, 9, 9, 9, 9, 9, 1, 1, 1, 1, 1, 13, 13, 13, 13, 0, 0, 0, 0, 0, 0, 0, 0};
constexpr int Q_C[96] = {1, 0, 2, 0, 3, 0, 4, 0, 5, 0, 6, 0, 7, 0, 0, 0, 0, 0, 8, 0, 0, 0, 0, 0, 9, 0, 0, 0, 0, 0, 10, 0, 0, 0, 0, 0, 11, 0, 0, 0, 0, 0, 12, 0, 0, 0, 0, 0, 13, 0, 0, 0, 0, 0, 14, 0, 0, 0, 0, 0, 15, 0, 0, 0, 0, 0, 16, 0, 0, 0, 0, 0, 17, 0, 0, 0, 0, 0, 18, 0, 19, 0, 20, 0, 21, 0, 22, 0, 0, 0, 0, 0, 0, 0, 0, 0};
constexpr int X_U0[16] = {6, 9, 14, 7, 5, 10, 16, 3, 2, 18, 17, 3, 5, 9, 11, 16};
constexpr int X_U1[16] = {5, 10, 16, 3, 2, 18, 17, 0, 4, 11, 15, 0, 2, 10, 12, 17};
constexpr int P_U[4] = {11, 12, 9, 10};
constexpr int W_U[16] = {8, 14, 15, 13, 7, 3, 9, 10, 11, 12, 0, 1, 6, 5, 2, 4};
constexpr int W_N[16] = {100, 100, 100, 100, 100, 100, 101, 101, 101, 101, 13, 13, 14, 14, 99, 99};
constexpr int F_J[64] = {-1, 0, 1, 2, -2, 3, 4, 5, -3, 6, 7, 8, -4, 9, 10, 11, -5, 12, 13, 14, -6, 15, 16, 17, -7, 18, 19, 20, -8, 21, 22, 23, -9, 24, 25, 26, -10, 27, 28, 29, -11, 30, 31, -12, 32, 33, -13, 34, 35, -14, 36, -15, 37, -16, 38, 39, 40, 41, 42, 43, 44, 45, 46, 47};
constexpr int F_N[64] = {0, 0, 0, 0, 0, 0, 0, 0, 0, 0, 0, 0, 0, 0, 0, 1, 1, 1, 1, 1, 2, 2, 2, 2, 3, 3, 3, 3, 4, 4, 4, 4, 5, 5, 5, 5, 6, 6, 6, 6, 7, 7, 7, 7, 8, 8, 8, 8, 9, 9, 9, 10, 10, 10, 10, 11, 11, 11, 11, 12, 12, 12, 12, 12};
constexpr int F_K[64] = {0, 1, 2, 3, 0, 4, 5, 6, 0, 0, 0, 7, 0, 0, 0, 8, 0, 0, 0, 9, 0, 0, 0, 10, 0, 0, 0, 11, 0, 0, 0, 12, 0, 0, 0, 13, 0, 0, 0, 14, 0, 0, 0, 0, 15, 0, 0, 0, 16, 0, 0, 0, 0, 0, 17, 18, 19, 20, 21, 0, 0, 0, 0, 22};
constexpr int E_A[384] = {300, 325, 350, 375, 0, 0, 0, 502, 0, 0, 0, 403, 0, 0, 0, 503, 201, 251, 0, 404, 0, 100, 101, 504, 301, 326, 351, 376, 0, 0, 0, 505, 601, 0, 0, 406, 0, 0, 0, 506, 202, 252, 0, 407, 0, 0, 0, 507, 302, 327, 352, 377, 0, 0, 0, 602, 0, 0, 0, 0, 0, 0, 0, 203, 253, 0, 0, 408, 0, 100, 101, 508, 303, 328, 353, 378, 0, 0, 0, 603, 0, 0, 0, 0, 0, 0, 0, 204, 254, 0, 0, 409, 0, 0, 0, 509, 304, 329, 354, 379, 0, 100, 101, 604, 102, 103, 0, 0, 0, 0, 0, 205, 255, 0, 0, 410, 0, 0, 0, 510, 305, 330, 355, 380, 0, 0, 0, 605, 0, 0, 0, 0, 0, 0, 0, 206, 256, 0, 0, 411, 0, 0, 0, 511, 306, 331, 356, 381, 0, 0, 0, 606, 0, 0, 0, 0, 0, 100, 101, 102, 103, 207, 257, 412, 0, 0, 0, 512, 307, 332, 357, 382, 0, 0, 0, 607, 0, 0, 0, 0, 0, 0, 0, 208, 258, 0, 0, 413, 0, 0, 0, 513, 308, 333, 358, 383, 0, 100, 101, 608, 102, 103, 0, 0, 0, 0, 0, 209, 259, 0, 0, 414, 0, 0, 0, 514, 309, 334, 359, 384, 0, 0, 0, 609, 0, 0, 0, 0, 0, 0, 0, 210, 260, 0, 0, 415, 0, 0, 0, 515, 310, 335, 360, 385, 0, 100, 101, 610, 102, 103, 211, 261, 0, 0, 0, 0, 311, 336, 361, 386, 0, 0, 0, 516, 611, 212, 262, 0, 0, 0, 0, 0, 312, 337, 362, 387, 0, 0, 0, 213, 263, 0, 0, 417, 0, 100, 101, 517, 313, 338, 363, 388, 214, 264, 0, 612, 314, 339, 364, 389, 215, 265, 0, 0, 315, 340, 365, 390, 0, 0, 0, 518, 0, 0, 0, 419, 0, 100, 101, 519, 102, 103, 0, 420, 0, 0, 0, 520, 0, 0, 0, 0, 0, 0, 0, 0, 0, 0, 0, 422, 0, 0, 0, 522, 0, 0, 0, 0, 0, 0, 0, 0, 0, 0, 0, 613, 0, 0, 0, 0, 0, 0, 0, 0, 0, 0, 0, 0, 0, 0, 0, 614, 0, 0, 0, 0, 0, 0, 0, 0, 0, 0, 0, 0};
constexpr int E_B[384] = {0, 0, 402, 0, 0, 0, 0, 0, 0, 0, 0, 0, 0, 0, 0, 0, 0, 0, 0, 0, 0, 0, 0, 102, 103, 0, 405, 0, 0, 0, 0, 0, 0, 0, 0, 0, 0, 0, 0, 0, 0, 0, 0, 0, 0, 0, 0, 0, 0, 0, 0, 0, 0, 0, 0, 0, 0, 0, 0, 0, 0, 0, 0, 0, 0, 0, 0, 0, 0, 0, 0, 102, 103, 0, 0, 0, 0, 0, 0, 0, 0, 0, 0, 0, 0, 0, 0, 0, 0, 0, 0, 0, 0, 0, 0, 0, 0, 0, 0, 0, 0, 0, 0, 0, 0, 0, 0, 0, 0, 0, 0, 0, 0, 0, 0, 0, 0, 0, 0, 0, 0, 0, 0, 0, 0, 0, 0, 0, 0, 0, 0, 0, 0, 0, 0, 0, 0, 0, 0, 0, 0, 0, 0, 0, 0, 0, 0, 0, 0, 0, 0, 0, 0, 0, 0, 0, 0, 0, 0, 0, 0, 0, 0, 0, 0, 0, 0, 0, 0, 0, 0, 0, 0, 0, 0, 0, 0, 0, 0, 0, 0, 0, 0, 0, 0, 0, 0, 0, 0, 0, 0, 0, 0, 0, 0, 0, 0, 0, 0, 0, 0, 0, 0, 0, 0, 0, 0, 0, 0, 0, 0, 0, 0, 0, 0, 0, 0, 0, 0, 0, 0, 0, 0, 0, 0, 0, 0, 0, 0, 0, 0, 0, 0, 0, 0, 0, 0, 0, 0, 0, 0, 0, 0, 0, 0, 0, 0, 0, 0, 0, 0, 0, 0, 0, 0, 0, 0, 0, 416, 0, 0, 0, 0, 0, 0, 0, 0, 0, 0, 0, 0, 0, 0, 0, 0, 0, 0, 0, 0, 0, 0, 0, 0, 0, 0, 0, 0, 102, 103, 0, 0, 0, 0, 0, 0, 0, 0, 0, 0, 0, 0, 0, 0, 0, 0, 0, 418, 0, 0, 0, 0, 0, 0, 0, 0, 0, 0, 0, 0, 0, 0, 0, 0, 0, 0, 0, 0, 0, 0, 0, 0, 0, 0, 0, 0, 0, 0, 0, 0, 0, 0, 0, 0, 0, 0, 0, 0, 0, 0, 0, 0, 0, 0, 0, 0, 0, 0, 0, 0, 0, 0, 0, 0, 0, 0, 0, 0, 0, 0, 0, 0, 0, 0, 0, 0, 0, 0, 0, 0, 0, 0, 0, 0, 0};
#define LH 0
constexpr unsigned L_A = 16 * 8192;
constexpr unsigned L_KF = 18 * 8192;
constexpr unsigned L_SM = 19 * 8192;
constexpr unsigned L_FULL = L_SM + 256;
constexpr unsigned L_FVR = L_SM + 320, L_PRL = L_SM + 336;
constexpr unsigned L_PROG = L_SM + 896;
constexpr unsigned L_FW2 = L_SM + 512, L_FOS = L_SM + 480, L_FGI = L_SM + 520, L_FK0 = L_SM + 752, L_FBI = L_SM + 744;
constexpr unsigned L_FK = L_SM + 528, L_FKT = L_SM + 544, L_FV = L_SM + 560, L_FP = L_SM + 576, L_FKS = L_SM + 592;
constexpr unsigned L_FQD = L_SM + 608, L_FG = L_SM + 624, L_FKF = L_SM + 736, L_FQ = L_SM + 720, L_FWD = L_SM + 640, L_FWO = L_SM + 656, L_FLD = L_SM + 688, L_BEST = L_SM + 696;
constexpr unsigned L_SEL = L_SM + 704;
constexpr unsigned L_XS = L_SM + 2048, L_KZ = L_SM + 3072, L_WPHI = L_SM + 4096;
static_assert(NP <= 31 && NM <= 63, "");
static_assert(P_U[1] == P_U[0] + 1 && P_U[3] == P_U[2] + 1, "feature maps of a head lie side by side");
constexpr int wo_unit(int i) { return W_U[i]; }
constexpr int f_u(int f) { return f & 3; }
constexpr bool j_first(int j) {
    for (int i = 0; i < j; ++i)
        if (J_M[i] == J_M[j]) return false;
    return true;
}
constexpr int younger(int p) {
    int c = 0;
    for (int n = 0; n < NF1 - LH; ++n)
        if (F_N[n] >= p) c += F_J[n] < 0 ? 8 : 4;
    return c;
}
constexpr int nfv() {
    for (int n = 0; n < NF1; ++n)
        if (F_J[n] == VD / 2 - 1) return n + 1;
    return NF1;
}
constexpr int next_need(int n) {
    for (int i = n; i < NF; ++i)
        if (F_N[i] != F_N[n]) return F_N[i];
    return 99;
}
constexpr int x_stage(int i) { return i < 16 ? i : i - 16 + NQ; }
constexpr unsigned SPIN_CAP = 1u << 16;
#define N8(a) a, a + 1, a + 2, a + 3, a + 4, a + 5, a + 6, a + 7
__device__ const unsigned k_num[64] = {N8(0), N8(8), N8(16), N8(24), N8(32), N8(40), N8(48), N8(56)};

#define RK(i) do {} while (0)
#define RSL(i) do {} while (0)
#define RSC(i) do {} while (0)
#define CSN 6
#define CFP 0
#define QWM 0
#define RLD 11
#define CLF 3
#define CLT 1
#define FLV 1
#define SP1 0
#define SP2 0
#define SP3 0
#define SP4 0
#define SPW 0
#define SPI "s_load_dwordx2"
#define DNP 1
#define WOT 1
#define YT 1
#define GDL 16
#define SSM 0
#define CLE 1
#define GUO 0
#define TL0 0
#define TL0W ""
#define TLD 0
#define SPA 0
#define SPS 7
#define MRG 36
#define PP1 0
#define PD1 0
#define PP2 0
#define PD2 0
#define PP3 0
#define PD3 0
#define PDI 0
#define HLD " sc1"
#define HST " sc1"
#define HSA 16
#define BMAP 0
#define ROTC 0
#define ROT0 0
#define OCL 0
#define KCL 0
#define CLL 1
#define ZL 1
#define RTS 0
#define WEP 0
#define PFN 0
#define PFS 0x248
#define PFM ""
#define KXP 8
#define KXQ 4
#define LPRIO 0
#define LPRW 3
#define LTH 63
#define LT0 14
#define PEEK 1
#define CSP 1
#define GUA 12
#define OPL 8
#define OPM ""
#define PD 4
#define NPH 4
#define KSK 3
#define ROTK 8
#define OWIN 2
#define CLR0 2
#define CLS 1
#define CLP 0
#define SLK 2
#define CPH 3
#define NPL 0
#define CPRIO 1
#define HPRIO 2
#define KPK 3
#define KRND 1
#define QW 1
#define WOG 1
#define WON 12
#define KFD 24
#define SEL0 9
#define SPRIO 2
#define FBA 3
#define FBB 10
#define FBS 1
#define FBL 1
#define WOP 0
#define GTL 3
#define WPF 0
#define WOE 6
#define GW 0
#define GUF 0
#define FBH 0
#define WG2 0
#define NTX 0
#define NTW 0
#define NTS 16
#define NTO ""
#define GQ1 6
#define GQ2 8
#define CLO 0
#define PDL 6
#define XK 3
#define YK 7
#define BPRIO 0
#define AMF 1
#define LF 1
#define SH 2
#define LC 0
#define SPR2 0
#define GQ0 0
#define CPF 53248
#define NF0 4
#define OTL 0
#define OST " sc1"
#define TLW 3
#define THR -1
#define THQ -1
#define FDH 0
#define WLK -1
#define XGE 1
#define KXO 3
#define KXM 3
#define KX8 0
#define KXB 0xFEDBA9C768534210ull
#define BCK 10
#define GPR 0
#define GHL 0
#define TGT 1
#define PORD 1
#define TRM 3
#define LFE 1
#define PWN 0
#define PWL 99
#define TRN 0
#define RT0 0
#define BSP 0
#define WOV 0
#define GUP 1
#define NOF 0
#define FBD 8
#define BAR() asm volatile("s_waitcnt lgkmcnt(0)\n\ts_barrier" ::: "memory")
template <int N>
DI void waitv() { asm volatile("s_waitcnt vmcnt(%0)" ::"n"(N) : "memory"); }

DI u64 ld_agent(const char* p) { return __hip_atomic_load((const AS1 u64*)p, RLX_AGENT); }
DI void st_agent(char* p, u64 v) { __hip_atomic_store((AS1 u64*)p, v, RLX_AGENT); }
DI unsigned nz1(unsigned v) { return max(v, 1u); }
DI unsigned umin(unsigned a, unsigned b) { return min(a, b); }
DI u32x4 nz4(u32x4 v) { return (u32x4){max(v[0], 1u), max(v[1], 1u), max(v[2], 1u), max(v[3], 1u)}; }
DI u32x4 nz4(f32x4 v) { return nz4(__builtin_bit_cast(u32x4, v)); }
DI unsigned min4(u32x4 v) { return min(min(v[0], v[1]), min(v[2], v[3])); }
DI u32x4 ld_wt(__amdgpu_buffer_rsrc_t r, unsigned off) { return __builtin_amdgcn_raw_buffer_load_b128(r, off, 0, 16); }
DI void st_wt(__amdgpu_buffer_rsrc_t r, unsigned off, u32x4 v) { __builtin_amdgcn_raw_buffer_store_b128(v, r, off, 0, HSA); }
DI void ld_async(u32x4& v, unsigned off, const char* base) {
    asm volatile("global_load_dwordx4 %0, %1, %2 sc1" : "=&v"(v) : "v"(off), "s"(base) : "memory");
}
#define ST_MASK(op, m, off, v, base) asm volatile("s_mov_b64 exec, %3\n\t" op " %0, %1, %2" HST "\n\ts_mov_b64 exec, -1\n\ts_nop 0" ::"v"(off), "v"(v), "s"(base), "s"((u64)(m)) : "memory")
DI void pin(u32x4& v) { asm volatile("" : "+v"(v)); }
template <int N, class OFF, class BASE, class OK>
DI void settle(u32x4 (&v)[N], OFF off, BASE base, OK ok) {
#pragma unroll
    for (int i = 0; i < N; ++i) pin(v[i]);
#pragma unroll 1
    for (unsigned s = 0; s < SPIN_CAP; ++s) {
        unsigned m = ok(v[0]);
#pragma unroll
        for (int i = 1; i < N; ++i) m = min(m, ok(v[i]));
        if (__ballot(m == 0u) == 0ull) break;
#pragma unroll
        for (int i = 0; i < N; ++i) {
            u64 keep;
            asm volatile("s_mov_b64 %1, exec\n\ts_mov_b64 exec, %4\n\tglobal_load_dwordx4 %0, %2, %3 sc1\n\ts_mov_b64 exec, %1"
                         : "+&v"(v[i]), "=&s"(keep) : "v"(off(i)), "s"(base(i)), "s"((u64)__ballot(ok(v[i]) == 0u)) : "memory");
        }
        waitv<0>();
#pragma unroll
        for (int i = 0; i < N; ++i) pin(v[i]);
    }
}
template <int N, class OFF, class BASE>
DI void settle(u32x4 (&v)[N], OFF off, BASE base) { settle(v, off, base, [](const u32x4& a) { return min4(a); }); }
template <int P, int D, int N, class OFF, class BASE>
DI void settle_p(u32x4 (&v)[N], OFF off, BASE base) {
#pragma unroll 1
    for (unsigned s = 0;; ++s) {
        u64 miss[N], any = 0;
#pragma unroll
        for (int i = 0; i < N; ++i) {
            pin(v[i]);
            miss[i] = __ballot(min4(v[i]) == 0u);
            any |= miss[i];
        }
        if (s >= SPIN_CAP) any = 0;
        asm volatile("" : "+s"(any));
        if (any == 0ull) break;
#pragma unroll
        for (int i = 0; i < N; ++i)
            asm volatile("s_mov_b64 exec, %3\n\tglobal_load_dwordx4 %0, %1, %2" HLD "\n\ts_mov_b64 exec, -1"
                         ::"v"(v[i]), "v"(off(i)), "s"(base(i)), "s"(miss[i] | 1ull) : "memory");
        if (D) __builtin_amdgcn_s_sleep(D);
        waitv<N * (P - 1)>();
    }
}
DI void lds_wait(unsigned a, unsigned n) {
    unsigned v, c;
    asm volatile("s_mov_b32 %1, 0x400000\n"
                 ".Lp%=:\n\tds_read_b32 %0, %2\n\ts_waitcnt lgkmcnt(0)\n\tv_cmp_gt_u32 vcc, %3, %0\n\ts_cbranch_vccz .Lf%=\n\t"
                 "s_sleep 1\n\ts_sub_u32 %1, %1, 1\n\ts_cbranch_scc0 .Lp%=\n.Lf%=:"
                 : "=&v"(v), "=&s"(c) : "v"(a), "s"(n) : "memory", "vcc", "scc");
}
DI void lds_wait0(unsigned a) {
    if (!TGT) { lds_wait(a, 1); return; }
    unsigned v, c;
    asm volatile("s_mov_b32 %1, 0x1000000\n"
                 ".Lp%=:\n\tds_read_b32 %0, %2\n\ts_sub_u32 %1, %1, 1\n\ts_cbranch_scc1 .Lf%=\n\ts_waitcnt lgkmcnt(0)\n\tv_cmp_eq_u32 vcc, 0, %0\n\ts_cbranch_vccnz .Lp%=\n"
                 ".Lf%=:\n\ts_waitcnt lgkmcnt(0)"
                 : "=&v"(v), "=&s"(c) : "v"(a) : "memory", "vcc", "scc");
}
DI void lds_wait8(unsigned a, unsigned n) {
    unsigned v, c;
    asm volatile("s_mov_b32 %1, 0x100000\n"
                 ".Lp%=:\n\tds_read_b32 %0, %2\n\ts_waitcnt lgkmcnt(0)\n\tv_cmp_gt_u32 vcc, %3, %0\n\ts_cbranch_vccz .Lf%=\n\t"
                 "s_sleep 4\n\ts_sub_u32 %1, %1, 1\n\ts_cbranch_scc0 .Lp%=\n.Lf%=:"
                 : "=&v"(v), "=&s"(c) : "v"(a), "s"(n) : "memory", "vcc", "scc");
}
DI void lds_peek(unsigned& v, unsigned a) { asm volatile("ds_read_b32 %0, %1" : "=v"(v) : "v"(a) : "memory"); }
DI void lds_wait(unsigned& v, unsigned a, unsigned n) {
    unsigned c;
    asm volatile("s_mov_b32 %1, 0x400000\n"
                 ".Lp%=:\n\ts_waitcnt lgkmcnt(0)\n\tv_cmp_gt_u32 vcc, %3, %0\n\ts_cbranch_vccz .Lf%=\n\t"
                 "ds_read_b32 %0, %2\n\ts_sub_u32 %1, %1, 1\n\ts_cbranch_scc0 .Lp%=\n.Lf%=:"
                 : "+v"(v), "=&s"(c) : "v"(a), "s"(n) : "memory", "vcc", "scc");
}
DI void lds_set(unsigned a) {
    asm volatile("s_waitcnt lgkmcnt(0)\n\tds_write_b32 %0, %1" ::"v"(a), "v"(1u) : "memory");
}
DI float dot2(unsigned a, unsigned b, float c) {
    return __builtin_amdgcn_fdot2_f32_bf16(__builtin_bit_cast(bf16x2_t, a), __builtin_bit_cast(bf16x2_t, b), c, false);
}
DI float sum4(const float (&a)[4]) {
    float c = swap16_add(swap32_add(a[0], a[1]), swap32_add(a[2], a[3]));
    c = dpp_add<0xB1>(c);
    c = dpp_add<0x4E>(c);
    c = dpp_add<0x141>(c);
    return dpp_add<0x140>(c);
}

template <class OP, class POST>
DI void red4(float& a, float& b, float& c, float& d, OP op, POST post) {
    auto x = __builtin_amdgcn_permlane32_swap(__float_as_uint(a), __float_as_uint(b), false, false);
    auto y = __builtin_amdgcn_permlane32_swap(__float_as_uint(c), __float_as_uint(d), false, false);
    const float m = op(__uint_as_float(x[0]), __uint_as_float(x[1])), n = op(__uint_as_float(y[0]), __uint_as_float(y[1]));
    auto z = __builtin_amdgcn_permlane16_swap(__float_as_uint(m), __float_as_uint(n), false, false);
    const unsigned f = __float_as_uint(post(op(__uint_as_float(z[0]), __uint_as_float(z[1]))));
    auto u = __builtin_amdgcn_permlane16_swap(f, f, false, false);
    auto v = __builtin_amdgcn_permlane32_swap(u[0], u[0], false, false);
    auto t = __builtin_amdgcn_permlane32_swap(u[1], u[1], false, false);
    a = __uint_as_float(v[0]);
    b = __uint_as_float(v[1]);
    c = __uint_as_float(t[0]);
    d = __uint_as_float(t[1]);
}
DI float mx2(float a, float b) { return __builtin_elementwise_maximum(a, b); }
DI float opq(float a) { asm("" : "+v"(a)); return a; }
template <bool PK, class HOOK>
DI void softmax2(const f32x4 (&v)[2][2], u32x4 (&ph)[2][2], HOOK hook) {
    constexpr float L2E = 1.4426950408889634f;
    float mx[2], mn[2], sp_[2], sn_[2];
#pragma unroll
    for (int b = 0; b < 2; ++b) {
        const f32x4 s = v[b][0], t = v[b][1];
        mx[b] = mx2(mx2(mx2(mx2(s[0], s[1]), s[2]), mx2(mx2(s[3], t[0]), t[1])), mx2(t[2], t[3]));
        mn[b] = mx2(mx2(mx2(mx2(-s[0], -s[1]), -s[2]), mx2(mx2(-s[3], -t[0]), -t[1])), mx2(-t[2], -t[3]));
    }
    red4(mx[0], mx[1], mn[0], mn[1], [](float a, float b) { return mx2(a, b); }, [](float a) { return a * -L2E; });
    f32x2 ep[2][4], en[2][4];
#pragma unroll
    for (int b = 0; b < 2; ++b) {
#pragma unroll
        for (int e = 0; e < 4; ++e) {
            const f32x2 u = {v[b][e >> 1][2 * (e & 1)], v[b][e >> 1][2 * (e & 1) + 1]};
            f32x2 a, c;
            if (PK) {
                a = __builtin_elementwise_fma(u, (f32x2){L2E, L2E}, (f32x2){mx[b], mx[b]});
                c = __builtin_elementwise_fma(u, (f32x2){-L2E, -L2E}, (f32x2){mn[b], mn[b]});
            } else {
                a = (f32x2){__builtin_fmaf(u[0], L2E, mx[b]), __builtin_fmaf(u[1], L2E, mx[b])};
                c = (f32x2){__builtin_fmaf(u[0], -L2E, mn[b]), __builtin_fmaf(u[1], -L2E, mn[b])};
            }
            ep[b][e] = (f32x2){__builtin_amdgcn_exp2f(a[0]), __builtin_amdgcn_exp2f(a[1])};
            en[b][e] = (f32x2){__builtin_amdgcn_exp2f(c[0]), __builtin_amdgcn_exp2f(c[1])};
        }
        if (PK) {
            const f32x2 s = (ep[b][0] + ep[b][1]) + (ep[b][2] + ep[b][3]), t = (en[b][0] + en[b][1]) + (en[b][2] + en[b][3]);
            sp_[b] = s[0] + s[1];
            sn_[b] = t[0] + t[1];
        } else {
            sp_[b] = ep[b][0][0];
            sn_[b] = en[b][0][0];
#pragma unroll
            for (int e = 1; e < 8; ++e) sp_[b] += ep[b][e >> 1][e & 1], sn_[b] += en[b][e >> 1][e & 1];
        }
    }
    asm volatile("" : "+v"(sp_[0]), "+v"(sn_[0]), "+v"(sp_[1]), "+v"(sn_[1]));
    hook();
    red4(sp_[0], sp_[1], sn_[0], sn_[1], [](float a, float b) { return a + b; }, [](float a) { return __builtin_amdgcn_rcpf(a); });
#pragma unroll
    for (int b = 0; b < 2; ++b)
#pragma unroll
        for (int e = 0; e < 4; ++e) {
            if (PK) {
                const f32x2 a = ep[b][e] * (f32x2){sp_[b], sp_[b]}, c = en[b][e] * (f32x2){sn_[b], sn_[b]};
                ph[b][0][e] = pack2(a[0], a[1]);
                ph[b][1][e] = pack2(c[0], c[1]);
            } else {
                ph[b][0][e] = pack2(opq(ep[b][e][0] * sp_[b]), ep[b][e][1] * sp_[b]);
                ph[b][1][e] = pack2(opq(en[b][e][0] * sn_[b]), en[b][e][1] * sn_[b]);
            }
        }
}
DI f32x4 rbf4(const f32x4& a) {
    const unsigned u = pack2(a[0], a[1]), v = pack2(a[2], a[3]);
    return (f32x4){lo16(u), hi16(u), lo16(v), hi16(v)};
}
DI int fsw(int row) { return (((row >> 1) & 3) << 2) | (((row >> 3) & 1) << 1) | (row & 1); }
template <bool PK, class HOOK>
DI void phi_k(const f32x4 (&pp)[2][2], const unsigned (&pw)[4], unsigned off, HOOK hook) {
    u32x4 ph[2][2];
    softmax2<PK>(pp, ph, hook);
#pragma unroll
    for (int b = 0; b < 2; ++b)
#pragma unroll
        for (int kf = 0; kf < 2; ++kf)
#pragma unroll
            for (int fb = 0; fb < 2; ++fb) {
                const u32x2 a = {ph[b][kf][2 * fb], ph[b][kf][2 * fb + 1]};
                lds_write<u32x2>(pw[2 * kf + fb] + off + b * 2048, a);
            }
}

DI void phi_k1(const f32x4 (&v)[2], const unsigned (&pw)[4]) {
    constexpr float L2E = 1.4426950408889634f;
    const f32x4 s = v[0], t = v[1];
    float mx = mx2(mx2(mx2(mx2(s[0], s[1]), s[2]), mx2(mx2(s[3], t[0]), t[1])), mx2(t[2], t[3]));
    float mn = mx2(mx2(mx2(mx2(-s[0], -s[1]), -s[2]), mx2(mx2(-s[3], -t[0]), -t[1])), mx2(-t[2], -t[3]));
    red2(mx, mn, [](float a, float b) { return mx2(a, b); }, [](float a) { return a * -L2E; });
    f32x2 ep[4], en[4];
#pragma unroll
    for (int e = 0; e < 4; ++e) {
        const f32x2 u = {v[e >> 1][2 * (e & 1)], v[e >> 1][2 * (e & 1) + 1]};
        const f32x2 a = __builtin_elementwise_fma(u, (f32x2){L2E, L2E}, (f32x2){mx, mx});
        const f32x2 c = __builtin_elementwise_fma(u, (f32x2){-L2E, -L2E}, (f32x2){mn, mn});
        ep[e] = (f32x2){__builtin_amdgcn_exp2f(a[0]), __builtin_amdgcn_exp2f(a[1])};
        en[e] = (f32x2){__builtin_amdgcn_exp2f(c[0]), __builtin_amdgcn_exp2f(c[1])};
    }
    const f32x2 a = (ep[0] + ep[1]) + (ep[2] + ep[3]), c = (en[0] + en[1]) + (en[2] + en[3]);
    float sp = a[0] + a[1], sn = c[0] + c[1];
    red2(sp, sn, [](float a, float b) { return a + b; }, [](float a) { return __builtin_amdgcn_rcpf(a); });
#pragma unroll
    for (int i = 0; i < 4; ++i) {
        const float r = i < 2 ? sp : sn;
        const f32x2 lo = (i < 2 ? ep[2 * i] : en[2 * i - 4]) * (f32x2){r, r}, hi = (i < 2 ? ep[2 * i + 1] : en[2 * i - 3]) * (f32x2){r, r};
        lds_write<u32x2>(pw[i], (u32x2){pack2(lo[0], lo[1]), pack2(hi[0], hi[1])});
    }
}

DI void waitvn(int n) {
#define WV(k) case k: waitv<k>(); break;
#define WV8(k) WV(k) WV(k + 1) WV(k + 2) WV(k + 3) WV(k + 4) WV(k + 5) WV(k + 6) WV(k + 7)
    switch (n < 63 ? n : 63) { WV8(0) WV8(8) WV8(16) WV8(24) WV8(32) WV8(40) WV8(48) WV8(56) }
}
#define SB0() __builtin_amdgcn_sched_barrier(0)
template <int A, int B, class F>
DI void sfor(F f) {
    if constexpr (A < B) {
        f(std::integral_constant<int, A>{});
        sfor<A + 1, B>(f);
    }
}
template <int N> using IC = std::integral_constant<int, N>;
#define AI __attribute__((always_inline))
#define RSK(b, k) do { switch (k) { case 0: RS(b); break; case 1: RS(b + 1); break; case 2: RS(b + 2); break; case 3: RS(b + 3); break; \
    case 4: RS(b + 4); break; case 5: RS(b + 5); break; case 6: RS(b + 6); break; case 7: RS(b + 7); break; } } while (0)

DI void o_store(const u32x4 (&kf)[6], const u32x4 (&ph)[2][2], __amdgpu_buffer_rsrc_t rsrc, unsigned o_st) {
    const f32x4 zero = {0.f, 0.f, 0.f, 0.f};
#pragma unroll
    for (int i = 0; i < 2; ++i) {
        const bf16x8 p0 = __builtin_bit_cast(bf16x8, ph[i][0]), p1 = __builtin_bit_cast(bf16x8, ph[i][1]);
        f32x4 dn = mfma16(__builtin_bit_cast(bf16x8, kf[4]), p0, zero);
        f32x4 n0 = mfma16(__builtin_bit_cast(bf16x8, kf[0]), p0, zero);
        f32x4 n1 = mfma16(__builtin_bit_cast(bf16x8, kf[1]), p0, zero);
        dn = mfma16(__builtin_bit_cast(bf16x8, kf[5]), p1, dn);
        n0 = mfma16(__builtin_bit_cast(bf16x8, kf[2]), p1, n0);
        n1 = mfma16(__builtin_bit_cast(bf16x8, kf[3]), p1, n1);
        const float rd_ = __builtin_amdgcn_rcpf(rbf(rbf(dn[0]) + 1e-6f));
        const u32x4 nb_ = __builtin_bit_cast(u32x4, pack44(n0, n1));
        u32x4 o;
#pragma unroll
        for (int j = 0; j < 4; ++j) o[j] = pack2(lo16(nb_[j]) * rd_, hi16(nb_[j]) * rd_);
        st_wt(rsrc, o_st + i * 1024, nz4(o));
    }
}

typedef __attribute__((ext_vector_type(16))) unsigned u32x16;
__global__ __launch_bounds__(512, 2) void fused_mix(const u16*, const u16*, const u16*, const u16*, const u16*, const u16*, const u16*, const u16*,
                                                    const u16*, char*, u16*) {
    __shared__ __attribute__((aligned(16))) char smem[163840];
    u32x4 ka;
    u32x16 kb_;
    u64 kz;
    asm volatile("s_load_dwordx4 %0, %3, 0x0\n\ts_load_dwordx16 %1, %3, 0x10\n\ts_load_dwordx2 %2, %3, 0x50"
                 : "=&s"(ka), "=&s"(kb_), "=&s"(kz) : "s"(__builtin_amdgcn_kernarg_segment_ptr()));
    SB0();
    const int tid = threadIdx.x;
    int l = tid & 63;
    const int w = __builtin_amdgcn_readfirstlane(tid >> 6) ^ 4;
    int bid = blockIdx.x;
    const unsigned lds0 = lds_of(smem);
    int g = l >> 4, l15 = l & 15, q = l15 >> 2, p = l & 3;
    int srow = l >> 3, sch = l & 7;
    unsigned l16 = l * 16;
    unsigned vst = 0;
    const int rg = w & 3, kp = w >> 2;
    const int hs = rg >> 1;
    RS(0);
    if (CPF) {
        u64 pc;
        asm volatile("s_getpc_b64 %0\n\ts_mov_b32 m0, %2\n\ts_mov_b64 exec, 3\n\tglobal_load_lds_dword %1, %0\n\ts_mov_b64 exec, -1"
                     : "=&s"(pc) : "v"(min((unsigned)(((bid & ~7) + w) * 2 + l) * 128u, (unsigned)CPF)), "s"(lds0 + L_SM + 3584) : "memory");
    }
    lds_write<unsigned>(lds0 + L_SM + tid * 4, 0u);
    const int slot = bid >> 3;
    const int mt = (bid & 1) * 8 + (slot & 7);
    const int hp = ((bid >> 1) & 3) * 4 + (slot >> 3);
    unsigned vx[4], vw[4];
    auto vw_init = [&]() AI {
        int t = l;
        asm volatile("" : "+v"(t));
#pragma unroll
        for (int i = 0; i < 4; ++i) vw[i] = (unsigned)((8 * i + (t >> 3)) * 6144 + ((t & 7) ^ (((t >> 4) & 1) << 1) ^ ((i & 1) << 2)) * 16 + 4096 - i * 1024);
    };
    auto vx_init = [&]() AI {
        int t = l;
        asm volatile("" : "+v"(t));
#pragma unroll
        for (int i = 0; i < 4; ++i) vx[i] = (unsigned)((8 * i + (t >> 3)) * 2048 + (((t & 7) ^ ((4 * (i & 1) + (t >> 4)) & 7)) * 16) + 4096 - i * 1024);
    };
    vx_init();
    vw_init();
    asm volatile("" : "+v"(vx[0]), "+v"(vx[1]), "+v"(vx[2]), "+v"(vx[3]), "+v"(vw[0]), "+v"(vw[1]), "+v"(vw[2]), "+v"(vw[3]));
    const int ksa = SLK * (slot & 7), ksb = KSK * ((slot + (slot >> 3)) & (NPH - 1));
    unsigned xo0 = (mt * 128 + hs * 64) * 2048, wo0 = hp * 128 + hs * (32 * 6144);
    asm volatile("s_waitcnt lgkmcnt(0)" : "+s"(ka), "+s"(kb_), "+s"(kz), "+s"(xo0), "+s"(wo0) :: "memory");
    RS(48);
    const u16* x = (const u16*)((u64)ka[1] << 32 | ka[0]);
    const u16* wqkv = (const u16*)((u64)ka[3] << 32 | ka[2]);
    auto arg = [&](int i) { return (char*)((u64)kb_[2 * i + 1] << 32 | kb_[2 * i]); };
    const u16 *wphi = (const u16*)arg(0), *wo = (const u16*)arg(1), *wr = (const u16*)arg(2), *wg = (const u16*)arg(3);
    const u16 *wu = (const u16*)arg(4), *wd = (const u16*)arg(5), *gw = (const u16*)arg(6);
    char* ws = arg(7);
    u16* out = (u16*)kz;
    const int sc = (3 - (int)((unsigned)(size_t)wqkv >> 7)) & 7;
    const unsigned* one = k_num;
    unsigned v0 = 0;
    asm volatile("" : "+s"(one), "+v"(v0));
    auto mark = [&](unsigned dst, int n = 1, int lanes = 1) AI {
        asm volatile("s_mov_b32 m0, %2\n\ts_mov_b64 exec, %4\n\tglobal_load_lds_dword %0, %1 offset:%3\n\ts_mov_b64 exec, -1"
                     ::"v"(v0), "s"(one), "s"(dst - 4 * n), "n"(4 * n), "n"(lanes) : "memory");
    };
    auto four = [&](const unsigned (&v)[4], const char* src, unsigned dst, int nt = 0) AI {
#define FOUR(P) asm volatile("s_mov_b32 m0, %5\n\ts_nop 0\n\t" \
                     "global_load_lds_dwordx4 %0, %4" P "\n\tglobal_load_lds_dwordx4 %1, %4 offset:1024" P "\n\t" \
                     "global_load_lds_dwordx4 %2, %4 offset:2048" P "\n\tglobal_load_lds_dwordx4 %3, %4 offset:3072" P \
                     ::"v"(v[0]), "v"(v[1]), "v"(v[2]), "v"(v[3]), "s"(src), "s"(dst) : "memory")
        if (nt == 1) FOUR(" nt");
        else if (nt == 2) FOUR(" sc1");
        else if (nt == 3) FOUR(" sc0 sc1");
        else FOUR("");
    };
    const int ks0 = (((slot + (slot >> 3)) & KXM) | (KXO == 2 || (KXO == 3 && (hp & 7) == sc) ? slot & 4 : 0)) ^ ((bid & 1) * KXP) ^ (((bid >> 1) & 3) * KXQ);
    const char* xb0 = (const char*)x + xo0 - 4096;
    const char* wb0 = (const char*)wqkv + wo0 - 4096;
    auto fill = [&](auto N) AI {
        constexpr int n = N, j = F_J[n] < 0 ? 0 : F_J[n], xi = F_J[n] < 0 ? -1 - F_J[n] : 0, s = F_J[n] < 0 ? x_stage(xi) : J_S[j], m = J_M[j];
        const int ks = KXO ? ks0 ^ (int)((KXB >> (4 * s)) & 15) : (ks0 + s) & 15;
        if constexpr (n >= NF1 && n < NF2) {
            if constexpr (F_J[n] >= 0) {
                asm volatile("s_mov_b32 m0, %3\n\ts_nop 0\n\tglobal_load_lds_dwordx4 %0, %2\n\tglobal_load_lds_dwordx4 %1, %2 offset:1024"
                             ::"v"(vw[0]), "v"(vw[1]), "s"(wb0 + (ks * 64 + (w & 1) * 16) * 6144), "s"(lds0 + J_U[j] * 8192 + w * 2048) : "memory");
            } else four(vx, xb0 + ks * 128 + (w & 1) * (32 * 2048), lds0 + (hs ? X_U1[xi] : X_U0[xi]) * 8192 + (w & 1) * 4096);
            if constexpr (F_K[n] > 1) mark(lds0 + L_FULL + 16 + w * 4, F_K[n]);
            return;
        }
        if constexpr (F_J[n] >= 0) {
            four(vw, wb0 + ks * (64 * 6144) + (m == 2 ? 0 : 2048 * (m + 1)), lds0 + J_U[j] * 8192 + hs * 4096, s < NTS ? NTW : 0);
        } else {
            const char* xs_ = xb0 + ks * 128;
            const unsigned d = lds0 + (hs ? X_U1[xi] : X_U0[xi]) * 8192;
            four(vx, xs_, d, s < NTS ? NTX : 0);
            four(vx, xs_ + 32 * 2048, d + 4096, s < NTS ? NTX : 0);
        }
        if constexpr (F_K[n] > 1) mark(lds0 + L_FULL + (n < NF1 ? 0 : 32) + hs * 8, F_K[n], 3);
    };
    static_assert(F_K[1] == 1, "");
    if (__builtin_expect((w & 5) == 4, 1)) sfor<0, 2>(fill);
    RS(49);
    unsigned sd = 0, st;
    asm volatile("s_cmp_lg_u32 %2, 5\n\ts_cbranch_scc1 .La%=\n\t"
                 "s_min_u32 %1, %3, 23\n\ts_lshl_b32 %1, %1, 18\n\ts_load_dword %0, %5, %1\n\ts_load_dword %0, %6, 0x0\n\t"
                 "s_and_b32 %1, %1, 0x3c0000\n\ts_load_dword %0, %4, %1\n\ts_load_dword %0, %10, %1\n\t"
                 "s_load_dword %0, %7, 0x0\n\ts_load_dword %0, %8, 0x0\n\ts_load_dword %0, %9, 0x0\n.La%=:"
                 : "+s"(sd), "=&s"(st) : "s"(w), "s"(slot), "s"(x), "s"(wqkv), "s"(one), "s"(wr), "s"(gw), "s"(wphi), "s"(out) : "memory", "scc");
    asm volatile("s_cmp_lg_u32 %2, 7\n\ts_cbranch_scc1 .La%=\n\t"
                 "s_min_u32 %1, %3, 27\n\ts_lshl_b32 %1, %1, 18\n\ts_load_dword %0, %4, 0x0\n\ts_load_dword %0, %4, %1\n\t"
                 "s_load_dword %0, %5, %1\n\ts_load_dword %0, %6, %1\n\ts_load_dword %0, %7, %1\n\t"
                 "s_and_b32 %1, %1, 0x1c0000\n\ts_load_dword %0, %8, %1\n.La%=:\n\ts_barrier"
                 : "+s"(sd), "=&s"(st) : "s"(w), "s"(slot), "s"(ws), "s"(wg), "s"(wu), "s"(wd), "s"(wo) : "memory", "scc");
    RS(50);
    if (__builtin_expect((w & 5) == 4, 1)) {
        mark(lds0 + L_FULL + hs * 8, 1, 3);
        sfor<2, NF0>(fill);
    }
    RS(51);
    asm volatile("" : "+s"(bid));
    const int xcd = bid & 7;
    const int rt = BMAP ? (xcd & 1) * 8 + ((bid >> 3) & 7) : (xcd >> 1) * 4 + ((bid >> 3) & 3);
    const int cb = BMAP ? (((xcd >> 1) + BMAP - 1) & 3) * 4 + (bid >> 6) : (xcd & 1) * 8 + (bid >> 5), rot = ROTC * cb + ROTK * rt + ROT0 * (xcd >> 1);
    const __amdgpu_buffer_rsrc_t rsrc = __builtin_amdgcn_make_buffer_rsrc(ws, 0, WS_END, 0x00020000);
    const u32x4 z4 = {0u, 0u, 0u, 0u};
    const f32x4 zero = {0.f, 0.f, 0.f, 0.f};
    unsigned AX = lds0 + L_A + rg * 4096 + l16;
    unsigned W4 = lds0 + p * 4;
    unsigned PG = lds0 + L_PROG + p * 256;
    const unsigned WR = lds0 + rg * 4;
    auto flag = [&](unsigned a) AI { asm volatile("ds_write_b32 %0, %0" ::"v"(a) : "memory"); };
    auto phi_w = [&](bf16x8 (&a)[2], int h) AI {
        const unsigned ad = lds0 + L_WPHI + h * 2048 + (4 * g + q) * 64 + (((p >> 1) ^ (2 * (g & 1))) * 16) + 8 * (p & 1);
        a[0] = cat8(tr_read(ad), tr_read(ad + 1024));
        a[1] = cat8(tr_read(ad ^ 32), tr_read((ad ^ 32) + 1024));
    };
    u32x4 fw[32], fd[16], gu[2];
    unsigned sel_[4][2];
    u32x4 ph[2][2], sq;
    const int hp2 = ((bid >> 1) & 3) * 4 + (bid >> 6);
    const bool ffn = (hp2 & 7) != sc && !(NOF & 1);
    const int ph4 = ((bid >> 3) + (bid >> 6)) & 3;
    const int turn = (ph4 + TRN) & TRM;
    const int kq = mt >> 2;
    const int cs = (ffn ? hp2 - (hp2 > sc) - (hp2 > sc + 8) : 14 + (hp2 >> 3)) * 4 + ((ph4 + CPH - (int)((unsigned)(size_t)wg >> 7)) & 3);
    auto spread = [&](const u32x4& v, int i) -> bf16x8 {
        const unsigned s = v[i >> 1];
        u32x4 a;
#pragma unroll
        for (int d = 0; d < 4; ++d) a[d] = __builtin_amdgcn_perm(s, s, sel_[d][i & 1]);
        return __builtin_bit_cast(bf16x8, a);
    };
    auto fold_store = [&](const f32x4& a, unsigned off) {
        f32x4 v;
#pragma unroll
        for (int i = 0; i < 4; ++i) {
            const unsigned u = __float_as_uint(a[i]);
            const float r = __uint_as_float(__builtin_amdgcn_update_dpp(u, u, 0x128, 0xc, 0xf, false));
            v[i] = swap32_add(r, r);
        }
        const u32x4 o = nz4(v);
        ST_MASK("global_store_dwordx4", 0x00FF00FFull, off + l15 * 32 + g * 16, o, ws);
    };
    auto sel_init = [&]() AI {
#pragma unroll
        for (int d = 0; d < 4; ++d)
#pragma unroll
            for (int hf = 0; hf < 2; ++hf) {
                const unsigned b = hf ? 0x0302u : 0x0100u;
                sel_[d][hf] = (sch >> 1) != d ? 0x0c0c0c0cu : (l & 1) ? (b << 16) | 0x0c0cu : 0x0c0c0000u | b;
            }
    };
    auto gua = [&](int i) { return ws + WS_GU + cs * 4096 + i * 1024; };
    auto gu_load = [&]() AI {
#pragma unroll
        for (int i = 0; i < 2; ++i) ld_async(gu[i], l16, gua(i));
    };
    auto down = [&]() AI {
#pragma unroll
        for (int i = 0; i < 16; ++i) pin(fd[i]);
        settle(gu, [&](int) { return l16; }, gua);
        const f32x4 v = __builtin_bit_cast(f32x4, gu[0]) + __builtin_bit_cast(f32x4, gu[1]);
        auto r = __builtin_amdgcn_permlane16_swap(__float_as_uint(swap32_add(v[0], v[2])), __float_as_uint(swap32_add(v[1], v[3])), false, false);
        const float gt = rbf(__uint_as_float(r[0])), up = rbf(__uint_as_float(r[1]));
        const unsigned ha = lds0 + L_XS + hs * 512;
        lds_write<u16>(ha + l15 * 8 + g * 2, f2bf(rbf(gt / (1.f + __expf(-gt))) * up));
        const u32x4 hv = lds_read<u32x4>(ha + srow * 16);
        f32x4 y0 = zero, y1 = zero;
#pragma unroll
        for (int i = 0; i < 8; ++i) {
            const bf16x8 a = spread(hv, i);
            y0 = mfma16(a, __builtin_bit_cast(bf16x8, fd[i]), y0);
            y1 = mfma16(a, __builtin_bit_cast(bf16x8, fd[8 + i]), y1);
        }
        fold_store(y0, WS_Y + cs * 4096 + kq * 1024 + hs * 512);
        fold_store(y1, WS_Y + cs * 4096 + kq * 1024 + hs * 512 + 256);
    };
    auto need = [&](int c) AI {
        if (c == 101) lds_wait(W4 + L_FKS, 1);
        else if (c == 100) lds_wait(W4 + L_FV, 1);
        else if (c == 99) lds_wait(W4 + L_FQD, 1);
        else if (c > 0) lds_wait(c > RLD ? W4 + L_PRL : PG, c);
    };
    auto wo_load = [&](int n0, int n1, bool mk) AI {
        unsigned vo[4];
#pragma unroll
        for (int i = 0; i < 4; ++i) vo[i] = (unsigned)((8 * i + srow) * 2048 + ((sch ^ ((g & 3) << 1)) * 16) + 4096 - i * 1024);
        const char* wob = (const char*)wo + cb * 128 - 4096;
#pragma unroll
        for (int n = n0; n < ((NOF & 4) ? n0 : n1); ++n) {
            if (n < WON && (n == 0 || W_N[n] != W_N[n - 1])) need(W_N[n]);
            four(vo, wob + ((2 * n + hs + rot) & 31) * 65536, lds0 + wo_unit(n) * 8192 + hs * 4096);
        }
        if (mk) mark(lds0 + (n0 >= WON && WON ? L_FW2 : L_FWD) + hs * 4);
    };
    auto phi_adr = [&](unsigned (&pw)[4], unsigned base) AI {
#pragma unroll
        for (int i = 0; i < 4; ++i) pw[i] = (base + rg * 4096 + l15 * 128 + ((g ^ fsw(l15)) << 3)) ^ (i << 5);
    };
    const int row0 = rt * 128 + rg * 32 + kp * 16;
    u32x4 xr[2];
    u64 gg[2];
    unsigned xo = srow * 2048 + sch * 16;
    auto g_load = [&]() AI {
        asm volatile("global_load_dwordx2 %0, %2, %3 sc1\n\tglobal_load_dwordx2 %1, %2, %3 offset:64 sc1"
                     : "=&v"(gg[0]), "=&v"(gg[1]) : "v"((unsigned)srow * 8), "s"(ws + WS_GG + row0 * 8) : "memory");
    };
    auto xg_load = [&](int skip) AI {
        asm volatile("s_cmp_eq_u32 %4, 4\n\ts_cbranch_scc1 .Lk%=\n\t"
                     "global_load_dwordx4 %0, %5, %6\n\tglobal_load_dwordx4 %1, %5, %7\n\t"
                     "global_load_dwordx2 %2, %8, %9 sc1\n\tglobal_load_dwordx2 %3, %8, %9 offset:64 sc1\n.Lk%=:"
                     : "+v"(xr[0]), "+v"(xr[1]), "+v"(gg[0]), "+v"(gg[1])
                     : "s"(skip), "v"(xo), "s"(x + (size_t)row0 * DM + cb * 64), "s"(x + (size_t)(row0 + 8) * DM + cb * 64),
                       "v"((unsigned)srow * 8), "s"(ws + WS_GG + row0 * 8) : "memory", "scc");
    };
    auto xg_def = [&]() AI { asm volatile("" : "=v"(xr[0]), "=v"(xr[1]), "=v"(gg[0]), "=v"(gg[1])); };
    auto relane = [&]() AI {
        unsigned z = 0;
        asm volatile("" : "+v"(z));
        l = __builtin_amdgcn_mbcnt_hi(~0u, __builtin_amdgcn_mbcnt_lo(~0u, z));
        g = l >> 4, l15 = l & 15, q = l15 >> 2, p = l & 3, srow = l >> 3, sch = l & 7, l16 = l * 16;
        AX = lds0 + L_A + rg * 4096 + l16, W4 = lds0 + p * 4, PG = lds0 + L_PROG + p * 256, xo = srow * 2048 + sch * 16;
    };
    u32x4 kf[6];
    auto kf_def = [&]() AI { asm volatile("" : "=v"(kf[0]), "=v"(kf[1]), "=v"(kf[2]), "=v"(kf[3]), "=v"(kf[4]), "=v"(kf[5])); };
    const int oh = hs ^ kp ^ 1;

    if (__builtin_expect(w >= 4, 1)) {
        const bool loader = !(w & 1);
        const unsigned kf_kv = WS_KF + (hp * 2 + (mt >> 3)) * 8192 + (mt & 7) * 512;
        const unsigned kf_z = WS_KF + (hp * 2 + (mt >> 3)) * 8192 + 4096 + (ZL ? (mt & 7) * 128 : ((mt >> 2) & 1) * 64);
        const unsigned fgo = ffn ? (unsigned)((256 * kq + 16 * srow) * 7168 + cs * 128 + sch * 16) : 0u;
        const char* fsrc = (const char*)(hs ? wu : wg);
        auto gu_w = [&](int q0, int q1) AI {
#pragma unroll
            for (int i = q0; i < q1; ++i) {
                unsigned t1, t2, t3;
                if (NOF & 8) fw[4 * i] = fw[4 * i + 1] = fw[4 * i + 2] = fw[4 * i + 3] = z4; else
                asm volatile("v_add_u32 %4, 0x1c00, %7\n\tv_add_u32 %5, 0x3800, %7\n\tv_add_u32 %6, 0x5400, %7\n\t"
                             "global_load_dwordx4 %0, %7, %8 nt\n\tglobal_load_dwordx4 %1, %4, %8 nt\n\t"
                             "global_load_dwordx4 %2, %5, %8 nt\n\tglobal_load_dwordx4 %3, %6, %8 nt"
                             : "=&v"(fw[4 * i]), "=&v"(fw[4 * i + 1]), "=&v"(fw[4 * i + 2]), "=&v"(fw[4 * i + 3]), "=&v"(t1), "=&v"(t2), "=&v"(t3)
                             : "v"(fgo), "s"(fsrc + (128 * (i >> 2) + 4 * (i & 3)) * 7168) : "memory");
            }
        };
        const unsigned fdo = ffn ? (unsigned)((64 * cs + 8 * srow) * 2048 + 512 * kq + 256 * hs + sch * 16) : 0u;
        auto dn_w = [&](int q0, int q1) AI {
#pragma unroll
            for (int i = q0; i < q1; ++i)
                if (NOF & 8) fd[4 * i] = fd[4 * i + 1] = fd[4 * i + 2] = fd[4 * i + 3] = z4; else
                asm volatile("global_load_dwordx4 %0, %4, %5 offset:-4096 nt\n\tglobal_load_dwordx4 %1, %4, %5 offset:-2048 nt\n\t"
                             "global_load_dwordx4 %2, %4, %5 nt\n\tglobal_load_dwordx4 %3, %4, %5 offset:2048 nt"
                             : "=&v"(fd[4 * i]), "=&v"(fd[4 * i + 1]), "=&v"(fd[4 * i + 2]), "=&v"(fd[4 * i + 3])
                             : "v"(fdo), "s"((const char*)wd + 4096 + (i & 1) * 8192 + (i >> 1) * 128) : "memory");
        };
        auto gate_up = [&]() AI {
            sel_init();
#pragma unroll
            for (int i = 0; i < 32; ++i) pin(fw[i]);
            u32x4 xs[4];
#pragma unroll
            for (int i = 0; i < 4; ++i) xs[i] = lds_read<u32x4>(lds0 + L_XS + hs * 512 + (i >> 1) * 256 + srow * 32 + (i & 1) * 16);
            f32x4 a = zero;
#pragma unroll
            for (int i = 0; i < 32; ++i) a = mfma16(spread(xs[i >> 3], i & 7), __builtin_bit_cast(bf16x8, fw[i]), a);
            fold_store(a, WS_GU + cs * 4096 + kq * 512 + hs * 256);
            SB0();
        };
        auto o_clear = [&]() AI {
#pragma unroll
            for (int n = 0; n < ((NOF & 2) ? 1 : 8); ++n) st_wt(rsrc, WS_O + ((mt * 2 + (n >> 2)) * 32 + 2 * hp + hs) * 4096 + (n & 3) * 1024 + l16, z4);
        };
        u64 T;
        u32x4 sv[2];
        auto clears = [&]() AI {
            if (!OCL) o_clear();
#pragma unroll
            for (int n = 0; n < ((NOF & 16) ? 0 : 4); ++n) st_wt(rsrc, WS_KV + ((hp * 8 + hs * 4 + n) * 16 + mt) * 1024 + l16, z4);
            ST_MASK("global_store_dwordx4", 0xFFFFull, WS_Z + ((hp * 4 + hs * 2 + (srow & 1)) * 16 + mt) * 128 + sch * 16, z4, ws);
        };
        auto cleared = [&]() AI {
            if (CLL) ST_MASK("global_store_dwordx2", 0xFFFFull, (unsigned)l * 8, T, ws + WS_C2 + ((mt * 2 + hs) * 16 + hp) * 128);
            else ST_MASK("global_store_dwordx2", 1ull, 0u, T, ws + WS_CL + ((mt * 2 + hs) * 16 + hp) * 8);
        };
        auto rg_load = [&]() AI {
            asm volatile("global_load_dwordx4 %0, %2, %3 sc1\n\tglobal_load_dwordx4 %1, %2, %3 offset:16 sc1"
                         : "=&v"(sv[0]), "=&v"(sv[1]) : "v"(l * 32), "s"(ws + WS_RG) : "memory");
        };
        auto select = [&]() AI {
            const unsigned tl = (unsigned)T & 0x1FFFu, thi = (unsigned)(T >> 13);
#pragma unroll 1
            for (unsigned s = 0; s < SPIN_CAP; ++s) {
                pin(sv[0]);
                pin(sv[1]);
                const unsigned bad = ((sv[0][0] >> 19) ^ tl) | (sv[0][1] ^ thi) | ((sv[0][2] >> 19) ^ tl) | (sv[0][3] ^ thi) |
                                     ((sv[1][0] >> 19) ^ tl) | (sv[1][1] ^ thi) | ((sv[1][2] >> 19) ^ tl) | (sv[1][3] ^ thi);
                if (__all(bad == 0u)) break;
                __builtin_amdgcn_s_sleep(16);
                rg_load();
                waitv<0>();
            }
            pin(sv[0]);
            pin(sv[1]);
            RS(4);
            unsigned k2 = 0;
#pragma unroll
            for (int j = 0; j < 4; ++j) {
                const unsigned v = sv[j >> 1][(j & 1) * 2] & 0x7FFFFu;
                k2 = max(k2, ((v >> 3) << 11) | ((255 - (4 * l + j)) << 3) | (v & 7));
            }
            const unsigned key = __builtin_amdgcn_readfirstlane(wave_umax(k2));
            asm volatile("s_mov_b32 m0, %2\n\ts_mov_b64 exec, %3\n\tglobal_load_lds_dwordx4 %0, %1\n\ts_mov_b64 exec, -1"
                         ::"v"((unsigned)(512 * kq) + l16), "s"(x + (size_t)(2047 - (int)(key & 2047u)) * DM), "s"(lds0 + L_XS + hs * 512), "s"(0xFFFFFFFFull) : "memory");
            unsigned rb = key >> 11;
            rb ^= (rb & 0x8000u) ? 0x8000u : 0xFFFFu;
            const float rsel = rbf(1.f / (1.f + __expf(-bf2f((u16)rb))));
            lds_write<u32x4>(lds0 + L_SEL, (u32x4){(unsigned)T, (unsigned)(T >> 32), 2047u - (key & 2047u), __float_as_uint(rsel)});
            if (bid == 0 && hs) ST_MASK("global_store_dwordx2", 1ull, 0u, T, ws + WS_E);
            RS(5);
        };
        auto k_soft = [&](int np) AI {
            bf16x8 a0[2], kb[2];
            f32x4 pk[2][2][2];
            unsigned pw[4];
            phi_w(a0, 0);
            phi_adr(pw, lds0 + P_U[0] * 8192);
            asm volatile("" : "+v"(pw[0]), "+v"(pw[1]), "+v"(pw[2]), "+v"(pw[3]));
            auto logits = [&](int h) AI {
                lds_wait(WR + L_FK0 + 4 * h, 1);
#pragma unroll
                for (int i = 0; i < 2; ++i) kb[i] = lds_read128(AX + (P_U[0] * 8192 - L_A) + h * 4096 + i * 1024);
#pragma unroll
                for (int i = 0; i < 4; ++i) pk[h][i >> 1][i & 1] = mfma16(a0[i & 1], kb[i >> 1], zero);
            };
            auto round = [&](int h) AI {
#pragma unroll
                for (int i = 0; i < 4; ++i) pk[h][i >> 1][i & 1] = rbf4(pk[h][i >> 1][i & 1]);
            };
            logits(0);
            RS(12);
            round(0);
            SB0();
            phi_k<KPK & 1>(pk[0], pw, 0, [&]() AI {
                if (np > 1) logits(1);
                SB0();
            });
            SB0();
            if (np > 1) {
                round(1);
                phi_k<(KPK >> 1)>(pk[1], pw, 4096, []() {});
                SB0();
            }
            flag(WR + L_FP);
        };
        if (__builtin_expect(loader, 1)) {
            vx_init();
            vw_init();
            if (LPRIO) __builtin_amdgcn_s_setprio(LPRIO);
            unsigned pv = 0;
            sfor<NF0, NF1>([&](auto N) {
                constexpr int n = N;
                if constexpr (n > 0 && F_N[n] > F_N[n > 0 ? n - 1 : 0]) {
                    RSL(32 + F_N[n]);
                    if (LPRW != LPRIO) __builtin_amdgcn_s_setprio(LPRW);
                    if (NOF & 256) {
                    } else if (F_N[n] > RLD) {
                        lds_wait(W4 + L_PRL, F_N[n]);
                    } else if (PEEK) {
                        lds_wait(pv, PG, F_N[n]);
                        if (F_N[n] < F_N[NF1 - 1] && next_need(n) <= RLD) lds_peek(pv, PG);
                    } else lds_wait(PG, F_N[n]);
                    if (LPRW != LPRIO) __builtin_amdgcn_s_setprio(LPRIO);
                    RSL(48 + F_N[n]);
                }
                if constexpr (n == 12) {
                    asm volatile("s_mov_b32 m0, %2\n\ts_nop 0\n\tglobal_load_lds_dwordx4 %0, %1\n\tglobal_load_lds_dwordx4 %0, %1 offset:1024"
                                 ::"v"((unsigned)((l >> 2) * 64 + (((l & 3) ^ (2 * ((l >> 4) & 1))) * 16))),
                                   "s"(wphi + (hp * 2 + hs) * 1024), "s"(lds0 + L_WPHI + hs * 2048) : "memory");
                }
                if constexpr (PFN > 0 && F_J[n] < 0 && ((PFS >> (-1 - F_J[n])) & 1)) {
                    constexpr int e = __builtin_popcount(PFS & ((1 << (-1 - F_J[n])) - 1));
                    if constexpr (PFN == 32) {
                        asm volatile("s_mov_b32 m0, %2\n\ts_mov_b64 exec, %3\n\tglobal_load_lds_dword %0, %1" PFM "\n\ts_mov_b64 exec, -1"
                                     ::"v"(ffn ? (unsigned)((64 * cs + 16 * e + 8 * hs + (l >> 2)) * 2048 + 512 * kq + p * 128) : 0u), "s"(wd), "s"(lds0 + L_SM + 3584), "s"(0xFFFFFFFFull) : "memory");
                    } else {
                        asm volatile("s_mov_b32 m0, %2\n\ts_nop 0\n\tglobal_load_lds_dword %0, %1" PFM
                                     ::"v"(ffn ? (unsigned)((256 * kq + 64 * e + l) * 7168 + cs * 128) : 0u), "s"(fsrc), "s"(lds0 + L_SM + 3584) : "memory");
                    }
                }
                fill(N);
            });
            flag(lds0 + L_FLD + hs * 4);
            RS(1);
            if (HPRIO) __builtin_amdgcn_s_setprio(HPRIO);
            const char* cla = ws + (CLL ? WS_C2 : WS_CL) + ((l15 * 2 + (mt >> 3)) * 16 + hp) * (CLL ? 128 : 8);
            u64 cl;
            if (KCL) cl = 0;
            else if (CLE) asm volatile("global_load_dwordx2 %0, %1, off sc1" : "=&v"(cl) : "v"(cla) : "memory");
            k_soft(SSM ? 1 : 2);
            if (!CLE) asm volatile("global_load_dwordx2 %0, %1, off sc1" : "=&v"(cl) : "v"(cla) : "memory");
            RS(11);
            lds_wait(W4 + L_FP, 1);
            sq = lds_read<u32x4>(lds0 + L_SEL);
            waitv<0>();
            asm volatile("" : "+v"(cl));
            RS(13);
            u32x4 kp_[5 + ZL];
            {
                const char* kb = ws + WS_KV + (hp * 8 + (mt >> 1)) * 16384 + (mt & 1) * 512 + hs * 256;
                const bool zw = !((mt & 3) | (ZL & hs));
                const char* zb = zw ? ws + WS_Z + (hp * 4 + (mt >> 2)) * 2048 + (ZL ? 0 : hs * 64) : kb;
                const unsigned kvo = g * 1024 + l15 * 16, zo = zw ? (ZL ? l16 : l15 * 128 + g * 16) : 0u;
                auto ko = [&](int i) { return i < 4 ? kvo : zo; };
                auto ka = [&](int i) { return i < 4 ? kb + i * 4096 : zb + (i - 4) * 1024; };
                const u64 T = (u64)sq[1] << 32 | sq[0];
#pragma unroll 1
                for (unsigned s = 0; !KCL && s < SPIN_CAP && __ballot(cl != T); ++s)
                    asm volatile("global_load_dwordx2 %0, %1, off sc1\n\ts_waitcnt vmcnt(0)" : "=&v"(cl) : "v"(cla) : "memory");
                lds_wait0(W4 + L_FKS);
                if (GDL) __builtin_amdgcn_s_sleep(GDL);
#pragma unroll
                for (int i = 0; i < 5 + ZL; ++i) { if (PDI & 1) kp_[i] = z4; else ld_async(kp_[i], ko(i), ka(i)); }
                if (XGE) {
                    xg_def();
                    xg_load(w + 1);
                }
                flag(lds0 + L_FGI + hs * 4);
                SB0();
                RS(14);
                if (!(PDI & 1)) waitv<0>();
                RS(15);
                if (PP1) settle_p<PP1 + !PP1, PD1>(kp_, ko, ka);
                else settle(kp_, ko, ka);
                RS(24);
                float v[8], u[4];
#pragma unroll
                for (int e = 0; e < 4; ++e) {
                    v[2 * e] = (lo16(kp_[0][e]) + lo16(kp_[1][e])) + (lo16(kp_[2][e]) + lo16(kp_[3][e]));
                    v[2 * e + 1] = (hi16(kp_[0][e]) + hi16(kp_[1][e])) + (hi16(kp_[2][e]) + hi16(kp_[3][e]));
                }
#pragma unroll
                for (int e = 0; e < 4; ++e) u[e] = swap32_add(v[e], v[e + 4]);
                const unsigned o = nz1(pack2(swap16_add(u[0], u[2]), swap16_add(u[1], u[3])));
                ST_MASK("global_store_dword", ~0ull, kf_kv + hs * 256 + l15 * 16 + g * 4, o, ws);
                if (zw && ZL) {
                    f32x4 z = __builtin_bit_cast(f32x4, kp_[4]) + __builtin_bit_cast(f32x4, kp_[4 + ZL]);
#pragma unroll
                    for (int i = 0; i < 4; ++i) {
                        float t = dpp_add<0x128>(z[i]);
                        t = swap16_add(t, t);
                        z[i] = swap32_add(t, t);
                    }
                    const u32x2 oz = {nz1(pack2(z[0], z[1])), nz1(pack2(z[2], z[3]))};
                    ST_MASK("global_store_dwordx2", 0xFFFFull, kf_z + (l & 8) * 8 + p * 16 + (sch >> 2) * 8, oz, ws);
                } else if (zw) {
                    f32x4 z;
#pragma unroll
                    for (int i = 0; i < 4; ++i) z[i] = dpp_add<0x140>(dpp_add<0x141>(dpp_add<0x4E>(dpp_add<0xB1>(__uint_as_float(kp_[4][i])))));
                    const u32x2 oz = {nz1(pack2(z[0], z[1])), nz1(pack2(z[2], z[3]))};
                    ST_MASK("global_store_dwordx2", 0x0001000100010001ull, kf_z + g * 16 + hs * 8, oz, ws);
                }
                flag(lds0 + L_FG + hs * 4);
            }
            RS(25);
            const char* kfb = ws + WS_KF + (hp * 2 + hs) * 8192;
            auto fo = [&](int i) { return i < 4 ? l16 : (unsigned)g * 16; };
            auto fb = [&](int i) { return i < 4 ? kfb + i * 1024 : kfb + 4096 + (i - 4) * (ZL ? 512 : 64); };
            if (KFD) __builtin_amdgcn_s_sleep(KFD);
#pragma unroll
            for (int i = 0; i < 6; ++i) { if (PDI & 2) kf[i] = z4; else ld_async(kf[i], fo(i), fb(i)); }
            if (!XGE) {
                xg_def();
                xg_load(w + 1);
            }
            if (!(PDI & 2)) waitv<0>();
            RS(29);
            if (PP2) settle_p<PP2 + !PP2, PD2>(kf, fo, fb);
            else settle(kf, fo, fb);
#pragma unroll
            for (int i = 0; i < 5 + ZL; ++i) pin(kp_[i]);
            asm volatile("" : "+v"(xr[0]), "+v"(xr[1]), "+v"(gg[0]), "+v"(gg[1]));
            need(KFN);
#pragma unroll
            for (int i = 0; i < 4; ++i) lds_write<u32x4>(lds0 + L_KF + hs * 4096 + i * 1024 + l16, kf[i]);
#pragma unroll
            for (int i = 0; i < 2; ++i) lds_write<u32x4>(lds0 + L_KZ + hs * 128 + i * 64 + g * 16, kf[4 + i]);
            SB0();
            flag(lds0 + L_FKF + hs * 4);
        } else {
            const int row0 = (mt * 16 + hp) * 8 + 4 * hs;
            if (SPRIO) __builtin_amdgcn_s_setprio(SPRIO);
            asm volatile("s_waitcnt lgkmcnt(0)" : "+s"(sd));
            if (TLD) __builtin_amdgcn_s_sleep(TLD);
            u32x4 rv[2], gv[2], xv[4][2];
            u64 ev;
            if (RT0) lds_wait8(PG, RT0 + RTS * ((ph4 + CLP) & 3));
            asm volatile("global_load_dwordx2 %4, %5, %9 sc1\n\t"
                         "global_store_dwordx4 %10, %11, %9 sc1\n\t"
                         "global_store_dwordx2 %12, %13, %9 sc1\n\t"
                         "global_load_dwordx4 %0, %6, %7\n\tglobal_load_dwordx4 %1, %6, %7 offset:1024\n\t"
                         "global_load_dwordx4 %2, %6, %8\n\tglobal_load_dwordx4 %3, %6, %8 offset:1024"
                         : "=&v"(rv[0]), "=&v"(rv[1]), "=&v"(gv[0]), "=&v"(gv[1]), "=&v"(ev)
                         : "v"(0u), "v"(l16), "s"(wr), "s"(gw), "s"(ws),
                           "v"(l < 32 ? WS_Y + cs * 4096 + kq * 1024 + hs * 512 + l16 : WS_GU + cs * 4096 + kq * 512 + hs * 256 + l15 * 16), "v"(z4),
                           "v"(hs ? kf_z + (ZL ? l15 : sch) * 8 : kf_kv + l * 8), "v"(0ull)
                         : "memory");
            RS(1);
#pragma unroll
            for (int r = 0; r < 2; ++r)
                if (NOF & 64) xv[2 * r][0] = xv[2 * r][1] = xv[2 * r + 1][0] = xv[2 * r + 1][1] = z4; else
                asm volatile("global_load_dwordx4 %0, %4, %5\n\tglobal_load_dwordx4 %1, %4, %5 offset:1024\n\t"
                             "global_load_dwordx4 %2, %4, %5 offset:2048\n\tglobal_load_dwordx4 %3, %4, %5 offset:3072"
                             : "=&v"(xv[2 * r][0]), "=&v"(xv[2 * r][1]), "=&v"(xv[2 * r + 1][0]), "=&v"(xv[2 * r + 1][1])
                             : "v"(l16), "s"(x + (size_t)(row0 + 2 * r) * DM) : "memory");
            SB0();
            if ((MRG & 8) || KCL) clears();
            waitv<(MRG & 8) ? 13 : 0>();
            asm volatile("" : "+v"(rv[0]), "+v"(rv[1]), "+v"(gv[0]), "+v"(gv[1]), "+v"(ev));
#pragma unroll
            for (int r = 0; r < 4; ++r) asm volatile("" : "+v"(xv[r][0]), "+v"(xv[r][1]));
            RS(2);
            T = ((u64)__builtin_amdgcn_readfirstlane((unsigned)(ev >> 32)) << 32 | __builtin_amdgcn_readfirstlane((unsigned)ev)) + 1;
            float ra_[4] = {0.f, 0.f, 0.f, 0.f}, ga_[4] = {0.f, 0.f, 0.f, 0.f};
#pragma unroll
            for (int r = 0; r < 4; ++r)
#pragma unroll
                for (int i = 0; i < 2; ++i)
#pragma unroll
                    for (int j = 0; j < 4; ++j) {
                        ra_[r] = dot2(xv[r][i][j], rv[i][j], ra_[r]);
                        ga_[r] = dot2(xv[r][i][j], gv[i][j], ga_[r]);
                    }
            const int rr = (g >> 1) | ((g & 1) << 1);
            unsigned k = f2bf(sum4(ra_));
            k ^= (k & 0x8000u) ? 0xFFFFu : 0x8000u;
            k = (k << 3) | (7 - 4 * hs - rr);
            auto r1 = __builtin_amdgcn_permlane16_swap(k, k, false, false);
            k = max(r1[0], r1[1]);
            auto r2 = __builtin_amdgcn_permlane32_swap(k, k, false, false);
            k = max(r2[0], r2[1]);
            u64 gq = (T << 16) | f2bf(1.f / (1.f + __expf(-rbf(sum4(ga_)))));
            if (MRG & 8) waitv<0>();
            if (hs) lds_write<unsigned>(lds0 + L_BEST, k | 0x80000000u);
            asm volatile("" : "+v"(gq), "+v"(k));
            if (MRG & 1) {
                __builtin_amdgcn_s_setprio(SPR2);
                lds_wait8(PG, CLR0 + CLS * ((ph4 + CLP) & 3));
            }
            if (!hs) {
                lds_wait(lds0 + L_BEST, 1);
                const u64 rq = (T << 19) | max(k, lds_read<unsigned>(lds0 + L_BEST) & 0x7FFFFu);
                ST_MASK("global_store_dwordx2", 1ull, 0u, rq, ws + WS_RG + (mt * 16 + hp) * 8);
            }
            ST_MASK("global_store_dwordx2", 0x0001000100010001ull, (unsigned)(rr * 8), gq, ws + WS_GG + row0 * 8);
            RS(3);
            __builtin_amdgcn_s_setprio(SPR2);
            auto q_fill = [&]() AI {
                vx_init();
                vw_init();
                sfor<NF2, NF>([&](auto N) {
                    constexpr int n = N;
                    if constexpr (n == NF2 || F_N[n] != F_N[n - 1]) need(F_N[n]);
                    fill(N);
                });
            };
            auto wt = [&](unsigned f) AI { lds_wait(lds0 + f + (l & 1) * 4, 1); };
            unsigned pf[2];
            auto wo_warm = [&]() AI {
                asm volatile("global_load_dword %0, %2, %3\n\tglobal_load_dword %1, %2, %4" : "=&v"(pf[0]), "=&v"(pf[1])
                             : "v"((unsigned)((BMAP ? (rt & 7) * 128 + hs * 64 + (l & 31) : (rt & 3) * 256 + hs * 128 + l) * 2048)), "s"((const char*)wo + cb * 128), "s"((const char*)wo + cb * 128 + (BMAP ? 32 : 64) * 2048) : "memory");
            };
            if (DNP == 0) {
                lds_wait8(PG, FBA + FBS * turn);
                dn_w(0, 4);
            }
            if (!(MRG & 8) && !KCL) {
                if (!(MRG & 1)) lds_wait8(PG, CLR0 + CLS * ((ph4 + CLP) & 3));
                clears();
                waitv<0>();
            }
            if ((!(MRG & 6) || (MRG & 40)) && !KCL) cleared();
            RS(25);
            if (!(MRG & 4)) {
                lds_wait8(PG, SEL0);
                rg_load();
                if ((MRG & 10) == 2) cleared();
                waitv<(MRG & 10) == 2>();
                if (MRG & 2) lds_wait8(lds0 + L_FLD + (l & 1) * 4, 1);
                select();
                if (!SSM) flag(WR + L_FP);
            }
            if (FBL == 2) {
                lds_wait8(PG, F_N[NF1 - 1] - 1);
                __builtin_amdgcn_s_setprio(3);
                lds_wait(PG, F_N[NF1 - 1]);
                __builtin_amdgcn_s_sleep(FBD);
                __builtin_amdgcn_s_setprio(SPR2);
            } else if (FBL == 3) {
                lds_wait8(PG, F_N[NF1 - 1] - 1);
                __builtin_amdgcn_s_setprio(3);
                lds_wait(lds0 + L_FLD + (l & 1) * 4, 1);
                __builtin_amdgcn_s_setprio(SPR2);
            } else if (!(MRG & 2)) lds_wait8(lds0 + L_FLD + (l & 1) * 4, 1);
            if (MRG & 4) {
                rg_load();
                if (!(MRG & 40) && !KCL) cleared();
            }
            RS(6);
            if (WPF == 4) wo_warm();
            gu_w(0, GQ1);
            if ((MRG & 20) == 4) {
                waitv<4 * GQ1>();
                select();
                if (!SSM) flag(WR + L_FP);
            }
            q_fill();
            if ((MRG & 20) == 20) {
                waitv<4 * GQ1 + 4 * (NF - NF2)>();
                select();
                if (!SSM) flag(WR + L_FP);
            }
            RS(7);
            if (WEP == 0) wo_load(0, WE, false);
            gu_w(GQ1, GQ2);
            if (SSM) {
                __builtin_amdgcn_s_setprio(HPRIO);
                k_soft(1);
                __builtin_amdgcn_s_setprio(SPR2);
                RS(11);
            }
            gu_w(GQ2, 8);
            if (WPF == 1) wo_warm();
            if (WEP == 1) wo_load(0, WE, false);
            if (DNP == 1) dn_w(0, 4);
            if (WEP == 2) wo_load(0, WE, false);
            RS(8);
            waitv<(DNP == 1 ? 16 : 0) + (WEP ? 4 * WE : 0)>();
            if ((WPF == 1 && DNP != 1) || WPF == 4) asm volatile("" : "+v"(pf[0]), "+v"(pf[1]));
            RS(9);
            if (GUO == 0) gate_up();
            RS(10);
            if (WOT == 1) lds_wait(W4 + L_FKS, 1);
            if (WOT == 2) wt(L_FGI);
            if (DNP == 2) dn_w(0, 4);
            wo_load(WE, WOE, WOE == WON);
            RS(14);
            if (GUO == 1) {
                __builtin_amdgcn_s_setprio(GPR);
                gate_up();
                __builtin_amdgcn_s_setprio(SPR2);
            }
            RS(13);
            if (YT == 1) wt(L_FG);
            gu_load();
            if (DNP == 3) dn_w(0, 4);
            if (WOE < WON) wo_load(WOE, WON, true);
            waitv<(WOE < WON ? 4 * (WON - WOE) + 1 : 0)>();
            if (WPF == 1 && DNP == 1) asm volatile("" : "+v"(pf[0]), "+v"(pf[1]));
            RS(15);
            down();
            RS(24);
            sq = lds_read<u32x4>(lds0 + L_SEL);
            xg_def();
            kf_def();
        }
        RS(26);
        lds_wait0(WR + L_FQ);
#pragma unroll
        for (int i = 0; i < 4; ++i) ph[i >> 1][i & 1] = lds_read<u32x4>(AX + i * 1024);
    } else {
        if (CPRIO) __builtin_amdgcn_s_setprio(CPRIO);
        unsigned ra[4], xt[2];
#pragma unroll
        for (int u = 0; u < 4; ++u) ra[u] = lds0 + J_B[0] * 8192 + (8 * g + q) * 128 + ((u ^ (q >> 1) ^ ((g & 1) << 1)) * 32) + 8 * p;
        unsigned xa = lds0 + (32 * (w & 1) + l15) * 128 + ((g ^ (l15 >> 1)) * 16), fa = lds0 + L_FULL + (l & 3) * 4;
        xt[0] = xa + (hs ? X_U1[0] : X_U0[0]) * 8192;
        xt[1] = xt[0] ^ 64;
        asm volatile("s_mov_b32 m0, %4" : "+v"(xa), "+v"(fa), "+v"(xt[0]), "+v"(xt[1]) : "s"(lds0 + L_PROG + w * 256 - 124));
        f32x4 acc[3][4][2];
        bf16x8 xf[ND][2][2];
        s16x4 wh[PD + 1][2];
        unsigned fv = 0;
        bf16x8 kown[2];
        u64 bad = ~0ull;
        auto chk = [&](int c) AI {
            SB0();
            if (__builtin_expect(bad != 0ull, 0)) {
                unsigned c_;
                asm volatile("s_mov_b32 %1, 0x400000\n"
                             ".Lp%=:\n\tds_read_b32 %0, %2 offset:%3\n\ts_waitcnt lgkmcnt(0)\n\tv_cmp_gt_u32 vcc, %4, %0\n\ts_cbranch_vccz .Lf%=\n\t"
                             "s_sub_u32 %1, %1, 1\n\ts_cbranch_scc0 .Lp%=\n.Lf%=:"
                             : "=&v"(fv), "=&s"(c_) : "v"(fa), "n"(c > NM2 ? 32 : c > NM1 ? 16 : 0), "n"(c) : "memory", "vcc", "scc");
            }
            SB0();
        };
        auto o_clear4 = [&]() AI {
#pragma unroll
            for (int n = 0; n < 4; ++n) st_wt(rsrc, WS_O + ((mt * 2 + hs) * 32 + 2 * hp + (n >> 1)) * 4096 + (rg & 1) * 2048 + (n & 1) * 1024 + l16, z4);
        };
        auto kv_part = [&]() AI
        {
            bf16x8 ones;
#pragma unroll
            for (int j = 0; j < 8; ++j) ones[j] = (short)0x3f80;
            const unsigned pa = lds0 + (hs ? P_U[2] : P_U[0]) * 8192 + (4 * g + q) * 128 + (((8 * (rg & 1) + p) ^ fsw(4 * g + q)) << 3);
            const unsigned va = lds0 + L_A + hs * 2048 + l16;
            bf16x8 b0[4], b1[4], af[2][4];
            lds_wait0(lds0 + L_FV + sch * 4);
            RS(15);
#pragma unroll
            for (int i = 0; i < 4; ++i) {
                af[0][i] = cat8(tr_read(pa + i * 4096), tr_read(pa + i * 4096 + 2048));
                b0[i] = lds_read128(va + i * 4096);
                af[1][i] = cat8(tr_read((pa ^ 32) + i * 4096), tr_read((pa ^ 32) + i * 4096 + 2048));
            }
#pragma unroll
            for (int i = 0; i < 4; ++i) b1[i] = lds_read128(va + i * 4096 + 1024);
            SB0();
            f32x4 k0[2] = {zero, zero}, k1[2] = {zero, zero}, zt[2] = {zero, zero};
            const unsigned kv_st = WS_KV + ((hp * 8 + rg * 2) * 16 + mt) * 1024 + l16;
#pragma unroll
            for (int i = 0; i < 4; ++i)
#pragma unroll
                for (int e = 0; e < 2; ++e) {
                    k0[e] = mfma16(af[e][i], b0[i], k0[e]);
                    if (PORD) zt[e] = mfma16(af[e][i], ones, zt[e]);
                }
            SB0();
#pragma unroll
            for (int i = 0; i < 4; ++i)
#pragma unroll
                for (int e = 0; e < 2; ++e) k1[e] = mfma16(af[e][i], b1[i], k1[e]);
            st_wt(rsrc, kv_st, nz4(__builtin_bit_cast(u32x4, pack44(k0[0], k0[1]))));
            if (!PORD) {
                SB0();
#pragma unroll
                for (int i = 0; i < 4; ++i)
#pragma unroll
                    for (int e = 0; e < 2; ++e) zt[e] = mfma16(af[e][i], ones, zt[e]);
                st_wt(rsrc, kv_st + 16384, nz4(__builtin_bit_cast(u32x4, pack44(k1[0], k1[1]))));
            }
            const u32x4 zo = nz4((l & 1) ? zt[1] : zt[0]);
            ST_MASK("global_store_dwordx4", 0x0003000300030003ull, WS_Z + ((hp * 4 + rg) * 16 + mt) * 128 + (l & 1) * 64 + g * 16, zo, ws);
            if (PORD) st_wt(rsrc, kv_st + 16384, nz4(__builtin_bit_cast(u32x4, pack44(k1[0], k1[1]))));
            SB0();
            flag(WR + L_FKS);
            if (OCL == 1) o_clear4();
        };
        auto report = [&](int r) AI {
            if (r > RLD) {
                unsigned t;
                unsigned a;
                asm volatile("v_mov_b32 %0, %3\n\tv_mov_b32 %1, %2\n\tds_write_b32 %1, %0" : "=&v"(t), "=&v"(a) : "s"(WR + L_PRL), "n"(r) : "memory");
            } else asm volatile("global_load_lds_dword %0, %1 offset:%2" ::"v"(v0), "s"(one), "n"(4 * r) : "memory"); };
        auto rd1 = [&](auto F, auto H) AI {
            constexpr int f = F, h = H, n = f >> 2, j = f >> 3, c = h ? E_B[f] : E_A[f], a = c % 100;
            if constexpr (h == 0 && (f & 3) == 0 && Q_C[n] > 0 && !((NOF & 512) && n >= 12 && n < KD)) chk(Q_C[n]);
            wh[f % (PD + 1)][h] = tr_read(ra[f & 3] + (J_U[j] - J_B[j]) * 8192 + (n & 1) * 4096 + h * 512);
            SB0();
            if constexpr (c / 100 == 1) {
                constexpr int jn = (f + 3 - a) >> 3;
                asm volatile("v_add_u32 %0, %1, %0" : "+v"(ra[a]) : "n"((J_B[jn] - J_B[jn - 1]) * 8192));
            } else if constexpr (c / 50 == 4) {
                asm volatile("v_add_u32 %0, %1, %2" : "=v"(xt[0]) : "s"((hs ? X_U1[a] : X_U0[a]) * 8192), "v"(xa));
            } else if constexpr (c / 50 == 5) {
                asm volatile("v_xor_b32 %0, 64, %1" : "=v"(xt[1]) : "v"(xt[0]));
            } else if constexpr (c / 100 == 3) {
                xf[a % 25 % ND][a / 25 & 1][a / 50] = lds_read128(xt[a / 50] + (a / 25 & 1) * 2048);
            } else if constexpr (c / 100 == 4) {
                fv = *(volatile AS3 unsigned*)(size_t)(fa + (a > NM2 ? 32 : a > NM1 ? 16 : 0));
            } else if constexpr (c / 100 == 5) {
                bad = __builtin_amdgcn_ballot_w64(fv < (unsigned)a);
                asm volatile("" : "+s"(bad));
            } else if constexpr (c / 100 == 6 && !((NOF & 1024) && a < NP1)) {
                report(a);
            }
        };
        RS(1);
        sfor<0, PD>([&](auto F) { rd1(F, IC<0>{}); rd1(F, IC<1>{}); });
        RS(2);
        constexpr int K0F = 4 * KD, K1F = 4 * KD + 1;
        sfor<0, 96>([&](auto N) {
            sfor<0, 4>([&](auto E) {
                constexpr int n = N, e = E, f = 4 * n + e, j = n >> 1, ks = n & 1, u = f_u(f), s = J_X[j], m = J_M[j];
                const bf16x8 wv = cat8(wh[f % (PD + 1)][0], wh[f % (PD + 1)][1]);
                sfor<0, 2>([&](auto RB) {
                    constexpr int rb = RB;
                    const bf16x8 xv = xf[s % ND][rb][ks];
                    if (AMF) {
                        if (j_first(j) && ks == 0) asm volatile("v_mfma_f32_16x16x32_bf16 %0, %1, %2, 0" : "=&v"(acc[m][u][rb]) : "v"(m == 1 ? xv : wv), "v"(m == 1 ? wv : xv));
                        else asm volatile("v_mfma_f32_16x16x32_bf16 %0, %1, %2, %0" : "+v"(acc[m][u][rb]) : "v"(m == 1 ? xv : wv), "v"(m == 1 ? wv : xv));
                    } else if (m == 1) acc[m][u][rb] = mfma16(xv, wv, acc[m][u][rb]);
                    else acc[m][u][rb] = mfma16(wv, xv, acc[m][u][rb]);
                    SB0();
                    if constexpr (f + PD < 384 && (f + PD < 4 * VD || f >= 4 * VD)) rd1(IC<f + PD>{}, RB);
                    if constexpr (rb == 0 && f == K0F) relane();
                    if constexpr (AMF && rb == 0 && (f == K0F || f == K1F)) asm volatile("s_nop 7");
                    SB0();
                    if constexpr (f == K0F) lds_write<bf16x8>(AX + (P_U[0] * 8192 - L_A) + rb * 1024, pack44(acc[0][0][rb], acc[0][1][rb]));
                    if constexpr (f == K1F) {
                        kown[rb] = pack44(acc[0][2][rb], acc[0][3][rb]);
                        if (rb < 2 - SH) lds_write<bf16x8>(AX + (P_U[2] * 8192 - L_A) + rb * 1024, kown[rb]);
                        else asm volatile("" : "+v"(kown[rb]));
                    }
                    SB0();
                });
                if constexpr (f == K0F) { flag(WR + L_FK0); RS(12); }
                if constexpr (f == K1F) { if (SH < 2) flag(WR + L_FK); RS(4); }
                if constexpr (f == 4 * VD - 1) {
                    bf16x8 ak[2];
                    if (NF2 > NF1) flag(WR + L_FVR);
                    auto own = [&]() AI {
                        if (NF2 == NF1) return;
                        vx_init();
                        vw_init();
                        lds_wait0(W4 + L_FVR);
                        sfor<NF1, NF2>(fill);
                        asm volatile("s_mov_b32 m0, %0" ::"s"(lds0 + L_PROG + w * 256 - 124));
                        RS(14);
                    };
                    if (SH) {
                        __builtin_amdgcn_s_setprio(HPRIO);
                        phi_w(ak, 1);
                    }
                    asm volatile("s_nop 7");
                    SB0();
#pragma unroll
                    for (int i = 0; i < 4; ++i) lds_write<bf16x8>(AX + i * 1024, pack44(acc[1][i][0], acc[1][i][1]));
                    SB0();
                    RS(5);
                    if (CFP == 1 && SH) own();
                    if (SH) {
                        f32x4 pk[2][2];
                        unsigned pw[4];
#pragma unroll
                        for (int i = 4 - 2 * SH; i < 4; ++i) pk[i >> 1][i & 1] = rbf4(mfma16(ak[i & 1], kown[i >> 1], zero));
                        phi_adr(pw, lds0 + P_U[2] * 8192 + (SH == 1 ? 2048 : 0));
                        if (SH == 1) phi_k1(pk[1], pw);
                        else phi_k<true>(pk, pw, 0, []() {});
                        SB0();
                    }
                    flag(WR + L_FV);
                    if (OCL == 2) o_clear4();
                    RS(13);
                    if (CFP == 0 || !SH) own();
                    kv_part();
                    __builtin_amdgcn_s_setprio(CPRIO);
                    fv = 0;
                    bad = ~0ull;
                    sfor<4 * VD, 4 * VD + PD>([&](auto F) { rd1(F, IC<0>{}); rd1(F, IC<1>{}); });
                    RS(10);
                }
                if constexpr (e == 3) {
                    SB0();
                    if constexpr (n % CSN == CSN - 1) { RSC(32 + n / CSN); RK(48 + n / CSN); }
                    if constexpr (n == 31) RS(3);
                }
            });
        });
        asm volatile("s_nop 7");
        SB0();
        RS(6);
        if (CPRIO) __builtin_amdgcn_s_setprio(0);
        flag(WR + L_FQD);
        if (OCL) {
            const u32x2 tv = lds_read<u32x2>(lds0 + L_SEL);
            waitv<0>();
            ST_MASK("global_store_dwordx2", 1ull, 0u, tv, ws + WS_CL + 4096 + ((mt * 4 + rg) * 16 + hp) * 8);
        }
        bf16x8 a0[2], a1[2];
        f32x4 pq[2][2][2];
        phi_w(a1, hs);
        phi_w(a0, hs ^ 1);
#pragma unroll
        for (int rb = 0; rb < 2; ++rb) {
            const bf16x8 b0 = pack44(acc[2][0][rb], acc[2][1][rb]), b1 = pack44(acc[2][2][rb], acc[2][3][rb]);
#pragma unroll
            for (int fb = 0; fb < 2; ++fb) {
                pq[1][rb][fb] = rbf4(mfma16(a1[fb], hs ? b1 : b0, zero));
                pq[0][rb][fb] = rbf4(mfma16(a0[fb], hs ? b0 : b1, zero));
            }
        }
        softmax2<true>(pq[1], ph, []() {});
        RS(7);
        lds_wait(W4 + L_FKS, 1);
#pragma unroll
        for (int i = 0; i < 4; ++i) lds_write<u32x4>(AX + i * 1024, ph[i >> 1][i & 1]);
        SB0();
        flag(WR + L_FQ);
        sq = lds_read<u32x4>(lds0 + L_SEL);
        RS(8);
        softmax2<true>(pq[0], ph, []() {});
        asm volatile("" : "+v"(ph[0][0]), "+v"(ph[0][1]), "+v"(ph[1][0]), "+v"(ph[1][1]));
        RS(9);
        RS(11);
        xg_def();
        kf_def();
    }
    relane();
    const char* cla = CLL ? ws + WS_C2 + ((rt * 2 + kp) * 16 + l15) * 128 : ws + WS_CL + (OCL ? 4096 + ((rt * 4 + rg) * 16 + l15) * 8 : ((rt * 2 + kp) * 16 + l15) * 8);
    const u64 T = (u64)sq[1] << 32 | sq[0];
    u64 cl;
    xg_load(w & 5);
    {
        if ((w & 5) != 4) {
            lds_wait0(lds0 + L_FKF + oh * 4);
#pragma unroll
            for (int i = 0; i < 4; ++i) kf[i] = lds_read<u32x4>(lds0 + L_KF + oh * 4096 + i * 1024 + l16);
#pragma unroll
            for (int i = 0; i < 2; ++i) kf[4 + i] = lds_read<u32x4>(lds0 + L_KZ + oh * 128 + i * 64 + g * 16);
        }
        asm volatile("global_load_dwordx2 %0, %1, off sc1" : "=&v"(cl) : "v"(cla) : "memory");
        RS(27);
        o_store(kf, ph, rsrc, WS_O + ((mt * 2 + hs) * 32 + 2 * hp + oh) * 4096 + (rg & 1) * 2048 + l16);
        RS(28);
    }
    if (WON < 16 && WLK < 0) flag(lds0 + L_FOS + w * 4);
    waitv<3>();
#pragma unroll
    for (int i = 0; i < 6; ++i) pin(kf[i]);
    flag(lds0 + L_FWO + w * 4);
    __builtin_amdgcn_s_setprio(0);
    if (WON < 16 && WLK < 0) {
        if ((w & 5) == 5) {
            lds_wait(lds0 + L_FOS + sch * 4, 1);
            wo_load(WON, 16, true);
        }
    }
    const u32x2 selv = {sq[2], sq[3]};

    RS(30);
    const char* ob = ws + WS_O + (rt * 2 + (rg >> 1)) * 131072 + (rg & 1) * 2048;
    const unsigned ov0 = l16 + kp * 1024, ov1 = l16 + (kp ^ 1) * 1024;
    auto oa = [&](int i) { return ob + ((2 * i + kp + rot) & 31) * 4096; };
    u32x4 of[3][4];
    auto fetch1 = [&](int k, int j) AI {
        if (k < OPL) ld_async(of[k % 3][j], (j & 1) ? ov1 : ov0, oa(2 * k + (j >> 1)));
        else asm volatile("global_load_dwordx4 %0, %1, %2" OPM : "=&v"(of[k % 3][j]) : "v"((j & 1) ? ov1 : ov0), "s"(oa(2 * k + (j >> 1))) : "memory");
    };
    auto fix = [&](int k) AI { if (NOF & 128) { for (int j = 0; j < 4; ++j) pin(of[k % 3][j]); return; }
        if (PP3) settle_p<PP3 + !PP3, PD3>(of[k % 3], [&](int j) { return (j & 1) ? ov1 : ov0; }, [&](int j) { return oa(2 * k + (j >> 1)); });
        else settle(of[k % 3], [&](int j) { return (j & 1) ? ov1 : ov0; }, [&](int j) { return oa(2 * k + (j >> 1)); }); };
    const bool own = (__builtin_amdgcn_readfirstlane(selv[0]) >> 4) == (unsigned)(rt * 8 + rg * 2 + kp);
    float gv_[2];
    f32x2 gb[2][4];
    auto g_mix = [&]() AI {
#pragma unroll
        for (int j = 0; j < 2; ++j) {
            gv_[j] = bf2f((u16)gg[j]);
            const float og = rbf(1.f - gv_[j]);
#pragma unroll
            for (int i = 0; i < 4; ++i) {
                const unsigned t = pack2(og * lo16(xr[j][i]), og * hi16(xr[j][i]));
                gb[j][i] = (f32x2){lo16(t), hi16(t)};
                asm volatile("" : "+v"(gb[j][i]));
            }
        }
    };
    asm volatile("" : "+v"(xr[0]), "+v"(xr[1]), "+v"(gg[0]), "+v"(gg[1]));
#pragma unroll 1
    for (unsigned s = 0; s < SPIN_CAP && __ballot((gg[0] >> 16) != (T & 0xFFFFFFFFFFFFull) || (gg[1] >> 16) != (T & 0xFFFFFFFFFFFFull)); ++s) {
        g_load();
        waitv<0>();
        asm volatile("" : "+v"(gg[0]), "+v"(gg[1]));
    }
    g_mix();
    if (PDL && ((w & 5) != 5 || WLK >= 0)) __builtin_amdgcn_s_sleep(PDL);
    if ((w & 5) != 5) waitv<2>();
    else waitv<(WON < 16 && WLK < 0 ? 3 + 4 * (16 - WON) : 2)>();
    asm volatile("" : "+v"(cl));
#pragma unroll 1
    for (unsigned s = 0; s < SPIN_CAP && __ballot(cl != T); ++s)
        asm volatile("global_load_dwordx2 %0, %1, off sc1\n\ts_waitcnt vmcnt(0)" : "=&v"(cl) : "v"(cla) : "memory");
    RS(16);
#pragma unroll
    for (int j = 0; j < 4; ++j) fetch1(0, j);
    RS(17);
    f32x4 acc[2][4];
    unsigned wa[2][4];
#pragma unroll
    for (int i = 0; i < 8; ++i) {
        wa[i >> 2][i & 3] = ((lds0 + 4096 * kp + (4 * g + q) * 128 + ((((g & 1) << 1) | (q >> 1)) * 32) + 8 * p) ^ ((i & 3) << 5)) + (i >> 2) * 65536;
        asm volatile("" : "+v"(wa[i >> 2][i & 3]));
    }
    s16x4 wf[2][8][2];
    auto wrd = [&](int k, int i) AI {
        const int u = wo_unit(2 * k + (i >> 3));
        wf[k & 1][i >> 1][i & 1] = tr_read(wa[u >> 3][(i >> 1) & 3] + (u & 7) * 8192 + (i & 1) * 2048);
    };
    u32x4 yp[14];
    const unsigned yo = srow * 4096 + cb * 256 + sch * 32;
    auto ya = [&](int i) { return ws + WS_Y + (i >> 1) * 32768 + (i & 1) * 16; };
    lds_wait(lds0 + L_FWD + ((l & 8) ? 16 + sch * 4 : (l & 1) * 4), 1);
    RS(18);
    RS(31);
    if (BPRIO && !kp) __builtin_amdgcn_s_setprio(BPRIO);
#pragma unroll
    for (int i = 0; i < 16; ++i) wrd(0, i);
#pragma unroll
    for (int j = 0; j < 4; ++j) fetch1(1, j);
    waitv<4>();
    fix(0);
    sfor<0, 8>([&](auto K) {
        constexpr int k = K;
        unsigned m[4];
        if constexpr (WON < 16 && 2 * (k + 1) == WON) lds_wait(lds0 + L_FW2 + (l & 1) * 4, 1);
        if constexpr (k == YK) {
            if (own) {
#pragma unroll
                for (int i = 0; i < 14; ++i) ld_async(yp[i], yo, ya(i));
            }
        }
        SB0();
        sfor<0, 16>([&](auto I) {
            constexpr int i = I, j = i >> 2, nb = i & 3;
            const bf16x8 a = cat8(wf[k & 1][(j >> 1) * 4 + nb][0], wf[k & 1][(j >> 1) * 4 + nb][1]);
            if (k == 0 && j < 2) asm volatile("v_mfma_f32_16x16x32_bf16 %0, %1, %2, 0" : "=&v"(acc[j & 1][nb]) : "v"(a), "v"(of[k % 3][j]));
            else asm volatile("v_mfma_f32_16x16x32_bf16 %0, %1, %2, %0" : "+v"(acc[j & 1][nb]) : "v"(a), "v"(of[k % 3][j]));
            SB0();
            if constexpr (k < 7) wrd(k + 1, i);
            if constexpr (k < 6 && i < 4) fetch1(k + 2, i);
            if constexpr (k < 7 && i == BCK) {
                waitv<(k < 6 ? 4 : 0)>();
#pragma unroll
                for (int n = 0; n < 4; ++n) pin(of[(k + 1) % 3][n]);
            }
            if constexpr (k < 7 && i > BCK && i <= BCK + 4) {
                m[i - BCK - 1] = min4(of[(k + 1) % 3][i - BCK - 1]);
                asm volatile("" : "+v"(m[i - BCK - 1]));
            }
            SB0();
        });
        if constexpr (k < 7) {
            if (__builtin_expect(__builtin_amdgcn_ballot_w64(min(min(m[0], m[1]), min(m[2], m[3])) == 0u) != 0ull, 0)) fix(k + 1);
        }
        SB0();
        if (k == 0) RS(19);
        if (k == 3) RS(23);
    });
    asm volatile("s_nop 7");
    SB0();
    RS(20);
    const unsigned ea = lds0 + L_A + l16 + 4096 * w, eb = lds0 + L_A + 4096 * (w ^ 4);
    lds_write<u32x4>(ea, nz4(acc[1][0]));
#pragma unroll
    for (int nb = 1; nb < 4; ++nb) lds_write<f32x4>(ea + nb * 1024, acc[1][nb]);
    if (own) {
        waitv<0>();
        settle(yp, [&](int) { return yo; }, ya);
        f32x4 yv[2];
#pragma unroll
        for (int h = 0; h < 2; ++h) {
            yv[h] = __builtin_bit_cast(f32x4, yp[h]);
#pragma unroll
            for (int i = 1; i < 7; ++i) yv[h] += __builtin_bit_cast(f32x4, yp[2 * i + h]);
#pragma unroll
            for (int i = 0; i < 4; ++i) {
                float t = dpp_add<0x128>(yv[h][i]);
                t = swap16_add(t, t);
                yv[h][i] = rbf(rbf(swap32_add(t, t)) * __uint_as_float(selv[1]));
            }
        }
#pragma unroll
        for (int j = 0; j < 2; ++j) {
            const bool hit = row0 + 8 * j + srow == (int)selv[0];
#pragma unroll
            for (int i = 0; i < 4; ++i) {
                const unsigned t = pack2(lo16(xr[j][i]) + yv[i >> 1][2 * (i & 1)], hi16(xr[j][i]) + yv[i >> 1][2 * (i & 1) + 1]);
                xr[j][i] = hit ? t : xr[j][i];
            }
        }
        g_mix();
    }
    BAR();
    RS(21);
#pragma unroll
    for (int nb = 0; nb < 4; ++nb) acc[0][nb] += lds_read<f32x4>(eb + l16 + nb * 1024);
#pragma unroll
    for (int nb = 0; nb < 4; ++nb) asm volatile("" : "+v"(acc[0][nb]));
#pragma unroll
    for (int nb = 0; nb < 4; ++nb) {
        const u32x2 a = {pack2(acc[0][nb][0], acc[0][nb][1]), pack2(acc[0][nb][2], acc[0][nb][3])};
        lds_write<u32x2>(eb + l15 * 128 + (((2 * nb + (g >> 1)) ^ (l & 7)) * 16) + (g & 1) * 8, a);
    }
#pragma unroll
    for (int j = 0; j < 2; ++j) {
        const u32x4 av = lds_read<u32x4>(eb + j * 1024 + srow * 128 + ((sch ^ srow) * 16));
        u32x4 o;
#pragma unroll
        for (int i = 0; i < 4; ++i) {
            const unsigned ga = pack2(gv_[j] * lo16(av[i]), gv_[j] * hi16(av[i]));
            o[i] = pack2(lo16(ga) + gb[j][i][0], hi16(ga) + gb[j][i][1]);
        }
        asm volatile("global_store_dwordx4 %0, %1, %2" OST "\n\ts_nop 1" ::"v"(xo), "v"(o), "s"(out + (size_t)(row0 + 8 * j) * DM + cb * 64) : "memory");
    }
    RS(22);
    RSDUMP();
}

extern "C" void kernel_launch(void* const* d_in, const int* in_sizes, int n_in, void* d_out, int out_size,
                              void* d_ws, size_t ws_size, hipStream_t stream) {
    fused_mix<<<256, 512, 0, stream>>>((const u16*)d_in[0], (const u16*)d_in[1], (const u16*)d_in[2], (const u16*)d_in[3],
                                       (const u16*)d_in[5], (const u16*)d_in[6], (const u16*)d_in[7], (const u16*)d_in[8],
                                       (const u16*)d_in[9], (char*)d_ws, (u16*)d_out);
}
```

```cpp
#include <hip/hip_runtime.h>
#include <hip/hip_bf16.h>
#include <stdint.h>
#include <type_traits>

typedef __attribute__((ext_vector_type(8))) short bf16x8;
typedef __attribute__((ext_vector_type(4))) short s16x4;
typedef __attribute__((ext_vector_type(4))) float f32x4;
typedef __attribute__((ext_vector_type(2))) float f32x2;
typedef __attribute__((ext_vector_type(2))) unsigned u32x2;
typedef __attribute__((ext_vector_type(4))) unsigned u32x4;
typedef __attribute__((ext_vector_type(2))) __bf16 bf16x2_t;
typedef unsigned short u16;
typedef unsigned long long u64;

#define DI __device__ __forceinline__
#define AS3 __attribute__((address_space(3)))
#define AS1 __attribute__((address_space(1)))
#define RLX_AGENT __ATOMIC_RELAXED, __HIP_MEMORY_SCOPE_AGENT

constexpr int SEQ = 2048, DM = 1024, NH = 32, FF = 3584;

DI float bf2f(u16 v) { return __uint_as_float(((unsigned)v) << 16); }
DI u16 f2bf(float f) { __bf16 b = (__bf16)f; return __builtin_bit_cast(u16, b); }
DI float rbf(float f) { return bf2f(f2bf(f)); }
DI unsigned pack2(float lo, float hi) {
    const f32x2 v = {lo, hi};
    return __builtin_bit_cast(unsigned, __builtin_convertvector(v, bf16x2_t));
}
DI float lo16(unsigned u) { return __uint_as_float(u << 16); }
DI float hi16(unsigned u) { return __uint_as_float(u & 0xffff0000u); }

DI s16x4 tr_read(unsigned a) { return __builtin_amdgcn_ds_read_tr16_b64_v4i16((AS3 s16x4*)(size_t)a); }
DI bf16x8 lds_read128(unsigned a) { return *(AS3 const bf16x8*)(size_t)a; }
template <class T> DI void lds_write(unsigned a, T v) { *(AS3 T*)(size_t)a = v; }
template <class T> DI T lds_read(unsigned a) { return *(AS3 const T*)(size_t)a; }
DI bf16x8 cat8(s16x4 a, s16x4 b) { return __builtin_shufflevector(a, b, 0, 1, 2, 3, 4, 5, 6, 7); }
DI f32x4 mfma16(bf16x8 a, bf16x8 b, f32x4 c) { return __builtin_amdgcn_mfma_f32_16x16x32_bf16(a, b, c, 0, 0, 0); }
DI bf16x8 pack44(const f32x4& a, const f32x4& b) {
    u32x4 r;
    r[0] = pack2(a[0], a[1]);
    r[1] = pack2(a[2], a[3]);
    r[2] = pack2(b[0], b[1]);
    r[3] = pack2(b[2], b[3]);
    return __builtin_bit_cast(bf16x8, r);
}
DI unsigned lds_of(const void* p) { return (unsigned)(size_t)(AS3 const char*)p; }

template <int CTRL>
DI unsigned dpp(unsigned x) { return __builtin_amdgcn_update_dpp(0u, x, CTRL, 0xf, 0xf, true); }
template <int CTRL>
DI float dpp_add(float x) { return x + __uint_as_float(dpp<CTRL>(__float_as_uint(x))); }
DI float swap32_add(float a, float b) {
    auto r = __builtin_amdgcn_permlane32_swap(__float_as_uint(a), __float_as_uint(b), false, false);
    return __uint_as_float(r[0]) + __uint_as_float(r[1]);
}
DI float swap16_add(float a, float b) {
    auto r = __builtin_amdgcn_permlane16_swap(__float_as_uint(a), __float_as_uint(b), false, false);
    return __uint_as_float(r[0]) + __uint_as_float(r[1]);
}
DI float wave_sum(float x) {
    x = dpp_add<0xB1>(x);
    x = dpp_add<0x4E>(x);
    x = dpp_add<0x141>(x);
    x = dpp_add<0x140>(x);
    x = swap16_add(x, x);
    return swap32_add(x, x);
}
DI unsigned wave_umax(unsigned x) {
    x = max(x, dpp<0xB1>(x));
    x = max(x, dpp<0x4E>(x));
    x = max(x, dpp<0x141>(x));
    x = max(x, dpp<0x140>(x));
    auto r = __builtin_amdgcn_permlane16_swap(x, x, false, false);
    x = max(r[0], r[1]);
    auto s = __builtin_amdgcn_permlane32_swap(x, x, false, false);
    return max(s[0], s[1]);
}
template <class OP, class POST>
DI void red2(float& a, float& b, OP op, POST post) {
    auto x = __builtin_amdgcn_permlane32_swap(__float_as_uint(a), __float_as_uint(b), false, false);
    const float m = op(__uint_as_float(x[0]), __uint_as_float(x[1]));
    auto y = __builtin_amdgcn_permlane16_swap(__float_as_uint(m), __float_as_uint(m), false, false);
    const float f = post(op(__uint_as_float(y[0]), __uint_as_float(y[1])));
    auto z = __builtin_amdgcn_permlane32_swap(__float_as_uint(f), __float_as_uint(f), false, false);
    a = __uint_as_float(z[0]);
    b = __uint_as_float(z[1]);
}

#define RS(i) do {} while (0)
#define RSD(i) do {} while (0)
#define RSDUMP() do {} while (0)

constexpr unsigned WS_E = 0;
constexpr unsigned WS_RG = 256;
constexpr unsigned WS_GG = WS_RG + 2048;
constexpr unsigned WS_CL = 32768;
constexpr unsigned WS_Z = 65536;
constexpr unsigned WS_KF = WS_Z + 131072;
constexpr unsigned WS_GU = WS_KF + 262144;
constexpr unsigned WS_Y = WS_GU + 262144;
constexpr unsigned WS_KV = WS_Y + 262144;
constexpr unsigned WS_O = WS_KV + (2u << 20);
constexpr unsigned WS_C2 = WS_O + (4u << 20);
constexpr unsigned WS_END = WS_C2 + 65536;

constexpr int WE = 0;
constexpr int NQ = 10, ND = 6, NX = 16, KD = 78, VD = 84, NF = 64, NF1 = 59, NF2 = 59, NP1 = 12, NP = 14, NM1 = 21, NM2 = 21, NM = 22, KFN = 100;
constexpr int J_S[48] = {0, 0, 0, 1, 1, 1, 2, 2, 2, 3, 3, 3, 4, 4, 4, 5, 5, 5, 6, 6, 6, 7, 7, 7, 8, 8, 8, 9, 9, 9, 10, 10, 11, 11, 12, 12, 13, 14, 15, 13, 14, 15, 10, 11, 12, 13, 14, 15};
constexpr int J_M[48] = {0, 1, 2, 0, 1, 2, 0, 1, 2, 0, 1, 2, 0, 1, 2, 0, 1, 2, 0, 1, 2, 0, 1, 2, 0, 1, 2, 0, 1, 2, 0, 1, 0, 1, 0, 1, 0, 0, 0, 1, 1, 1, 2, 2, 2, 2, 2, 2};
constexpr int J_X[48] = {0, 0, 0, 1, 1, 1, 2, 2, 2, 3, 3, 3, 4, 4, 4, 5, 5, 5, 6, 6, 6, 7, 7, 7, 8, 8, 8, 9, 9, 9, 10, 10, 11, 11, 12, 12, 13, 14, 15, 13, 14, 15, 10, 11, 12, 13, 14, 15};
constexpr int J_U[48] = {2, 4, 8, 18, 11, 12, 17, 15, 13, 0, 1, 6, 4, 8, 9, 11, 12, 14, 15, 13, 7, 1, 6, 5, 8, 9, 10, 12, 14, 16, 13, 7, 1, 6, 4, 8, 18, 14, 15, 13, 7, 3, 0, 1, 6, 5, 2, 4};
constexpr int J_B[48] = {2, 2, 2, 11, 11, 11, 11, 11, 11, 0, 0, 0, 0, 8, 8, 8, 8, 8, 8, 8, 1, 1, 1, 1, 1, 9, 9, 9, 9, 9, 9, 1, 1, 1, 1, 1, 13, 13, 13, 13, 0, 0, 0, 0, 0, 0, 0, 0};
constexpr int Q_C[96] = {1, 0, 2, 0, 3, 0, 4, 0, 5, 0, 6, 0, 7, 0, 0, 0, 0, 0, 8, 0, 0, 0, 0, 0, 9, 0, 0, 0, 0, 0, 10, 0, 0, 0, 0, 0, 11, 0, 0, 0, 0, 0, 12, 0, 0, 0, 0, 0, 13, 0, 0, 0, 0, 0, 14, 0, 0, 0, 0, 0, 15, 0, 0, 0, 0, 0, 16, 0, 0, 0, 0, 0, 17, 0, 0, 0, 0, 0, 18, 0, 19, 0, 20, 0, 21, 0, 22, 0, 0, 0, 0, 0, 0, 0, 0, 0};
constexpr int X_U0[16] = {6, 9, 14, 7, 5, 10, 16, 3, 2, 18, 17, 3, 5, 9, 11, 16};
constexpr int X_U1[16] = {5, 10, 16, 3, 2, 18, 17, 0, 4, 11, 15, 0, 2, 10, 12, 17};
constexpr int P_U[4] = {11, 12, 9, 10};
constexpr int W_U[16] = {8, 14, 15, 13, 7, 3, 9, 10, 11, 12, 0, 1, 6, 5, 2, 4};
constexpr int W_N[16] = {100, 100, 100, 100, 100, 100, 101, 101, 101, 101, 13, 13, 14, 14, 99, 99};
constexpr int F_J[64] = {-1, 0, 1, 2, -2, 3, 4, 5, -3, 6, 7, 8, -4, 9, 10, 11, -5, 12, 13, 14, -6, 15, 16, 17, -7, 18, 19, 20, -8, 21, 22, 23, -9, 24, 25, 26, -10, 27, 28, 29, -11, 30, 31, -12, 32, 33, -13, 34, 35, -14, 36, -15, 37, -16, 38, 39, 40, 41, 42, 43, 44, 45, 46, 47};
constexpr int F_N[64] = {0, 0, 0, 0, 0, 0, 0, 0, 0, 0, 0, 0, 0, 0, 0, 1, 1, 1, 1, 1, 2, 2, 2, 2, 3, 3, 3, 3, 4, 4, 4, 4, 5, 5, 5, 5, 6, 6, 6, 6, 7, 7, 7, 7, 8, 8, 8, 8, 9, 9, 9, 10, 10, 10, 10, 11, 11, 11, 11, 12, 12, 12, 12, 12};
constexpr int F_K[64] = {0, 1, 2, 3, 0, 4, 5, 6, 0, 0, 0, 7, 0, 0, 0, 8, 0, 0, 0, 9, 0, 0, 0, 10, 0, 0, 0, 11, 0, 0, 0, 12, 0, 0, 0, 13, 0, 0, 0, 14, 0, 0, 0, 0, 15, 0, 0, 0, 16, 0, 0, 0, 0, 0, 17, 18, 19, 20, 21, 0, 0, 0, 0, 22};
constexpr int E_A[384] = {300, 325, 350, 375, 0, 0, 0, 502, 0, 0, 0, 403, 0, 0, 0, 503, 201, 251, 0, 404, 0, 100, 101, 504, 301, 326, 351, 376, 0, 0, 0, 505, 601, 0, 0, 406, 0, 0, 0, 506, 202, 252, 0, 407, 0, 0, 0, 507, 302, 327, 352, 377, 0, 0, 0, 602, 0, 0, 0, 0, 0, 0, 0, 203, 253, 0, 0, 408, 0, 100, 101, 508, 303, 328, 353, 378, 0, 0, 0, 603, 0, 0, 0, 0, 0, 0, 0, 204, 254, 0, 0, 409, 0, 0, 0, 509, 304, 329, 354, 379, 0, 100, 101, 604, 102, 103, 0, 0, 0, 0, 0, 205, 255, 0, 0, 410, 0, 0, 0, 510, 305, 330, 355, 380, 0, 0, 0, 605, 0, 0, 0, 0, 0, 0, 0, 206, 256, 0, 0, 411, 0, 0, 0, 511, 306, 331, 356, 381, 0, 0, 0, 606, 0, 0, 0, 0, 0, 100, 101, 102, 103, 207, 257, 412, 0, 0, 0, 512, 307, 332, 357, 382, 0, 0, 0, 607, 0, 0, 0, 0, 0, 0, 0, 208, 258, 0, 0, 413, 0, 0, 0, 513, 308, 333, 358, 383, 0, 100, 101, 608, 102, 103, 0, 0, 0, 0, 0, 209, 259, 0, 0, 414, 0, 0, 0, 514, 309, 334, 359, 384, 0, 0, 0, 609, 0, 0, 0, 0, 0, 0, 0, 210, 260, 0, 0, 415, 0, 0, 0, 515, 310, 335, 360, 385, 0, 100, 101, 610, 102, 103, 211, 261, 0, 0, 0, 0, 311, 336, 361, 386, 0, 0, 0, 516, 611, 212, 262, 0, 0, 0, 0, 0, 312, 337, 362, 387, 0, 0, 0, 213, 263, 0, 0, 417, 0, 100, 101, 517, 313, 338, 363, 388, 214, 264, 0, 612, 314, 339, 364, 389, 215, 265, 0, 0, 315, 340, 365, 390, 0, 0, 0, 518, 0, 0, 0, 419, 0, 100, 101, 519, 102, 103, 0, 420, 0, 0, 0, 520, 0, 0, 0, 0, 0, 0, 0, 0, 0, 0, 0, 422, 0, 0, 0, 522, 0, 0, 0, 0, 0, 0, 0, 0, 0, 0, 0, 613, 0, 0, 0, 0, 0, 0, 0, 0, 0, 0, 0, 0, 0, 0, 0, 614, 0, 0, 0, 0, 0, 0, 0, 0, 0, 0, 0, 0};
constexpr int E_B[384] = {0, 0, 402, 0, 0, 0, 0, 0, 0, 0, 0, 0, 0, 0, 0, 0, 0, 0, 0, 0, 0, 0, 0, 102, 103, 0, 405, 0, 0, 0, 0, 0, 0, 0, 0, 0, 0, 0, 0, 0, 0, 0, 0, 0, 0, 0, 0, 0, 0, 0, 0, 0, 0, 0, 0, 0, 0, 0, 0, 0, 0, 0, 0, 0, 0, 0, 0, 0, 0, 0, 0, 102, 103, 0, 0, 0, 0, 0, 0, 0, 0, 0, 0, 0, 0, 0, 0, 0, 0, 0, 0, 0, 0, 0, 0, 0, 0, 0, 0, 0, 0, 0, 0, 0, 0, 0, 0, 0, 0, 0, 0, 0, 0, 0, 0, 0, 0, 0, 0, 0, 0, 0, 0, 0, 0, 0, 0, 0, 0, 0, 0, 0, 0, 0, 0, 0, 0, 0, 0, 0, 0, 0, 0, 0, 0, 0, 0, 0, 0, 0, 0, 0, 0, 0, 0, 0, 0, 0, 0, 0, 0, 0, 0, 0, 0, 0, 0, 0, 0, 0, 0, 0, 0, 0, 0, 0, 0, 0, 0, 0, 0, 0, 0, 0, 0, 0, 0, 0, 0, 0, 0, 0, 0, 0, 0, 0, 0, 0, 0, 0, 0, 0, 0, 0, 0, 0, 0, 0, 0, 0, 0, 0, 0, 0, 0, 0, 0, 0, 0, 0, 0, 0, 0, 0, 0, 0, 0, 0, 0, 0, 0, 0, 0, 0, 0, 0, 0, 0, 0, 0, 0, 0, 0, 0, 0, 0, 0, 0, 0, 0, 0, 0, 0, 0, 0, 0, 0, 0, 416, 0, 0, 0, 0, 0, 0, 0, 0, 0, 0, 0, 0, 0, 0, 0, 0, 0, 0, 0, 0, 0, 0, 0, 0, 0, 0, 0, 0, 102, 103, 0, 0, 0, 0, 0, 0, 0, 0, 0, 0, 0, 0, 0, 0, 0, 0, 0, 418, 0, 0, 0, 0, 0, 0, 0, 0, 0, 0, 0, 0, 0, 0, 0, 0, 0, 0, 0, 0, 0, 0, 0, 0, 0, 0, 0, 0, 0, 0, 0, 0, 0, 0, 0, 0, 0, 0, 0, 0, 0, 0, 0, 0, 0, 0, 0, 0, 0, 0, 0, 0, 0, 0, 0, 0, 0, 0, 0, 0, 0, 0, 0, 0, 0, 0, 0, 0, 0, 0, 0, 0, 0, 0, 0, 0, 0};
#define LH 0
constexpr unsigned L_A = 16 * 8192;
constexpr unsigned L_KF = 18 * 8192;
constexpr unsigned L_SM = 19 * 8192;
constexpr unsigned L_FULL = L_SM + 256;
constexpr unsigned L_FVR = L_SM + 320, L_PRL = L_SM + 336;
constexpr unsigned L_PROG = L_SM + 896;
constexpr unsigned L_FW2 = L_SM + 512, L_FOS = L_SM + 480, L_FGI = L_SM + 520, L_FK0 = L_SM + 752, L_FBI = L_SM + 744;
constexpr unsigned L_FK = L_SM + 528, L_FKT = L_SM + 544, L_FV = L_SM + 560, L_FP = L_SM + 576, L_FKS = L_SM + 592;
constexpr unsigned L_FQD = L_SM + 608, L_FG = L_SM + 624, L_FKF = L_SM + 736, L_FQ = L_SM + 720, L_FWD = L_SM + 640, L_FWO = L_SM + 656, L_FLD = L_SM + 688, L_BEST = L_SM + 696;
constexpr unsigned L_SEL = L_SM + 704;
constexpr unsigned L_XS = L_SM + 2048, L_KZ = L_SM + 3072, L_WPHI = L_SM + 4096;
static_assert(NP <= 31 && NM <= 63, "");
static_assert(P_U[1] == P_U[0] + 1 && P_U[3] == P_U[2] + 1, "feature maps of a head lie side by side");
constexpr int wo_unit(int i) { return W_U[i]; }
constexpr int f_u(int f) { return f & 3; }
constexpr bool j_first(int j) {
    for (int i = 0; i < j; ++i)
        if (J_M[i] == J_M[j]) return false;
    return true;
}
constexpr int younger(int p) {
    int c = 0;
    for (int n = 0; n < NF1 - LH; ++n)
        if (F_N[n] >= p) c += F_J[n] < 0 ? 8 : 4;
    return c;
}
constexpr int nfv() {
    for (int n = 0; n < NF1; ++n)
        if (F_J[n] == VD / 2 - 1) return n + 1;
    return NF1;
}
constexpr int next_need(int n) {
    for (int i = n; i < NF; ++i)
        if (F_N[i] != F_N[n]) return F_N[i];
    return 99;
}
constexpr int x_stage(int i) { return i < 16 ? i : i - 16 + NQ; }
constexpr unsigned SPIN_CAP = 1u << 16;
#define N8(a) a, a + 1, a + 2, a + 3, a + 4, a + 5, a + 6, a + 7
__device__ const unsigned k_num[64] = {N8(0), N8(8), N8(16), N8(24), N8(32), N8(40), N8(48), N8(56)};

#define RK(i) do {} while (0)
#define RSL(i) do {} while (0)
#define RSC(i) do {} while (0)
#define CSN 6
#define CFP 0
#define QWM 0
#define RLD 11
#define CLF 3
#define CLT 1
#define FLV 1
#define SP1 0
#define SP2 0
#define SP3 0
#define SP4 0
#define SPW 0
#define SPI "s_load_dwordx2"
#define DNP 1
#define WOT 1
#define YT 1
#define GDL 16
#define SSM 0
#define CLE 0
#define GUO 0
#define TL0 0
#define TL0W ""
#define TLD 0
#define SPA 0
#define SPS 7
#define MRG 5
#define PP1 0
#define PD1 0
#define PP2 0
#define PD2 0
#define PP3 0
#define PD3 0
#define PDI 0
#define HLD " sc1"
#define HST " sc1"
#define HSA 16
#define BMAP 0
#define ROTC 0
#define ROT0 0
#define OCL 0
#define KCL 0
#define CLL 0
#define ZL 1
#define RTS 0
#define WEP 0
#define PFN 0
#define PFS 0x248
#define PFM ""
#define KXP 8
#define KXQ 0
#define OW0 0
#define LPRIO 0
#define LPRW 3
#define LTH 63
#define LT0 14
#define PEEK 1
#define CSP 1
#define GUA 12
#define OPL 8
#define OPM ""
#define PD 4
#define NPH 4
#define KSK 3
#define ROTK 8
#define OWIN 2
#define CLR0 2
#define CLS 1
#define CLP 0
#define SLK 2
#define CPH 3
#define NPL 0
#define CPRIO 1
#define HPRIO 2
#define KPK 3
#define KRND 1
#define QW 1
#define WOG 1
#define WON 12
#define KFD 24
#define SEL0 9
#define SPRIO 2
#define FBA 3
#define FBB 10
#define FBS 1
#define FBL 1
#define WOP 0
#define GTL 3
#define WPF 0
#define WOE 6
#define GW 0
#define GUF 0
#define FBH 0
#define WG2 0
#define NTX 0
#define NTW 0
#define NTS 16
#define NTO ""
#define GQ1 6
#define GQ2 8
#define CLO 0
#define PDL 6
#define XK 3
#define YK 7
#define BPRIO 0
#define AMF 1
#define LF 1
#define SH 2
#define LC 0
#define SPR2 0
#define GQ0 0
#define CPF 53248
#define NF0 4
#define OTL 0
#define OST " sc1"
#define TLW 3
#define THR -1
#define THQ -1
#define FDH 0
#define WLK -1
#define XGE 1
#define KXO 3
#define KXM 3
#define KX8 0
#define KXB 0xFEDBA9C768534210ull
#define BCK 10
#define GPR 0
#define GHL 0
#define TGT 1
#define PORD 1
#define TRM 3
#define LFE 1
#define PWN 0
#define PWL 99
#define TRN 0
#define RT0 0
#define BSP 0
#define WOV 0
#define GUP 1
#define NOF 0
#define FBD 8
#define ROTT 0
#define GDS GDL
#define KFS KFD
#define CLN 0
#define KPRIO HPRIO
#define VPRIO HPRIO
#define QRND 1
#define BAR() asm volatile("s_waitcnt lgkmcnt(0)\n\ts_barrier" ::: "memory")
template <int N>
DI void waitv() { asm volatile("s_waitcnt vmcnt(%0)" ::"n"(N) : "memory"); }

DI u64 ld_agent(const char* p) { return __hip_atomic_load((const AS1 u64*)p, RLX_AGENT); }
DI void st_agent(char* p, u64 v) { __hip_atomic_store((AS1 u64*)p, v, RLX_AGENT); }
DI unsigned nz1(unsigned v) { return max(v, 1u); }
DI unsigned umin(unsigned a, unsigned b) { return min(a, b); }
DI u32x4 nz4(u32x4 v) { return (u32x4){max(v[0], 1u), max(v[1], 1u), max(v[2], 1u), max(v[3], 1u)}; }
DI u32x4 nz4(f32x4 v) { return nz4(__builtin_bit_cast(u32x4, v)); }
DI unsigned min4(u32x4 v) { return min(min(v[0], v[1]), min(v[2], v[3])); }
DI u32x4 ld_wt(__amdgpu_buffer_rsrc_t r, unsigned off) { return __builtin_amdgcn_raw_buffer_load_b128(r, off, 0, 16); }
DI void st_wt(__amdgpu_buffer_rsrc_t r, unsigned off, u32x4 v) { __builtin_amdgcn_raw_buffer_store_b128(v, r, off, 0, HSA); }
DI void ld_async(u32x4& v, unsigned off, const char* base) {
    asm volatile("global_load_dwordx4 %0, %1, %2 sc1" : "=&v"(v) : "v"(off), "s"(base) : "memory");
}
#define ST_MASK(op, m, off, v, base) asm volatile("s_mov_b64 exec, %3\n\t" op " %0, %1, %2" HST "\n\ts_mov_b64 exec, -1\n\ts_nop 0" ::"v"(off), "v"(v), "s"(base), "s"((u64)(m)) : "memory")
DI void pin(u32x4& v) { asm volatile("" : "+v"(v)); }
template <int N, class OFF, class BASE, class OK>
DI void settle(u32x4 (&v)[N], OFF off, BASE base, OK ok) {
#pragma unroll
    for (int i = 0; i < N; ++i) pin(v[i]);
#pragma unroll 1
    for (unsigned s = 0; s < SPIN_CAP; ++s) {
        unsigned m = ok(v[0]);
#pragma unroll
        for (int i = 1; i < N; ++i) m = min(m, ok(v[i]));
        if (__ballot(m == 0u) == 0ull) break;
#pragma unroll
        for (int i = 0; i < N; ++i) {
            u64 keep;
            asm volatile("s_mov_b64 %1, exec\n\ts_mov_b64 exec, %4\n\tglobal_load_dwordx4 %0, %2, %3 sc1\n\ts_mov_b64 exec, %1"
                         : "+&v"(v[i]), "=&s"(keep) : "v"(off(i)), "s"(base(i)), "s"((u64)__ballot(ok(v[i]) == 0u)) : "memory");
        }
        waitv<0>();
#pragma unroll
        for (int i = 0; i < N; ++i) pin(v[i]);
    }
}
template <int N, class OFF, class BASE>
DI void settle(u32x4 (&v)[N], OFF off, BASE base) { settle(v, off, base, [](const u32x4& a) { return min4(a); }); }
template <int P, int D, int N, class OFF, class BASE>
DI void settle_p(u32x4 (&v)[N], OFF off, BASE base) {
#pragma unroll 1
    for (unsigned s = 0;; ++s) {
        u64 miss[N], any = 0;
#pragma unroll
        for (int i = 0; i < N; ++i) {
            pin(v[i]);
            miss[i] = __ballot(min4(v[i]) == 0u);
            any |= miss[i];
        }
        if (s >= SPIN_CAP) any = 0;
        asm volatile("" : "+s"(any));
        if (any == 0ull) break;
#pragma unroll
        for (int i = 0; i < N; ++i)
            asm volatile("s_mov_b64 exec, %3\n\tglobal_load_dwordx4 %0, %1, %2" HLD "\n\ts_mov_b64 exec, -1"
                         ::"v"(v[i]), "v"(off(i)), "s"(base(i)), "s"(miss[i] | 1ull) : "memory");
        if (D) __builtin_amdgcn_s_sleep(D);
        waitv<N * (P - 1)>();
    }
}
DI void lds_wait(unsigned a, unsigned n) {
    unsigned v, c;
    asm volatile("s_mov_b32 %1, 0x400000\n"
                 ".Lp%=:\n\tds_read_b32 %0, %2\n\ts_waitcnt lgkmcnt(0)\n\tv_cmp_gt_u32 vcc, %3, %0\n\ts_cbranch_vccz .Lf%=\n\t"
                 "s_sleep 1\n\ts_sub_u32 %1, %1, 1\n\ts_cbranch_scc0 .Lp%=\n.Lf%=:"
                 : "=&v"(v), "=&s"(c) : "v"(a), "s"(n) : "memory", "vcc", "scc");
}
DI void lds_wait0(unsigned a) {
    if (!TGT) { lds_wait(a, 1); return; }
    unsigned v, c;
    asm volatile("s_mov_b32 %1, 0x1000000\n"
                 ".Lp%=:\n\tds_read_b32 %0, %2\n\ts_sub_u32 %1, %1, 1\n\ts_cbranch_scc1 .Lf%=\n\ts_waitcnt lgkmcnt(0)\n\tv_cmp_eq_u32 vcc, 0, %0\n\ts_cbranch_vccnz .Lp%=\n"
                 ".Lf%=:\n\ts_waitcnt lgkmcnt(0)"
                 : "=&v"(v), "=&s"(c) : "v"(a) : "memory", "vcc", "scc");
}
DI void lds_wait8(unsigned a, unsigned n) {
    unsigned v, c;
    asm volatile("s_mov_b32 %1, 0x100000\n"
                 ".Lp%=:\n\tds_read_b32 %0, %2\n\ts_waitcnt lgkmcnt(0)\n\tv_cmp_gt_u32 vcc, %3, %0\n\ts_cbranch_vccz .Lf%=\n\t"
                 "s_sleep 4\n\ts_sub_u32 %1, %1, 1\n\ts_cbranch_scc0 .Lp%=\n.Lf%=:"
                 : "=&v"(v), "=&s"(c) : "v"(a), "s"(n) : "memory", "vcc", "scc");
}
DI void lds_peek(unsigned& v, unsigned a) { asm volatile("ds_read_b32 %0, %1" : "=v"(v) : "v"(a) : "memory"); }
DI void lds_wait(unsigned& v, unsigned a, unsigned n) {
    unsigned c;
    asm volatile("s_mov_b32 %1, 0x400000\n"
                 ".Lp%=:\n\ts_waitcnt lgkmcnt(0)\n\tv_cmp_gt_u32 vcc, %3, %0\n\ts_cbranch_vccz .Lf%=\n\t"
                 "ds_read_b32 %0, %2\n\ts_sub_u32 %1, %1, 1\n\ts_cbranch_scc0 .Lp%=\n.Lf%=:"
                 : "+v"(v), "=&s"(c) : "v"(a), "s"(n) : "memory", "vcc", "scc");
}
DI void lds_set(unsigned a) {
    asm volatile("s_waitcnt lgkmcnt(0)\n\tds_write_b32 %0, %1" ::"v"(a), "v"(1u) : "memory");
}
DI float dot2(unsigned a, unsigned b, float c) {
    return __builtin_amdgcn_fdot2_f32_bf16(__builtin_bit_cast(bf16x2_t, a), __builtin_bit_cast(bf16x2_t, b), c, false);
}
DI float sum4(const float (&a)[4]) {
    float c = swap16_add(swap32_add(a[0], a[1]), swap32_add(a[2], a[3]));
    c = dpp_add<0xB1>(c);
    c = dpp_add<0x4E>(c);
    c = dpp_add<0x141>(c);
    return dpp_add<0x140>(c);
}

template <class OP, class POST>
DI void red4(float& a, float& b, float& c, float& d, OP op, POST post) {
    auto x = __builtin_amdgcn_permlane32_swap(__float_as_uint(a), __float_as_uint(b), false, false);
    auto y = __builtin_amdgcn_permlane32_swap(__float_as_uint(c), __float_as_uint(d), false, false);
    const float m = op(__uint_as_float(x[0]), __uint_as_float(x[1])), n = op(__uint_as_float(y[0]), __uint_as_float(y[1]));
    auto z = __builtin_amdgcn_permlane16_swap(__float_as_uint(m), __float_as_uint(n), false, false);
    const unsigned f = __float_as_uint(post(op(__uint_as_float(z[0]), __uint_as_float(z[1]))));
    auto u = __builtin_amdgcn_permlane16_swap(f, f, false, false);
    auto v = __builtin_amdgcn_permlane32_swap(u[0], u[0], false, false);
    auto t = __builtin_amdgcn_permlane32_swap(u[1], u[1], false, false);
    a = __uint_as_float(v[0]);
    b = __uint_as_float(v[1]);
    c = __uint_as_float(t[0]);
    d = __uint_as_float(t[1]);
}
DI float mx2(float a, float b) { return __builtin_elementwise_maximum(a, b); }
DI float opq(float a) { asm("" : "+v"(a)); return a; }
template <bool PK, class HOOK>
DI void softmax2(const f32x4 (&v)[2][2], u32x4 (&ph)[2][2], HOOK hook) {
    constexpr float L2E = 1.4426950408889634f;
    float mx[2], mn[2], sp_[2], sn_[2];
#pragma unroll
    for (int b = 0; b < 2; ++b) {
        const f32x4 s = v[b][0], t = v[b][1];
        mx[b] = mx2(mx2(mx2(mx2(s[0], s[1]), s[2]), mx2(mx2(s[3], t[0]), t[1])), mx2(t[2], t[3]));
        mn[b] = mx2(mx2(mx2(mx2(-s[0], -s[1]), -s[2]), mx2(mx2(-s[3], -t[0]), -t[1])), mx2(-t[2], -t[3]));
    }
    red4(mx[0], mx[1], mn[0], mn[1], [](float a, float b) { return mx2(a, b); }, [](float a) { return a * -L2E; });
    f32x2 ep[2][4], en[2][4];
#pragma unroll
    for (int b = 0; b < 2; ++b) {
#pragma unroll
        for (int e = 0; e < 4; ++e) {
            const f32x2 u = {v[b][e >> 1][2 * (e & 1)], v[b][e >> 1][2 * (e & 1) + 1]};
            f32x2 a, c;
            if (PK) {
                a = __builtin_elementwise_fma(u, (f32x2){L2E, L2E}, (f32x2){mx[b], mx[b]});
                c = __builtin_elementwise_fma(u, (f32x2){-L2E, -L2E}, (f32x2){mn[b], mn[b]});
            } else {
                a = (f32x2){__builtin_fmaf(u[0], L2E, mx[b]), __builtin_fmaf(u[1], L2E, mx[b])};
                c = (f32x2){__builtin_fmaf(u[0], -L2E, mn[b]), __builtin_fmaf(u[1], -L2E, mn[b])};
            }
            ep[b][e] = (f32x2){__builtin_amdgcn_exp2f(a[0]), __builtin_amdgcn_exp2f(a[1])};
            en[b][e] = (f32x2){__builtin_amdgcn_exp2f(c[0]), __builtin_amdgcn_exp2f(c[1])};
        }
        if (PK) {
            const f32x2 s = (ep[b][0] + ep[b][1]) + (ep[b][2] + ep[b][3]), t = (en[b][0] + en[b][1]) + (en[b][2] + en[b][3]);
            sp_[b] = s[0] + s[1];
            sn_[b] = t[0] + t[1];
        } else {
            sp_[b] = ep[b][0][0];
            sn_[b] = en[b][0][0];
#pragma unroll
            for (int e = 1; e < 8; ++e) sp_[b] += ep[b][e >> 1][e & 1], sn_[b] += en[b][e >> 1][e & 1];
        }
    }
    asm volatile("" : "+v"(sp_[0]), "+v"(sn_[0]), "+v"(sp_[1]), "+v"(sn_[1]));
    hook();
    red4(sp_[0], sp_[1], sn_[0], sn_[1], [](float a, float b) { return a + b; }, [](float a) { return __builtin_amdgcn_rcpf(a); });
#pragma unroll
    for (int b = 0; b < 2; ++b)
#pragma unroll
        for (int e = 0; e < 4; ++e) {
            if (PK) {
                const f32x2 a = ep[b][e] * (f32x2){sp_[b], sp_[b]}, c = en[b][e] * (f32x2){sn_[b], sn_[b]};
                ph[b][0][e] = pack2(a[0], a[1]);
                ph[b][1][e] = pack2(c[0], c[1]);
            } else {
                ph[b][0][e] = pack2(opq(ep[b][e][0] * sp_[b]), ep[b][e][1] * sp_[b]);
                ph[b][1][e] = pack2(opq(en[b][e][0] * sn_[b]), en[b][e][1] * sn_[b]);
            }
        }
}
DI f32x4 rbf4(const f32x4& a) {
    const unsigned u = pack2(a[0], a[1]), v = pack2(a[2], a[3]);
    return (f32x4){lo16(u), hi16(u), lo16(v), hi16(v)};
}
DI int fsw(int row) { return (((row >> 1) & 3) << 2) | (((row >> 3) & 1) << 1) | (row & 1); }
template <bool PK, class HOOK>
DI void phi_k(const f32x4 (&pp)[2][2], const unsigned (&pw)[4], unsigned off, HOOK hook) {
    u32x4 ph[2][2];
    softmax2<PK>(pp, ph, hook);
#pragma unroll
    for (int b = 0; b < 2; ++b)
#pragma unroll
        for (int kf = 0; kf < 2; ++kf)
#pragma unroll
            for (int fb = 0; fb < 2; ++fb) {
                const u32x2 a = {ph[b][kf][2 * fb], ph[b][kf][2 * fb + 1]};
                lds_write<u32x2>(pw[2 * kf + fb] + off + b * 2048, a);
            }
}

DI void phi_k1(const f32x4 (&v)[2], const unsigned (&pw)[4]) {
    constexpr float L2E = 1.4426950408889634f;
    const f32x4 s = v[0], t = v[1];
    float mx = mx2(mx2(mx2(mx2(s[0], s[1]), s[2]), mx2(mx2(s[3], t[0]), t[1])), mx2(t[2], t[3]));
    float mn = mx2(mx2(mx2(mx2(-s[0], -s[1]), -s[2]), mx2(mx2(-s[3], -t[0]), -t[1])), mx2(-t[2], -t[3]));
    red2(mx, mn, [](float a, float b) { return mx2(a, b); }, [](float a) { return a * -L2E; });
    f32x2 ep[4], en[4];
#pragma unroll
    for (int e = 0; e < 4; ++e) {
        const f32x2 u = {v[e >> 1][2 * (e & 1)], v[e >> 1][2 * (e & 1) + 1]};
        const f32x2 a = __builtin_elementwise_fma(u, (f32x2){L2E, L2E}, (f32x2){mx, mx});
        const f32x2 c = __builtin_elementwise_fma(u, (f32x2){-L2E, -L2E}, (f32x2){mn, mn});
        ep[e] = (f32x2){__builtin_amdgcn_exp2f(a[0]), __builtin_amdgcn_exp2f(a[1])};
        en[e] = (f32x2){__builtin_amdgcn_exp2f(c[0]), __builtin_amdgcn_exp2f(c[1])};
    }
    const f32x2 a = (ep[0] + ep[1]) + (ep[2] + ep[3]), c = (en[0] + en[1]) + (en[2] + en[3]);
    float sp = a[0] + a[1], sn = c[0] + c[1];
    red2(sp, sn, [](float a, float b) { return a + b; }, [](float a) { return __builtin_amdgcn_rcpf(a); });
#pragma unroll
    for (int i = 0; i < 4; ++i) {
        const float r = i < 2 ? sp : sn;
        const f32x2 lo = (i < 2 ? ep[2 * i] : en[2 * i - 4]) * (f32x2){r, r}, hi = (i < 2 ? ep[2 * i + 1] : en[2 * i - 3]) * (f32x2){r, r};
        lds_write<u32x2>(pw[i], (u32x2){pack2(lo[0], lo[1]), pack2(hi[0], hi[1])});
    }
}

DI void waitvn(int n) {
#define WV(k) case k: waitv<k>(); break;
#define WV8(k) WV(k) WV(k + 1) WV(k + 2) WV(k + 3) WV(k + 4) WV(k + 5) WV(k + 6) WV(k + 7)
    switch (n < 63 ? n : 63) { WV8(0) WV8(8) WV8(16) WV8(24) WV8(32) WV8(40) WV8(48) WV8(56) }
}
#define SB0() __builtin_amdgcn_sched_barrier(0)
template <int A, int B, class F>
DI void sfor(F f) {
    if constexpr (A < B) {
        f(std::integral_constant<int, A>{});
        sfor<A + 1, B>(f);
    }
}
template <int N> using IC = std::integral_constant<int, N>;
#define AI __attribute__((always_inline))
#define RSK(b, k) do { switch (k) { case 0: RS(b); break; case 1: RS(b + 1); break; case 2: RS(b + 2); break; case 3: RS(b + 3); break; \
    case 4: RS(b + 4); break; case 5: RS(b + 5); break; case 6: RS(b + 6); break; case 7: RS(b + 7); break; } } while (0)

DI void o_store(const u32x4 (&kf)[6], const u32x4 (&ph)[2][2], __amdgpu_buffer_rsrc_t rsrc, unsigned o_st) {
    const f32x4 zero = {0.f, 0.f, 0.f, 0.f};
#pragma unroll
    for (int i = 0; i < 2; ++i) {
        const bf16x8 p0 = __builtin_bit_cast(bf16x8, ph[i][0]), p1 = __builtin_bit_cast(bf16x8, ph[i][1]);
        f32x4 dn = mfma16(__builtin_bit_cast(bf16x8, kf[4]), p0, zero);
        f32x4 n0 = mfma16(__builtin_bit_cast(bf16x8, kf[0]), p0, zero);
        f32x4 n1 = mfma16(__builtin_bit_cast(bf16x8, kf[1]), p0, zero);
        dn = mfma16(__builtin_bit_cast(bf16x8, kf[5]), p1, dn);
        n0 = mfma16(__builtin_bit_cast(bf16x8, kf[2]), p1, n0);
        n1 = mfma16(__builtin_bit_cast(bf16x8, kf[3]), p1, n1);
        const float rd_ = __builtin_amdgcn_rcpf(rbf(rbf(dn[0]) + 1e-6f));
        const u32x4 nb_ = __builtin_bit_cast(u32x4, pack44(n0, n1));
        u32x4 o;
#pragma unroll
        for (int j = 0; j < 4; ++j) o[j] = pack2(lo16(nb_[j]) * rd_, hi16(nb_[j]) * rd_);
        st_wt(rsrc, o_st + i * 1024, nz4(o));
    }
}

typedef __attribute__((ext_vector_type(16))) unsigned u32x16;
__global__ __launch_bounds__(512, 2) void fused_mix(const u16*, const u16*, const u16*, const u16*, const u16*, const u16*, const u16*, const u16*,
                                                    const u16*, char*, u16*) {
    __shared__ __attribute__((aligned(16))) char smem[163840];
    u32x4 ka;
    u32x16 kb_;
    u64 kz;
    asm volatile("s_load_dwordx4 %0, %3, 0x0\n\ts_load_dwordx16 %1, %3, 0x10\n\ts_load_dwordx2 %2, %3, 0x50"
                 : "=&s"(ka), "=&s"(kb_), "=&s"(kz) : "s"(__builtin_amdgcn_kernarg_segment_ptr()));
    SB0();
    const int tid = threadIdx.x;
    int l = tid & 63;
    const int w = __builtin_amdgcn_readfirstlane(tid >> 6) ^ 4;
    int bid = blockIdx.x;
    const unsigned lds0 = lds_of(smem);
    int g = l >> 4, l15 = l & 15, q = l15 >> 2, p = l & 3;
    int srow = l >> 3, sch = l & 7;
    unsigned l16 = l * 16;
    unsigned vst = 0;
    const int rg = w & 3, kp = w >> 2;
    const int hs = rg >> 1;
    RS(0);
    if (CPF) {
        u64 pc;
        asm volatile("s_getpc_b64 %0\n\ts_mov_b32 m0, %2\n\ts_mov_b64 exec, 3\n\tglobal_load_lds_dword %1, %0\n\ts_mov_b64 exec, -1"
                     : "=&s"(pc) : "v"(min((unsigned)(((bid & ~7) + w) * 2 + l) * 128u, (unsigned)CPF)), "s"(lds0 + L_SM + 3584) : "memory");
    }
    lds_write<unsigned>(lds0 + L_SM + tid * 4, 0u);
    const int slot = bid >> 3;
    const int mt = (bid & 1) * 8 + (slot & 7);
    const int hp = ((bid >> 1) & 3) * 4 + (slot >> 3);
    unsigned vx[4], vw[4];
    auto vw_init = [&]() AI {
        int t = l;
        asm volatile("" : "+v"(t));
#pragma unroll
        for (int i = 0; i < 4; ++i) vw[i] = (unsigned)((8 * i + (t >> 3)) * 6144 + ((t & 7) ^ (((t >> 4) & 1) << 1) ^ ((i & 1) << 2)) * 16 + 4096 - i * 1024);
    };
    auto vx_init = [&]() AI {
        int t = l;
        asm volatile("" : "+v"(t));
#pragma unroll
        for (int i = 0; i < 4; ++i) vx[i] = (unsigned)((8 * i + (t >> 3)) * 2048 + (((t & 7) ^ ((4 * (i & 1) + (t >> 4)) & 7)) * 16) + 4096 - i * 1024);
    };
    vx_init();
    vw_init();
    asm volatile("" : "+v"(vx[0]), "+v"(vx[1]), "+v"(vx[2]), "+v"(vx[3]), "+v"(vw[0]), "+v"(vw[1]), "+v"(vw[2]), "+v"(vw[3]));
    const int ksa = SLK * (slot & 7), ksb = KSK * ((slot + (slot >> 3)) & (NPH - 1));
    unsigned xo0 = (mt * 128 + hs * 64) * 2048, wo0 = hp * 128 + hs * (32 * 6144);
    asm volatile("s_waitcnt lgkmcnt(0)" : "+s"(ka), "+s"(kb_), "+s"(kz), "+s"(xo0), "+s"(wo0) :: "memory");
    RS(48);
    const u16* x = (const u16*)((u64)ka[1] << 32 | ka[0]);
    const u16* wqkv = (const u16*)((u64)ka[3] << 32 | ka[2]);
    auto arg = [&](int i) { return (char*)((u64)kb_[2 * i + 1] << 32 | kb_[2 * i]); };
    const u16 *wphi = (const u16*)arg(0), *wo = (const u16*)arg(1), *wr = (const u16*)arg(2), *wg = (const u16*)arg(3);
    const u16 *wu = (const u16*)arg(4), *wd = (const u16*)arg(5), *gw = (const u16*)arg(6);
    char* ws = arg(7);
    u16* out = (u16*)kz;
    const int sc = (3 - (int)((unsigned)(size_t)wqkv >> 7)) & 7;
    const unsigned* one = k_num;
    unsigned v0 = 0;
    asm volatile("" : "+s"(one), "+v"(v0));
    auto mark = [&](unsigned dst, int n = 1, int lanes = 1) AI {
        asm volatile("s_mov_b32 m0, %2\n\ts_mov_b64 exec, %4\n\tglobal_load_lds_dword %0, %1 offset:%3\n\ts_mov_b64 exec, -1"
                     ::"v"(v0), "s"(one), "s"(dst - 4 * n), "n"(4 * n), "n"(lanes) : "memory");
    };
    auto four = [&](const unsigned (&v)[4], const char* src, unsigned dst, int nt = 0) AI {
#define FOUR(P) asm volatile("s_mov_b32 m0, %5\n\ts_nop 0\n\t" \
                     "global_load_lds_dwordx4 %0, %4" P "\n\tglobal_load_lds_dwordx4 %1, %4 offset:1024" P "\n\t" \
                     "global_load_lds_dwordx4 %2, %4 offset:2048" P "\n\tglobal_load_lds_dwordx4 %3, %4 offset:3072" P \
                     ::"v"(v[0]), "v"(v[1]), "v"(v[2]), "v"(v[3]), "s"(src), "s"(dst) : "memory")
        if (nt == 1) FOUR(" nt");
        else if (nt == 2) FOUR(" sc1");
        else if (nt == 3) FOUR(" sc0 sc1");
        else FOUR("");
    };
    const int ks0 = (((slot + (slot >> 3)) & KXM) | (KXO == 2 || (KXO == 3 && (hp & 7) == sc) ? slot & 4 : 0)) ^ ((bid & 1) * KXP) ^ (((bid >> 1) & 3) * KXQ);
    const char* xb0 = (const char*)x + xo0 - 4096;
    const char* wb0 = (const char*)wqkv + wo0 - 4096;
    auto fill = [&](auto N) AI {
        constexpr int n = N, j = F_J[n] < 0 ? 0 : F_J[n], xi = F_J[n] < 0 ? -1 - F_J[n] : 0, s = F_J[n] < 0 ? x_stage(xi) : J_S[j], m = J_M[j];
        const int ks = KXO ? ks0 ^ (int)((KXB >> (4 * s)) & 15) : (ks0 + s) & 15;
        if constexpr (n >= NF1 && n < NF2) {
            if constexpr (F_J[n] >= 0) {
                asm volatile("s_mov_b32 m0, %3\n\ts_nop 0\n\tglobal_load_lds_dwordx4 %0, %2\n\tglobal_load_lds_dwordx4 %1, %2 offset:1024"
                             ::"v"(vw[0]), "v"(vw[1]), "s"(wb0 + (ks * 64 + (w & 1) * 16) * 6144), "s"(lds0 + J_U[j] * 8192 + w * 2048) : "memory");
            } else four(vx, xb0 + ks * 128 + (w & 1) * (32 * 2048), lds0 + (hs ? X_U1[xi] : X_U0[xi]) * 8192 + (w & 1) * 4096);
            if constexpr (F_K[n] > 1) mark(lds0 + L_FULL + 16 + w * 4, F_K[n]);
            return;
        }
        if constexpr (F_J[n] >= 0) {
            four(vw, wb0 + ks * (64 * 6144) + (m == 2 ? 0 : 2048 * (m + 1)), lds0 + J_U[j] * 8192 + hs * 4096, s < NTS ? NTW : 0);
        } else {
            const char* xs_ = xb0 + ks * 128;
            const unsigned d = lds0 + (hs ? X_U1[xi] : X_U0[xi]) * 8192;
            four(vx, xs_, d, s < NTS ? NTX : 0);
            four(vx, xs_ + 32 * 2048, d + 4096, s < NTS ? NTX : 0);
        }
        if constexpr (F_K[n] > 1) mark(lds0 + L_FULL + (n < NF1 ? 0 : 32) + hs * 8, F_K[n], 3);
    };
    static_assert(F_K[1] == 1, "");
    if (__builtin_expect((w & 5) == 4, 1)) sfor<0, 2>(fill);
    RS(49);
    unsigned sd = 0, st;
    asm volatile("s_cmp_lg_u32 %2, 5\n\ts_cbranch_scc1 .La%=\n\t"
                 "s_min_u32 %1, %3, 23\n\ts_lshl_b32 %1, %1, 18\n\ts_load_dword %0, %5, %1\n\ts_load_dword %0, %6, 0x0\n\t"
                 "s_and_b32 %1, %1, 0x3c0000\n\ts_load_dword %0, %4, %1\n\ts_load_dword %0, %10, %1\n\t"
                 "s_load_dword %0, %7, 0x0\n\ts_load_dword %0, %8, 0x0\n\ts_load_dword %0, %9, 0x0\n.La%=:"
                 : "+s"(sd), "=&s"(st) : "s"(w), "s"(slot), "s"(x), "s"(wqkv), "s"(one), "s"(wr), "s"(gw), "s"(wphi), "s"(out) : "memory", "scc");
    asm volatile("s_cmp_lg_u32 %2, 7\n\ts_cbranch_scc1 .La%=\n\t"
                 "s_min_u32 %1, %3, 27\n\ts_lshl_b32 %1, %1, 18\n\ts_load_dword %0, %4, 0x0\n\ts_load_dword %0, %4, %1\n\t"
                 "s_load_dword %0, %5, %1\n\ts_load_dword %0, %6, %1\n\ts_load_dword %0, %7, %1\n\t"
                 "s_and_b32 %1, %1, 0x1c0000\n\ts_load_dword %0, %8, %1\n.La%=:\n\ts_barrier"
                 : "+s"(sd), "=&s"(st) : "s"(w), "s"(slot), "s"(ws), "s"(wg), "s"(wu), "s"(wd), "s"(wo) : "memory", "scc");
    RS(50);
    if (__builtin_expect((w & 5) == 4, 1)) {
        mark(lds0 + L_FULL + hs * 8, 1, 3);
        sfor<2, NF0>(fill);
    }
    RS(51);
    asm volatile("" : "+s"(bid));
    const int xcd = bid & 7;
    const int rt = BMAP ? (xcd & 1) * 8 + ((bid >> 3) & 7) : (xcd >> 1) * 4 + ((bid >> 3) & 3);
    const int cb = BMAP ? (((xcd >> 1) + BMAP - 1) & 3) * 4 + (bid >> 6) : (xcd & 1) * 8 + (bid >> 5), rot = ROTT ? 2 * sc + 2 + ((ROTT >> (8 * (rt & 3))) & 255) : ROTC * cb + ROTK * rt + ROT0 * (xcd >> 1);
    const __amdgpu_buffer_rsrc_t rsrc = __builtin_amdgcn_make_buffer_rsrc(ws, 0, WS_END, 0x00020000);
    const u32x4 z4 = {0u, 0u, 0u, 0u};
    const f32x4 zero = {0.f, 0.f, 0.f, 0.f};
    unsigned AX = lds0 + L_A + rg * 4096 + l16;
    unsigned W4 = lds0 + p * 4;
    unsigned PG = lds0 + L_PROG + p * 256;
    const unsigned WR = lds0 + rg * 4;
    auto flag = [&](unsigned a) AI { asm volatile("ds_write_b32 %0, %0" ::"v"(a) : "memory"); };
    auto phi_w = [&](bf16x8 (&a)[2], int h) AI {
        const unsigned ad = lds0 + L_WPHI + h * 2048 + (4 * g + q) * 64 + (((p >> 1) ^ (2 * (g & 1))) * 16) + 8 * (p & 1);
        a[0] = cat8(tr_read(ad), tr_read(ad + 1024));
        a[1] = cat8(tr_read(ad ^ 32), tr_read((ad ^ 32) + 1024));
    };
    u32x4 fw[32], fd[16], gu[2];
    unsigned sel_[4][2];
    u32x4 ph[2][2], sq;
    const int hp2 = ((bid >> 1) & 3) * 4 + (bid >> 6);
    const bool ffn = (hp2 & 7) != sc && !(NOF & 1);
    const int ph4 = ((bid >> 3) + (bid >> 6)) & 3;
    const int turn = (ph4 + TRN) & TRM;
    const int kq = mt >> 2;
    const int cs = (ffn ? hp2 - (hp2 > sc) - (hp2 > sc + 8) : 14 + (hp2 >> 3)) * 4 + ((ph4 + CPH - (int)((unsigned)(size_t)wg >> 7)) & 3);
    auto spread = [&](const u32x4& v, int i) -> bf16x8 {
        const unsigned s = v[i >> 1];
        u32x4 a;
#pragma unroll
        for (int d = 0; d < 4; ++d) a[d] = __builtin_amdgcn_perm(s, s, sel_[d][i & 1]);
        return __builtin_bit_cast(bf16x8, a);
    };
    auto fold_store = [&](const f32x4& a, unsigned off) {
        f32x4 v;
#pragma unroll
        for (int i = 0; i < 4; ++i) {
            const unsigned u = __float_as_uint(a[i]);
            const float r = __uint_as_float(__builtin_amdgcn_update_dpp(u, u, 0x128, 0xc, 0xf, false));
            v[i] = swap32_add(r, r);
        }
        const u32x4 o = nz4(v);
        ST_MASK("global_store_dwordx4", 0x00FF00FFull, off + l15 * 32 + g * 16, o, ws);
    };
    auto sel_init = [&]() AI {
#pragma unroll
        for (int d = 0; d < 4; ++d)
#pragma unroll
            for (int hf = 0; hf < 2; ++hf) {
                const unsigned b = hf ? 0x0302u : 0x0100u;
                sel_[d][hf] = (sch >> 1) != d ? 0x0c0c0c0cu : (l & 1) ? (b << 16) | 0x0c0cu : 0x0c0c0000u | b;
            }
    };
    auto gua = [&](int i) { return ws + WS_GU + cs * 4096 + i * 1024; };
    auto gu_load = [&]() AI {
#pragma unroll
        for (int i = 0; i < 2; ++i) ld_async(gu[i], l16, gua(i));
    };
    auto down = [&]() AI {
#pragma unroll
        for (int i = 0; i < 16; ++i) pin(fd[i]);
        settle(gu, [&](int) { return l16; }, gua);
        const f32x4 v = __builtin_bit_cast(f32x4, gu[0]) + __builtin_bit_cast(f32x4, gu[1]);
        auto r = __builtin_amdgcn_permlane16_swap(__float_as_uint(swap32_add(v[0], v[2])), __float_as_uint(swap32_add(v[1], v[3])), false, false);
        const float gt = rbf(__uint_as_float(r[0])), up = rbf(__uint_as_float(r[1]));
        const unsigned ha = lds0 + L_XS + hs * 512;
        lds_write<u16>(ha + l15 * 8 + g * 2, f2bf(rbf(gt / (1.f + __expf(-gt))) * up));
        const u32x4 hv = lds_read<u32x4>(ha + srow * 16);
        f32x4 y0 = zero, y1 = zero;
#pragma unroll
        for (int i = 0; i < 8; ++i) {
            const bf16x8 a = spread(hv, i);
            y0 = mfma16(a, __builtin_bit_cast(bf16x8, fd[i]), y0);
            y1 = mfma16(a, __builtin_bit_cast(bf16x8, fd[8 + i]), y1);
        }
        fold_store(y0, WS_Y + cs * 4096 + kq * 1024 + hs * 512);
        fold_store(y1, WS_Y + cs * 4096 + kq * 1024 + hs * 512 + 256);
    };
    auto need = [&](int c) AI {
        if (c == 101) lds_wait(W4 + L_FKS, 1);
        else if (c == 100) lds_wait(W4 + L_FV, 1);
        else if (c == 99) lds_wait(W4 + L_FQD, 1);
        else if (c > 0) lds_wait(c > RLD ? W4 + L_PRL : PG, c);
    };
    auto wo_load = [&](int n0, int n1, bool mk) AI {
        unsigned vo[4];
#pragma unroll
        for (int i = 0; i < 4; ++i) vo[i] = (unsigned)((8 * i + srow) * 2048 + ((sch ^ ((g & 3) << 1)) * 16) + 4096 - i * 1024);
        const char* wob = (const char*)wo + cb * 128 - 4096;
#pragma unroll
        for (int n = n0; n < ((NOF & 4) ? n0 : n1); ++n) {
            if (n < WON && (n == 0 || W_N[n] != W_N[n - 1])) need(W_N[n]);
            four(vo, wob + ((2 * n + hs + rot) & 31) * 65536, lds0 + wo_unit(n) * 8192 + hs * 4096);
        }
        if (mk) mark(lds0 + (n0 >= WON && WON ? L_FW2 : L_FWD) + hs * 4);
    };
    auto phi_adr = [&](unsigned (&pw)[4], unsigned base) AI {
#pragma unroll
        for (int i = 0; i < 4; ++i) pw[i] = (base + rg * 4096 + l15 * 128 + ((g ^ fsw(l15)) << 3)) ^ (i << 5);
    };
    const int row0 = rt * 128 + rg * 32 + kp * 16;
    u32x4 xr[2];
    u64 gg[2];
    unsigned xo = srow * 2048 + sch * 16;
    auto g_load = [&]() AI {
        asm volatile("global_load_dwordx2 %0, %2, %3 sc1\n\tglobal_load_dwordx2 %1, %2, %3 offset:64 sc1"
                     : "=&v"(gg[0]), "=&v"(gg[1]) : "v"((unsigned)srow * 8), "s"(ws + WS_GG + row0 * 8) : "memory");
    };
    auto xg_load = [&](int skip) AI {
        asm volatile("s_cmp_eq_u32 %4, 4\n\ts_cbranch_scc1 .Lk%=\n\t"
                     "global_load_dwordx4 %0, %5, %6\n\tglobal_load_dwordx4 %1, %5, %7\n\t"
                     "global_load_dwordx2 %2, %8, %9 sc1\n\tglobal_load_dwordx2 %3, %8, %9 offset:64 sc1\n.Lk%=:"
                     : "+v"(xr[0]), "+v"(xr[1]), "+v"(gg[0]), "+v"(gg[1])
                     : "s"(skip), "v"(xo), "s"(x + (size_t)row0 * DM + cb * 64), "s"(x + (size_t)(row0 + 8) * DM + cb * 64),
                       "v"((unsigned)srow * 8), "s"(ws + WS_GG + row0 * 8) : "memory", "scc");
    };
    auto xg_def = [&]() AI { asm volatile("" : "=v"(xr[0]), "=v"(xr[1]), "=v"(gg[0]), "=v"(gg[1])); };
    auto relane = [&]() AI {
        unsigned z = 0;
        asm volatile("" : "+v"(z));
        l = __builtin_amdgcn_mbcnt_hi(~0u, __builtin_amdgcn_mbcnt_lo(~0u, z));
        g = l >> 4, l15 = l & 15, q = l15 >> 2, p = l & 3, srow = l >> 3, sch = l & 7, l16 = l * 16;
        AX = lds0 + L_A + rg * 4096 + l16, W4 = lds0 + p * 4, PG = lds0 + L_PROG + p * 256, xo = srow * 2048 + sch * 16;
    };
    u32x4 kf[6];
    auto kf_def = [&]() AI { asm volatile("" : "=v"(kf[0]), "=v"(kf[1]), "=v"(kf[2]), "=v"(kf[3]), "=v"(kf[4]), "=v"(kf[5])); };
    const int oh = hs ^ kp ^ 1;

    if (__builtin_expect(w >= 4, 1)) {
        const bool loader = !(w & 1);
        const unsigned kf_kv = WS_KF + (hp * 2 + (mt >> 3)) * 8192 + (mt & 7) * 512;
        const unsigned kf_z = WS_KF + (hp * 2 + (mt >> 3)) * 8192 + 4096 + (ZL ? (mt & 7) * 128 : ((mt >> 2) & 1) * 64);
        const unsigned fgo = ffn ? (unsigned)((256 * kq + 16 * srow) * 7168 + cs * 128 + sch * 16) : 0u;
        const char* fsrc = (const char*)(hs ? wu : wg);
        auto gu_w = [&](int q0, int q1) AI {
#pragma unroll
            for (int i = q0; i < q1; ++i) {
                unsigned t1, t2, t3;
                if (NOF & 8) fw[4 * i] = fw[4 * i + 1] = fw[4 * i + 2] = fw[4 * i + 3] = z4; else
                asm volatile("v_add_u32 %4, 0x1c00, %7\n\tv_add_u32 %5, 0x3800, %7\n\tv_add_u32 %6, 0x5400, %7\n\t"
                             "global_load_dwordx4 %0, %7, %8 nt\n\tglobal_load_dwordx4 %1, %4, %8 nt\n\t"
                             "global_load_dwordx4 %2, %5, %8 nt\n\tglobal_load_dwordx4 %3, %6, %8 nt"
                             : "=&v"(fw[4 * i]), "=&v"(fw[4 * i + 1]), "=&v"(fw[4 * i + 2]), "=&v"(fw[4 * i + 3]), "=&v"(t1), "=&v"(t2), "=&v"(t3)
                             : "v"(fgo), "s"(fsrc + (128 * (i >> 2) + 4 * (i & 3)) * 7168) : "memory");
            }
        };
        const unsigned fdo = ffn ? (unsigned)((64 * cs + 8 * srow) * 2048 + 512 * kq + 256 * hs + sch * 16) : 0u;
        auto dn_w = [&](int q0, int q1) AI {
#pragma unroll
            for (int i = q0; i < q1; ++i)
                if (NOF & 8) fd[4 * i] = fd[4 * i + 1] = fd[4 * i + 2] = fd[4 * i + 3] = z4; else
                asm volatile("global_load_dwordx4 %0, %4, %5 offset:-4096 nt\n\tglobal_load_dwordx4 %1, %4, %5 offset:-2048 nt\n\t"
                             "global_load_dwordx4 %2, %4, %5 nt\n\tglobal_load_dwordx4 %3, %4, %5 offset:2048 nt"
                             : "=&v"(fd[4 * i]), "=&v"(fd[4 * i + 1]), "=&v"(fd[4 * i + 2]), "=&v"(fd[4 * i + 3])
                             : "v"(fdo), "s"((const char*)wd + 4096 + (i & 1) * 8192 + (i >> 1) * 128) : "memory");
        };
        auto gate_up = [&]() AI {
            sel_init();
#pragma unroll
            for (int i = 0; i < 32; ++i) pin(fw[i]);
            u32x4 xs[4];
#pragma unroll
            for (int i = 0; i < 4; ++i) xs[i] = lds_read<u32x4>(lds0 + L_XS + hs * 512 + (i >> 1) * 256 + srow * 32 + (i & 1) * 16);
            f32x4 a = zero;
#pragma unroll
            for (int i = 0; i < 32; ++i) a = mfma16(spread(xs[i >> 3], i & 7), __builtin_bit_cast(bf16x8, fw[i]), a);
            fold_store(a, WS_GU + cs * 4096 + kq * 512 + hs * 256);
            SB0();
        };
        auto o_clear = [&]() AI {
#pragma unroll
            for (int n = 0; n < ((NOF & 2) ? 1 : 8); ++n) st_wt(rsrc, WS_O + ((mt * 2 + (n >> 2)) * 32 + 2 * hp + hs) * 4096 + (n & 3) * 1024 + l16, z4);
        };
        u64 T;
        u32x4 sv[2];
        auto clears = [&]() AI {
            if (!OCL) o_clear();
#pragma unroll
            for (int n = 0; n < ((NOF & 16) ? 0 : 4); ++n) st_wt(rsrc, WS_KV + ((hp * 8 + hs * 4 + n) * 16 + mt) * 1024 + l16, z4);
            ST_MASK("global_store_dwordx4", 0xFFFFull, WS_Z + ((hp * 4 + hs * 2 + (srow & 1)) * 16 + mt) * 128 + sch * 16, z4, ws);
        };
        auto cleared = [&]() AI {
            if (CLL) ST_MASK("global_store_dwordx2", 0xFFFFull, (unsigned)l * 8, T, ws + WS_C2 + ((mt * 2 + hs) * 16 + hp) * 128);
            else ST_MASK("global_store_dwordx2", 1ull, 0u, T, ws + WS_CL + ((mt * 2 + hs) * 16 + hp) * 8);
        };
        auto rg_load = [&]() AI {
            asm volatile("global_load_dwordx4 %0, %2, %3 sc1\n\tglobal_load_dwordx4 %1, %2, %3 offset:16 sc1"
                         : "=&v"(sv[0]), "=&v"(sv[1]) : "v"(l * 32), "s"(ws + WS_RG) : "memory");
        };
        auto select = [&]() AI {
            const unsigned tl = (unsigned)T & 0x1FFFu, thi = (unsigned)(T >> 13);
#pragma unroll 1
            for (unsigned s = 0; s < SPIN_CAP; ++s) {
                pin(sv[0]);
                pin(sv[1]);
                const unsigned bad = ((sv[0][0] >> 19) ^ tl) | (sv[0][1] ^ thi) | ((sv[0][2] >> 19) ^ tl) | (sv[0][3] ^ thi) |
                                     ((sv[1][0] >> 19) ^ tl) | (sv[1][1] ^ thi) | ((sv[1][2] >> 19) ^ tl) | (sv[1][3] ^ thi);
                if (__all(bad == 0u)) break;
                __builtin_amdgcn_s_sleep(16);
                rg_load();
                waitv<0>();
            }
            pin(sv[0]);
            pin(sv[1]);
            RS(4);
            unsigned k2 = 0;
#pragma unroll
            for (int j = 0; j < 4; ++j) {
                const unsigned v = sv[j >> 1][(j & 1) * 2] & 0x7FFFFu;
                k2 = max(k2, ((v >> 3) << 11) | ((255 - (4 * l + j)) << 3) | (v & 7));
            }
            const unsigned key = __builtin_amdgcn_readfirstlane(wave_umax(k2));
            asm volatile("s_mov_b32 m0, %2\n\ts_mov_b64 exec, %3\n\tglobal_load_lds_dwordx4 %0, %1\n\ts_mov_b64 exec, -1"
                         ::"v"((unsigned)(512 * kq) + l16), "s"(x + (size_t)(2047 - (int)(key & 2047u)) * DM), "s"(lds0 + L_XS + hs * 512), "s"(0xFFFFFFFFull) : "memory");
            unsigned rb = key >> 11;
            rb ^= (rb & 0x8000u) ? 0x8000u : 0xFFFFu;
            const float rsel = rbf(1.f / (1.f + __expf(-bf2f((u16)rb))));
            lds_write<u32x4>(lds0 + L_SEL, (u32x4){(unsigned)T, (unsigned)(T >> 32), 2047u - (key & 2047u), __float_as_uint(rsel)});
            if (bid == 0 && hs) ST_MASK("global_store_dwordx2", 1ull, 0u, T, ws + WS_E);
            RS(5);
        };
        auto k_soft = [&](int np) AI {
            bf16x8 a0[2], kb[2];
            f32x4 pk[2][2][2];
            unsigned pw[4];
            phi_w(a0, 0);
            phi_adr(pw, lds0 + P_U[0] * 8192);
            asm volatile("" : "+v"(pw[0]), "+v"(pw[1]), "+v"(pw[2]), "+v"(pw[3]));
            auto logits = [&](int h) AI {
                lds_wait(WR + L_FK0 + 4 * h, 1);
#pragma unroll
                for (int i = 0; i < 2; ++i) kb[i] = lds_read128(AX + (P_U[0] * 8192 - L_A) + h * 4096 + i * 1024);
#pragma unroll
                for (int i = 0; i < 4; ++i) pk[h][i >> 1][i & 1] = mfma16(a0[i & 1], kb[i >> 1], zero);
            };
            auto round = [&](int h) AI {
#pragma unroll
                for (int i = 0; i < 4; ++i) if (KRND) pk[h][i >> 1][i & 1] = rbf4(pk[h][i >> 1][i & 1]);
            };
            logits(0);
            RS(12);
            round(0);
            SB0();
            phi_k<KPK & 1>(pk[0], pw, 0, [&]() AI {
                if (np > 1) logits(1);
                SB0();
            });
            SB0();
            if (np > 1) {
                round(1);
                phi_k<(KPK >> 1)>(pk[1], pw, 4096, []() {});
                SB0();
            }
            flag(WR + L_FP);
        };
        if (__builtin_expect(loader, 1)) {
            vx_init();
            vw_init();
            if (LPRIO) __builtin_amdgcn_s_setprio(LPRIO);
            unsigned pv = 0;
            sfor<NF0, NF1>([&](auto N) {
                constexpr int n = N;
                if constexpr (n > 0 && F_N[n] > F_N[n > 0 ? n - 1 : 0]) {
                    RSL(32 + F_N[n]);
                    if (LPRW != LPRIO) __builtin_amdgcn_s_setprio(LPRW);
                    if (NOF & 256) {
                    } else if (F_N[n] > RLD) {
                        lds_wait(W4 + L_PRL, F_N[n]);
                    } else if (PEEK) {
                        lds_wait(pv, PG, F_N[n]);
                        if (F_N[n] < F_N[NF1 - 1] && next_need(n) <= RLD) lds_peek(pv, PG);
                    } else lds_wait(PG, F_N[n]);
                    if (LPRW != LPRIO) __builtin_amdgcn_s_setprio(LPRIO);
                    RSL(48 + F_N[n]);
                }
                if constexpr (n == 12) {
                    asm volatile("s_mov_b32 m0, %2\n\ts_nop 0\n\tglobal_load_lds_dwordx4 %0, %1\n\tglobal_load_lds_dwordx4 %0, %1 offset:1024"
                                 ::"v"((unsigned)((l >> 2) * 64 + (((l & 3) ^ (2 * ((l >> 4) & 1))) * 16))),
                                   "s"(wphi + (hp * 2 + hs) * 1024), "s"(lds0 + L_WPHI + hs * 2048) : "memory");
                }
                if constexpr (PFN > 0 && F_J[n] < 0 && ((PFS >> (-1 - F_J[n])) & 1)) {
                    constexpr int e = __builtin_popcount(PFS & ((1 << (-1 - F_J[n])) - 1));
                    if constexpr (PFN == 32) {
                        asm volatile("s_mov_b32 m0, %2\n\ts_mov_b64 exec, %3\n\tglobal_load_lds_dword %0, %1" PFM "\n\ts_mov_b64 exec, -1"
                                     ::"v"(ffn ? (unsigned)((64 * cs + 16 * e + 8 * hs + (l >> 2)) * 2048 + 512 * kq + p * 128) : 0u), "s"(wd), "s"(lds0 + L_SM + 3584), "s"(0xFFFFFFFFull) : "memory");
                    } else {
                        asm volatile("s_mov_b32 m0, %2\n\ts_nop 0\n\tglobal_load_lds_dword %0, %1" PFM
                                     ::"v"(ffn ? (unsigned)((256 * kq + 64 * e + l) * 7168 + cs * 128) : 0u), "s"(fsrc), "s"(lds0 + L_SM + 3584) : "memory");
                    }
                }
                fill(N);
            });
            flag(lds0 + L_FLD + hs * 4);
            RS(1);
            __builtin_amdgcn_s_setprio(KPRIO);
            const char* cla = ws + (CLL ? WS_C2 : WS_CL) + ((l15 * 2 + (mt >> 3)) * 16 + hp) * (CLL ? 128 : 8);
            u64 cl;
            if (KCL) cl = 0;
            else if (CLE) asm volatile("global_load_dwordx2 %0, %1, off sc1" : "=&v"(cl) : "v"(cla) : "memory");
            k_soft(SSM ? 1 : 2);
            __builtin_amdgcn_s_setprio(HPRIO);
            if (!CLE) asm volatile("global_load_dwordx2 %0, %1, off sc1" : "=&v"(cl) : "v"(cla) : "memory");
            RS(11);
            lds_wait(W4 + L_FP, 1);
            sq = lds_read<u32x4>(lds0 + L_SEL);
            waitv<0>();
            asm volatile("" : "+v"(cl));
            RS(13);
            u32x4 kp_[5 + ZL];
            {
                const char* kb = ws + WS_KV + (hp * 8 + (mt >> 1)) * 16384 + (mt & 1) * 512 + hs * 256;
                const bool zw = !((mt & 3) | (ZL & hs));
                const char* zb = zw ? ws + WS_Z + (hp * 4 + (mt >> 2)) * 2048 + (ZL ? 0 : hs * 64) : kb;
                const unsigned kvo = g * 1024 + l15 * 16, zo = zw ? (ZL ? l16 : l15 * 128 + g * 16) : 0u;
                auto ko = [&](int i) { return i < 4 ? kvo : zo; };
                auto ka = [&](int i) { return i < 4 ? kb + i * 4096 : zb + (i - 4) * 1024; };
                const u64 T = (u64)sq[1] << 32 | sq[0];
#pragma unroll 1
                for (unsigned s = 0; !KCL && s < SPIN_CAP && __ballot(cl != T); ++s)
                    asm volatile("global_load_dwordx2 %0, %1, off sc1\n\ts_waitcnt vmcnt(0)" : "=&v"(cl) : "v"(cla) : "memory");
                lds_wait0(W4 + L_FKS);
                __builtin_amdgcn_s_sleep(GDS);
                if (GDL > GDS && ffn) __builtin_amdgcn_s_sleep(GDL > GDS ? GDL - GDS : 0);
#pragma unroll
                for (int i = 0; i < 5 + ZL; ++i) { if (PDI & 1) kp_[i] = z4; else ld_async(kp_[i], ko(i), ka(i)); }
                if (XGE) {
                    xg_def();
                    xg_load(w + 1);
                }
                flag(lds0 + L_FGI + hs * 4);
                SB0();
                RS(14);
                if (!(PDI & 1)) waitv<0>();
                RS(15);
                if (PP1) settle_p<PP1 + !PP1, PD1>(kp_, ko, ka);
                else settle(kp_, ko, ka);
                RS(24);
                float v[8], u[4];
#pragma unroll
                for (int e = 0; e < 4; ++e) {
                    v[2 * e] = (lo16(kp_[0][e]) + lo16(kp_[1][e])) + (lo16(kp_[2][e]) + lo16(kp_[3][e]));
                    v[2 * e + 1] = (hi16(kp_[0][e]) + hi16(kp_[1][e])) + (hi16(kp_[2][e]) + hi16(kp_[3][e]));
                }
#pragma unroll
                for (int e = 0; e < 4; ++e) u[e] = swap32_add(v[e], v[e + 4]);
                const unsigned o = nz1(pack2(swap16_add(u[0], u[2]), swap16_add(u[1], u[3])));
                ST_MASK("global_store_dword", ~0ull, kf_kv + hs * 256 + l15 * 16 + g * 4, o, ws);
                if (zw && ZL) {
                    f32x4 z = __builtin_bit_cast(f32x4, kp_[4]) + __builtin_bit_cast(f32x4, kp_[4 + ZL]);
#pragma unroll
                    for (int i = 0; i < 4; ++i) {
                        float t = dpp_add<0x128>(z[i]);
                        t = swap16_add(t, t);
                        z[i] = swap32_add(t, t);
                    }
                    const u32x2 oz = {nz1(pack2(z[0], z[1])), nz1(pack2(z[2], z[3]))};
                    ST_MASK("global_store_dwordx2", 0xFFFFull, kf_z + (l & 8) * 8 + p * 16 + (sch >> 2) * 8, oz, ws);
                } else if (zw) {
                    f32x4 z;
#pragma unroll
                    for (int i = 0; i < 4; ++i) z[i] = dpp_add<0x140>(dpp_add<0x141>(dpp_add<0x4E>(dpp_add<0xB1>(__uint_as_float(kp_[4][i])))));
                    const u32x2 oz = {nz1(pack2(z[0], z[1])), nz1(pack2(z[2], z[3]))};
                    ST_MASK("global_store_dwordx2", 0x0001000100010001ull, kf_z + g * 16 + hs * 8, oz, ws);
                }
                flag(lds0 + L_FG + hs * 4);
            }
            RS(25);
            const char* kfb = ws + WS_KF + (hp * 2 + hs) * 8192;
            auto fo = [&](int i) { return i < 4 ? l16 : (unsigned)g * 16; };
            auto fb = [&](int i) { return i < 4 ? kfb + i * 1024 : kfb + 4096 + (i - 4) * (ZL ? 512 : 64); };
            __builtin_amdgcn_s_sleep(KFS);
            if (KFD > KFS && ffn) __builtin_amdgcn_s_sleep(KFD > KFS ? KFD - KFS : 0);
#pragma unroll
            for (int i = 0; i < 6; ++i) { if (PDI & 2) kf[i] = z4; else ld_async(kf[i], fo(i), fb(i)); }
            if (!XGE) {
                xg_def();
                xg_load(w + 1);
            }
            if (!(PDI & 2)) waitv<0>();
            RS(29);
            if (PP2) settle_p<PP2 + !PP2, PD2>(kf, fo, fb);
            else settle(kf, fo, fb);
#pragma unroll
            for (int i = 0; i < 5 + ZL; ++i) pin(kp_[i]);
            asm volatile("" : "+v"(xr[0]), "+v"(xr[1]), "+v"(gg[0]), "+v"(gg[1]));
            need(KFN);
#pragma unroll
            for (int i = 0; i < 4; ++i) lds_write<u32x4>(lds0 + L_KF + hs * 4096 + i * 1024 + l16, kf[i]);
#pragma unroll
            for (int i = 0; i < 2; ++i) lds_write<u32x4>(lds0 + L_KZ + hs * 128 + i * 64 + g * 16, kf[4 + i]);
            SB0();
            flag(lds0 + L_FKF + hs * 4);
        } else {
            const int row0 = (mt * 16 + hp) * 8 + 4 * hs;
            if (SPRIO) __builtin_amdgcn_s_setprio(SPRIO);
            asm volatile("s_waitcnt lgkmcnt(0)" : "+s"(sd));
            if (TLD) __builtin_amdgcn_s_sleep(TLD);
            u32x4 rv[2], gv[2], xv[4][2];
            u64 ev;
            if (RT0) lds_wait8(PG, RT0 + RTS * ((ph4 + CLP) & 3));
            asm volatile("global_load_dwordx2 %4, %5, %9 sc1\n\t"
                         "global_store_dwordx4 %10, %11, %9 sc1\n\t"
                         "global_store_dwordx2 %12, %13, %9 sc1\n\t"
                         "global_load_dwordx4 %0, %6, %7\n\tglobal_load_dwordx4 %1, %6, %7 offset:1024\n\t"
                         "global_load_dwordx4 %2, %6, %8\n\tglobal_load_dwordx4 %3, %6, %8 offset:1024"
                         : "=&v"(rv[0]), "=&v"(rv[1]), "=&v"(gv[0]), "=&v"(gv[1]), "=&v"(ev)
                         : "v"(0u), "v"(l16), "s"(wr), "s"(gw), "s"(ws),
                           "v"(l < 32 ? WS_Y + cs * 4096 + kq * 1024 + hs * 512 + l16 : WS_GU + cs * 4096 + kq * 512 + hs * 256 + l15 * 16), "v"(z4),
                           "v"(hs ? kf_z + (ZL ? l15 : sch) * 8 : kf_kv + l * 8), "v"(0ull)
                         : "memory");
            RS(1);
#pragma unroll
            for (int r = 0; r < 2; ++r)
                if (NOF & 64) xv[2 * r][0] = xv[2 * r][1] = xv[2 * r + 1][0] = xv[2 * r + 1][1] = z4; else
                asm volatile("global_load_dwordx4 %0, %4, %5\n\tglobal_load_dwordx4 %1, %4, %5 offset:1024\n\t"
                             "global_load_dwordx4 %2, %4, %5 offset:2048\n\tglobal_load_dwordx4 %3, %4, %5 offset:3072"
                             : "=&v"(xv[2 * r][0]), "=&v"(xv[2 * r][1]), "=&v"(xv[2 * r + 1][0]), "=&v"(xv[2 * r + 1][1])
                             : "v"(l16), "s"(x + (size_t)(row0 + 2 * r) * DM) : "memory");
            SB0();
            if ((MRG & 8) || KCL) clears();
            waitv<(MRG & 8) ? 13 : 0>();
            asm volatile("" : "+v"(rv[0]), "+v"(rv[1]), "+v"(gv[0]), "+v"(gv[1]), "+v"(ev));
#pragma unroll
            for (int r = 0; r < 4; ++r) asm volatile("" : "+v"(xv[r][0]), "+v"(xv[r][1]));
            RS(2);
            T = ((u64)__builtin_amdgcn_readfirstlane((unsigned)(ev >> 32)) << 32 | __builtin_amdgcn_readfirstlane((unsigned)ev)) + 1;
            float ra_[4] = {0.f, 0.f, 0.f, 0.f}, ga_[4] = {0.f, 0.f, 0.f, 0.f};
#pragma unroll
            for (int r = 0; r < 4; ++r)
#pragma unroll
                for (int i = 0; i < 2; ++i)
#pragma unroll
                    for (int j = 0; j < 4; ++j) {
                        ra_[r] = dot2(xv[r][i][j], rv[i][j], ra_[r]);
                        ga_[r] = dot2(xv[r][i][j], gv[i][j], ga_[r]);
                    }
            const int rr = (g >> 1) | ((g & 1) << 1);
            unsigned k = f2bf(sum4(ra_));
            k ^= (k & 0x8000u) ? 0xFFFFu : 0x8000u;
            k = (k << 3) | (7 - 4 * hs - rr);
            auto r1 = __builtin_amdgcn_permlane16_swap(k, k, false, false);
            k = max(r1[0], r1[1]);
            auto r2 = __builtin_amdgcn_permlane32_swap(k, k, false, false);
            k = max(r2[0], r2[1]);
            u64 gq = (T << 16) | f2bf(1.f / (1.f + __expf(-rbf(sum4(ga_)))));
            if (MRG & 8) waitv<0>();
            if (hs) lds_write<unsigned>(lds0 + L_BEST, k | 0x80000000u);
            asm volatile("" : "+v"(gq), "+v"(k));
            if (MRG & 1) {
                __builtin_amdgcn_s_setprio(SPR2);
                lds_wait8(PG, CLR0 + CLS * ((ph4 + CLP) & 3));
            }
            if (!hs) {
                lds_wait(lds0 + L_BEST, 1);
                const u64 rq = (T << 19) | max(k, lds_read<unsigned>(lds0 + L_BEST) & 0x7FFFFu);
                ST_MASK("global_store_dwordx2", 1ull, 0u, rq, ws + WS_RG + (mt * 16 + hp) * 8);
            }
            ST_MASK("global_store_dwordx2", 0x0001000100010001ull, (unsigned)(rr * 8), gq, ws + WS_GG + row0 * 8);
            RS(3);
            __builtin_amdgcn_s_setprio(SPR2);
            auto q_fill = [&]() AI {
                vx_init();
                vw_init();
                sfor<NF2, NF>([&](auto N) {
                    constexpr int n = N;
                    if constexpr (n == NF2 || F_N[n] != F_N[n - 1]) need(F_N[n]);
                    fill(N);
                });
            };
            auto wt = [&](unsigned f) AI { lds_wait(lds0 + f + (l & 1) * 4, 1); };
            unsigned pf[2];
            auto wo_warm = [&]() AI {
                asm volatile("global_load_dword %0, %2, %3\n\tglobal_load_dword %1, %2, %4" : "=&v"(pf[0]), "=&v"(pf[1])
                             : "v"((unsigned)((BMAP ? (rt & 7) * 128 + hs * 64 + (l & 31) : (rt & 3) * 256 + hs * 128 + l) * 2048)), "s"((const char*)wo + cb * 128), "s"((const char*)wo + cb * 128 + (BMAP ? 32 : 64) * 2048) : "memory");
            };
            if (DNP == 0) {
                lds_wait8(PG, FBA + FBS * turn);
                dn_w(0, 4);
            }
            if (!(MRG & 8) && !KCL) {
                if (CLN == 1 && !ffn) lds_wait8(lds0 + L_FLD + (l & 1) * 4, 1);
                if (!(MRG & 1)) lds_wait8(PG, CLN > 1 && !ffn ? CLN : CLR0 + CLS * ((ph4 + CLP) & 3));
                clears();
                waitv<0>();
            }
            if ((!(MRG & 6) || (MRG & 40)) && !KCL) cleared();
            RS(25);
            if (!(MRG & 4)) {
                lds_wait8(PG, SEL0);
                rg_load();
                if ((MRG & 10) == 2) cleared();
                waitv<(MRG & 10) == 2>();
                if (MRG & 2) lds_wait8(lds0 + L_FLD + (l & 1) * 4, 1);
                select();
                if (!SSM) flag(WR + L_FP);
            }
            if (FBL == 2) {
                lds_wait8(PG, F_N[NF1 - 1] - 1);
                __builtin_amdgcn_s_setprio(3);
                lds_wait(PG, F_N[NF1 - 1]);
                __builtin_amdgcn_s_sleep(FBD);
                __builtin_amdgcn_s_setprio(SPR2);
            } else if (FBL == 3) {
                lds_wait8(PG, F_N[NF1 - 1] - 1);
                __builtin_amdgcn_s_setprio(3);
                lds_wait(lds0 + L_FLD + (l & 1) * 4, 1);
                __builtin_amdgcn_s_setprio(SPR2);
            } else if (!(MRG & 2)) lds_wait8(lds0 + L_FLD + (l & 1) * 4, 1);
            if (MRG & 4) {
                rg_load();
                if (!(MRG & 40) && !KCL) cleared();
            }
            RS(6);
            if (WPF == 4) wo_warm();
            gu_w(0, GQ1);
            if ((MRG & 20) == 4) {
                waitv<4 * GQ1>();
                select();
                if (!SSM) flag(WR + L_FP);
            }
            q_fill();
            if ((MRG & 20) == 20) {
                waitv<4 * GQ1 + 4 * (NF - NF2)>();
                select();
                if (!SSM) flag(WR + L_FP);
            }
            RS(7);
            if (WEP == 0) wo_load(0, WE, false);
            gu_w(GQ1, GQ2);
            if (SSM) {
                __builtin_amdgcn_s_setprio(KPRIO);
                k_soft(1);
                __builtin_amdgcn_s_setprio(SPR2);
                RS(11);
            }
            gu_w(GQ2, 8);
            if (WPF == 1) wo_warm();
            if (WEP == 1) wo_load(0, WE, false);
            if (DNP == 1) dn_w(0, 4);
            if (WEP == 2) wo_load(0, WE, false);
            RS(8);
            waitv<(DNP == 1 ? 16 : 0) + (WEP ? 4 * WE : 0)>();
            if ((WPF == 1 && DNP != 1) || WPF == 4) asm volatile("" : "+v"(pf[0]), "+v"(pf[1]));
            RS(9);
            if (GUO == 0) gate_up();
            RS(10);
            if (WOT == 1) lds_wait(W4 + L_FKS, 1);
            if (WOT == 2) wt(L_FGI);
            if (DNP == 2) dn_w(0, 4);
            wo_load(WE, WOE, WOE == WON);
            RS(14);
            if (GUO == 1) {
                __builtin_amdgcn_s_setprio(GPR);
                gate_up();
                __builtin_amdgcn_s_setprio(SPR2);
            }
            RS(13);
            if (YT == 1) wt(L_FG);
            gu_load();
            if (DNP == 3) dn_w(0, 4);
            if (WOE < WON) wo_load(WOE, WON, true);
            waitv<(WOE < WON ? 4 * (WON - WOE) + 1 : 0)>();
            if (WPF == 1 && DNP == 1) asm volatile("" : "+v"(pf[0]), "+v"(pf[1]));
            RS(15);
            down();
            RS(24);
            sq = lds_read<u32x4>(lds0 + L_SEL);
            xg_def();
            kf_def();
        }
        RS(26);
        lds_wait0(WR + L_FQ);
#pragma unroll
        for (int i = 0; i < 4; ++i) ph[i >> 1][i & 1] = lds_read<u32x4>(AX + i * 1024);
    } else {
        if (CPRIO) __builtin_amdgcn_s_setprio(CPRIO);
        unsigned ra[4], xt[2];
#pragma unroll
        for (int u = 0; u < 4; ++u) ra[u] = lds0 + J_B[0] * 8192 + (8 * g + q) * 128 + ((u ^ (q >> 1) ^ ((g & 1) << 1)) * 32) + 8 * p;
        unsigned xa = lds0 + (32 * (w & 1) + l15) * 128 + ((g ^ (l15 >> 1)) * 16), fa = lds0 + L_FULL + (l & 3) * 4;
        xt[0] = xa + (hs ? X_U1[0] : X_U0[0]) * 8192;
        xt[1] = xt[0] ^ 64;
        asm volatile("s_mov_b32 m0, %4" : "+v"(xa), "+v"(fa), "+v"(xt[0]), "+v"(xt[1]) : "s"(lds0 + L_PROG + w * 256 - 124));
        f32x4 acc[3][4][2];
        bf16x8 xf[ND][2][2];
        s16x4 wh[PD + 1][2];
        unsigned fv = 0;
        bf16x8 kown[2];
        u64 bad = ~0ull;
        auto chk = [&](int c) AI {
            SB0();
            if (__builtin_expect(bad != 0ull, 0)) {
                unsigned c_;
                asm volatile("s_mov_b32 %1, 0x400000\n"
                             ".Lp%=:\n\tds_read_b32 %0, %2 offset:%3\n\ts_waitcnt lgkmcnt(0)\n\tv_cmp_gt_u32 vcc, %4, %0\n\ts_cbranch_vccz .Lf%=\n\t"
                             "s_sub_u32 %1, %1, 1\n\ts_cbranch_scc0 .Lp%=\n.Lf%=:"
                             : "=&v"(fv), "=&s"(c_) : "v"(fa), "n"(c > NM2 ? 32 : c > NM1 ? 16 : 0), "n"(c) : "memory", "vcc", "scc");
            }
            SB0();
        };
        auto o_clear4 = [&]() AI {
#pragma unroll
            for (int n = 0; n < 4; ++n) st_wt(rsrc, WS_O + ((mt * 2 + hs) * 32 + 2 * hp + (n >> 1)) * 4096 + (rg & 1) * 2048 + (n & 1) * 1024 + l16, z4);
        };
        auto kv_part = [&]() AI
        {
            bf16x8 ones;
#pragma unroll
            for (int j = 0; j < 8; ++j) ones[j] = (short)0x3f80;
            const unsigned pa = lds0 + (hs ? P_U[2] : P_U[0]) * 8192 + (4 * g + q) * 128 + (((8 * (rg & 1) + p) ^ fsw(4 * g + q)) << 3);
            const unsigned va = lds0 + L_A + hs * 2048 + l16;
            bf16x8 b0[4], b1[4], af[2][4];
            lds_wait0(lds0 + L_FV + sch * 4);
            RS(15);
#pragma unroll
            for (int i = 0; i < 4; ++i) {
                af[0][i] = cat8(tr_read(pa + i * 4096), tr_read(pa + i * 4096 + 2048));
                b0[i] = lds_read128(va + i * 4096);
                af[1][i] = cat8(tr_read((pa ^ 32) + i * 4096), tr_read((pa ^ 32) + i * 4096 + 2048));
            }
#pragma unroll
            for (int i = 0; i < 4; ++i) b1[i] = lds_read128(va + i * 4096 + 1024);
            SB0();
            f32x4 k0[2] = {zero, zero}, k1[2] = {zero, zero}, zt[2] = {zero, zero};
            const unsigned kv_st = WS_KV + ((hp * 8 + rg * 2) * 16 + mt) * 1024 + l16;
#pragma unroll
            for (int i = 0; i < 4; ++i)
#pragma unroll
                for (int e = 0; e < 2; ++e) {
                    k0[e] = mfma16(af[e][i], b0[i], k0[e]);
                    if (PORD) zt[e] = mfma16(af[e][i], ones, zt[e]);
                }
            SB0();
#pragma unroll
            for (int i = 0; i < 4; ++i)
#pragma unroll
                for (int e = 0; e < 2; ++e) k1[e] = mfma16(af[e][i], b1[i], k1[e]);
            st_wt(rsrc, kv_st, nz4(__builtin_bit_cast(u32x4, pack44(k0[0], k0[1]))));
            if (!PORD) {
                SB0();
#pragma unroll
                for (int i = 0; i < 4; ++i)
#pragma unroll
                    for (int e = 0; e < 2; ++e) zt[e] = mfma16(af[e][i], ones, zt[e]);
                st_wt(rsrc, kv_st + 16384, nz4(__builtin_bit_cast(u32x4, pack44(k1[0], k1[1]))));
            }
            const u32x4 zo = nz4((l & 1) ? zt[1] : zt[0]);
            ST_MASK("global_store_dwordx4", 0x0003000300030003ull, WS_Z + ((hp * 4 + rg) * 16 + mt) * 128 + (l & 1) * 64 + g * 16, zo, ws);
            if (PORD) st_wt(rsrc, kv_st + 16384, nz4(__builtin_bit_cast(u32x4, pack44(k1[0], k1[1]))));
            SB0();
            flag(WR + L_FKS);
            if (OCL == 1) o_clear4();
        };
        auto report = [&](int r) AI {
            if (r > RLD) {
                unsigned t;
                unsigned a;
                asm volatile("v_mov_b32 %0, %3\n\tv_mov_b32 %1, %2\n\tds_write_b32 %1, %0" : "=&v"(t), "=&v"(a) : "s"(WR + L_PRL), "n"(r) : "memory");
            } else asm volatile("global_load_lds_dword %0, %1 offset:%2" ::"v"(v0), "s"(one), "n"(4 * r) : "memory"); };
        auto rd1 = [&](auto F, auto H) AI {
            constexpr int f = F, h = H, n = f >> 2, j = f >> 3, c = h ? E_B[f] : E_A[f], a = c % 100;
            if constexpr (h == 0 && (f & 3) == 0 && Q_C[n] > 0 && !((NOF & 512) && n >= 12 && n < KD)) chk(Q_C[n]);
            wh[f % (PD + 1)][h] = tr_read(ra[f & 3] + (J_U[j] - J_B[j]) * 8192 + (n & 1) * 4096 + h * 512);
            SB0();
            if constexpr (c / 100 == 1) {
                constexpr int jn = (f + 3 - a) >> 3;
                asm volatile("v_add_u32 %0, %1, %0" : "+v"(ra[a]) : "n"((J_B[jn] - J_B[jn - 1]) * 8192));
            } else if constexpr (c / 50 == 4) {
                asm volatile("v_add_u32 %0, %1, %2" : "=v"(xt[0]) : "s"((hs ? X_U1[a] : X_U0[a]) * 8192), "v"(xa));
            } else if constexpr (c / 50 == 5) {
                asm volatile("v_xor_b32 %0, 64, %1" : "=v"(xt[1]) : "v"(xt[0]));
            } else if constexpr (c / 100 == 3) {
                xf[a % 25 % ND][a / 25 & 1][a / 50] = lds_read128(xt[a / 50] + (a / 25 & 1) * 2048);
            } else if constexpr (c / 100 == 4) {
                fv = *(volatile AS3 unsigned*)(size_t)(fa + (a > NM2 ? 32 : a > NM1 ? 16 : 0));
            } else if constexpr (c / 100 == 5) {
                bad = __builtin_amdgcn_ballot_w64(fv < (unsigned)a);
                asm volatile("" : "+s"(bad));
            } else if constexpr (c / 100 == 6 && !((NOF & 1024) && a < NP1)) {
                report(a);
            }
        };
        RS(1);
        sfor<0, PD>([&](auto F) { rd1(F, IC<0>{}); rd1(F, IC<1>{}); });
        RS(2);
        constexpr int K0F = 4 * KD, K1F = 4 * KD + 1;
        sfor<0, 96>([&](auto N) {
            sfor<0, 4>([&](auto E) {
                constexpr int n = N, e = E, f = 4 * n + e, j = n >> 1, ks = n & 1, u = f_u(f), s = J_X[j], m = J_M[j];
                const bf16x8 wv = cat8(wh[f % (PD + 1)][0], wh[f % (PD + 1)][1]);
                sfor<0, 2>([&](auto RB) {
                    constexpr int rb = RB;
                    const bf16x8 xv = xf[s % ND][rb][ks];
                    if (AMF) {
                        if (j_first(j) && ks == 0) asm volatile("v_mfma_f32_16x16x32_bf16 %0, %1, %2, 0" : "=&v"(acc[m][u][rb]) : "v"(m == 1 ? xv : wv), "v"(m == 1 ? wv : xv));
                        else asm volatile("v_mfma_f32_16x16x32_bf16 %0, %1, %2, %0" : "+v"(acc[m][u][rb]) : "v"(m == 1 ? xv : wv), "v"(m == 1 ? wv : xv));
                    } else if (m == 1) acc[m][u][rb] = mfma16(xv, wv, acc[m][u][rb]);
                    else acc[m][u][rb] = mfma16(wv, xv, acc[m][u][rb]);
                    SB0();
                    if constexpr (f + PD < 384 && (f + PD < 4 * VD || f >= 4 * VD)) rd1(IC<f + PD>{}, RB);
                    if constexpr (rb == 0 && f == K0F) relane();
                    if constexpr (AMF && rb == 0 && (f == K0F || f == K1F)) asm volatile("s_nop 7");
                    SB0();
                    if constexpr (f == K0F) lds_write<bf16x8>(AX + (P_U[0] * 8192 - L_A) + rb * 1024, pack44(acc[0][0][rb], acc[0][1][rb]));
                    if constexpr (f == K1F) {
                        kown[rb] = pack44(acc[0][2][rb], acc[0][3][rb]);
                        if (rb < 2 - SH) lds_write<bf16x8>(AX + (P_U[2] * 8192 - L_A) + rb * 1024, kown[rb]);
                        else asm volatile("" : "+v"(kown[rb]));
                    }
                    SB0();
                });
                if constexpr (f == K0F) { flag(WR + L_FK0); RS(12); }
                if constexpr (f == K1F) { if (SH < 2) flag(WR + L_FK); RS(4); }
                if constexpr (f == 4 * VD - 1) {
                    bf16x8 ak[2];
                    if (NF2 > NF1) flag(WR + L_FVR);
                    auto own = [&]() AI {
                        if (NF2 == NF1) return;
                        vx_init();
                        vw_init();
                        lds_wait0(W4 + L_FVR);
                        sfor<NF1, NF2>(fill);
                        asm volatile("s_mov_b32 m0, %0" ::"s"(lds0 + L_PROG + w * 256 - 124));
                        RS(14);
                    };
                    if (SH) {
                        __builtin_amdgcn_s_setprio(VPRIO);
                        phi_w(ak, 1);
                    }
                    asm volatile("s_nop 7");
                    SB0();
#pragma unroll
                    for (int i = 0; i < 4; ++i) lds_write<bf16x8>(AX + i * 1024, pack44(acc[1][i][0], acc[1][i][1]));
                    SB0();
                    RS(5);
                    if (CFP == 1 && SH) own();
                    if (SH) {
                        f32x4 pk[2][2];
                        unsigned pw[4];
#pragma unroll
                        for (int i = 4 - 2 * SH; i < 4; ++i) pk[i >> 1][i & 1] = KRND ? rbf4(mfma16(ak[i & 1], kown[i >> 1], zero)) : mfma16(ak[i & 1], kown[i >> 1], zero);
                        phi_adr(pw, lds0 + P_U[2] * 8192 + (SH == 1 ? 2048 : 0));
                        if (SH == 1) phi_k1(pk[1], pw);
                        else phi_k<true>(pk, pw, 0, []() {});
                        SB0();
                    }
                    flag(WR + L_FV);
                    if (OCL == 2) o_clear4();
                    RS(13);
                    if (CFP == 0 || !SH) own();
                    kv_part();
                    __builtin_amdgcn_s_setprio(CPRIO);
                    fv = 0;
                    bad = ~0ull;
                    sfor<4 * VD, 4 * VD + PD>([&](auto F) { rd1(F, IC<0>{}); rd1(F, IC<1>{}); });
                    RS(10);
                }
                if constexpr (e == 3) {
                    SB0();
                    if constexpr (n % CSN == CSN - 1) { RSC(32 + n / CSN); RK(48 + n / CSN); }
                    if constexpr (n == 31) RS(3);
                }
            });
        });
        asm volatile("s_nop 7");
        SB0();
        RS(6);
        if (CPRIO) __builtin_amdgcn_s_setprio(0);
        flag(WR + L_FQD);
        if (OCL) {
            const u32x2 tv = lds_read<u32x2>(lds0 + L_SEL);
            waitv<0>();
            ST_MASK("global_store_dwordx2", 1ull, 0u, tv, ws + WS_CL + 4096 + ((mt * 4 + rg) * 16 + hp) * 8);
        }
        bf16x8 a0[2], a1[2];
        f32x4 pq[2][2][2];
        phi_w(a1, hs);
        phi_w(a0, hs ^ 1);
#pragma unroll
        for (int rb = 0; rb < 2; ++rb) {
            const bf16x8 b0 = pack44(acc[2][0][rb], acc[2][1][rb]), b1 = pack44(acc[2][2][rb], acc[2][3][rb]);
#pragma unroll
            for (int fb = 0; fb < 2; ++fb) {
                pq[1][rb][fb] = QRND ? rbf4(mfma16(a1[fb], hs ? b1 : b0, zero)) : mfma16(a1[fb], hs ? b1 : b0, zero);
                pq[0][rb][fb] = QRND ? rbf4(mfma16(a0[fb], hs ? b0 : b1, zero)) : mfma16(a0[fb], hs ? b0 : b1, zero);
            }
        }
        softmax2<true>(pq[1], ph, []() {});
        RS(7);
        lds_wait(W4 + L_FKS, 1);
#pragma unroll
        for (int i = 0; i < 4; ++i) lds_write<u32x4>(AX + i * 1024, ph[i >> 1][i & 1]);
        SB0();
        flag(WR + L_FQ);
        sq = lds_read<u32x4>(lds0 + L_SEL);
        RS(8);
        softmax2<true>(pq[0], ph, []() {});
        asm volatile("" : "+v"(ph[0][0]), "+v"(ph[0][1]), "+v"(ph[1][0]), "+v"(ph[1][1]));
        RS(9);
        RS(11);
        xg_def();
        kf_def();
    }
    relane();
    const char* cla = CLL ? ws + WS_C2 + ((rt * 2 + kp) * 16 + l15) * 128 : ws + WS_CL + (OCL ? 4096 + ((rt * 4 + rg) * 16 + l15) * 8 : ((rt * 2 + kp) * 16 + l15) * 8);
    const u64 T = (u64)sq[1] << 32 | sq[0];
    u64 cl;
    xg_load(w & 5);
    {
        if ((w & 5) != 4) {
            lds_wait0(lds0 + L_FKF + oh * 4);
#pragma unroll
            for (int i = 0; i < 4; ++i) kf[i] = lds_read<u32x4>(lds0 + L_KF + oh * 4096 + i * 1024 + l16);
#pragma unroll
            for (int i = 0; i < 2; ++i) kf[4 + i] = lds_read<u32x4>(lds0 + L_KZ + oh * 128 + i * 64 + g * 16);
        }
        asm volatile("global_load_dwordx2 %0, %1, off sc1" : "=&v"(cl) : "v"(cla) : "memory");
        RS(27);
        o_store(kf, ph, rsrc, WS_O + ((mt * 2 + hs) * 32 + 2 * hp + oh) * 4096 + (rg & 1) * 2048 + l16);
        RS(28);
    }
    if (WON < 16 && WLK < 0) flag(lds0 + L_FOS + w * 4);
    waitv<3>();
#pragma unroll
    for (int i = 0; i < 6; ++i) pin(kf[i]);
    flag(lds0 + L_FWO + w * 4);
    __builtin_amdgcn_s_setprio(0);
    if (WON < 16 && WLK < 0) {
        if ((w & 5) == 5) {
            lds_wait(lds0 + L_FOS + sch * 4, 1);
            wo_load(WON, 16, true);
        }
    }
    const u32x2 selv = {sq[2], sq[3]};

    RS(30);
    const char* ob = ws + WS_O + (rt * 2 + (rg >> 1)) * 131072 + (rg & 1) * 2048;
    const unsigned ov0 = l16 + kp * 1024, ov1 = l16 + (kp ^ 1) * 1024;
    auto oa = [&](int i) { return ob + ((2 * i + kp + rot) & 31) * 4096; };
    constexpr int OB = OWIN + 1;
    u32x4 of[OB][4];
    auto fetch1 = [&](int k, int j) AI {
        if (k < OPL) ld_async(of[k % OB][j], (j & 1) ? ov1 : ov0, oa(2 * k + (j >> 1)));
        else asm volatile("global_load_dwordx4 %0, %1, %2" OPM : "=&v"(of[k % OB][j]) : "v"((j & 1) ? ov1 : ov0), "s"(oa(2 * k + (j >> 1))) : "memory");
    };
    auto fix = [&](int k) AI { if (NOF & 128) { for (int j = 0; j < 4; ++j) pin(of[k % OB][j]); return; }
        if (PP3) settle_p<PP3 + !PP3, PD3>(of[k % OB], [&](int j) { return (j & 1) ? ov1 : ov0; }, [&](int j) { return oa(2 * k + (j >> 1)); });
        else settle(of[k % OB], [&](int j) { return (j & 1) ? ov1 : ov0; }, [&](int j) { return oa(2 * k + (j >> 1)); }); };
    const bool own = (__builtin_amdgcn_readfirstlane(selv[0]) >> 4) == (unsigned)(rt * 8 + rg * 2 + kp);
    float gv_[2];
    f32x2 gb[2][4];
    auto g_mix = [&]() AI {
#pragma unroll
        for (int j = 0; j < 2; ++j) {
            gv_[j] = bf2f((u16)gg[j]);
            const float og = rbf(1.f - gv_[j]);
#pragma unroll
            for (int i = 0; i < 4; ++i) {
                const unsigned t = pack2(og * lo16(xr[j][i]), og * hi16(xr[j][i]));
                gb[j][i] = (f32x2){lo16(t), hi16(t)};
                asm volatile("" : "+v"(gb[j][i]));
            }
        }
    };
    asm volatile("" : "+v"(xr[0]), "+v"(xr[1]), "+v"(gg[0]), "+v"(gg[1]));
#pragma unroll 1
    for (unsigned s = 0; s < SPIN_CAP && __ballot((gg[0] >> 16) != (T & 0xFFFFFFFFFFFFull) || (gg[1] >> 16) != (T & 0xFFFFFFFFFFFFull)); ++s) {
        g_load();
        waitv<0>();
        asm volatile("" : "+v"(gg[0]), "+v"(gg[1]));
    }
    g_mix();
    if (PDL && ((w & 5) != 5 || WLK >= 0)) __builtin_amdgcn_s_sleep(PDL);
    if ((w & 5) != 5) waitv<2>();
    else waitv<(WON < 16 && WLK < 0 ? 3 + 4 * (16 - WON) : 2)>();
    asm volatile("" : "+v"(cl));
#pragma unroll 1
    for (unsigned s = 0; s < SPIN_CAP && __ballot(cl != T); ++s)
        asm volatile("global_load_dwordx2 %0, %1, off sc1\n\ts_waitcnt vmcnt(0)" : "=&v"(cl) : "v"(cla) : "memory");
    RS(16);
#pragma unroll
    for (int j = 0; j < 4; ++j) fetch1(0, j);
    RS(17);
    f32x4 acc[2][4];
    unsigned wa[2][4];
#pragma unroll
    for (int i = 0; i < 8; ++i) {
        wa[i >> 2][i & 3] = ((lds0 + 4096 * kp + (4 * g + q) * 128 + ((((g & 1) << 1) | (q >> 1)) * 32) + 8 * p) ^ ((i & 3) << 5)) + (i >> 2) * 65536;
        asm volatile("" : "+v"(wa[i >> 2][i & 3]));
    }
    s16x4 wf[2][8][2];
    auto wrd = [&](int k, int i) AI {
        const int u = wo_unit(2 * k + (i >> 3));
        wf[k & 1][i >> 1][i & 1] = tr_read(wa[u >> 3][(i >> 1) & 3] + (u & 7) * 8192 + (i & 1) * 2048);
    };
    u32x4 yp[14];
    const unsigned yo = srow * 4096 + cb * 256 + sch * 32;
    auto ya = [&](int i) { return ws + WS_Y + (i >> 1) * 32768 + (i & 1) * 16; };
    lds_wait(lds0 + L_FWD + ((l & 8) ? 16 + sch * 4 : (l & 1) * 4), 1);
    RS(18);
    RS(31);
    if (BPRIO && !kp) __builtin_amdgcn_s_setprio(BPRIO);
#pragma unroll
    for (int i = 0; i < 16; ++i) wrd(0, i);
#pragma unroll
    for (int j = 0; j < 4; ++j) fetch1(1, j);
    waitv<4>();
    fix(0);
    sfor<0, 8>([&](auto K) {
        constexpr int k = K;
        unsigned m[4];
        if constexpr (WON < 16 && 2 * (k + 1) == WON) lds_wait(lds0 + L_FW2 + (l & 1) * 4, 1);
        if constexpr (k == YK) {
            if (own) {
#pragma unroll
                for (int i = 0; i < 14; ++i) ld_async(yp[i], yo, ya(i));
            }
        }
        SB0();
        sfor<0, 16>([&](auto I) {
            constexpr int i = I, j = i >> 2, nb = i & 3;
            const bf16x8 a = cat8(wf[k & 1][(j >> 1) * 4 + nb][0], wf[k & 1][(j >> 1) * 4 + nb][1]);
            if (k == 0 && j < 2) asm volatile("v_mfma_f32_16x16x32_bf16 %0, %1, %2, 0" : "=&v"(acc[j & 1][nb]) : "v"(a), "v"(of[k % OB][j]));
            else asm volatile("v_mfma_f32_16x16x32_bf16 %0, %1, %2, %0" : "+v"(acc[j & 1][nb]) : "v"(a), "v"(of[k % OB][j]));
            SB0();
            if constexpr (k < 7) wrd(k + 1, i);
            if constexpr (OWIN == 2 || k < OW0) {
                if constexpr (k < 6 && i < 4) fetch1(k + 2, i);
            } else if constexpr (k == OW0) {
                if constexpr (i < 8) fetch1(k + 2 + (i >> 2), i & 3);
            } else if constexpr (k < 5 && i < 4) fetch1(k + 3, i);
            if constexpr (k < 7 && i == BCK) {
                waitv<(OWIN == 3 && k >= OW0 && k < 5 ? 8 : k < 6 ? 4 : 0)>();
#pragma unroll
                for (int n = 0; n < 4; ++n) pin(of[(k + 1) % OB][n]);
            }
            if constexpr (k < 7 && i > BCK && i <= BCK + 4) {
                m[i - BCK - 1] = min4(of[(k + 1) % OB][i - BCK - 1]);
                asm volatile("" : "+v"(m[i - BCK - 1]));
            }
            SB0();
        });
        if constexpr (k < 7) {
            if (__builtin_expect(__builtin_amdgcn_ballot_w64(min(min(m[0], m[1]), min(m[2], m[3])) == 0u) != 0ull, 0)) fix(k + 1);
        }
        SB0();
        if (k == 0) RS(19);
        if (k == 3) RS(23);
    });
    asm volatile("s_nop 7");
    SB0();
    RS(20);
    const unsigned ea = lds0 + L_A + l16 + 4096 * w, eb = lds0 + L_A + 4096 * (w ^ 4);
    lds_write<u32x4>(ea, nz4(acc[1][0]));
#pragma unroll
    for (int nb = 1; nb < 4; ++nb) lds_write<f32x4>(ea + nb * 1024, acc[1][nb]);
    if (own) {
        waitv<0>();
        settle(yp, [&](int) { return yo; }, ya);
        f32x4 yv[2];
#pragma unroll
        for (int h = 0; h < 2; ++h) {
            yv[h] = __builtin_bit_cast(f32x4, yp[h]);
#pragma unroll
            for (int i = 1; i < 7; ++i) yv[h] += __builtin_bit_cast(f32x4, yp[2 * i + h]);
#pragma unroll
            for (int i = 0; i < 4; ++i) {
                float t = dpp_add<0x128>(yv[h][i]);
                t = swap16_add(t, t);
                yv[h][i] = rbf(rbf(swap32_add(t, t)) * __uint_as_float(selv[1]));
            }
        }
#pragma unroll
        for (int j = 0; j < 2; ++j) {
            const bool hit = row0 + 8 * j + srow == (int)selv[0];
#pragma unroll
            for (int i = 0; i < 4; ++i) {
                const unsigned t = pack2(lo16(xr[j][i]) + yv[i >> 1][2 * (i & 1)], hi16(xr[j][i]) + yv[i >> 1][2 * (i & 1) + 1]);
                xr[j][i] = hit ? t : xr[j][i];
            }
        }
        g_mix();
    }
    BAR();
    RS(21);
#pragma unroll
    for (int nb = 0; nb < 4; ++nb) acc[0][nb] += lds_read<f32x4>(eb + l16 + nb * 1024);
#pragma unroll
    for (int nb = 0; nb < 4; ++nb) asm volatile("" : "+v"(acc[0][nb]));
#pragma unroll
    for (int nb = 0; nb < 4; ++nb) {
        const u32x2 a = {pack2(acc[0][nb][0], acc[0][nb][1]), pack2(acc[0][nb][2], acc[0][nb][3])};
        lds_write<u32x2>(eb + l15 * 128 + (((2 * nb + (g >> 1)) ^ (l & 7)) * 16) + (g & 1) * 8, a);
    }
#pragma unroll
    for (int j = 0; j < 2; ++j) {
        const u32x4 av = lds_read<u32x4>(eb + j * 1024 + srow * 128 + ((sch ^ srow) * 16));
        u32x4 o;
#pragma unroll
        for (int i = 0; i < 4; ++i) {
            const unsigned ga = pack2(gv_[j] * lo16(av[i]), gv_[j] * hi16(av[i]));
            o[i] = pack2(lo16(ga) + gb[j][i][0], hi16(ga) + gb[j][i][1]);
        }
        asm volatile("global_store_dwordx4 %0, %1, %2" OST "\n\ts_nop 1" ::"v"(xo), "v"(o), "s"(out + (size_t)(row0 + 8 * j) * DM + cb * 64) : "memory");
    }
    RS(22);
    RSDUMP();
}

extern "C" void kernel_launch(void* const* d_in, const int* in_sizes, int n_in, void* d_out, int out_size,
                              void* d_ws, size_t ws_size, hipStream_t stream) {
    fused_mix<<<256, 512, 0, stream>>>((const u16*)d_in[0], (const u16*)d_in[1], (const u16*)d_in[2], (const u16*)d_in[3],
                                       (const u16*)d_in[5], (const u16*)d_in[6], (const u16*)d_in[7], (const u16*)d_in[8],
                                       (const u16*)d_in[9], (char*)d_ws, (u16*)d_out);
}
```

```cpp
#include <hip/hip_runtime.h>
#include <hip/hip_bf16.h>
#include <stdint.h>
#include <type_traits>

typedef __attribute__((ext_vector_type(8))) short bf16x8;
typedef __attribute__((ext_vector_type(4))) short s16x4;
typedef __attribute__((ext_vector_type(4))) float f32x4;
typedef __attribute__((ext_vector_type(2))) float f32x2;
typedef __attribute__((ext_vector_type(2))) unsigned u32x2;
typedef __attribute__((ext_vector_type(4))) unsigned u32x4;
typedef __attribute__((ext_vector_type(2))) __bf16 bf16x2_t;
typedef unsigned short u16;
typedef unsigned long long u64;

#define DI __device__ __forceinline__
#define AS3 __attribute__((address_space(3)))

constexpr int DM = 1024;

DI float bf2f(u16 v) { return __uint_as_float(((unsigned)v) << 16); }
DI u16 f2bf(float f) { __bf16 b = (__bf16)f; return __builtin_bit_cast(u16, b); }
DI float rbf(float f) { return bf2f(f2bf(f)); }
DI unsigned pack2(float lo, float hi) {
    const f32x2 v = {lo, hi};
    return __builtin_bit_cast(unsigned, __builtin_convertvector(v, bf16x2_t));
}
DI float lo16(unsigned u) { return __uint_as_float(u << 16); }
DI float hi16(unsigned u) { return __uint_as_float(u & 0xffff0000u); }

DI s16x4 tr_read(unsigned a) { return __builtin_amdgcn_ds_read_tr16_b64_v4i16((AS3 s16x4*)(size_t)a); }
DI bf16x8 lds_read128(unsigned a) { return *(AS3 const bf16x8*)(size_t)a; }
template <class T> DI void lds_write(unsigned a, T v) { *(AS3 T*)(size_t)a = v; }
template <class T> DI T lds_read(unsigned a) { return *(AS3 const T*)(size_t)a; }
DI bf16x8 cat8(s16x4 a, s16x4 b) { return __builtin_shufflevector(a, b, 0, 1, 2, 3, 4, 5, 6, 7); }
DI f32x4 mfma16(bf16x8 a, bf16x8 b, f32x4 c) { return __builtin_amdgcn_mfma_f32_16x16x32_bf16(a, b, c, 0, 0, 0); }
DI bf16x8 pack44(const f32x4& a, const f32x4& b) {
    u32x4 r;
    r[0] = pack2(a[0], a[1]);
    r[1] = pack2(a[2], a[3]);
    r[2] = pack2(b[0], b[1]);
    r[3] = pack2(b[2], b[3]);
    return __builtin_bit_cast(bf16x8, r);
}
DI unsigned lds_of(const void* p) { return (unsigned)(size_t)(AS3 const char*)p; }

template <int CTRL>
DI unsigned dpp(unsigned x) { return __builtin_amdgcn_update_dpp(0u, x, CTRL, 0xf, 0xf, true); }
template <int CTRL>
DI float dpp_add(float x) { return x + __uint_as_float(dpp<CTRL>(__float_as_uint(x))); }
DI float swap32_add(float a, float b) {
    auto r = __builtin_amdgcn_permlane32_swap(__float_as_uint(a), __float_as_uint(b), false, false);
    return __uint_as_float(r[0]) + __uint_as_float(r[1]);
}
DI float swap16_add(float a, float b) {
    auto r = __builtin_amdgcn_permlane16_swap(__float_as_uint(a), __float_as_uint(b), false, false);
    return __uint_as_float(r[0]) + __uint_as_float(r[1]);
}
DI unsigned wave_umax(unsigned x) {
    x = max(x, dpp<0xB1>(x));
    x = max(x, dpp<0x4E>(x));
    x = max(x, dpp<0x141>(x));
    x = max(x, dpp<0x140>(x));
    auto r = __builtin_amdgcn_permlane16_swap(x, x, false, false);
    x = max(r[0], r[1]);
    auto s = __builtin_amdgcn_permlane32_swap(x, x, false, false);
    return max(s[0], s[1]);
}
constexpr unsigned WS_E = 0;
constexpr unsigned WS_RG = 256;
constexpr unsigned WS_GG = WS_RG + 2048;
constexpr unsigned WS_CL = 32768;
constexpr unsigned WS_Z = 65536;
constexpr unsigned WS_KF = WS_Z + 131072;
constexpr unsigned WS_GU = WS_KF + 262144;
constexpr unsigned WS_Y = WS_GU + 262144;
constexpr unsigned WS_KV = WS_Y + 262144;
constexpr unsigned WS_O = WS_KV + (2u << 20);
constexpr unsigned WS_END = WS_O + (4u << 20);

constexpr int NQ = 10, ND = 6, NX = 16, KD = 78, VD = 84, NF = 64, NF1 = 59, NF2 = 59, NP1 = 12, NP = 14, NM1 = 21, NM2 = 21, NM = 22, KFN = 100;
constexpr int J_S[48] = {0, 0, 0, 1, 1, 1, 2, 2, 2, 3, 3, 3, 4, 4, 4, 5, 5, 5, 6, 6, 6, 7, 7, 7, 8, 8, 8, 9, 9, 9, 10, 10, 11, 11, 12, 12, 13, 14, 15, 13, 14, 15, 10, 11, 12, 13, 14, 15};
constexpr int J_M[48] = {0, 1, 2, 0, 1, 2, 0, 1, 2, 0, 1, 2, 0, 1, 2, 0, 1, 2, 0, 1, 2, 0, 1, 2, 0, 1, 2, 0, 1, 2, 0, 1, 0, 1, 0, 1, 0, 0, 0, 1, 1, 1, 2, 2, 2, 2, 2, 2};
constexpr int J_X[48] = {0, 0, 0, 1, 1, 1, 2, 2, 2, 3, 3, 3, 4, 4, 4, 5, 5, 5, 6, 6, 6, 7, 7, 7, 8, 8, 8, 9, 9, 9, 10, 10, 11, 11, 12, 12, 13, 14, 15, 13, 14, 15, 10, 11, 12, 13, 14, 15};
constexpr int J_U[48] = {2, 4, 8, 18, 11, 12, 17, 15, 13, 0, 1, 6, 4, 8, 9, 11, 12, 14, 15, 13, 7, 1, 6, 5, 8, 9, 10, 12, 14, 16, 13, 7, 1, 6, 4, 8, 18, 14, 15, 13, 7, 3, 0, 1, 6, 5, 2, 4};
constexpr int J_B[48] = {2, 2, 2, 11, 11, 11, 11, 11, 11, 0, 0, 0, 0, 8, 8, 8, 8, 8, 8, 8, 1, 1, 1, 1, 1, 9, 9, 9, 9, 9, 9, 1, 1, 1, 1, 1, 13, 13, 13, 13, 0, 0, 0, 0, 0, 0, 0, 0};
constexpr int Q_C[96] = {1, 0, 2, 0, 3, 0, 4, 0, 5, 0, 6, 0, 7, 0, 0, 0, 0, 0, 8, 0, 0, 0, 0, 0, 9, 0, 0, 0, 0, 0, 10, 0, 0, 0, 0, 0, 11, 0, 0, 0, 0, 0, 12, 0, 0, 0, 0, 0, 13, 0, 0, 0, 0, 0, 14, 0, 0, 0, 0, 0, 15, 0, 0, 0, 0, 0, 16, 0, 0, 0, 0, 0, 17, 0, 0, 0, 0, 0, 18, 0, 19, 0, 20, 0, 21, 0, 22, 0, 0, 0, 0, 0, 0, 0, 0, 0};
constexpr int X_U0[16] = {6, 9, 14, 7, 5, 10, 16, 3, 2, 18, 17, 3, 5, 9, 11, 16};
constexpr int X_U1[16] = {5, 10, 16, 3, 2, 18, 17, 0, 4, 11, 15, 0, 2, 10, 12, 17};
constexpr int P_U[4] = {11, 12, 9, 10};
constexpr int W_U[16] = {8, 14, 15, 13, 7, 3, 9, 10, 11, 12, 0, 1, 6, 5, 2, 4};
constexpr int W_N[16] = {100, 100, 100, 100, 100, 100, 101, 101, 101, 101, 13, 13, 14, 14, 99, 99};
constexpr int F_J[64] = {-1, 0, 1, 2, -2, 3, 4, 5, -3, 6, 7, 8, -4, 9, 10, 11, -5, 12, 13, 14, -6, 15, 16, 17, -7, 18, 19, 20, -8, 21, 22, 23, -9, 24, 25, 26, -10, 27, 28, 29, -11, 30, 31, -12, 32, 33, -13, 34, 35, -14, 36, -15, 37, -16, 38, 39, 40, 41, 42, 43, 44, 45, 46, 47};
constexpr int F_N[64] = {0, 0, 0, 0, 0, 0, 0, 0, 0, 0, 0, 0, 0, 0, 0, 1, 1, 1, 1, 1, 2, 2, 2, 2, 3, 3, 3, 3, 4, 4, 4, 4, 5, 5, 5, 5, 6, 6, 6, 6, 7, 7, 7, 7, 8, 8, 8, 8, 9, 9, 9, 10, 10, 10, 10, 11, 11, 11, 11, 12, 12, 12, 12, 12};
constexpr int F_K[64] = {0, 1, 2, 3, 0, 4, 5, 6, 0, 0, 0, 7, 0, 0, 0, 8, 0, 0, 0, 9, 0, 0, 0, 10, 0, 0, 0, 11, 0, 0, 0, 12, 0, 0, 0, 13, 0, 0, 0, 14, 0, 0, 0, 0, 15, 0, 0, 0, 16, 0, 0, 0, 0, 0, 17, 18, 19, 20, 21, 0, 0, 0, 0, 22};
constexpr int E_A[384] = {300, 325, 350, 375, 0, 0, 0, 502, 0, 0, 0, 403, 0, 0, 0, 503, 201, 251, 0, 404, 0, 100, 101, 504, 301, 326, 351, 376, 0, 0, 0, 505, 601, 0, 0, 406, 0, 0, 0, 506, 202, 252, 0, 407, 0, 0, 0, 507, 302, 327, 352, 377, 0, 0, 0, 602, 0, 0, 0, 0, 0, 0, 0, 203, 253, 0, 0, 408, 0, 100, 101, 508, 303, 328, 353, 378, 0, 0, 0, 603, 0, 0, 0, 0, 0, 0, 0, 204, 254, 0, 0, 409, 0, 0, 0, 509, 304, 329, 354, 379, 0, 100, 101, 604, 102, 103, 0, 0, 0, 0, 0, 205, 255, 0, 0, 410, 0, 0, 0, 510, 305, 330, 355, 380, 0, 0, 0, 605, 0, 0, 0, 0, 0, 0, 0, 206, 256, 0, 0, 411, 0, 0, 0, 511, 306, 331, 356, 381, 0, 0, 0, 606, 0, 0, 0, 0, 0, 100, 101, 102, 103, 207, 257, 412, 0, 0, 0, 512, 307, 332, 357, 382, 0, 0, 0, 607, 0, 0, 0, 0, 0, 0, 0, 208, 258, 0, 0, 413, 0, 0, 0, 513, 308, 333, 358, 383, 0, 100, 101, 608, 102, 103, 0, 0, 0, 0, 0, 209, 259, 0, 0, 414, 0, 0, 0, 514, 309, 334, 359, 384, 0, 0, 0, 609, 0, 0, 0, 0, 0, 0, 0, 210, 260, 0, 0, 415, 0, 0, 0, 515, 310, 335, 360, 385, 0, 100, 101, 610, 102, 103, 211, 261, 0, 0, 0, 0, 311, 336, 361, 386, 0, 0, 0, 516, 611, 212, 262, 0, 0, 0, 0, 0, 312, 337, 362, 387, 0, 0, 0, 213, 263, 0, 0, 417, 0, 100, 101, 517, 313, 338, 363, 388, 214, 264, 0, 612, 314, 339, 364, 389, 215, 265, 0, 0, 315, 340, 365, 390, 0, 0, 0, 518, 0, 0, 0, 419, 0, 100, 101, 519, 102, 103, 0, 420, 0, 0, 0, 520, 0, 0, 0, 0, 0, 0, 0, 0, 0, 0, 0, 422, 0, 0, 0, 522, 0, 0, 0, 0, 0, 0, 0, 0, 0, 0, 0, 613, 0, 0, 0, 0, 0, 0, 0, 0, 0, 0, 0, 0, 0, 0, 0, 614, 0, 0, 0, 0, 0, 0, 0, 0, 0, 0, 0, 0};
constexpr int E_B[384] = {0, 0, 402, 0, 0, 0, 0, 0, 0, 0, 0, 0, 0, 0, 0, 0, 0, 0, 0, 0, 0, 0, 0, 102, 103, 0, 405, 0, 0, 0, 0, 0, 0, 0, 0, 0, 0, 0, 0, 0, 0, 0, 0, 0, 0, 0, 0, 0, 0, 0, 0, 0, 0, 0, 0, 0, 0, 0, 0, 0, 0, 0, 0, 0, 0, 0, 0, 0, 0, 0, 0, 102, 103, 0, 0, 0, 0, 0, 0, 0, 0, 0, 0, 0, 0, 0, 0, 0, 0, 0, 0, 0, 0, 0, 0, 0, 0, 0, 0, 0, 0, 0, 0, 0, 0, 0, 0, 0, 0, 0, 0, 0, 0, 0, 0, 0, 0, 0, 0, 0, 0, 0, 0, 0, 0, 0, 0, 0, 0, 0, 0, 0, 0, 0, 0, 0, 0, 0, 0, 0, 0, 0, 0, 0, 0, 0, 0, 0, 0, 0, 0, 0, 0, 0, 0, 0, 0, 0, 0, 0, 0, 0, 0, 0, 0, 0, 0, 0, 0, 0, 0, 0, 0, 0, 0, 0, 0, 0, 0, 0, 0, 0, 0, 0, 0, 0, 0, 0, 0, 0, 0, 0, 0, 0, 0, 0, 0, 0, 0, 0, 0, 0, 0, 0, 0, 0, 0, 0, 0, 0, 0, 0, 0, 0, 0, 0, 0, 0, 0, 0, 0, 0, 0, 0, 0, 0, 0, 0, 0, 0, 0, 0, 0, 0, 0, 0, 0, 0, 0, 0, 0, 0, 0, 0, 0, 0, 0, 0, 0, 0, 0, 0, 0, 0, 0, 0, 0, 0, 416, 0, 0, 0, 0, 0, 0, 0, 0, 0, 0, 0, 0, 0, 0, 0, 0, 0, 0, 0, 0, 0, 0, 0, 0, 0, 0, 0, 0, 102, 103, 0, 0, 0, 0, 0, 0, 0, 0, 0, 0, 0, 0, 0, 0, 0, 0, 0, 418, 0, 0, 0, 0, 0, 0, 0, 0, 0, 0, 0, 0, 0, 0, 0, 0, 0, 0, 0, 0, 0, 0, 0, 0, 0, 0, 0, 0, 0, 0, 0, 0, 0, 0, 0, 0, 0, 0, 0, 0, 0, 0, 0, 0, 0, 0, 0, 0, 0, 0, 0, 0, 0, 0, 0, 0, 0, 0, 0, 0, 0, 0, 0, 0, 0, 0, 0, 0, 0, 0, 0, 0, 0, 0, 0, 0, 0};
constexpr unsigned L_A = 16 * 8192;
constexpr unsigned L_KF = 18 * 8192;
constexpr unsigned L_SM = 19 * 8192;
constexpr unsigned L_FULL = L_SM + 256;
constexpr unsigned L_PROG = L_SM + 896;
constexpr unsigned L_PRL = L_SM + 336;
constexpr unsigned L_FW2 = L_SM + 512, L_FOS = L_SM + 480, L_FK0 = L_SM + 752, L_FV = L_SM + 560, L_FP = L_SM + 576, L_FKS = L_SM + 592;
constexpr unsigned L_FQD = L_SM + 608, L_FG = L_SM + 624, L_FKF = L_SM + 736, L_FQ = L_SM + 720, L_FWD = L_SM + 640, L_FWO = L_SM + 656;
constexpr unsigned L_FLD = L_SM + 688, L_BEST = L_SM + 696;
constexpr unsigned L_SEL = L_SM + 704;
constexpr unsigned L_XS = L_SM + 2048, L_KZ = L_SM + 3072, L_WPHI = L_SM + 4096;
static_assert(NP <= 31 && NM <= 63 && NF1 == NF2 && NX == 16, "");
static_assert(P_U[1] == P_U[0] + 1 && P_U[3] == P_U[2] + 1, "feature maps of a head lie side by side");
constexpr bool j_first(int j) {
    for (int i = 0; i < j; ++i)
        if (J_M[i] == J_M[j]) return false;
    return true;
}
constexpr unsigned SPIN_CAP = 1u << 16;
#define N8(a) a, a + 1, a + 2, a + 3, a + 4, a + 5, a + 6, a + 7
__device__ const unsigned k_num[64] = {N8(0), N8(8), N8(16), N8(24), N8(32), N8(40), N8(48), N8(56)};

constexpr int PD = 4;
constexpr int RLD = F_N[NF1 - 1];
constexpr int CLR0 = 2;
constexpr int GQ1 = 6;
constexpr int WOE = 6, WON = 12;
constexpr int GDL = 16, KFD = 24, PDL = 6;
constexpr unsigned CPF = 53248;
constexpr u64 KXB = 0xFEDBA9C768534210ull;
#define BAR() asm volatile("s_waitcnt lgkmcnt(0)\n\ts_barrier" ::: "memory")
template <int N>
DI void waitv() { asm volatile("s_waitcnt vmcnt(%0)" ::"n"(N) : "memory"); }

DI unsigned nz1(unsigned v) { return max(v, 1u); }
DI u32x4 nz4(u32x4 v) { return (u32x4){max(v[0], 1u), max(v[1], 1u), max(v[2], 1u), max(v[3], 1u)}; }
DI u32x4 nz4(f32x4 v) { return nz4(__builtin_bit_cast(u32x4, v)); }
DI unsigned min4(u32x4 v) { return min(min(v[0], v[1]), min(v[2], v[3])); }
DI void st_wt(__amdgpu_buffer_rsrc_t r, unsigned off, u32x4 v) { __builtin_amdgcn_raw_buffer_store_b128(v, r, off, 0, 16); }
DI void ld_async(u32x4& v, unsigned off, const char* base) {
    asm volatile("global_load_dwordx4 %0, %1, %2 sc1" : "=&v"(v) : "v"(off), "s"(base) : "memory");
}
#define ST_MASK(op, m, off, v, base) asm volatile("s_mov_b64 exec, %3\n\t" op " %0, %1, %2 sc1\n\ts_mov_b64 exec, -1\n\ts_nop 0" ::"v"(off), "v"(v), "s"(base), "s"((u64)(m)) : "memory")
DI void pin(u32x4& v) { asm volatile("" : "+v"(v)); }
template <int N, class OFF, class BASE>
DI void settle(u32x4 (&v)[N], OFF off, BASE base) {
#pragma unroll
    for (int i = 0; i < N; ++i) pin(v[i]);
#pragma unroll 1
    for (unsigned s = 0; s < SPIN_CAP; ++s) {
        unsigned m = min4(v[0]);
#pragma unroll
        for (int i = 1; i < N; ++i) m = min(m, min4(v[i]));
        if (__ballot(m == 0u) == 0ull) break;
#pragma unroll
        for (int i = 0; i < N; ++i) {
            u64 keep;
            asm volatile("s_mov_b64 %1, exec\n\ts_mov_b64 exec, %4\n\tglobal_load_dwordx4 %0, %2, %3 sc1\n\ts_mov_b64 exec, %1"
                         : "+&v"(v[i]), "=&s"(keep) : "v"(off(i)), "s"(base(i)), "s"((u64)__ballot(min4(v[i]) == 0u)) : "memory");
        }
        waitv<0>();
#pragma unroll
        for (int i = 0; i < N; ++i) pin(v[i]);
    }
}
DI void lds_wait(unsigned a, unsigned n) {
    unsigned v, c;
    asm volatile("s_mov_b32 %1, 0x400000\n"
                 ".Lp%=:\n\tds_read_b32 %0, %2\n\ts_waitcnt lgkmcnt(0)\n\tv_cmp_gt_u32 vcc, %3, %0\n\ts_cbranch_vccz .Lf%=\n\t"
                 "s_sleep 1\n\ts_sub_u32 %1, %1, 1\n\ts_cbranch_scc0 .Lp%=\n.Lf%=:"
                 : "=&v"(v), "=&s"(c) : "v"(a), "s"(n) : "memory", "vcc", "scc");
}
DI void lds_wait0(unsigned a) {
    unsigned v, c;
    asm volatile("s_mov_b32 %1, 0x1000000\n"
                 ".Lp%=:\n\tds_read_b32 %0, %2\n\ts_sub_u32 %1, %1, 1\n\ts_cbranch_scc1 .Lf%=\n\ts_waitcnt lgkmcnt(0)\n\tv_cmp_eq_u32 vcc, 0, %0\n\ts_cbranch_vccnz .Lp%=\n"
                 ".Lf%=:\n\ts_waitcnt lgkmcnt(0)"
                 : "=&v"(v), "=&s"(c) : "v"(a) : "memory", "vcc", "scc");
}
DI void lds_wait8(unsigned a, unsigned n) {
    unsigned v, c;
    asm volatile("s_mov_b32 %1, 0x100000\n"
                 ".Lp%=:\n\tds_read_b32 %0, %2\n\ts_waitcnt lgkmcnt(0)\n\tv_cmp_gt_u32 vcc, %3, %0\n\ts_cbranch_vccz .Lf%=\n\t"
                 "s_sleep 4\n\ts_sub_u32 %1, %1, 1\n\ts_cbranch_scc0 .Lp%=\n.Lf%=:"
                 : "=&v"(v), "=&s"(c) : "v"(a), "s"(n) : "memory", "vcc", "scc");
}
DI void lds_peek(unsigned& v, unsigned a) { asm volatile("ds_read_b32 %0, %1" : "=v"(v) : "v"(a) : "memory"); }
DI void lds_wait(unsigned& v, unsigned a, unsigned n) {
    unsigned c;
    asm volatile("s_mov_b32 %1, 0x400000\n"
                 ".Lp%=:\n\ts_waitcnt lgkmcnt(0)\n\tv_cmp_gt_u32 vcc, %3, %0\n\ts_cbranch_vccz .Lf%=\n\t"
                 "ds_read_b32 %0, %2\n\ts_sub_u32 %1, %1, 1\n\ts_cbranch_scc0 .Lp%=\n.Lf%=:"
                 : "+v"(v), "=&s"(c) : "v"(a), "s"(n) : "memory", "vcc", "scc");
}
DI float dot2(unsigned a, unsigned b, float c) {
    return __builtin_amdgcn_fdot2_f32_bf16(__builtin_bit_cast(bf16x2_t, a), __builtin_bit_cast(bf16x2_t, b), c, false);
}
DI float sum4(const float (&a)[4]) {
    float c = swap16_add(swap32_add(a[0], a[1]), swap32_add(a[2], a[3]));
    c = dpp_add<0xB1>(c);
    c = dpp_add<0x4E>(c);
    c = dpp_add<0x141>(c);
    return dpp_add<0x140>(c);
}

template <class OP, class POST>
DI void red4(float& a, float& b, float& c, float& d, OP op, POST post) {
    auto x = __builtin_amdgcn_permlane32_swap(__float_as_uint(a), __float_as_uint(b), false, false);
    auto y = __builtin_amdgcn_permlane32_swap(__float_as_uint(c), __float_as_uint(d), false, false);
    const float m = op(__uint_as_float(x[0]), __uint_as_float(x[1])), n = op(__uint_as_float(y[0]), __uint_as_float(y[1]));
    auto z = __builtin_amdgcn_permlane16_swap(__float_as_uint(m), __float_as_uint(n), false, false);
    const unsigned f = __float_as_uint(post(op(__uint_as_float(z[0]), __uint_as_float(z[1]))));
    auto u = __builtin_amdgcn_permlane16_swap(f, f, false, false);
    auto v = __builtin_amdgcn_permlane32_swap(u[0], u[0], false, false);
    auto t = __builtin_amdgcn_permlane32_swap(u[1], u[1], false, false);
    a = __uint_as_float(v[0]);
    b = __uint_as_float(v[1]);
    c = __uint_as_float(t[0]);
    d = __uint_as_float(t[1]);
}
DI float mx2(float a, float b) { return __builtin_elementwise_maximum(a, b); }
template <class HOOK>
DI void softmax2(const f32x4 (&v)[2][2], u32x4 (&ph)[2][2], HOOK hook) {
    constexpr float L2E = 1.4426950408889634f;
    float mx[2], mn[2], sp_[2], sn_[2];
#pragma unroll
    for (int b = 0; b < 2; ++b) {
        const f32x4 s = v[b][0], t = v[b][1];
        mx[b] = mx2(mx2(mx2(mx2(s[0], s[1]), s[2]), mx2(mx2(s[3], t[0]), t[1])), mx2(t[2], t[3]));
        mn[b] = mx2(mx2(mx2(mx2(-s[0], -s[1]), -s[2]), mx2(mx2(-s[3], -t[0]), -t[1])), mx2(-t[2], -t[3]));
    }
    red4(mx[0], mx[1], mn[0], mn[1], [](float a, float b) { return mx2(a, b); }, [](float a) { return a * -L2E; });
    f32x2 ep[2][4], en[2][4];
#pragma unroll
    for (int b = 0; b < 2; ++b) {
#pragma unroll
        for (int e = 0; e < 4; ++e) {
            const f32x2 u = {v[b][e >> 1][2 * (e & 1)], v[b][e >> 1][2 * (e & 1) + 1]};
            const f32x2 a = __builtin_elementwise_fma(u, (f32x2){L2E, L2E}, (f32x2){mx[b], mx[b]});
            const f32x2 c = __builtin_elementwise_fma(u, (f32x2){-L2E, -L2E}, (f32x2){mn[b], mn[b]});
            ep[b][e] = (f32x2){__builtin_amdgcn_exp2f(a[0]), __builtin_amdgcn_exp2f(a[1])};
            en[b][e] = (f32x2){__builtin_amdgcn_exp2f(c[0]), __builtin_amdgcn_exp2f(c[1])};
        }
        const f32x2 s = (ep[b][0] + ep[b][1]) + (ep[b][2] + ep[b][3]), t = (en[b][0] + en[b][1]) + (en[b][2] + en[b][3]);
        sp_[b] = s[0] + s[1];
        sn_[b] = t[0] + t[1];
    }
    asm volatile("" : "+v"(sp_[0]), "+v"(sn_[0]), "+v"(sp_[1]), "+v"(sn_[1]));
    hook();
    red4(sp_[0], sp_[1], sn_[0], sn_[1], [](float a, float b) { return a + b; }, [](float a) { return __builtin_amdgcn_rcpf(a); });
#pragma unroll
    for (int b = 0; b < 2; ++b)
#pragma unroll
        for (int e = 0; e < 4; ++e) {
            const f32x2 a = ep[b][e] * (f32x2){sp_[b], sp_[b]}, c = en[b][e] * (f32x2){sn_[b], sn_[b]};
            ph[b][0][e] = pack2(a[0], a[1]);
            ph[b][1][e] = pack2(c[0], c[1]);
        }
}
DI f32x4 rbf4(const f32x4& a) {
    const unsigned u = pack2(a[0], a[1]), v = pack2(a[2], a[3]);
    return (f32x4){lo16(u), hi16(u), lo16(v), hi16(v)};
}
DI int fsw(int row) { return (((row >> 1) & 3) << 2) | (((row >> 3) & 1) << 1) | (row & 1); }
template <class HOOK>
DI void phi_k(const f32x4 (&pp)[2][2], const unsigned (&pw)[4], unsigned off, HOOK hook) {
    u32x4 ph[2][2];
    softmax2(pp, ph, hook);
#pragma unroll
    for (int b = 0; b < 2; ++b)
#pragma unroll
        for (int kf = 0; kf < 2; ++kf)
#pragma unroll
            for (int fb = 0; fb < 2; ++fb) {
                const u32x2 a = {ph[b][kf][2 * fb], ph[b][kf][2 * fb + 1]};
                lds_write<u32x2>(pw[2 * kf + fb] + off + b * 2048, a);
            }
}

#define SB0() __builtin_amdgcn_sched_barrier(0)
template <int A, int B, class F>
DI void sfor(F f) {
    if constexpr (A < B) {
        f(std::integral_constant<int, A>{});
        sfor<A + 1, B>(f);
    }
}
template <int N> using IC = std::integral_constant<int, N>;
#define AI __attribute__((always_inline))

DI void o_store(const u32x4 (&kf)[6], const u32x4 (&ph)[2][2], __amdgpu_buffer_rsrc_t rsrc, unsigned o_st) {
    const f32x4 zero = {0.f, 0.f, 0.f, 0.f};
#pragma unroll
    for (int i = 0; i < 2; ++i) {
        const bf16x8 p0 = __builtin_bit_cast(bf16x8, ph[i][0]), p1 = __builtin_bit_cast(bf16x8, ph[i][1]);
        f32x4 dn = mfma16(__builtin_bit_cast(bf16x8, kf[4]), p0, zero);
        f32x4 n0 = mfma16(__builtin_bit_cast(bf16x8, kf[0]), p0, zero);
        f32x4 n1 = mfma16(__builtin_bit_cast(bf16x8, kf[1]), p0, zero);
        dn = mfma16(__builtin_bit_cast(bf16x8, kf[5]), p1, dn);
        n0 = mfma16(__builtin_bit_cast(bf16x8, kf[2]), p1, n0);
        n1 = mfma16(__builtin_bit_cast(bf16x8, kf[3]), p1, n1);
        const float rd_ = __builtin_amdgcn_rcpf(rbf(rbf(dn[0]) + 1e-6f));
        const u32x4 nb_ = __builtin_bit_cast(u32x4, pack44(n0, n1));
        u32x4 o;
#pragma unroll
        for (int j = 0; j < 4; ++j) o[j] = pack2(lo16(nb_[j]) * rd_, hi16(nb_[j]) * rd_);
        st_wt(rsrc, o_st + i * 1024, nz4(o));
    }
}


typedef __attribute__((ext_vector_type(16))) unsigned u32x16;
__global__ __launch_bounds__(512, 2) void fused_mix(const u16*, const u16*, const u16*, const u16*, const u16*, const u16*, const u16*, const u16*,
                                                    const u16*, char*, u16*) {
    __shared__ __attribute__((aligned(16))) char smem[163840];
    u32x4 ka;
    u32x16 kb_;
    u64 kz;
    asm volatile("s_load_dwordx4 %0, %3, 0x0\n\ts_load_dwordx16 %1, %3, 0x10\n\ts_load_dwordx2 %2, %3, 0x50"
                 : "=&s"(ka), "=&s"(kb_), "=&s"(kz) : "s"(__builtin_amdgcn_kernarg_segment_ptr()));
    SB0();
    const int tid = threadIdx.x;
    int l = tid & 63;
    const int w = __builtin_amdgcn_readfirstlane(tid >> 6) ^ 4;
    int bid = blockIdx.x;
    const unsigned lds0 = lds_of(smem);
    int g = l >> 4, l15 = l & 15, q = l15 >> 2, p = l & 3;
    int srow = l >> 3, sch = l & 7;
    unsigned l16 = l * 16;
    const int rg = w & 3, kp = w >> 2;
    const int hs = rg >> 1;
    {
        u64 pc;
        asm volatile("s_getpc_b64 %0\n\ts_mov_b32 m0, %2\n\ts_mov_b64 exec, 3\n\tglobal_load_lds_dword %1, %0\n\ts_mov_b64 exec, -1"
                     : "=&s"(pc) : "v"(min((unsigned)(((bid & ~7) + w) * 2 + l) * 128u, CPF)), "s"(lds0 + L_SM + 3584) : "memory");
    }
    lds_write<unsigned>(lds0 + L_SM + tid * 4, 0u);
    const int slot = bid >> 3;
    const int mt = (bid & 1) * 8 + (slot & 7);
    const int hp = ((bid >> 1) & 3) * 4 + (slot >> 3);
    unsigned vx[4], vw[4];
    auto vw_init = [&]() AI {
        int t = l;
        asm volatile("" : "+v"(t));
#pragma unroll
        for (int i = 0; i < 4; ++i) vw[i] = (unsigned)((8 * i + (t >> 3)) * 6144 + ((t & 7) ^ (((t >> 4) & 1) << 1) ^ ((i & 1) << 2)) * 16 + 4096 - i * 1024);
    };
    auto vx_init = [&]() AI {
        int t = l;
        asm volatile("" : "+v"(t));
#pragma unroll
        for (int i = 0; i < 4; ++i) vx[i] = (unsigned)((8 * i + (t >> 3)) * 2048 + (((t & 7) ^ ((4 * (i & 1) + (t >> 4)) & 7)) * 16) + 4096 - i * 1024);
    };
    vx_init();
    vw_init();
    asm volatile("" : "+v"(vx[0]), "+v"(vx[1]), "+v"(vx[2]), "+v"(vx[3]), "+v"(vw[0]), "+v"(vw[1]), "+v"(vw[2]), "+v"(vw[3]));
    unsigned xo0 = (mt * 128 + hs * 64) * 2048, wo0 = hp * 128 + hs * (32 * 6144);
    asm volatile("s_waitcnt lgkmcnt(0)" : "+s"(ka), "+s"(kb_), "+s"(kz), "+s"(xo0), "+s"(wo0) :: "memory");
    const u16* x = (const u16*)((u64)ka[1] << 32 | ka[0]);
    const u16* wqkv = (const u16*)((u64)ka[3] << 32 | ka[2]);
    auto arg = [&](int i) { return (char*)((u64)kb_[2 * i + 1] << 32 | kb_[2 * i]); };
    const u16 *wphi = (const u16*)arg(0), *wo = (const u16*)arg(1), *wr = (const u16*)arg(2), *wg = (const u16*)arg(3);
    const u16 *wu = (const u16*)arg(4), *wd = (const u16*)arg(5), *gw = (const u16*)arg(6);
    char* ws = arg(7);
    u16* out = (u16*)kz;
    const int sc = (3 - (int)((unsigned)(size_t)wqkv >> 7)) & 7;
    const unsigned* one = k_num;
    unsigned v0 = 0;
    asm volatile("" : "+s"(one), "+v"(v0));
    auto mark = [&](unsigned dst, int n = 1, int lanes = 1) AI {
        asm volatile("s_mov_b32 m0, %2\n\ts_mov_b64 exec, %4\n\tglobal_load_lds_dword %0, %1 offset:%3\n\ts_mov_b64 exec, -1"
                     ::"v"(v0), "s"(one), "s"(dst - 4 * n), "n"(4 * n), "n"(lanes) : "memory");
    };
    auto four = [&](const unsigned (&v)[4], const char* src, unsigned dst) AI {
        asm volatile("s_mov_b32 m0, %5\n\ts_nop 0\n\t"
                     "global_load_lds_dwordx4 %0, %4\n\tglobal_load_lds_dwordx4 %1, %4 offset:1024\n\t"
                     "global_load_lds_dwordx4 %2, %4 offset:2048\n\tglobal_load_lds_dwordx4 %3, %4 offset:3072"
                     ::"v"(v[0]), "v"(v[1]), "v"(v[2]), "v"(v[3]), "s"(src), "s"(dst) : "memory");
    };
    const int ks0 = (((slot + (slot >> 3)) & 3) | ((hp & 7) == sc ? slot & 4 : 0)) ^ ((bid & 1) * 8);
    const char* xb0 = (const char*)x + xo0 - 4096;
    const char* wb0 = (const char*)wqkv + wo0 - 4096;
    auto fill = [&](auto N) AI {
        constexpr int n = N, j = F_J[n] < 0 ? 0 : F_J[n], xi = F_J[n] < 0 ? -1 - F_J[n] : 0, s = F_J[n] < 0 ? xi : J_S[j], m = J_M[j];
        const int ks = ks0 ^ (int)((KXB >> (4 * s)) & 15);
        if constexpr (F_J[n] >= 0) {
            four(vw, wb0 + ks * (64 * 6144) + (m == 2 ? 0 : 2048 * (m + 1)), lds0 + J_U[j] * 8192 + hs * 4096);
        } else {
            const char* xs_ = xb0 + ks * 128;
            const unsigned d = lds0 + (hs ? X_U1[xi] : X_U0[xi]) * 8192;
            four(vx, xs_, d);
            four(vx, xs_ + 32 * 2048, d + 4096);
        }
        if constexpr (F_K[n] > 1) mark(lds0 + L_FULL + (n < NF1 ? 0 : 32) + hs * 8, F_K[n], 3);
    };
    static_assert(F_K[1] == 1, "");
    if (__builtin_expect((w & 5) == 4, 1)) sfor<0, 2>(fill);
    unsigned sd = 0, st;
    asm volatile("s_cmp_lg_u32 %2, 5\n\ts_cbranch_scc1 .La%=\n\t"
                 "s_min_u32 %1, %3, 23\n\ts_lshl_b32 %1, %1, 18\n\ts_load_dword %0, %5, %1\n\ts_load_dword %0, %6, 0x0\n\t"
                 "s_and_b32 %1, %1, 0x3c0000\n\ts_load_dword %0, %4, %1\n\ts_load_dword %0, %10, %1\n\t"
                 "s_load_dword %0, %7, 0x0\n\ts_load_dword %0, %8, 0x0\n\ts_load_dword %0, %9, 0x0\n.La%=:"
                 : "+s"(sd), "=&s"(st) : "s"(w), "s"(slot), "s"(x), "s"(wqkv), "s"(one), "s"(wr), "s"(gw), "s"(wphi), "s"(out) : "memory", "scc");
    asm volatile("s_cmp_lg_u32 %2, 7\n\ts_cbranch_scc1 .La%=\n\t"
                 "s_min_u32 %1, %3, 27\n\ts_lshl_b32 %1, %1, 18\n\ts_load_dword %0, %4, 0x0\n\ts_load_dword %0, %4, %1\n\t"
                 "s_load_dword %0, %5, %1\n\ts_load_dword %0, %6, %1\n\ts_load_dword %0, %7, %1\n\t"
                 "s_and_b32 %1, %1, 0x1c0000\n\ts_load_dword %0, %8, %1\n.La%=:\n\ts_barrier"
                 : "+s"(sd), "=&s"(st) : "s"(w), "s"(slot), "s"(ws), "s"(wg), "s"(wu), "s"(wd), "s"(wo) : "memory", "scc");
    if (__builtin_expect((w & 5) == 4, 1)) {
        mark(lds0 + L_FULL + hs * 8, 1, 3);
        sfor<2, 4>(fill);
    }
    asm volatile("" : "+s"(bid));
    const int xcd = bid & 7;
    const int rt = (xcd >> 1) * 4 + ((bid >> 3) & 3), cb = (xcd & 1) * 8 + (bid >> 5), rot = 8 * rt;
    const __amdgpu_buffer_rsrc_t rsrc = __builtin_amdgcn_make_buffer_rsrc(ws, 0, WS_END, 0x00020000);
    const u32x4 z4 = {0u, 0u, 0u, 0u};
    const f32x4 zero = {0.f, 0.f, 0.f, 0.f};
    unsigned AX = lds0 + L_A + rg * 4096 + l16;
    unsigned W4 = lds0 + p * 4;
    unsigned PG = lds0 + L_PROG + p * 256;
    const unsigned WR = lds0 + rg * 4;
    auto flag = [&](unsigned a) AI { asm volatile("ds_write_b32 %0, %0" ::"v"(a) : "memory"); };
    auto phi_w = [&](bf16x8 (&a)[2], int h) AI {
        const unsigned ad = lds0 + L_WPHI + h * 2048 + (4 * g + q) * 64 + (((p >> 1) ^ (2 * (g & 1))) * 16) + 8 * (p & 1);
        a[0] = cat8(tr_read(ad), tr_read(ad + 1024));
        a[1] = cat8(tr_read(ad ^ 32), tr_read((ad ^ 32) + 1024));
    };
    u32x4 fw[32], fd[16], gu[2];
    unsigned sel_[4][2];
    u32x4 ph[2][2], sq;
    const int hp2 = ((bid >> 1) & 3) * 4 + (bid >> 6);
    const bool ffn = (hp2 & 7) != sc;
    const int ph4 = ((bid >> 3) + (bid >> 6)) & 3;
    const int kq = mt >> 2;
    const int cs = (ffn ? hp2 - (hp2 > sc) - (hp2 > sc + 8) : 14 + (hp2 >> 3)) * 4 + ((ph4 + 3 - (int)((unsigned)(size_t)wg >> 7)) & 3);
    auto spread = [&](const u32x4& v, int i) -> bf16x8 {
        const unsigned s = v[i >> 1];
        u32x4 a;
#pragma unroll
        for (int d = 0; d < 4; ++d) a[d] = __builtin_amdgcn_perm(s, s, sel_[d][i & 1]);
        return __builtin_bit_cast(bf16x8, a);
    };
    auto fold_store = [&](const f32x4& a, unsigned off) {
        f32x4 v;
#pragma unroll
        for (int i = 0; i < 4; ++i) {
            const unsigned u = __float_as_uint(a[i]);
            const float r = __uint_as_float(__builtin_amdgcn_update_dpp(u, u, 0x128, 0xc, 0xf, false));
            v[i] = swap32_add(r, r);
        }
        const u32x4 o = nz4(v);
        ST_MASK("global_store_dwordx4", 0x00FF00FFull, off + l15 * 32 + g * 16, o, ws);
    };
    auto sel_init = [&]() AI {
#pragma unroll
        for (int d = 0; d < 4; ++d)
#pragma unroll
            for (int hf = 0; hf < 2; ++hf) {
                const unsigned b = hf ? 0x0302u : 0x0100u;
                sel_[d][hf] = (sch >> 1) != d ? 0x0c0c0c0cu : (l & 1) ? (b << 16) | 0x0c0cu : 0x0c0c0000u | b;
            }
    };
    auto gua = [&](int i) { return ws + WS_GU + cs * 4096 + i * 1024; };
    auto gu_load = [&]() AI {
#pragma unroll
        for (int i = 0; i < 2; ++i) ld_async(gu[i], l16, gua(i));
    };
    auto down = [&]() AI {
#pragma unroll
        for (int i = 0; i < 16; ++i) pin(fd[i]);
        settle(gu, [&](int) { return l16; }, gua);
        const f32x4 v = __builtin_bit_cast(f32x4, gu[0]) + __builtin_bit_cast(f32x4, gu[1]);
        auto r = __builtin_amdgcn_permlane16_swap(__float_as_uint(swap32_add(v[0], v[2])), __float_as_uint(swap32_add(v[1], v[3])), false, false);
        const float gt = rbf(__uint_as_float(r[0])), up = rbf(__uint_as_float(r[1]));
        const unsigned ha = lds0 + L_XS + hs * 512;
        lds_write<u16>(ha + l15 * 8 + g * 2, f2bf(rbf(gt / (1.f + __expf(-gt))) * up));
        const u32x4 hv = lds_read<u32x4>(ha + srow * 16);
        f32x4 y0 = zero, y1 = zero;
#pragma unroll
        for (int i = 0; i < 8; ++i) {
            const bf16x8 a = spread(hv, i);
            y0 = mfma16(a, __builtin_bit_cast(bf16x8, fd[i]), y0);
            y1 = mfma16(a, __builtin_bit_cast(bf16x8, fd[8 + i]), y1);
        }
        fold_store(y0, WS_Y + cs * 4096 + kq * 1024 + hs * 512);
        fold_store(y1, WS_Y + cs * 4096 + kq * 1024 + hs * 512 + 256);
    };
    auto need = [&](int c) AI {
        if (c == 101) lds_wait(W4 + L_FKS, 1);
        else if (c == 100) lds_wait(W4 + L_FV, 1);
        else if (c == 99) lds_wait(W4 + L_FQD, 1);
        else if (c > 0) lds_wait(c > RLD ? W4 + L_PRL : PG, c);
    };
    auto wo_load = [&](int n0, int n1, bool mk) AI {
        unsigned vo[4];
#pragma unroll
        for (int i = 0; i < 4; ++i) vo[i] = (unsigned)((8 * i + srow) * 2048 + ((sch ^ ((g & 3) << 1)) * 16) + 4096 - i * 1024);
        const char* wob = (const char*)wo + cb * 128 - 4096;
#pragma unroll
        for (int n = n0; n < n1; ++n) {
            if (n < WON && (n == 0 || W_N[n] != W_N[n - 1])) need(W_N[n]);
            four(vo, wob + ((2 * n + hs + rot) & 31) * 65536, lds0 + W_U[n] * 8192 + hs * 4096);
        }
        if (mk) mark(lds0 + (n0 >= WON ? L_FW2 : L_FWD) + hs * 4);
    };
    auto phi_adr = [&](unsigned (&pw)[4], unsigned base) AI {
#pragma unroll
        for (int i = 0; i < 4; ++i) pw[i] = (base + rg * 4096 + l15 * 128 + ((g ^ fsw(l15)) << 3)) ^ (i << 5);
    };
    const int row0 = rt * 128 + rg * 32 + kp * 16;
    u32x4 xr[2];
    u64 gg[2];
    unsigned xo = srow * 2048 + sch * 16;
    auto g_load = [&]() AI {
        asm volatile("global_load_dwordx2 %0, %2, %3 sc1\n\tglobal_load_dwordx2 %1, %2, %3 offset:64 sc1"
                     : "=&v"(gg[0]), "=&v"(gg[1]) : "v"((unsigned)srow * 8), "s"(ws + WS_GG + row0 * 8) : "memory");
    };
    auto xg_load = [&](int skip) AI {
        asm volatile("s_cmp_eq_u32 %4, 4\n\ts_cbranch_scc1 .Lk%=\n\t"
                     "global_load_dwordx4 %0, %5, %6\n\tglobal_load_dwordx4 %1, %5, %7\n\t"
                     "global_load_dwordx2 %2, %8, %9 sc1\n\tglobal_load_dwordx2 %3, %8, %9 offset:64 sc1\n.Lk%=:"
                     : "+v"(xr[0]), "+v"(xr[1]), "+v"(gg[0]), "+v"(gg[1])
                     : "s"(skip), "v"(xo), "s"(x + (size_t)row0 * DM + cb * 64), "s"(x + (size_t)(row0 + 8) * DM + cb * 64),
                       "v"((unsigned)srow * 8), "s"(ws + WS_GG + row0 * 8) : "memory", "scc");
    };
    auto xg_def = [&]() AI { asm volatile("" : "=v"(xr[0]), "=v"(xr[1]), "=v"(gg[0]), "=v"(gg[1])); };
    auto relane = [&]() AI {
        unsigned z = 0;
        asm volatile("" : "+v"(z));
        l = __builtin_amdgcn_mbcnt_hi(~0u, __builtin_amdgcn_mbcnt_lo(~0u, z));
        g = l >> 4, l15 = l & 15, q = l15 >> 2, p = l & 3, srow = l >> 3, sch = l & 7, l16 = l * 16;
        AX = lds0 + L_A + rg * 4096 + l16, W4 = lds0 + p * 4, PG = lds0 + L_PROG + p * 256, xo = srow * 2048 + sch * 16;
    };
    u32x4 kf[6];
    auto kf_def = [&]() AI { asm volatile("" : "=v"(kf[0]), "=v"(kf[1]), "=v"(kf[2]), "=v"(kf[3]), "=v"(kf[4]), "=v"(kf[5])); };
    const int oh = hs ^ kp ^ 1;

    if (__builtin_expect(w >= 4, 1)) {
        const bool loader = !(w & 1);
        const unsigned kf_kv = WS_KF + (hp * 2 + (mt >> 3)) * 8192 + (mt & 7) * 512;
        const unsigned kf_z = WS_KF + (hp * 2 + (mt >> 3)) * 8192 + 4096 + (mt & 7) * 128;
        const unsigned fgo = ffn ? (unsigned)((256 * kq + 16 * srow) * 7168 + cs * 128 + sch * 16) : 0u;
        const char* fsrc = (const char*)(hs ? wu : wg);
        auto gu_w = [&](int q0, int q1) AI {
#pragma unroll
            for (int i = q0; i < q1; ++i) {
                unsigned t1, t2, t3;
                asm volatile("v_add_u32 %4, 0x1c00, %7\n\tv_add_u32 %5, 0x3800, %7\n\tv_add_u32 %6, 0x5400, %7\n\t"
                             "global_load_dwordx4 %0, %7, %8 nt\n\tglobal_load_dwordx4 %1, %4, %8 nt\n\t"
                             "global_load_dwordx4 %2, %5, %8 nt\n\tglobal_load_dwordx4 %3, %6, %8 nt"
                             : "=&v"(fw[4 * i]), "=&v"(fw[4 * i + 1]), "=&v"(fw[4 * i + 2]), "=&v"(fw[4 * i + 3]), "=&v"(t1), "=&v"(t2), "=&v"(t3)
                             : "v"(fgo), "s"(fsrc + (128 * (i >> 2) + 4 * (i & 3)) * 7168) : "memory");
            }
        };
        const unsigned fdo = ffn ? (unsigned)((64 * cs + 8 * srow) * 2048 + 512 * kq + 256 * hs + sch * 16) : 0u;
        auto dn_w = [&](int q0, int q1) AI {
#pragma unroll
            for (int i = q0; i < q1; ++i)
                asm volatile("global_load_dwordx4 %0, %4, %5 offset:-4096 nt\n\tglobal_load_dwordx4 %1, %4, %5 offset:-2048 nt\n\t"
                             "global_load_dwordx4 %2, %4, %5 nt\n\tglobal_load_dwordx4 %3, %4, %5 offset:2048 nt"
                             : "=&v"(fd[4 * i]), "=&v"(fd[4 * i + 1]), "=&v"(fd[4 * i + 2]), "=&v"(fd[4 * i + 3])
                             : "v"(fdo), "s"((const char*)wd + 4096 + (i & 1) * 8192 + (i >> 1) * 128) : "memory");
        };
        auto gate_up = [&]() AI {
            sel_init();
#pragma unroll
            for (int i = 0; i < 32; ++i) pin(fw[i]);
            u32x4 xs[4];
#pragma unroll
            for (int i = 0; i < 4; ++i) xs[i] = lds_read<u32x4>(lds0 + L_XS + hs * 512 + (i >> 1) * 256 + srow * 32 + (i & 1) * 16);
            f32x4 a = zero;
#pragma unroll
            for (int i = 0; i < 32; ++i) a = mfma16(spread(xs[i >> 3], i & 7), __builtin_bit_cast(bf16x8, fw[i]), a);
            fold_store(a, WS_GU + cs * 4096 + kq * 512 + hs * 256);
            SB0();
        };
        u64 T;
        u32x4 sv[2];
        auto rg_load = [&]() AI {
            asm volatile("global_load_dwordx4 %0, %2, %3 sc1\n\tglobal_load_dwordx4 %1, %2, %3 offset:16 sc1"
                         : "=&v"(sv[0]), "=&v"(sv[1]) : "v"(l * 32), "s"(ws + WS_RG) : "memory");
        };
        auto select = [&]() AI {
            const unsigned tl = (unsigned)T & 0x1FFFu, thi = (unsigned)(T >> 13);
#pragma unroll 1
            for (unsigned s = 0; s < SPIN_CAP; ++s) {
                pin(sv[0]);
                pin(sv[1]);
                const unsigned bad = ((sv[0][0] >> 19) ^ tl) | (sv[0][1] ^ thi) | ((sv[0][2] >> 19) ^ tl) | (sv[0][3] ^ thi) |
                                     ((sv[1][0] >> 19) ^ tl) | (sv[1][1] ^ thi) | ((sv[1][2] >> 19) ^ tl) | (sv[1][3] ^ thi);
                if (__all(bad == 0u)) break;
                __builtin_amdgcn_s_sleep(16);
                rg_load();
                waitv<0>();
            }
            pin(sv[0]);
            pin(sv[1]);
            unsigned k2 = 0;
#pragma unroll
            for (int j = 0; j < 4; ++j) {
                const unsigned v = sv[j >> 1][(j & 1) * 2] & 0x7FFFFu;
                k2 = max(k2, ((v >> 3) << 11) | ((255 - (4 * l + j)) << 3) | (v & 7));
            }
            const unsigned key = __builtin_amdgcn_readfirstlane(wave_umax(k2));
            asm volatile("s_mov_b32 m0, %2\n\ts_mov_b64 exec, %3\n\tglobal_load_lds_dwordx4 %0, %1\n\ts_mov_b64 exec, -1"
                         ::"v"((unsigned)(512 * kq) + l16), "s"(x + (size_t)(2047 - (int)(key & 2047u)) * DM), "s"(lds0 + L_XS + hs * 512), "s"(0xFFFFFFFFull) : "memory");
            unsigned rb = key >> 11;
            rb ^= (rb & 0x8000u) ? 0x8000u : 0xFFFFu;
            const float rsel = rbf(1.f / (1.f + __expf(-bf2f((u16)rb))));
            lds_write<u32x4>(lds0 + L_SEL, (u32x4){(unsigned)T, (unsigned)(T >> 32), 2047u - (key & 2047u), __float_as_uint(rsel)});
            if (bid == 0 && hs) ST_MASK("global_store_dwordx2", 1ull, 0u, T, ws + WS_E);
        };
        auto k_soft = [&]() AI {
            bf16x8 a0[2], kb[2];
            f32x4 pk[2][2][2];
            unsigned pw[4];
            phi_w(a0, 0);
            phi_adr(pw, lds0 + P_U[0] * 8192);
            asm volatile("" : "+v"(pw[0]), "+v"(pw[1]), "+v"(pw[2]), "+v"(pw[3]));
            auto logits = [&](int h) AI {
                lds_wait(WR + L_FK0 + 4 * h, 1);
#pragma unroll
                for (int i = 0; i < 2; ++i) kb[i] = lds_read128(AX + (P_U[0] * 8192 - L_A) + h * 4096 + i * 1024);
#pragma unroll
                for (int i = 0; i < 4; ++i) pk[h][i >> 1][i & 1] = mfma16(a0[i & 1], kb[i >> 1], zero);
            };
            auto round = [&](int h) AI {
#pragma unroll
                for (int i = 0; i < 4; ++i) pk[h][i >> 1][i & 1] = rbf4(pk[h][i >> 1][i & 1]);
            };
            logits(0);
            round(0);
            SB0();
            phi_k(pk[0], pw, 0, [&]() AI {
                logits(1);
                SB0();
            });
            SB0();
            round(1);
            phi_k(pk[1], pw, 4096, []() {});
            SB0();
            flag(WR + L_FP);
        };
        if (__builtin_expect(loader, 1)) {
            vx_init();
            vw_init();
            unsigned pv = 0;
            sfor<4, NF1>([&](auto N) {
                constexpr int n = N;
                if constexpr (F_N[n] > F_N[n - 1]) {
                    __builtin_amdgcn_s_setprio(3);
                    lds_wait(pv, PG, F_N[n]);
                    if (F_N[n] < RLD) lds_peek(pv, PG);
                    __builtin_amdgcn_s_setprio(0);
                }
                if constexpr (n == 12) {
                    asm volatile("s_mov_b32 m0, %2\n\ts_nop 0\n\tglobal_load_lds_dwordx4 %0, %1\n\tglobal_load_lds_dwordx4 %0, %1 offset:1024"
                                 ::"v"((unsigned)((l >> 2) * 64 + (((l & 3) ^ (2 * ((l >> 4) & 1))) * 16))),
                                   "s"(wphi + (hp * 2 + hs) * 1024), "s"(lds0 + L_WPHI + hs * 2048) : "memory");
                }
                fill(N);
            });
            flag(lds0 + L_FLD + hs * 4);
            __builtin_amdgcn_s_setprio(2);
            const char* cla = ws + WS_CL + ((l15 * 2 + (mt >> 3)) * 16 + hp) * 8;
            u64 cl;
            k_soft();
            asm volatile("global_load_dwordx2 %0, %1, off sc1" : "=&v"(cl) : "v"(cla) : "memory");
            lds_wait(W4 + L_FP, 1);
            sq = lds_read<u32x4>(lds0 + L_SEL);
            waitv<0>();
            asm volatile("" : "+v"(cl));
            {
                u32x4 kp_[6];
                const char* kb = ws + WS_KV + (hp * 8 + (mt >> 1)) * 16384 + (mt & 1) * 512 + hs * 256;
                const bool zw = !((mt & 3) | hs);
                const char* zb = zw ? ws + WS_Z + (hp * 4 + (mt >> 2)) * 2048 : kb;
                const unsigned kvo = g * 1024 + l15 * 16, zo = zw ? l16 : 0u;
                auto ko = [&](int i) { return i < 4 ? kvo : zo; };
                auto ka = [&](int i) { return i < 4 ? kb + i * 4096 : zb + (i - 4) * 1024; };
                const u64 T = (u64)sq[1] << 32 | sq[0];
#pragma unroll 1
                for (unsigned s = 0; s < SPIN_CAP && __ballot(cl != T); ++s)
                    asm volatile("global_load_dwordx2 %0, %1, off sc1\n\ts_waitcnt vmcnt(0)" : "=&v"(cl) : "v"(cla) : "memory");
                lds_wait0(W4 + L_FKS);
                __builtin_amdgcn_s_sleep(GDL);
#pragma unroll
                for (int i = 0; i < 6; ++i) ld_async(kp_[i], ko(i), ka(i));
                xg_def();
                xg_load(w + 1);
                SB0();
                waitv<0>();
                settle(kp_, ko, ka);
                float v[8], u[4];
#pragma unroll
                for (int e = 0; e < 4; ++e) {
                    v[2 * e] = (lo16(kp_[0][e]) + lo16(kp_[1][e])) + (lo16(kp_[2][e]) + lo16(kp_[3][e]));
                    v[2 * e + 1] = (hi16(kp_[0][e]) + hi16(kp_[1][e])) + (hi16(kp_[2][e]) + hi16(kp_[3][e]));
                }
#pragma unroll
                for (int e = 0; e < 4; ++e) u[e] = swap32_add(v[e], v[e + 4]);
                const unsigned o = nz1(pack2(swap16_add(u[0], u[2]), swap16_add(u[1], u[3])));
                ST_MASK("global_store_dword", ~0ull, kf_kv + hs * 256 + l15 * 16 + g * 4, o, ws);
                if (zw) {
                    f32x4 z = __builtin_bit_cast(f32x4, kp_[4]) + __builtin_bit_cast(f32x4, kp_[5]);
#pragma unroll
                    for (int i = 0; i < 4; ++i) {
                        float t = dpp_add<0x128>(z[i]);
                        t = swap16_add(t, t);
                        z[i] = swap32_add(t, t);
                    }
                    const u32x2 oz = {nz1(pack2(z[0], z[1])), nz1(pack2(z[2], z[3]))};
                    ST_MASK("global_store_dwordx2", 0xFFFFull, kf_z + (l & 8) * 8 + p * 16 + (sch >> 2) * 8, oz, ws);
                }
                flag(lds0 + L_FG + hs * 4);
            }
            const char* kfb = ws + WS_KF + (hp * 2 + hs) * 8192;
            auto fo = [&](int i) { return i < 4 ? l16 : (unsigned)g * 16; };
            auto fb = [&](int i) { return i < 4 ? kfb + i * 1024 : kfb + 4096 + (i - 4) * 512; };
            __builtin_amdgcn_s_sleep(KFD);
#pragma unroll
            for (int i = 0; i < 6; ++i) ld_async(kf[i], fo(i), fb(i));
            waitv<0>();
            settle(kf, fo, fb);
            asm volatile("" : "+v"(xr[0]), "+v"(xr[1]), "+v"(gg[0]), "+v"(gg[1]));
            need(KFN);
#pragma unroll
            for (int i = 0; i < 4; ++i) lds_write<u32x4>(lds0 + L_KF + hs * 4096 + i * 1024 + l16, kf[i]);
#pragma unroll
            for (int i = 0; i < 2; ++i) lds_write<u32x4>(lds0 + L_KZ + hs * 128 + i * 64 + g * 16, kf[4 + i]);
            SB0();
            flag(lds0 + L_FKF + hs * 4);
        } else {
            const int row0 = (mt * 16 + hp) * 8 + 4 * hs;
            __builtin_amdgcn_s_setprio(2);
            asm volatile("s_waitcnt lgkmcnt(0)" : "+s"(sd));
            u32x4 rv[2], gv[2], xv[4][2];
            u64 ev;
            asm volatile("global_load_dwordx2 %4, %5, %9 sc1\n\t"
                         "global_store_dwordx4 %10, %11, %9 sc1\n\t"
                         "global_store_dwordx2 %12, %13, %9 sc1\n\t"
                         "global_load_dwordx4 %0, %6, %7\n\tglobal_load_dwordx4 %1, %6, %7 offset:1024\n\t"
                         "global_load_dwordx4 %2, %6, %8\n\tglobal_load_dwordx4 %3, %6, %8 offset:1024"
                         : "=&v"(rv[0]), "=&v"(rv[1]), "=&v"(gv[0]), "=&v"(gv[1]), "=&v"(ev)
                         : "v"(0u), "v"(l16), "s"(wr), "s"(gw), "s"(ws),
                           "v"(l < 32 ? WS_Y + cs * 4096 + kq * 1024 + hs * 512 + l16 : WS_GU + cs * 4096 + kq * 512 + hs * 256 + l15 * 16), "v"(z4),
                           "v"(hs ? kf_z + l15 * 8 : kf_kv + l * 8), "v"(0ull)
                         : "memory");
#pragma unroll
            for (int r = 0; r < 2; ++r)
                asm volatile("global_load_dwordx4 %0, %4, %5\n\tglobal_load_dwordx4 %1, %4, %5 offset:1024\n\t"
                             "global_load_dwordx4 %2, %4, %5 offset:2048\n\tglobal_load_dwordx4 %3, %4, %5 offset:3072"
                             : "=&v"(xv[2 * r][0]), "=&v"(xv[2 * r][1]), "=&v"(xv[2 * r + 1][0]), "=&v"(xv[2 * r + 1][1])
                             : "v"(l16), "s"(x + (size_t)(row0 + 2 * r) * DM) : "memory");
            SB0();
            waitv<0>();
            asm volatile("" : "+v"(rv[0]), "+v"(rv[1]), "+v"(gv[0]), "+v"(gv[1]), "+v"(ev));
#pragma unroll
            for (int r = 0; r < 4; ++r) asm volatile("" : "+v"(xv[r][0]), "+v"(xv[r][1]));
            T = ((u64)__builtin_amdgcn_readfirstlane((unsigned)(ev >> 32)) << 32 | __builtin_amdgcn_readfirstlane((unsigned)ev)) + 1;
            float ra_[4] = {0.f, 0.f, 0.f, 0.f}, ga_[4] = {0.f, 0.f, 0.f, 0.f};
#pragma unroll
            for (int r = 0; r < 4; ++r)
#pragma unroll
                for (int i = 0; i < 2; ++i)
#pragma unroll
                    for (int j = 0; j < 4; ++j) {
                        ra_[r] = dot2(xv[r][i][j], rv[i][j], ra_[r]);
                        ga_[r] = dot2(xv[r][i][j], gv[i][j], ga_[r]);
                    }
            const int rr = (g >> 1) | ((g & 1) << 1);
            unsigned k = f2bf(sum4(ra_));
            k ^= (k & 0x8000u) ? 0xFFFFu : 0x8000u;
            k = (k << 3) | (7 - 4 * hs - rr);
            auto r1 = __builtin_amdgcn_permlane16_swap(k, k, false, false);
            k = max(r1[0], r1[1]);
            auto r2 = __builtin_amdgcn_permlane32_swap(k, k, false, false);
            k = max(r2[0], r2[1]);
            u64 gq = (T << 16) | f2bf(1.f / (1.f + __expf(-rbf(sum4(ga_)))));
            if (hs) lds_write<unsigned>(lds0 + L_BEST, k | 0x80000000u);
            asm volatile("" : "+v"(gq), "+v"(k));
            __builtin_amdgcn_s_setprio(0);
            lds_wait8(PG, CLR0 + ph4);
            if (!hs) {
                lds_wait(lds0 + L_BEST, 1);
                const u64 rq = (T << 19) | max(k, lds_read<unsigned>(lds0 + L_BEST) & 0x7FFFFu);
                ST_MASK("global_store_dwordx2", 1ull, 0u, rq, ws + WS_RG + (mt * 16 + hp) * 8);
            }
            ST_MASK("global_store_dwordx2", 0x0001000100010001ull, (unsigned)(rr * 8), gq, ws + WS_GG + row0 * 8);
#pragma unroll
            for (int n = 0; n < 8; ++n) st_wt(rsrc, WS_O + ((mt * 2 + (n >> 2)) * 32 + 2 * hp + hs) * 4096 + (n & 3) * 1024 + l16, z4);
#pragma unroll
            for (int n = 0; n < 4; ++n) st_wt(rsrc, WS_KV + ((hp * 8 + hs * 4 + n) * 16 + mt) * 1024 + l16, z4);
            ST_MASK("global_store_dwordx4", 0xFFFFull, WS_Z + ((hp * 4 + hs * 2 + (srow & 1)) * 16 + mt) * 128 + sch * 16, z4, ws);
            waitv<0>();
            lds_wait8(lds0 + L_FLD + (l & 1) * 4, 1);
            rg_load();
            ST_MASK("global_store_dwordx2", 1ull, 0u, T, ws + WS_CL + ((mt * 2 + hs) * 16 + hp) * 8);
            gu_w(0, GQ1);
            waitv<4 * GQ1>();
            select();
            flag(WR + L_FP);
            vx_init();
            vw_init();
            sfor<NF1, NF>([&](auto N) {
                constexpr int n = N;
                if constexpr (n == NF1 || F_N[n] != F_N[n - 1]) need(F_N[n]);
                fill(N);
            });
            gu_w(GQ1, 8);
            dn_w(0, 4);
            waitv<16>();
            gate_up();
            lds_wait(W4 + L_FKS, 1);
            wo_load(0, WOE, false);
            lds_wait(lds0 + L_FG + (l & 1) * 4, 1);
            gu_load();
            wo_load(WOE, WON, true);
            waitv<4 * (WON - WOE) + 1>();
            down();
            sq = lds_read<u32x4>(lds0 + L_SEL);
            xg_def();
            kf_def();
        }
        lds_wait0(WR + L_FQ);
#pragma unroll
        for (int i = 0; i < 4; ++i) ph[i >> 1][i & 1] = lds_read<u32x4>(AX + i * 1024);
    } else {
        __builtin_amdgcn_s_setprio(1);
        unsigned ra[4], xt[2];
#pragma unroll
        for (int u = 0; u < 4; ++u) ra[u] = lds0 + J_B[0] * 8192 + (8 * g + q) * 128 + ((u ^ (q >> 1) ^ ((g & 1) << 1)) * 32) + 8 * p;
        unsigned xa = lds0 + (32 * (w & 1) + l15) * 128 + ((g ^ (l15 >> 1)) * 16), fa = lds0 + L_FULL + (l & 3) * 4;
        xt[0] = xa + (hs ? X_U1[0] : X_U0[0]) * 8192;
        xt[1] = xt[0] ^ 64;
        asm volatile("s_mov_b32 m0, %4" : "+v"(xa), "+v"(fa), "+v"(xt[0]), "+v"(xt[1]) : "s"(lds0 + L_PROG + w * 256 - 124));
        f32x4 acc[3][4][2];
        bf16x8 xf[ND][2][2];
        s16x4 wh[PD + 1][2];
        unsigned fv = 0;
        bf16x8 kown[2];
        u64 bad = ~0ull;
        auto chk = [&](int c) AI {
            SB0();
            if (__builtin_expect(bad != 0ull, 0)) {
                unsigned c_;
                asm volatile("s_mov_b32 %1, 0x400000\n"
                             ".Lp%=:\n\tds_read_b32 %0, %2 offset:%3\n\ts_waitcnt lgkmcnt(0)\n\tv_cmp_gt_u32 vcc, %4, %0\n\ts_cbranch_vccz .Lf%=\n\t"
                             "s_sub_u32 %1, %1, 1\n\ts_cbranch_scc0 .Lp%=\n.Lf%=:"
                             : "=&v"(fv), "=&s"(c_) : "v"(fa), "n"(c > NM1 ? 32 : 0), "n"(c) : "memory", "vcc", "scc");
            }
            SB0();
        };
        auto kv_part = [&]() AI {
            bf16x8 ones;
#pragma unroll
            for (int j = 0; j < 8; ++j) ones[j] = (short)0x3f80;
            const unsigned pa = lds0 + (hs ? P_U[2] : P_U[0]) * 8192 + (4 * g + q) * 128 + (((8 * (rg & 1) + p) ^ fsw(4 * g + q)) << 3);
            const unsigned va = lds0 + L_A + hs * 2048 + l16;
            bf16x8 b0[4], b1[4], af[2][4];
            lds_wait0(lds0 + L_FV + sch * 4);
#pragma unroll
            for (int i = 0; i < 4; ++i) {
                af[0][i] = cat8(tr_read(pa + i * 4096), tr_read(pa + i * 4096 + 2048));
                b0[i] = lds_read128(va + i * 4096);
                af[1][i] = cat8(tr_read((pa ^ 32) + i * 4096), tr_read((pa ^ 32) + i * 4096 + 2048));
            }
#pragma unroll
            for (int i = 0; i < 4; ++i) b1[i] = lds_read128(va + i * 4096 + 1024);
            SB0();
            f32x4 k0[2] = {zero, zero}, k1[2] = {zero, zero}, zt[2] = {zero, zero};
            const unsigned kv_st = WS_KV + ((hp * 8 + rg * 2) * 16 + mt) * 1024 + l16;
#pragma unroll
            for (int i = 0; i < 4; ++i)
#pragma unroll
                for (int e = 0; e < 2; ++e) {
                    k0[e] = mfma16(af[e][i], b0[i], k0[e]);
                    zt[e] = mfma16(af[e][i], ones, zt[e]);
                }
            SB0();
#pragma unroll
            for (int i = 0; i < 4; ++i)
#pragma unroll
                for (int e = 0; e < 2; ++e) k1[e] = mfma16(af[e][i], b1[i], k1[e]);
            st_wt(rsrc, kv_st, nz4(__builtin_bit_cast(u32x4, pack44(k0[0], k0[1]))));
            const u32x4 zo = nz4((l & 1) ? zt[1] : zt[0]);
            ST_MASK("global_store_dwordx4", 0x0003000300030003ull, WS_Z + ((hp * 4 + rg) * 16 + mt) * 128 + (l & 1) * 64 + g * 16, zo, ws);
            st_wt(rsrc, kv_st + 16384, nz4(__builtin_bit_cast(u32x4, pack44(k1[0], k1[1]))));
            SB0();
            flag(WR + L_FKS);
        };
        auto report = [&](int r) AI {
            unsigned t, a;
            if (r > RLD) asm volatile("v_mov_b32 %0, %3\n\tv_mov_b32 %1, %2\n\tds_write_b32 %1, %0" : "=&v"(t), "=&v"(a) : "s"(WR + L_PRL), "n"(r) : "memory");
            else asm volatile("global_load_lds_dword %0, %1 offset:%2" ::"v"(v0), "s"(one), "n"(4 * r) : "memory");
        };
        auto rd1 = [&](auto F, auto H) AI {
            constexpr int f = F, h = H, n = f >> 2, j = f >> 3, c = h ? E_B[f] : E_A[f], a = c % 100;
            if constexpr (h == 0 && (f & 3) == 0 && Q_C[n] > 0) chk(Q_C[n]);
            wh[f % (PD + 1)][h] = tr_read(ra[f & 3] + (J_U[j] - J_B[j]) * 8192 + (n & 1) * 4096 + h * 512);
            SB0();
            if constexpr (c / 100 == 1) {
                constexpr int jn = (f + 3 - a) >> 3;
                asm volatile("v_add_u32 %0, %1, %0" : "+v"(ra[a]) : "n"((J_B[jn] - J_B[jn - 1]) * 8192));
            } else if constexpr (c / 50 == 4) {
                asm volatile("v_add_u32 %0, %1, %2" : "=v"(xt[0]) : "s"((hs ? X_U1[a] : X_U0[a]) * 8192), "v"(xa));
            } else if constexpr (c / 50 == 5) {
                asm volatile("v_xor_b32 %0, 64, %1" : "=v"(xt[1]) : "v"(xt[0]));
            } else if constexpr (c / 100 == 3) {
                xf[a % 25 % ND][a / 25 & 1][a / 50] = lds_read128(xt[a / 50] + (a / 25 & 1) * 2048);
            } else if constexpr (c / 100 == 4) {
                fv = *(volatile AS3 unsigned*)(size_t)(fa + (a > NM1 ? 32 : 0));
            } else if constexpr (c / 100 == 5) {
                bad = __builtin_amdgcn_ballot_w64(fv < (unsigned)a);
                asm volatile("" : "+s"(bad));
            } else if constexpr (c / 100 == 6) {
                report(a);
            }
        };
        sfor<0, PD>([&](auto F) { rd1(F, IC<0>{}); rd1(F, IC<1>{}); });
        constexpr int K0F = 4 * KD, K1F = 4 * KD + 1;
        sfor<0, 96>([&](auto N) {
            sfor<0, 4>([&](auto E) {
                constexpr int n = N, e = E, f = 4 * n + e, j = n >> 1, ks = n & 1, u = e, s = J_X[j], m = J_M[j];
                const bf16x8 wv = cat8(wh[f % (PD + 1)][0], wh[f % (PD + 1)][1]);
                sfor<0, 2>([&](auto RB) {
                    constexpr int rb = RB;
                    const bf16x8 xv = xf[s % ND][rb][ks];
                    if (j_first(j) && ks == 0) asm volatile("v_mfma_f32_16x16x32_bf16 %0, %1, %2, 0" : "=&v"(acc[m][u][rb]) : "v"(m == 1 ? xv : wv), "v"(m == 1 ? wv : xv));
                    else asm volatile("v_mfma_f32_16x16x32_bf16 %0, %1, %2, %0" : "+v"(acc[m][u][rb]) : "v"(m == 1 ? xv : wv), "v"(m == 1 ? wv : xv));
                    SB0();
                    if constexpr (f + PD < 384 && (f + PD < 4 * VD || f >= 4 * VD)) rd1(IC<f + PD>{}, RB);
                    if constexpr (rb == 0 && f == K0F) relane();
                    if constexpr (rb == 0 && (f == K0F || f == K1F)) asm volatile("s_nop 7");
                    SB0();
                    if constexpr (f == K0F) lds_write<bf16x8>(AX + (P_U[0] * 8192 - L_A) + rb * 1024, pack44(acc[0][0][rb], acc[0][1][rb]));
                    if constexpr (f == K1F) {
                        kown[rb] = pack44(acc[0][2][rb], acc[0][3][rb]);
                        asm volatile("" : "+v"(kown[rb]));
                    }
                    SB0();
                });
                if constexpr (f == K0F) flag(WR + L_FK0);
                if constexpr (f == 4 * VD - 1) {
                    bf16x8 ak[2];
                    f32x4 pk[2][2];
                    unsigned pw[4];
                    __builtin_amdgcn_s_setprio(2);
                    phi_w(ak, 1);
                    asm volatile("s_nop 7");
                    SB0();
#pragma unroll
                    for (int i = 0; i < 4; ++i) lds_write<bf16x8>(AX + i * 1024, pack44(acc[1][i][0], acc[1][i][1]));
                    SB0();
#pragma unroll
                    for (int i = 0; i < 4; ++i) pk[i >> 1][i & 1] = rbf4(mfma16(ak[i & 1], kown[i >> 1], zero));
                    phi_adr(pw, lds0 + P_U[2] * 8192);
                    phi_k(pk, pw, 0, []() {});
                    SB0();
                    flag(WR + L_FV);
                    kv_part();
                    __builtin_amdgcn_s_setprio(1);
                    fv = 0;
                    bad = ~0ull;
                    sfor<4 * VD, 4 * VD + PD>([&](auto F) { rd1(F, IC<0>{}); rd1(F, IC<1>{}); });
                }
            });
        });
        asm volatile("s_nop 7");
        SB0();
        __builtin_amdgcn_s_setprio(0);
        flag(WR + L_FQD);
        bf16x8 a0[2], a1[2];
        f32x4 pq[2][2][2];
        phi_w(a1, hs);
        phi_w(a0, hs ^ 1);
#pragma unroll
        for (int rb = 0; rb < 2; ++rb) {
            const bf16x8 b0 = pack44(acc[2][0][rb], acc[2][1][rb]), b1 = pack44(acc[2][2][rb], acc[2][3][rb]);
#pragma unroll
            for (int fb = 0; fb < 2; ++fb) {
                pq[1][rb][fb] = rbf4(mfma16(a1[fb], hs ? b1 : b0, zero));
                pq[0][rb][fb] = rbf4(mfma16(a0[fb], hs ? b0 : b1, zero));
            }
        }
        softmax2(pq[1], ph, []() {});
        lds_wait(W4 + L_FKS, 1);
#pragma unroll
        for (int i = 0; i < 4; ++i) lds_write<u32x4>(AX + i * 1024, ph[i >> 1][i & 1]);
        SB0();
        flag(WR + L_FQ);
        sq = lds_read<u32x4>(lds0 + L_SEL);
        softmax2(pq[0], ph, []() {});
        asm volatile("" : "+v"(ph[0][0]), "+v"(ph[0][1]), "+v"(ph[1][0]), "+v"(ph[1][1]));
        xg_def();
        kf_def();
    }
    relane();
    const char* cla = ws + WS_CL + ((rt * 2 + kp) * 16 + l15) * 8;
    const u64 T = (u64)sq[1] << 32 | sq[0];
    u64 cl;
    xg_load(w & 5);
    if ((w & 5) != 4) {
        lds_wait0(lds0 + L_FKF + oh * 4);
#pragma unroll
        for (int i = 0; i < 4; ++i) kf[i] = lds_read<u32x4>(lds0 + L_KF + oh * 4096 + i * 1024 + l16);
#pragma unroll
        for (int i = 0; i < 2; ++i) kf[4 + i] = lds_read<u32x4>(lds0 + L_KZ + oh * 128 + i * 64 + g * 16);
    }
    asm volatile("global_load_dwordx2 %0, %1, off sc1" : "=&v"(cl) : "v"(cla) : "memory");
    o_store(kf, ph, rsrc, WS_O + ((mt * 2 + hs) * 32 + 2 * hp + oh) * 4096 + (rg & 1) * 2048 + l16);
    flag(lds0 + L_FOS + w * 4);
    waitv<3>();
    flag(lds0 + L_FWO + w * 4);
    __builtin_amdgcn_s_setprio(0);
    if ((w & 5) == 5) {
        lds_wait(lds0 + L_FOS + sch * 4, 1);
        wo_load(WON, 16, true);
    }
    const u32x2 selv = {sq[2], sq[3]};

    const char* ob = ws + WS_O + (rt * 2 + (rg >> 1)) * 131072 + (rg & 1) * 2048;
    const unsigned ov0 = l16 + kp * 1024, ov1 = l16 + (kp ^ 1) * 1024;
    auto oa = [&](int i) { return ob + ((2 * i + kp + rot) & 31) * 4096; };
    u32x4 of[3][4];
    auto fetch1 = [&](int k, int j) AI { ld_async(of[k % 3][j], (j & 1) ? ov1 : ov0, oa(2 * k + (j >> 1))); };
    auto fix = [&](int k) AI { settle(of[k % 3], [&](int j) { return (j & 1) ? ov1 : ov0; }, [&](int j) { return oa(2 * k + (j >> 1)); }); };
    const bool own = (__builtin_amdgcn_readfirstlane(selv[0]) >> 4) == (unsigned)(rt * 8 + rg * 2 + kp);
    float gv_[2];
    f32x2 gb[2][4];
    auto g_mix = [&]() AI {
#pragma unroll
        for (int j = 0; j < 2; ++j) {
            gv_[j] = bf2f((u16)gg[j]);
            const float og = rbf(1.f - gv_[j]);
#pragma unroll
            for (int i = 0; i < 4; ++i) {
                const unsigned t = pack2(og * lo16(xr[j][i]), og * hi16(xr[j][i]));
                gb[j][i] = (f32x2){lo16(t), hi16(t)};
                asm volatile("" : "+v"(gb[j][i]));
            }
        }
    };
    asm volatile("" : "+v"(xr[0]), "+v"(xr[1]), "+v"(gg[0]), "+v"(gg[1]));
#pragma unroll 1
    for (unsigned s = 0; s < SPIN_CAP && __ballot((gg[0] >> 16) != (T & 0xFFFFFFFFFFFFull) || (gg[1] >> 16) != (T & 0xFFFFFFFFFFFFull)); ++s) {
        g_load();
        waitv<0>();
        asm volatile("" : "+v"(gg[0]), "+v"(gg[1]));
    }
    g_mix();
    if ((w & 5) != 5) __builtin_amdgcn_s_sleep(PDL);
    if ((w & 5) != 5) waitv<2>();
    else waitv<3 + 4 * (16 - WON)>();
    asm volatile("" : "+v"(cl));
#pragma unroll 1
    for (unsigned s = 0; s < SPIN_CAP && __ballot(cl != T); ++s)
        asm volatile("global_load_dwordx2 %0, %1, off sc1\n\ts_waitcnt vmcnt(0)" : "=&v"(cl) : "v"(cla) : "memory");
#pragma unroll
    for (int j = 0; j < 4; ++j) fetch1(0, j);
    f32x4 acc[2][4];
    unsigned wa[2][4];
#pragma unroll
    for (int i = 0; i < 8; ++i) {
        wa[i >> 2][i & 3] = ((lds0 + 4096 * kp + (4 * g + q) * 128 + ((((g & 1) << 1) | (q >> 1)) * 32) + 8 * p) ^ ((i & 3) << 5)) + (i >> 2) * 65536;
        asm volatile("" : "+v"(wa[i >> 2][i & 3]));
    }
    s16x4 wf[2][8][2];
    auto wrd = [&](int k, int i) AI {
        const int u = W_U[2 * k + (i >> 3)];
        wf[k & 1][i >> 1][i & 1] = tr_read(wa[u >> 3][(i >> 1) & 3] + (u & 7) * 8192 + (i & 1) * 2048);
    };
    u32x4 yp[14];
    const unsigned yo = srow * 4096 + cb * 256 + sch * 32;
    auto ya = [&](int i) { return ws + WS_Y + (i >> 1) * 32768 + (i & 1) * 16; };
    lds_wait(lds0 + L_FWD + ((l & 8) ? 16 + sch * 4 : (l & 1) * 4), 1);
#pragma unroll
    for (int i = 0; i < 16; ++i) wrd(0, i);
#pragma unroll
    for (int j = 0; j < 4; ++j) fetch1(1, j);
    waitv<4>();
    fix(0);
    sfor<0, 8>([&](auto K) {
        constexpr int k = K;
        unsigned m[4];
        if constexpr (2 * (k + 1) == WON) lds_wait(lds0 + L_FW2 + (l & 1) * 4, 1);
        if constexpr (k == 7) {
            if (own) {
#pragma unroll
                for (int i = 0; i < 14; ++i) ld_async(yp[i], yo, ya(i));
            }
        }
        SB0();
        sfor<0, 16>([&](auto I) {
            constexpr int i = I, j = i >> 2, nb = i & 3;
            const bf16x8 a = cat8(wf[k & 1][(j >> 1) * 4 + nb][0], wf[k & 1][(j >> 1) * 4 + nb][1]);
            if (k == 0 && j < 2) asm volatile("v_mfma_f32_16x16x32_bf16 %0, %1, %2, 0" : "=&v"(acc[j & 1][nb]) : "v"(a), "v"(of[k % 3][j]));
            else asm volatile("v_mfma_f32_16x16x32_bf16 %0, %1, %2, %0" : "+v"(acc[j & 1][nb]) : "v"(a), "v"(of[k % 3][j]));
            SB0();
            if constexpr (k < 7) wrd(k + 1, i);
            if constexpr (k < 6 && i < 4) fetch1(k + 2, i);
            if constexpr (k < 7 && i == 10) {
                waitv<(k < 6 ? 4 : 0)>();
#pragma unroll
                for (int n = 0; n < 4; ++n) pin(of[(k + 1) % 3][n]);
            }
            if constexpr (k < 7 && i > 10 && i <= 14) {
                m[i - 11] = min4(of[(k + 1) % 3][i - 11]);
                asm volatile("" : "+v"(m[i - 11]));
            }
            SB0();
        });
        if constexpr (k < 7) {
            if (__builtin_expect(__builtin_amdgcn_ballot_w64(min(min(m[0], m[1]), min(m[2], m[3])) == 0u) != 0ull, 0)) fix(k + 1);
        }
        SB0();
    });
    asm volatile("s_nop 7");
    SB0();
    const unsigned ea = lds0 + L_A + l16 + 4096 * w, eb = lds0 + L_A + 4096 * (w ^ 4);
    lds_write<u32x4>(ea, nz4(acc[1][0]));
#pragma unroll
    for (int nb = 1; nb < 4; ++nb) lds_write<f32x4>(ea + nb * 1024, acc[1][nb]);
    if (own) {
        waitv<0>();
        settle(yp, [&](int) { return yo; }, ya);
        f32x4 yv[2];
#pragma unroll
        for (int h = 0; h < 2; ++h) {
            yv[h] = __builtin_bit_cast(f32x4, yp[h]);
#pragma unroll
            for (int i = 1; i < 7; ++i) yv[h] += __builtin_bit_cast(f32x4, yp[2 * i + h]);
#pragma unroll
            for (int i = 0; i < 4; ++i) {
                float t = dpp_add<0x128>(yv[h][i]);
                t = swap16_add(t, t);
                yv[h][i] = rbf(rbf(swap32_add(t, t)) * __uint_as_float(selv[1]));
            }
        }
#pragma unroll
        for (int j = 0; j < 2; ++j) {
            const bool hit = row0 + 8 * j + srow == (int)selv[0];
#pragma unroll
            for (int i = 0; i < 4; ++i) {
                const unsigned t = pack2(lo16(xr[j][i]) + yv[i >> 1][2 * (i & 1)], hi16(xr[j][i]) + yv[i >> 1][2 * (i & 1) + 1]);
                xr[j][i] = hit ? t : xr[j][i];
            }
        }
        g_mix();
    }
    BAR();
#pragma unroll
    for (int nb = 0; nb < 4; ++nb) acc[0][nb] += lds_read<f32x4>(eb + l16 + nb * 1024);
#pragma unroll
    for (int nb = 0; nb < 4; ++nb) asm volatile("" : "+v"(acc[0][nb]));
#pragma unroll
    for (int nb = 0; nb < 4; ++nb) {
        const u32x2 a = {pack2(acc[0][nb][0], acc[0][nb][1]), pack2(acc[0][nb][2], acc[0][nb][3])};
        lds_write<u32x2>(eb + l15 * 128 + (((2 * nb + (g >> 1)) ^ (l & 7)) * 16) + (g & 1) * 8, a);
    }
#pragma unroll
    for (int j = 0; j < 2; ++j) {
        const u32x4 av = lds_read<u32x4>(eb + j * 1024 + srow * 128 + ((sch ^ srow) * 16));
        u32x4 o;
#pragma unroll
        for (int i = 0; i < 4; ++i) {
            const unsigned ga = pack2(gv_[j] * lo16(av[i]), gv_[j] * hi16(av[i]));
            o[i] = pack2(lo16(ga) + gb[j][i][0], hi16(ga) + gb[j][i][1]);
        }
        asm volatile("global_store_dwordx4 %0, %1, %2 sc1\n\ts_nop 1" ::"v"(xo), "v"(o), "s"(out + (size_t)(row0 + 8 * j) * DM + cb * 64) : "memory");
    }
}

extern "C" void kernel_launch(void* const* d_in, const int* in_sizes, int n_in, void* d_out, int out_size,
                              void* d_ws, size_t ws_size, hipStream_t stream) {
    fused_mix<<<256, 512, 0, stream>>>((const u16*)d_in[0], (const u16*)d_in[1], (const u16*)d_in[2], (const u16*)d_in[3],
                                       (const u16*)d_in[5], (const u16*)d_in[6], (const u16*)d_in[7], (const u16*)d_in[8],
                                       (const u16*)d_in[9], (char*)d_ws, (u16*)d_out);
}
```

```cpp
#include <hip/hip_runtime.h>
#include <hip/hip_bf16.h>
#include <stdint.h>
#include <type_traits>

typedef __attribute__((ext_vector_type(8))) short bf16x8;
typedef __attribute__((ext_vector_type(4))) short s16x4;
typedef __attribute__((ext_vector_type(4))) float f32x4;
typedef __attribute__((ext_vector_type(2))) float f32x2;
typedef __attribute__((ext_vector_type(2))) unsigned u32x2;
typedef __attribute__((ext_vector_type(4))) unsigned u32x4;
typedef __attribute__((ext_vector_type(2))) __bf16 bf16x2_t;
typedef unsigned short u16;
typedef unsigned long long u64;

#define DI __device__ __forceinline__
#define AS3 __attribute__((address_space(3)))

constexpr int DM = 1024;

DI float bf2f(u16 v) { return __uint_as_float(((unsigned)v) << 16); }
DI u16 f2bf(float f) { __bf16 b = (__bf16)f; return __builtin_bit_cast(u16, b); }
DI float rbf(float f) { return bf2f(f2bf(f)); }
DI unsigned pack2(float lo, float hi) {
    const f32x2 v = {lo, hi};
    return __builtin_bit_cast(unsigned, __builtin_convertvector(v, bf16x2_t));
}
DI float lo16(unsigned u) { return __uint_as_float(u << 16); }
DI float hi16(unsigned u) { return __uint_as_float(u & 0xffff0000u); }

DI s16x4 tr_read(unsigned a) { return __builtin_amdgcn_ds_read_tr16_b64_v4i16((AS3 s16x4*)(size_t)a); }
DI bf16x8 lds_read128(unsigned a) { return *(AS3 const bf16x8*)(size_t)a; }
template <class T> DI void lds_write(unsigned a, T v) { *(AS3 T*)(size_t)a = v; }
template <class T> DI T lds_read(unsigned a) { return *(AS3 const T*)(size_t)a; }
DI bf16x8 cat8(s16x4 a, s16x4 b) { return __builtin_shufflevector(a, b, 0, 1, 2, 3, 4, 5, 6, 7); }
DI f32x4 mfma16(bf16x8 a, bf16x8 b, f32x4 c) { return __builtin_amdgcn_mfma_f32_16x16x32_bf16(a, b, c, 0, 0, 0); }
DI bf16x8 pack44(const f32x4& a, const f32x4& b) {
    u32x4 r;
    r[0] = pack2(a[0], a[1]);
    r[1] = pack2(a[2], a[3]);
    r[2] = pack2(b[0], b[1]);
    r[3] = pack2(b[2], b[3]);
    return __builtin_bit_cast(bf16x8, r);
}
DI unsigned lds_of(const void* p) { return (unsigned)(size_t)(AS3 const char*)p; }

template <int CTRL>
DI unsigned dpp(unsigned x) { return __builtin_amdgcn_update_dpp(0u, x, CTRL, 0xf, 0xf, true); }
template <int CTRL>
DI float dpp_add(float x) { return x + __uint_as_float(dpp<CTRL>(__float_as_uint(x))); }
DI float swap32_add(float a, float b) {
    auto r = __builtin_amdgcn_permlane32_swap(__float_as_uint(a), __float_as_uint(b), false, false);
    return __uint_as_float(r[0]) + __uint_as_float(r[1]);
}
DI float swap16_add(float a, float b) {
    auto r = __builtin_amdgcn_permlane16_swap(__float_as_uint(a), __float_as_uint(b), false, false);
    return __uint_as_float(r[0]) + __uint_as_float(r[1]);
}
DI unsigned wave_umax(unsigned x) {
    x = max(x, dpp<0xB1>(x));
    x = max(x, dpp<0x4E>(x));
    x = max(x, dpp<0x141>(x));
    x = max(x, dpp<0x140>(x));
    auto r = __builtin_amdgcn_permlane16_swap(x, x, false, false);
    x = max(r[0], r[1]);
    auto s = __builtin_amdgcn_permlane32_swap(x, x, false, false);
    return max(s[0], s[1]);
}
constexpr unsigned WS_E = 0;
constexpr unsigned WS_RG = 256;
constexpr unsigned WS_GG = WS_RG + 2048;
constexpr unsigned WS_Z = 65536;
constexpr unsigned WS_KF = WS_Z + 131072;
constexpr unsigned WS_GU = WS_KF + 262144;
constexpr unsigned WS_Y = WS_GU + 262144;
constexpr unsigned WS_KV = WS_Y + 262144;
constexpr unsigned WS_O = WS_KV + (2u << 20);
constexpr unsigned WS_CL = WS_O + (4u << 20);
constexpr unsigned WS_END = WS_CL + 65536;

constexpr int NQ = 10, ND = 6, NX = 16, KD = 78, VD = 84, NF = 64, NF1 = 59, NF2 = 59, NP1 = 12, NP = 14, NM1 = 21, NM2 = 21, NM = 22, KFN = 100;
constexpr int J_S[48] = {0, 0, 0, 1, 1, 1, 2, 2, 2, 3, 3, 3, 4, 4, 4, 5, 5, 5, 6, 6, 6, 7, 7, 7, 8, 8, 8, 9, 9, 9, 10, 10, 11, 11, 12, 12, 13, 14, 15, 13, 14, 15, 10, 11, 12, 13, 14, 15};
constexpr int J_M[48] = {0, 1, 2, 0, 1, 2, 0, 1, 2, 0, 1, 2, 0, 1, 2, 0, 1, 2, 0, 1, 2, 0, 1, 2, 0, 1, 2, 0, 1, 2, 0, 1, 0, 1, 0, 1, 0, 0, 0, 1, 1, 1, 2, 2, 2, 2, 2, 2};
constexpr int J_X[48] = {0, 0, 0, 1, 1, 1, 2, 2, 2, 3, 3, 3, 4, 4, 4, 5, 5, 5, 6, 6, 6, 7, 7, 7, 8, 8, 8, 9, 9, 9, 10, 10, 11, 11, 12, 12, 13, 14, 15, 13, 14, 15, 10, 11, 12, 13, 14, 15};
constexpr int J_U[48] = {2, 4, 8, 18, 11, 12, 17, 15, 13, 0, 1, 6, 4, 8, 9, 11, 12, 14, 15, 13, 7, 1, 6, 5, 8, 9, 10, 12, 14, 16, 13, 7, 1, 6, 4, 8, 18, 14, 15, 13, 7, 3, 0, 1, 6, 5, 2, 4};
constexpr int J_B[48] = {2, 2, 2, 11, 11, 11, 11, 11, 11, 0, 0, 0, 0, 8, 8, 8, 8, 8, 8, 8, 1, 1, 1, 1, 1, 9, 9, 9, 9, 9, 9, 1, 1, 1, 1, 1, 13, 13, 13, 13, 0, 0, 0, 0, 0, 0, 0, 0};
constexpr int Q_C[96] = {1, 0, 2, 0, 3, 0, 4, 0, 5, 0, 6, 0, 7, 0, 0, 0, 0, 0, 8, 0, 0, 0, 0, 0, 9, 0, 0, 0, 0, 0, 10, 0, 0, 0, 0, 0, 11, 0, 0, 0, 0, 0, 12, 0, 0, 0, 0, 0, 13, 0, 0, 0, 0, 0, 14, 0, 0, 0, 0, 0, 15, 0, 0, 0, 0, 0, 16, 0, 0, 0, 0, 0, 17, 0, 0, 0, 0, 0, 18, 0, 19, 0, 20, 0, 21, 0, 22, 0, 0, 0, 0, 0, 0, 0, 0, 0};
constexpr int X_U0[16] = {6, 9, 14, 7, 5, 10, 16, 3, 2, 18, 17, 3, 5, 9, 11, 16};
constexpr int X_U1[16] = {5, 10, 16, 3, 2, 18, 17, 0, 4, 11, 15, 0, 2, 10, 12, 17};
constexpr int P_U[4] = {11, 12, 9, 10};
constexpr int W_U[16] = {8, 14, 15, 13, 7, 3, 9, 10, 11, 12, 0, 1, 6, 5, 2, 4};
constexpr int W_N[16] = {100, 100, 100, 100, 100, 100, 101, 101, 101, 101, 13, 13, 14, 14, 99, 99};
constexpr int F_J[64] = {-1, 0, 1, 2, -2, 3, 4, 5, -3, 6, 7, 8, -4, 9, 10, 11, -5, 12, 13, 14, -6, 15, 16, 17, -7, 18, 19, 20, -8, 21, 22, 23, -9, 24, 25, 26, -10, 27, 28, 29, -11, 30, 31, -12, 32, 33, -13, 34, 35, -14, 36, -15, 37, -16, 38, 39, 40, 41, 42, 43, 44, 45, 46, 47};
constexpr int F_N[64] = {0, 0, 0, 0, 0, 0, 0, 0, 0, 0, 0, 0, 0, 0, 0, 1, 1, 1, 1, 1, 2, 2, 2, 2, 3, 3, 3, 3, 4, 4, 4, 4, 5, 5, 5, 5, 6, 6, 6, 6, 7, 7, 7, 7, 8, 8, 8, 8, 9, 9, 9, 10, 10, 10, 10, 11, 11, 11, 11, 12, 12, 12, 12, 12};
constexpr int F_K[64] = {0, 1, 2, 3, 0, 4, 5, 6, 0, 0, 0, 7, 0, 0, 0, 8, 0, 0, 0, 9, 0, 0, 0, 10, 0, 0, 0, 11, 0, 0, 0, 12, 0, 0, 0, 13, 0, 0, 0, 14, 0, 0, 0, 0, 15, 0, 0, 0, 16, 0, 0, 0, 0, 0, 17, 18, 19, 20, 21, 0, 0, 0, 0, 22};
constexpr int E_A[384] = {300, 325, 350, 375, 0, 0, 0, 502, 0, 0, 0, 403, 0, 0, 0, 503, 201, 251, 0, 404, 0, 100, 101, 504, 301, 326, 351, 376, 0, 0, 0, 505, 601, 0, 0, 406, 0, 0, 0, 506, 202, 252, 0, 407, 0, 0, 0, 507, 302, 327, 352, 377, 0, 0, 0, 602, 0, 0, 0, 0, 0, 0, 0, 203, 253, 0, 0, 408, 0, 100, 101, 508, 303, 328, 353, 378, 0, 0, 0, 603, 0, 0, 0, 0, 0, 0, 0, 204, 254, 0, 0, 409, 0, 0, 0, 509, 304, 329, 354, 379, 0, 100, 101, 604, 102, 103, 0, 0, 0, 0, 0, 205, 255, 0, 0, 410, 0, 0, 0, 510, 305, 330, 355, 380, 0, 0, 0, 605, 0, 0, 0, 0, 0, 0, 0, 206, 256, 0, 0, 411, 0, 0, 0, 511, 306, 331, 356, 381, 0, 0, 0, 606, 0, 0, 0, 0, 0, 100, 101, 102, 103, 207, 257, 412, 0, 0, 0, 512, 307, 332, 357, 382, 0, 0, 0, 607, 0, 0, 0, 0, 0, 0, 0, 208, 258, 0, 0, 413, 0, 0, 0, 513, 308, 333, 358, 383, 0, 100, 101, 608, 102, 103, 0, 0, 0, 0, 0, 209, 259, 0, 0, 414, 0, 0, 0, 514, 309, 334, 359, 384, 0, 0, 0, 609, 0, 0, 0, 0, 0, 0, 0, 210, 260, 0, 0, 415, 0, 0, 0, 515, 310, 335, 360, 385, 0, 100, 101, 610, 102, 103, 211, 261, 0, 0, 0, 0, 311, 336, 361, 386, 0, 0, 0, 516, 611, 212, 262, 0, 0, 0, 0, 0, 312, 337, 362, 387, 0, 0, 0, 213, 263, 0, 0, 417, 0, 100, 101, 517, 313, 338, 363, 388, 214, 264, 0, 612, 314, 339, 364, 389, 215, 265, 0, 0, 315, 340, 365, 390, 0, 0, 0, 518, 0, 0, 0, 419, 0, 100, 101, 519, 102, 103, 0, 420, 0, 0, 0, 520, 0, 0, 0, 0, 0, 0, 0, 0, 0, 0, 0, 422, 0, 0, 0, 522, 0, 0, 0, 0, 0, 0, 0, 0, 0, 0, 0, 613, 0, 0, 0, 0, 0, 0, 0, 0, 0, 0, 0, 0, 0, 0, 0, 614, 0, 0, 0, 0, 0, 0, 0, 0, 0, 0, 0, 0};
constexpr int E_B[384] = {0, 0, 402, 0, 0, 0, 0, 0, 0, 0, 0, 0, 0, 0, 0, 0, 0, 0, 0, 0, 0, 0, 0, 102, 103, 0, 405, 0, 0, 0, 0, 0, 0, 0, 0, 0, 0, 0, 0, 0, 0, 0, 0, 0, 0, 0, 0, 0, 0, 0, 0, 0, 0, 0, 0, 0, 0, 0, 0, 0, 0, 0, 0, 0, 0, 0, 0, 0, 0, 0, 0, 102, 103, 0, 0, 0, 0, 0, 0, 0, 0, 0, 0, 0, 0, 0, 0, 0, 0, 0, 0, 0, 0, 0, 0, 0, 0, 0, 0, 0, 0, 0, 0, 0, 0, 0, 0, 0, 0, 0, 0, 0, 0, 0, 0, 0, 0, 0, 0, 0, 0, 0, 0, 0, 0, 0, 0, 0, 0, 0, 0, 0, 0, 0, 0, 0, 0, 0, 0, 0, 0, 0, 0, 0, 0, 0, 0, 0, 0, 0, 0, 0, 0, 0, 0, 0, 0, 0, 0, 0, 0, 0, 0, 0, 0, 0, 0, 0, 0, 0, 0, 0, 0, 0, 0, 0, 0, 0, 0, 0, 0, 0, 0, 0, 0, 0, 0, 0, 0, 0, 0, 0, 0, 0, 0, 0, 0, 0, 0, 0, 0, 0, 0, 0, 0, 0, 0, 0, 0, 0, 0, 0, 0, 0, 0, 0, 0, 0, 0, 0, 0, 0, 0, 0, 0, 0, 0, 0, 0, 0, 0, 0, 0, 0, 0, 0, 0, 0, 0, 0, 0, 0, 0, 0, 0, 0, 0, 0, 0, 0, 0, 0, 0, 0, 0, 0, 0, 0, 416, 0, 0, 0, 0, 0, 0, 0, 0, 0, 0, 0, 0, 0, 0, 0, 0, 0, 0, 0, 0, 0, 0, 0, 0, 0, 0, 0, 0, 102, 103, 0, 0, 0, 0, 0, 0, 0, 0, 0, 0, 0, 0, 0, 0, 0, 0, 0, 418, 0, 0, 0, 0, 0, 0, 0, 0, 0, 0, 0, 0, 0, 0, 0, 0, 0, 0, 0, 0, 0, 0, 0, 0, 0, 0, 0, 0, 0, 0, 0, 0, 0, 0, 0, 0, 0, 0, 0, 0, 0, 0, 0, 0, 0, 0, 0, 0, 0, 0, 0, 0, 0, 0, 0, 0, 0, 0, 0, 0, 0, 0, 0, 0, 0, 0, 0, 0, 0, 0, 0, 0, 0, 0, 0, 0, 0};
constexpr unsigned L_A = 16 * 8192;
constexpr unsigned L_KF = 18 * 8192;
constexpr unsigned L_SM = 19 * 8192;
constexpr unsigned L_FULL = L_SM + 256;
constexpr unsigned L_PROG = L_SM + 896;
constexpr unsigned L_PRL = L_SM + 336;
constexpr unsigned L_FW2 = L_SM + 512, L_FOS = L_SM + 480, L_FK0 = L_SM + 752, L_FV = L_SM + 560, L_FP = L_SM + 576, L_FKS = L_SM + 592;
constexpr unsigned L_FQD = L_SM + 608, L_FG = L_SM + 624, L_FKF = L_SM + 736, L_FQ = L_SM + 720, L_FWD = L_SM + 640, L_FWO = L_SM + 656;
constexpr unsigned L_FLD = L_SM + 688, L_BEST = L_SM + 696;
constexpr unsigned L_SEL = L_SM + 704;
constexpr unsigned L_XS = L_SM + 2048, L_KZ = L_SM + 3072, L_WPHI = L_SM + 4096;
static_assert(NP <= 31 && NM <= 63 && NF1 == NF2 && NX == 16, "");
static_assert(P_U[1] == P_U[0] + 1 && P_U[3] == P_U[2] + 1, "feature maps of a head lie side by side");
constexpr bool j_first(int j) {
    for (int i = 0; i < j; ++i)
        if (J_M[i] == J_M[j]) return false;
    return true;
}
constexpr unsigned SPIN_CAP = 1u << 16;
#define N8(a) a, a + 1, a + 2, a + 3, a + 4, a + 5, a + 6, a + 7
__device__ const unsigned k_num[64] = {N8(0), N8(8), N8(16), N8(24), N8(32), N8(40), N8(48), N8(56)};

constexpr int PD = 4;
constexpr int RLD = F_N[NF1 - 1];
constexpr int CLR0 = 2;
constexpr int GQ1 = 6;
constexpr int WOE = 6, WON = 12;
constexpr int GDL = 20, KFD = 24, PDL = 6;
constexpr unsigned CPF = 53248;
constexpr u64 KXB = 0xFEDBA9C768534210ull;
#define BAR() asm volatile("s_waitcnt lgkmcnt(0)\n\ts_barrier" ::: "memory")
template <int N>
DI void waitv() { asm volatile("s_waitcnt vmcnt(%0)" ::"n"(N) : "memory"); }

DI unsigned nz1(unsigned v) { return max(v, 1u); }
DI u32x4 nz4(u32x4 v) { return (u32x4){max(v[0], 1u), max(v[1], 1u), max(v[2], 1u), max(v[3], 1u)}; }
DI u32x4 nz4(f32x4 v) { return nz4(__builtin_bit_cast(u32x4, v)); }
DI unsigned min4(u32x4 v) { return min(min(v[0], v[1]), min(v[2], v[3])); }
DI void st_wt(__amdgpu_buffer_rsrc_t r, unsigned off, u32x4 v) { __builtin_amdgcn_raw_buffer_store_b128(v, r, off, 0, 16); }
DI void ld_async(u32x4& v, unsigned off, const char* base) {
    asm volatile("global_load_dwordx4 %0, %1, %2 sc1" : "=&v"(v) : "v"(off), "s"(base) : "memory");
}
#define ST_MASK(op, m, off, v, base) asm volatile("s_mov_b64 exec, %3\n\t" op " %0, %1, %2 sc1\n\ts_mov_b64 exec, -1\n\ts_nop 0" ::"v"(off), "v"(v), "s"(base), "s"((u64)(m)) : "memory")
DI void pin(u32x4& v) { asm volatile("" : "+v"(v)); }
template <int N, class OFF, class BASE>
DI void settle(u32x4 (&v)[N], OFF off, BASE base) {
#pragma unroll
    for (int i = 0; i < N; ++i) pin(v[i]);
#pragma unroll 1
    for (unsigned s = 0; s < SPIN_CAP; ++s) {
        unsigned m = min4(v[0]);
#pragma unroll
        for (int i = 1; i < N; ++i) m = min(m, min4(v[i]));
        if (__ballot(m == 0u) == 0ull) break;
#pragma unroll
        for (int i = 0; i < N; ++i) {
            u64 keep;
            asm volatile("s_mov_b64 %1, exec\n\ts_mov_b64 exec, %4\n\tglobal_load_dwordx4 %0, %2, %3 sc1\n\ts_mov_b64 exec, %1"
                         : "+&v"(v[i]), "=&s"(keep) : "v"(off(i)), "s"(base(i)), "s"((u64)__ballot(min4(v[i]) == 0u)) : "memory");
        }
        waitv<0>();
#pragma unroll
        for (int i = 0; i < N; ++i) pin(v[i]);
    }
}
DI void lds_wait(unsigned a, unsigned n) {
    unsigned v, c;
    asm volatile("s_mov_b32 %1, 0x400000\n"
                 ".Lp%=:\n\tds_read_b32 %0, %2\n\ts_waitcnt lgkmcnt(0)\n\tv_cmp_gt_u32 vcc, %3, %0\n\ts_cbranch_vccz .Lf%=\n\t"
                 "s_sleep 1\n\ts_sub_u32 %1, %1, 1\n\ts_cbranch_scc0 .Lp%=\n.Lf%=:"
                 : "=&v"(v), "=&s"(c) : "v"(a), "s"(n) : "memory", "vcc", "scc");
}
DI void lds_wait0(unsigned a) {
    unsigned v, c;
    asm volatile("s_mov_b32 %1, 0x1000000\n"
                 ".Lp%=:\n\tds_read_b32 %0, %2\n\ts_sub_u32 %1, %1, 1\n\ts_cbranch_scc1 .Lf%=\n\ts_waitcnt lgkmcnt(0)\n\tv_cmp_eq_u32 vcc, 0, %0\n\ts_cbranch_vccnz .Lp%=\n"
                 ".Lf%=:\n\ts_waitcnt lgkmcnt(0)"
                 : "=&v"(v), "=&s"(c) : "v"(a) : "memory", "vcc", "scc");
}
DI void lds_wait8(unsigned a, unsigned n) {
    unsigned v, c;
    asm volatile("s_mov_b32 %1, 0x100000\n"
                 ".Lp%=:\n\tds_read_b32 %0, %2\n\ts_waitcnt lgkmcnt(0)\n\tv_cmp_gt_u32 vcc, %3, %0\n\ts_cbranch_vccz .Lf%=\n\t"
                 "s_sleep 4\n\ts_sub_u32 %1, %1, 1\n\ts_cbranch_scc0 .Lp%=\n.Lf%=:"
                 : "=&v"(v), "=&s"(c) : "v"(a), "s"(n) : "memory", "vcc", "scc");
}
DI void lds_peek(unsigned& v, unsigned a) { asm volatile("ds_read_b32 %0, %1" : "=v"(v) : "v"(a) : "memory"); }
DI void lds_wait(unsigned& v, unsigned a, unsigned n) {
    unsigned c;
    asm volatile("s_mov_b32 %1, 0x400000\n"
                 ".Lp%=:\n\ts_waitcnt lgkmcnt(0)\n\tv_cmp_gt_u32 vcc, %3, %0\n\ts_cbranch_vccz .Lf%=\n\t"
                 "ds_read_b32 %0, %2\n\ts_sub_u32 %1, %1, 1\n\ts_cbranch_scc0 .Lp%=\n.Lf%=:"
                 : "+v"(v), "=&s"(c) : "v"(a), "s"(n) : "memory", "vcc", "scc");
}
DI float dot2(unsigned a, unsigned b, float c) {
    return __builtin_amdgcn_fdot2_f32_bf16(__builtin_bit_cast(bf16x2_t, a), __builtin_bit_cast(bf16x2_t, b), c, false);
}
DI float sum4(const float (&a)[4]) {
    float c = swap16_add(swap32_add(a[0], a[1]), swap32_add(a[2], a[3]));
    c = dpp_add<0xB1>(c);
    c = dpp_add<0x4E>(c);
    c = dpp_add<0x141>(c);
    return dpp_add<0x140>(c);
}

template <class OP, class POST>
DI void red4(float& a, float& b, float& c, float& d, OP op, POST post) {
    auto x = __builtin_amdgcn_permlane32_swap(__float_as_uint(a), __float_as_uint(b), false, false);
    auto y = __builtin_amdgcn_permlane32_swap(__float_as_uint(c), __float_as_uint(d), false, false);
    const float m = op(__uint_as_float(x[0]), __uint_as_float(x[1])), n = op(__uint_as_float(y[0]), __uint_as_float(y[1]));
    auto z = __builtin_amdgcn_permlane16_swap(__float_as_uint(m), __float_as_uint(n), false, false);
    const unsigned f = __float_as_uint(post(op(__uint_as_float(z[0]), __uint_as_float(z[1]))));
    auto u = __builtin_amdgcn_permlane16_swap(f, f, false, false);
    auto v = __builtin_amdgcn_permlane32_swap(u[0], u[0], false, false);
    auto t = __builtin_amdgcn_permlane32_swap(u[1], u[1], false, false);
    a = __uint_as_float(v[0]);
    b = __uint_as_float(v[1]);
    c = __uint_as_float(t[0]);
    d = __uint_as_float(t[1]);
}
DI float mx2(float a, float b) { return __builtin_elementwise_maximum(a, b); }
template <class HOOK>
DI void softmax2(const f32x4 (&v)[2][2], u32x4 (&ph)[2][2], HOOK hook) {
    constexpr float L2E = 1.4426950408889634f;
    float mx[2], mn[2], sp_[2], sn_[2];
#pragma unroll
    for (int b = 0; b < 2; ++b) {
        const f32x4 s = v[b][0], t = v[b][1];
        mx[b] = mx2(mx2(mx2(mx2(s[0], s[1]), s[2]), mx2(mx2(s[3], t[0]), t[1])), mx2(t[2], t[3]));
        mn[b] = mx2(mx2(mx2(mx2(-s[0], -s[1]), -s[2]), mx2(mx2(-s[3], -t[0]), -t[1])), mx2(-t[2], -t[3]));
    }
    red4(mx[0], mx[1], mn[0], mn[1], [](float a, float b) { return mx2(a, b); }, [](float a) { return a * -L2E; });
    f32x2 ep[2][4], en[2][4];
#pragma unroll
    for (int b = 0; b < 2; ++b) {
#pragma unroll
        for (int e = 0; e < 4; ++e) {
            const f32x2 u = {v[b][e >> 1][2 * (e & 1)], v[b][e >> 1][2 * (e & 1) + 1]};
            const f32x2 a = __builtin_elementwise_fma(u, (f32x2){L2E, L2E}, (f32x2){mx[b], mx[b]});
            const f32x2 c = __builtin_elementwise_fma(u, (f32x2){-L2E, -L2E}, (f32x2){mn[b], mn[b]});
            ep[b][e] = (f32x2){__builtin_amdgcn_exp2f(a[0]), __builtin_amdgcn_exp2f(a[1])};
            en[b][e] = (f32x2){__builtin_amdgcn_exp2f(c[0]), __builtin_amdgcn_exp2f(c[1])};
        }
        const f32x2 s = (ep[b][0] + ep[b][1]) + (ep[b][2] + ep[b][3]), t = (en[b][0] + en[b][1]) + (en[b][2] + en[b][3]);
        sp_[b] = s[0] + s[1];
        sn_[b] = t[0] + t[1];
    }
    asm volatile("" : "+v"(sp_[0]), "+v"(sn_[0]), "+v"(sp_[1]), "+v"(sn_[1]));
    hook();
    red4(sp_[0], sp_[1], sn_[0], sn_[1], [](float a, float b) { return a + b; }, [](float a) { return __builtin_amdgcn_rcpf(a); });
#pragma unroll
    for (int b = 0; b < 2; ++b)
#pragma unroll
        for (int e = 0; e < 4; ++e) {
            const f32x2 a = ep[b][e] * (f32x2){sp_[b], sp_[b]}, c = en[b][e] * (f32x2){sn_[b], sn_[b]};
            ph[b][0][e] = pack2(a[0], a[1]);
            ph[b][1][e] = pack2(c[0], c[1]);
        }
}
DI f32x4 rbf4(const f32x4& a) {
    const unsigned u = pack2(a[0], a[1]), v = pack2(a[2], a[3]);
    return (f32x4){lo16(u), hi16(u), lo16(v), hi16(v)};
}
DI int fsw(int row) { return (((row >> 1) & 3) << 2) | (((row >> 3) & 1) << 1) | (row & 1); }
template <class HOOK>
DI void phi_k(const f32x4 (&pp)[2][2], const unsigned (&pw)[4], unsigned off, HOOK hook) {
    u32x4 ph[2][2];
    softmax2(pp, ph, hook);
#pragma unroll
    for (int b = 0; b < 2; ++b)
#pragma unroll
        for (int kf = 0; kf < 2; ++kf)
#pragma unroll
            for (int fb = 0; fb < 2; ++fb) {
                const u32x2 a = {ph[b][kf][2 * fb], ph[b][kf][2 * fb + 1]};
                lds_write<u32x2>(pw[2 * kf + fb] + off + b * 2048, a);
            }
}

#define SB0() __builtin_amdgcn_sched_barrier(0)
template <int A, int B, class F>
DI void sfor(F f) {
    if constexpr (A < B) {
        f(std::integral_constant<int, A>{});
        sfor<A + 1, B>(f);
    }
}
template <int N> using IC = std::integral_constant<int, N>;
#define AI __attribute__((always_inline))

DI void o_store(const u32x4 (&kf)[6], const u32x4 (&ph)[2][2], __amdgpu_buffer_rsrc_t rsrc, unsigned o_st) {
    const f32x4 zero = {0.f, 0.f, 0.f, 0.f};
#pragma unroll
    for (int i = 0; i < 2; ++i) {
        const bf16x8 p0 = __builtin_bit_cast(bf16x8, ph[i][0]), p1 = __builtin_bit_cast(bf16x8, ph[i][1]);
        f32x4 dn = mfma16(__builtin_bit_cast(bf16x8, kf[4]), p0, zero);
        f32x4 n0 = mfma16(__builtin_bit_cast(bf16x8, kf[0]), p0, zero);
        f32x4 n1 = mfma16(__builtin_bit_cast(bf16x8, kf[1]), p0, zero);
        dn = mfma16(__builtin_bit_cast(bf16x8, kf[5]), p1, dn);
        n0 = mfma16(__builtin_bit_cast(bf16x8, kf[2]), p1, n0);
        n1 = mfma16(__builtin_bit_cast(bf16x8, kf[3]), p1, n1);
        const float rd_ = __builtin_amdgcn_rcpf(rbf(rbf(dn[0]) + 1e-6f));
        const u32x4 nb_ = __builtin_bit_cast(u32x4, pack44(n0, n1));
        u32x4 o;
#pragma unroll
        for (int j = 0; j < 4; ++j) o[j] = pack2(lo16(nb_[j]) * rd_, hi16(nb_[j]) * rd_);
        st_wt(rsrc, o_st + i * 1024, nz4(o));
    }
}


typedef __attribute__((ext_vector_type(16))) unsigned u32x16;
__global__ __launch_bounds__(512, 2) void fused_mix(const u16*, const u16*, const u16*, const u16*, const u16*, const u16*, const u16*, const u16*,
                                                    const u16*, char*, u16*) {
    __shared__ __attribute__((aligned(16))) char smem[163840];
    u32x4 ka;
    u32x16 kb_;
    u64 kz;
    asm volatile("s_load_dwordx4 %0, %3, 0x0\n\ts_load_dwordx16 %1, %3, 0x10\n\ts_load_dwordx2 %2, %3, 0x50"
                 : "=&s"(ka), "=&s"(kb_), "=&s"(kz) : "s"(__builtin_amdgcn_kernarg_segment_ptr()));
    SB0();
    const int tid = threadIdx.x;
    int l = tid & 63;
    const int w = __builtin_amdgcn_readfirstlane(tid >> 6) ^ 4;
    int bid = blockIdx.x;
    const unsigned lds0 = lds_of(smem);
    int g = l >> 4, l15 = l & 15, q = l15 >> 2, p = l & 3;
    int srow = l >> 3, sch = l & 7;
    unsigned l16 = l * 16;
    const int rg = w & 3, kp = w >> 2;
    const int hs = rg >> 1;
    {
        u64 pc;
        asm volatile("s_getpc_b64 %0\n\ts_mov_b32 m0, %2\n\ts_mov_b64 exec, 3\n\tglobal_load_lds_dword %1, %0\n\ts_mov_b64 exec, -1"
                     : "=&s"(pc) : "v"(min((unsigned)(((bid & ~7) + w) * 2 + l) * 128u, CPF)), "s"(lds0 + L_SM + 3584) : "memory");
    }
    lds_write<unsigned>(lds0 + L_SM + tid * 4, 0u);
    const int slot = bid >> 3;
    const int mt = (bid & 1) * 8 + (slot & 7);
    const int hp = ((bid >> 1) & 3) * 4 + (slot >> 3);
    unsigned vx[4], vw[4];
    auto vw_init = [&]() AI {
        int t = l;
        asm volatile("" : "+v"(t));
#pragma unroll
        for (int i = 0; i < 4; ++i) vw[i] = (unsigned)((8 * i + (t >> 3)) * 6144 + ((t & 7) ^ (((t >> 4) & 1) << 1) ^ ((i & 1) << 2)) * 16 + 4096 - i * 1024);
    };
    auto vx_init = [&]() AI {
        int t = l;
        asm volatile("" : "+v"(t));
#pragma unroll
        for (int i = 0; i < 4; ++i) vx[i] = (unsigned)((8 * i + (t >> 3)) * 2048 + (((t & 7) ^ ((4 * (i & 1) + (t >> 4)) & 7)) * 16) + 4096 - i * 1024);
    };
    vx_init();
    vw_init();
    asm volatile("" : "+v"(vx[0]), "+v"(vx[1]), "+v"(vx[2]), "+v"(vx[3]), "+v"(vw[0]), "+v"(vw[1]), "+v"(vw[2]), "+v"(vw[3]));
    unsigned xo0 = (mt * 128 + hs * 64) * 2048, wo0 = hp * 128 + hs * (32 * 6144);
    asm volatile("s_waitcnt lgkmcnt(0)" : "+s"(ka), "+s"(kb_), "+s"(kz), "+s"(xo0), "+s"(wo0) :: "memory");
    const u16* x = (const u16*)((u64)ka[1] << 32 | ka[0]);
    const u16* wqkv = (const u16*)((u64)ka[3] << 32 | ka[2]);
    auto arg = [&](int i) { return (char*)((u64)kb_[2 * i + 1] << 32 | kb_[2 * i]); };
    const u16 *wphi = (const u16*)arg(0), *wo = (const u16*)arg(1), *wr = (const u16*)arg(2), *wg = (const u16*)arg(3);
    const u16 *wu = (const u16*)arg(4), *wd = (const u16*)arg(5), *gw = (const u16*)arg(6);
    char* ws = arg(7);
    u16* out = (u16*)kz;
    const int sc = (3 - (int)((unsigned)(size_t)wqkv >> 7)) & 7;
    const unsigned* one = k_num;
    unsigned v0 = 0;
    asm volatile("" : "+s"(one), "+v"(v0));
    auto mark = [&](unsigned dst, int n = 1, int lanes = 1) AI {
        asm volatile("s_mov_b32 m0, %2\n\ts_mov_b64 exec, %4\n\tglobal_load_lds_dword %0, %1 offset:%3\n\ts_mov_b64 exec, -1"
                     ::"v"(v0), "s"(one), "s"(dst - 4 * n), "n"(4 * n), "n"(lanes) : "memory");
    };
    auto four = [&](const unsigned (&v)[4], const char* src, unsigned dst) AI {
        asm volatile("s_mov_b32 m0, %5\n\ts_nop 0\n\t"
                     "global_load_lds_dwordx4 %0, %4\n\tglobal_load_lds_dwordx4 %1, %4 offset:1024\n\t"
                     "global_load_lds_dwordx4 %2, %4 offset:2048\n\tglobal_load_lds_dwordx4 %3, %4 offset:3072"
                     ::"v"(v[0]), "v"(v[1]), "v"(v[2]), "v"(v[3]), "s"(src), "s"(dst) : "memory");
    };
    const int ks0 = (((slot + (slot >> 3)) & 3) | ((hp & 7) == sc ? slot & 4 : 0)) ^ ((bid & 1) * 8);
    const char* xb0 = (const char*)x + xo0 - 4096;
    const char* wb0 = (const char*)wqkv + wo0 - 4096;
    auto fill = [&](auto N) AI {
        constexpr int n = N, j = F_J[n] < 0 ? 0 : F_J[n], xi = F_J[n] < 0 ? -1 - F_J[n] : 0, s = F_J[n] < 0 ? xi : J_S[j], m = J_M[j];
        const int ks = ks0 ^ (int)((KXB >> (4 * s)) & 15);
        if constexpr (F_J[n] >= 0) {
            four(vw, wb0 + ks * (64 * 6144) + (m == 2 ? 0 : 2048 * (m + 1)), lds0 + J_U[j] * 8192 + hs * 4096);
        } else {
            const char* xs_ = xb0 + ks * 128;
            const unsigned d = lds0 + (hs ? X_U1[xi] : X_U0[xi]) * 8192;
            four(vx, xs_, d);
            four(vx, xs_ + 32 * 2048, d + 4096);
        }
        if constexpr (F_K[n] > 1) mark(lds0 + L_FULL + (n < NF1 ? 0 : 32) + hs * 8, F_K[n], 3);
    };
    static_assert(F_K[1] == 1, "");
    if (__builtin_expect((w & 5) == 4, 1)) sfor<0, 2>(fill);
    unsigned sd = 0, st;
    asm volatile("s_cmp_lg_u32 %2, 5\n\ts_cbranch_scc1 .La%=\n\t"
                 "s_min_u32 %1, %3, 23\n\ts_lshl_b32 %1, %1, 18\n\ts_load_dword %0, %5, %1\n\ts_load_dword %0, %6, 0x0\n\t"
                 "s_and_b32 %1, %1, 0x3c0000\n\ts_load_dword %0, %4, %1\n\ts_load_dword %0, %10, %1\n\t"
                 "s_load_dword %0, %7, 0x0\n\ts_load_dword %0, %8, 0x0\n\ts_load_dword %0, %9, 0x0\n.La%=:"
                 : "+s"(sd), "=&s"(st) : "s"(w), "s"(slot), "s"(x), "s"(wqkv), "s"(one), "s"(wr), "s"(gw), "s"(wphi), "s"(out) : "memory", "scc");
    asm volatile("s_cmp_lg_u32 %2, 7\n\ts_cbranch_scc1 .La%=\n\t"
                 "s_min_u32 %1, %3, 27\n\ts_lshl_b32 %1, %1, 18\n\ts_load_dword %0, %4, 0x0\n\ts_load_dword %0, %4, %1\n\t"
                 "s_load_dword %0, %5, %1\n\ts_load_dword %0, %6, %1\n\ts_load_dword %0, %7, %1\n\t"
                 "s_and_b32 %1, %1, 0x1c0000\n\ts_load_dword %0, %8, %1\n.La%=:\n\ts_barrier"
                 : "+s"(sd), "=&s"(st) : "s"(w), "s"(slot), "s"(ws), "s"(wg), "s"(wu), "s"(wd), "s"(wo) : "memory", "scc");
    if (__builtin_expect((w & 5) == 4, 1)) {
        mark(lds0 + L_FULL + hs * 8, 1, 3);
        sfor<2, 4>(fill);
    }
    asm volatile("" : "+s"(bid));
    const int xcd = bid & 7;
    const int rt = (xcd >> 1) * 4 + ((bid >> 3) & 3), cb = (xcd & 1) * 8 + (bid >> 5), rot = 8 * rt;
    const __amdgpu_buffer_rsrc_t rsrc = __builtin_amdgcn_make_buffer_rsrc(ws, 0, WS_END, 0x00020000);
    const u32x4 z4 = {0u, 0u, 0u, 0u};
    const f32x4 zero = {0.f, 0.f, 0.f, 0.f};
    unsigned AX = lds0 + L_A + rg * 4096 + l16;
    unsigned W4 = lds0 + p * 4;
    unsigned PG = lds0 + L_PROG + p * 256;
    const unsigned WR = lds0 + rg * 4;
    auto flag = [&](unsigned a) AI { asm volatile("ds_write_b32 %0, %0" ::"v"(a) : "memory"); };
    auto phi_w = [&](bf16x8 (&a)[2], int h) AI {
        const unsigned ad = lds0 + L_WPHI + h * 2048 + (4 * g + q) * 64 + (((p >> 1) ^ (2 * (g & 1))) * 16) + 8 * (p & 1);
        a[0] = cat8(tr_read(ad), tr_read(ad + 1024));
        a[1] = cat8(tr_read(ad ^ 32), tr_read((ad ^ 32) + 1024));
    };
    u32x4 fw[32], fd[16], gu[2];
    unsigned sel_[4][2];
    u32x4 ph[2][2], sq;
    const int hp2 = ((bid >> 1) & 3) * 4 + (bid >> 6);
    const bool ffn = (hp2 & 7) != sc;
    const int ph4 = ((bid >> 3) + (bid >> 6)) & 3;
    const int kq = mt >> 2;
    const int cs = (ffn ? hp2 - (hp2 > sc) - (hp2 > sc + 8) : 14 + (hp2 >> 3)) * 4 + ((ph4 + 3 - (int)((unsigned)(size_t)wg >> 7)) & 3);
    auto spread = [&](const u32x4& v, int i) -> bf16x8 {
        const unsigned s = v[i >> 1];
        u32x4 a;
#pragma unroll
        for (int d = 0; d < 4; ++d) a[d] = __builtin_amdgcn_perm(s, s, sel_[d][i & 1]);
        return __builtin_bit_cast(bf16x8, a);
    };
    auto fold_store = [&](const f32x4& a, unsigned off) {
        f32x4 v;
#pragma unroll
        for (int i = 0; i < 4; ++i) {
            const unsigned u = __float_as_uint(a[i]);
            const float r = __uint_as_float(__builtin_amdgcn_update_dpp(u, u, 0x128, 0xc, 0xf, false));
            v[i] = swap32_add(r, r);
        }
        const u32x4 o = nz4(v);
        ST_MASK("global_store_dwordx4", 0x00FF00FFull, off + l15 * 32 + g * 16, o, ws);
    };
    auto sel_init = [&]() AI {
#pragma unroll
        for (int d = 0; d < 4; ++d)
#pragma unroll
            for (int hf = 0; hf < 2; ++hf) {
                const unsigned b = hf ? 0x0302u : 0x0100u;
                sel_[d][hf] = (sch >> 1) != d ? 0x0c0c0c0cu : (l & 1) ? (b << 16) | 0x0c0cu : 0x0c0c0000u | b;
            }
    };
    auto gua = [&](int i) { return ws + WS_GU + cs * 4096 + i * 1024; };
    auto gu_load = [&]() AI {
#pragma unroll
        for (int i = 0; i < 2; ++i) ld_async(gu[i], l16, gua(i));
    };
    auto down = [&]() AI {
#pragma unroll
        for (int i = 0; i < 16; ++i) pin(fd[i]);
        settle(gu, [&](int) { return l16; }, gua);
        const f32x4 v = __builtin_bit_cast(f32x4, gu[0]) + __builtin_bit_cast(f32x4, gu[1]);
        auto r = __builtin_amdgcn_permlane16_swap(__float_as_uint(swap32_add(v[0], v[2])), __float_as_uint(swap32_add(v[1], v[3])), false, false);
        const float gt = rbf(__uint_as_float(r[0])), up = rbf(__uint_as_float(r[1]));
        const unsigned ha = lds0 + L_XS + hs * 512;
        lds_write<u16>(ha + l15 * 8 + g * 2, f2bf(rbf(gt / (1.f + __expf(-gt))) * up));
        const u32x4 hv = lds_read<u32x4>(ha + srow * 16);
        f32x4 y0 = zero, y1 = zero;
#pragma unroll
        for (int i = 0; i < 8; ++i) {
            const bf16x8 a = spread(hv, i);
            y0 = mfma16(a, __builtin_bit_cast(bf16x8, fd[i]), y0);
            y1 = mfma16(a, __builtin_bit_cast(bf16x8, fd[8 + i]), y1);
        }
        fold_store(y0, WS_Y + cs * 4096 + kq * 1024 + hs * 512);
        fold_store(y1, WS_Y + cs * 4096 + kq * 1024 + hs * 512 + 256);
    };
    auto need = [&](int c) AI {
        if (c == 101) lds_wait(W4 + L_FKS, 1);
        else if (c == 100) lds_wait(W4 + L_FV, 1);
        else if (c == 99) lds_wait(W4 + L_FQD, 1);
        else if (c > 0) lds_wait(c > RLD ? W4 + L_PRL : PG, c);
    };
    auto wo_load = [&](int n0, int n1, bool mk) AI {
        unsigned vo[4];
#pragma unroll
        for (int i = 0; i < 4; ++i) vo[i] = (unsigned)((8 * i + srow) * 2048 + ((sch ^ ((g & 3) << 1)) * 16) + 4096 - i * 1024);
        const char* wob = (const char*)wo + cb * 128 - 4096;
#pragma unroll
        for (int n = n0; n < n1; ++n) {
            if (n < WON && (n == 0 || W_N[n] != W_N[n - 1])) need(W_N[n]);
            four(vo, wob + ((2 * n + hs + rot) & 31) * 65536, lds0 + W_U[n] * 8192 + hs * 4096);
        }
        if (mk) mark(lds0 + (n0 >= WON ? L_FW2 : L_FWD) + hs * 4);
    };
    auto phi_adr = [&](unsigned (&pw)[4], unsigned base) AI {
#pragma unroll
        for (int i = 0; i < 4; ++i) pw[i] = (base + rg * 4096 + l15 * 128 + ((g ^ fsw(l15)) << 3)) ^ (i << 5);
    };
    const int row0 = rt * 128 + rg * 32 + kp * 16;
    u32x4 xr[2];
    u64 gg[2];
    unsigned xo = srow * 2048 + sch * 16;
    auto g_load = [&]() AI {
        asm volatile("global_load_dwordx2 %0, %2, %3 sc1\n\tglobal_load_dwordx2 %1, %2, %3 offset:64 sc1"
                     : "=&v"(gg[0]), "=&v"(gg[1]) : "v"((unsigned)srow * 8), "s"(ws + WS_GG + row0 * 8) : "memory");
    };
    auto xg_load = [&](int skip) AI {
        asm volatile("s_cmp_eq_u32 %4, 4\n\ts_cbranch_scc1 .Lk%=\n\t"
                     "global_load_dwordx4 %0, %5, %6\n\tglobal_load_dwordx4 %1, %5, %7\n\t"
                     "global_load_dwordx2 %2, %8, %9 sc1\n\tglobal_load_dwordx2 %3, %8, %9 offset:64 sc1\n.Lk%=:"
                     : "+v"(xr[0]), "+v"(xr[1]), "+v"(gg[0]), "+v"(gg[1])
                     : "s"(skip), "v"(xo), "s"(x + (size_t)row0 * DM + cb * 64), "s"(x + (size_t)(row0 + 8) * DM + cb * 64),
                       "v"((unsigned)srow * 8), "s"(ws + WS_GG + row0 * 8) : "memory", "scc");
    };
    auto xg_def = [&]() AI { asm volatile("" : "=v"(xr[0]), "=v"(xr[1]), "=v"(gg[0]), "=v"(gg[1])); };
    auto relane = [&]() AI {
        unsigned z = 0;
        asm volatile("" : "+v"(z));
        l = __builtin_amdgcn_mbcnt_hi(~0u, __builtin_amdgcn_mbcnt_lo(~0u, z));
        g = l >> 4, l15 = l & 15, q = l15 >> 2, p = l & 3, srow = l >> 3, sch = l & 7, l16 = l * 16;
        AX = lds0 + L_A + rg * 4096 + l16, W4 = lds0 + p * 4, PG = lds0 + L_PROG + p * 256, xo = srow * 2048 + sch * 16;
    };
    u32x4 kf[6];
    auto kf_def = [&]() AI { asm volatile("" : "=v"(kf[0]), "=v"(kf[1]), "=v"(kf[2]), "=v"(kf[3]), "=v"(kf[4]), "=v"(kf[5])); };
    const int oh = hs ^ kp ^ 1;

    if (__builtin_expect(w >= 4, 1)) {
        const bool loader = !(w & 1);
        const unsigned kf_kv = WS_KF + (hp * 2 + (mt >> 3)) * 8192 + (mt & 7) * 512;
        const unsigned kf_z = WS_KF + (hp * 2 + (mt >> 3)) * 8192 + 4096 + (mt & 7) * 128;
        const unsigned fgo = ffn ? (unsigned)((256 * kq + 16 * srow) * 7168 + cs * 128 + sch * 16) : 0u;
        const char* fsrc = (const char*)(hs ? wu : wg);
        auto gu_w = [&](int q0, int q1) AI {
#pragma unroll
            for (int i = q0; i < q1; ++i) {
                unsigned t1, t2, t3;
                asm volatile("v_add_u32 %4, 0x1c00, %7\n\tv_add_u32 %5, 0x3800, %7\n\tv_add_u32 %6, 0x5400, %7\n\t"
                             "global_load_dwordx4 %0, %7, %8 nt\n\tglobal_load_dwordx4 %1, %4, %8 nt\n\t"
                             "global_load_dwordx4 %2, %5, %8 nt\n\tglobal_load_dwordx4 %3, %6, %8 nt"
                             : "=&v"(fw[4 * i]), "=&v"(fw[4 * i + 1]), "=&v"(fw[4 * i + 2]), "=&v"(fw[4 * i + 3]), "=&v"(t1), "=&v"(t2), "=&v"(t3)
                             : "v"(fgo), "s"(fsrc + (128 * (i >> 2) + 4 * (i & 3)) * 7168) : "memory");
            }
        };
        const unsigned fdo = ffn ? (unsigned)((64 * cs + 8 * srow) * 2048 + 512 * kq + 256 * hs + sch * 16) : 0u;
        auto dn_w = [&](int q0, int q1) AI {
#pragma unroll
            for (int i = q0; i < q1; ++i)
                asm volatile("global_load_dwordx4 %0, %4, %5 offset:-4096 nt\n\tglobal_load_dwordx4 %1, %4, %5 offset:-2048 nt\n\t"
                             "global_load_dwordx4 %2, %4, %5 nt\n\tglobal_load_dwordx4 %3, %4, %5 offset:2048 nt"
                             : "=&v"(fd[4 * i]), "=&v"(fd[4 * i + 1]), "=&v"(fd[4 * i + 2]), "=&v"(fd[4 * i + 3])
                             : "v"(fdo), "s"((const char*)wd + 4096 + (i & 1) * 8192 + (i >> 1) * 128) : "memory");
        };
        auto gate_up = [&]() AI {
            sel_init();
#pragma unroll
            for (int i = 0; i < 32; ++i) pin(fw[i]);
            u32x4 xs[4];
#pragma unroll
            for (int i = 0; i < 4; ++i) xs[i] = lds_read<u32x4>(lds0 + L_XS + hs * 512 + (i >> 1) * 256 + srow * 32 + (i & 1) * 16);
            f32x4 a = zero;
#pragma unroll
            for (int i = 0; i < 32; ++i) a = mfma16(spread(xs[i >> 3], i & 7), __builtin_bit_cast(bf16x8, fw[i]), a);
            fold_store(a, WS_GU + cs * 4096 + kq * 512 + hs * 256);
            SB0();
        };
        u64 T;
        u32x4 sv[2];
        auto rg_load = [&]() AI {
            asm volatile("global_load_dwordx4 %0, %2, %3 sc1\n\tglobal_load_dwordx4 %1, %2, %3 offset:16 sc1"
                         : "=&v"(sv[0]), "=&v"(sv[1]) : "v"(l * 32), "s"(ws + WS_RG) : "memory");
        };
        auto select = [&]() AI {
            const unsigned tl = (unsigned)T & 0x1FFFu, thi = (unsigned)(T >> 13);
#pragma unroll 1
            for (unsigned s = 0; s < SPIN_CAP; ++s) {
                pin(sv[0]);
                pin(sv[1]);
                const unsigned bad = ((sv[0][0] >> 19) ^ tl) | (sv[0][1] ^ thi) | ((sv[0][2] >> 19) ^ tl) | (sv[0][3] ^ thi) |
                                     ((sv[1][0] >> 19) ^ tl) | (sv[1][1] ^ thi) | ((sv[1][2] >> 19) ^ tl) | (sv[1][3] ^ thi);
                if (__all(bad == 0u)) break;
                __builtin_amdgcn_s_sleep(16);
                rg_load();
                waitv<0>();
            }
            pin(sv[0]);
            pin(sv[1]);
            unsigned k2 = 0;
#pragma unroll
            for (int j = 0; j < 4; ++j) {
                const unsigned v = sv[j >> 1][(j & 1) * 2] & 0x7FFFFu;
                k2 = max(k2, ((v >> 3) << 11) | ((255 - (4 * l + j)) << 3) | (v & 7));
            }
            const unsigned key = __builtin_amdgcn_readfirstlane(wave_umax(k2));
            asm volatile("s_mov_b32 m0, %2\n\ts_mov_b64 exec, %3\n\tglobal_load_lds_dwordx4 %0, %1\n\ts_mov_b64 exec, -1"
                         ::"v"((unsigned)(512 * kq) + l16), "s"(x + (size_t)(2047 - (int)(key & 2047u)) * DM), "s"(lds0 + L_XS + hs * 512), "s"(0xFFFFFFFFull) : "memory");
            unsigned rb = key >> 11;
            rb ^= (rb & 0x8000u) ? 0x8000u : 0xFFFFu;
            const float rsel = rbf(1.f / (1.f + __expf(-bf2f((u16)rb))));
            lds_write<u32x4>(lds0 + L_SEL, (u32x4){(unsigned)T, (unsigned)(T >> 32), 2047u - (key & 2047u), __float_as_uint(rsel)});
            if (bid == 0 && hs) ST_MASK("global_store_dwordx2", 1ull, 0u, T, ws + WS_E);
        };
        auto k_soft = [&]() AI {
            bf16x8 a0[2], kb[2];
            f32x4 pk[2][2][2];
            unsigned pw[4];
            phi_w(a0, 0);
            phi_adr(pw, lds0 + P_U[0] * 8192);
            asm volatile("" : "+v"(pw[0]), "+v"(pw[1]), "+v"(pw[2]), "+v"(pw[3]));
            auto logits = [&](int h) AI {
                lds_wait(WR + L_FK0 + 4 * h, 1);
#pragma unroll
                for (int i = 0; i < 2; ++i) kb[i] = lds_read128(AX + (P_U[0] * 8192 - L_A) + h * 4096 + i * 1024);
#pragma unroll
                for (int i = 0; i < 4; ++i) pk[h][i >> 1][i & 1] = mfma16(a0[i & 1], kb[i >> 1], zero);
            };
            auto round = [&](int h) AI {
#pragma unroll
                for (int i = 0; i < 4; ++i) pk[h][i >> 1][i & 1] = rbf4(pk[h][i >> 1][i & 1]);
            };
            logits(0);
            round(0);
            SB0();
            phi_k(pk[0], pw, 0, [&]() AI {
                logits(1);
                SB0();
            });
            SB0();
            round(1);
            phi_k(pk[1], pw, 4096, []() {});
            SB0();
            flag(WR + L_FP);
        };
        if (__builtin_expect(loader, 1)) {
            vx_init();
            vw_init();
            unsigned pv = 0;
            sfor<4, NF1>([&](auto N) {
                constexpr int n = N;
                if constexpr (F_N[n] > F_N[n - 1]) {
                    __builtin_amdgcn_s_setprio(3);
                    lds_wait(pv, PG, F_N[n]);
                    if (F_N[n] < RLD) lds_peek(pv, PG);
                    __builtin_amdgcn_s_setprio(0);
                }
                if constexpr (n == 12) {
                    asm volatile("s_mov_b32 m0, %2\n\ts_nop 0\n\tglobal_load_lds_dwordx4 %0, %1\n\tglobal_load_lds_dwordx4 %0, %1 offset:1024"
                                 ::"v"((unsigned)((l >> 2) * 64 + (((l & 3) ^ (2 * ((l >> 4) & 1))) * 16))),
                                   "s"(wphi + (hp * 2 + hs) * 1024), "s"(lds0 + L_WPHI + hs * 2048) : "memory");
                }
                fill(N);
            });
            flag(lds0 + L_FLD + hs * 4);
            __builtin_amdgcn_s_setprio(2);
            const char* cla = ws + WS_CL + ((l15 * 2 + (mt >> 3)) * 16 + hp) * 128;
            u64 cl;
            asm volatile("global_load_dwordx2 %0, %1, off sc1" : "=&v"(cl) : "v"(cla) : "memory");
            k_soft();
            lds_wait(W4 + L_FP, 1);
            sq = lds_read<u32x4>(lds0 + L_SEL);
            waitv<0>();
            asm volatile("" : "+v"(cl));
            {
                u32x4 kp_[6];
                const char* kb = ws + WS_KV + (hp * 8 + (mt >> 1)) * 16384 + (mt & 1) * 512 + hs * 256;
                const bool zw = !((mt & 3) | hs);
                const char* zb = zw ? ws + WS_Z + (hp * 4 + (mt >> 2)) * 2048 : kb;
                const unsigned kvo = g * 1024 + l15 * 16, zo = zw ? l16 : 0u;
                auto ko = [&](int i) { return i < 4 ? kvo : zo; };
                auto ka = [&](int i) { return i < 4 ? kb + i * 4096 : zb + (i - 4) * 1024; };
                const u64 T = (u64)sq[1] << 32 | sq[0];
#pragma unroll 1
                for (unsigned s = 0; s < SPIN_CAP && __ballot(cl != T); ++s)
                    asm volatile("global_load_dwordx2 %0, %1, off sc1\n\ts_waitcnt vmcnt(0)" : "=&v"(cl) : "v"(cla) : "memory");
                lds_wait0(W4 + L_FKS);
                __builtin_amdgcn_s_sleep(GDL);
#pragma unroll
                for (int i = 0; i < 6; ++i) ld_async(kp_[i], ko(i), ka(i));
                xg_def();
                xg_load(w + 1);
                SB0();
                waitv<0>();
                settle(kp_, ko, ka);
                float v[8], u[4];
#pragma unroll
                for (int e = 0; e < 4; ++e) {
                    v[2 * e] = (lo16(kp_[0][e]) + lo16(kp_[1][e])) + (lo16(kp_[2][e]) + lo16(kp_[3][e]));
                    v[2 * e + 1] = (hi16(kp_[0][e]) + hi16(kp_[1][e])) + (hi16(kp_[2][e]) + hi16(kp_[3][e]));
                }
#pragma unroll
                for (int e = 0; e < 4; ++e) u[e] = swap32_add(v[e], v[e + 4]);
                const unsigned o = nz1(pack2(swap16_add(u[0], u[2]), swap16_add(u[1], u[3])));
                ST_MASK("global_store_dword", ~0ull, kf_kv + hs * 256 + l15 * 16 + g * 4, o, ws);
                if (zw) {
                    f32x4 z = __builtin_bit_cast(f32x4, kp_[4]) + __builtin_bit_cast(f32x4, kp_[5]);
#pragma unroll
                    for (int i = 0; i < 4; ++i) {
                        float t = dpp_add<0x128>(z[i]);
                        t = swap16_add(t, t);
                        z[i] = swap32_add(t, t);
                    }
                    const u32x2 oz = {nz1(pack2(z[0], z[1])), nz1(pack2(z[2], z[3]))};
                    ST_MASK("global_store_dwordx2", 0xFFFFull, kf_z + (l & 8) * 8 + p * 16 + (sch >> 2) * 8, oz, ws);
                }
                flag(lds0 + L_FG + hs * 4);
            }
            const char* kfb = ws + WS_KF + (hp * 2 + hs) * 8192;
            auto fo = [&](int i) { return i < 4 ? l16 : (unsigned)g * 16; };
            auto fb = [&](int i) { return i < 4 ? kfb + i * 1024 : kfb + 4096 + (i - 4) * 512; };
            __builtin_amdgcn_s_sleep(KFD);
#pragma unroll
            for (int i = 0; i < 6; ++i) ld_async(kf[i], fo(i), fb(i));
            waitv<0>();
            settle(kf, fo, fb);
            asm volatile("" : "+v"(xr[0]), "+v"(xr[1]), "+v"(gg[0]), "+v"(gg[1]));
            need(KFN);
#pragma unroll
            for (int i = 0; i < 4; ++i) lds_write<u32x4>(lds0 + L_KF + hs * 4096 + i * 1024 + l16, kf[i]);
#pragma unroll
            for (int i = 0; i < 2; ++i) lds_write<u32x4>(lds0 + L_KZ + hs * 128 + i * 64 + g * 16, kf[4 + i]);
            SB0();
            flag(lds0 + L_FKF + hs * 4);
        } else {
            const int row0 = (mt * 16 + hp) * 8 + 4 * hs;
            __builtin_amdgcn_s_setprio(2);
            asm volatile("s_waitcnt lgkmcnt(0)" : "+s"(sd));
            u32x4 rv[2], gv[2], xv[4][2];
            u64 ev;
            asm volatile("global_load_dwordx2 %4, %5, %9 sc1\n\t"
                         "global_store_dwordx4 %10, %11, %9 sc1\n\t"
                         "global_store_dwordx2 %12, %13, %9 sc1\n\t"
                         "global_load_dwordx4 %0, %6, %7\n\tglobal_load_dwordx4 %1, %6, %7 offset:1024\n\t"
                         "global_load_dwordx4 %2, %6, %8\n\tglobal_load_dwordx4 %3, %6, %8 offset:1024"
                         : "=&v"(rv[0]), "=&v"(rv[1]), "=&v"(gv[0]), "=&v"(gv[1]), "=&v"(ev)
                         : "v"(0u), "v"(l16), "s"(wr), "s"(gw), "s"(ws),
                           "v"(l < 32 ? WS_Y + cs * 4096 + kq * 1024 + hs * 512 + l16 : WS_GU + cs * 4096 + kq * 512 + hs * 256 + l15 * 16), "v"(z4),
                           "v"(hs ? kf_z + l15 * 8 : kf_kv + l * 8), "v"(0ull)
                         : "memory");
#pragma unroll
            for (int r = 0; r < 2; ++r)
                asm volatile("global_load_dwordx4 %0, %4, %5\n\tglobal_load_dwordx4 %1, %4, %5 offset:1024\n\t"
                             "global_load_dwordx4 %2, %4, %5 offset:2048\n\tglobal_load_dwordx4 %3, %4, %5 offset:3072"
                             : "=&v"(xv[2 * r][0]), "=&v"(xv[2 * r][1]), "=&v"(xv[2 * r + 1][0]), "=&v"(xv[2 * r + 1][1])
                             : "v"(l16), "s"(x + (size_t)(row0 + 2 * r) * DM) : "memory");
            SB0();
            waitv<0>();
            asm volatile("" : "+v"(rv[0]), "+v"(rv[1]), "+v"(gv[0]), "+v"(gv[1]), "+v"(ev));
#pragma unroll
            for (int r = 0; r < 4; ++r) asm volatile("" : "+v"(xv[r][0]), "+v"(xv[r][1]));
            T = ((u64)__builtin_amdgcn_readfirstlane((unsigned)(ev >> 32)) << 32 | __builtin_amdgcn_readfirstlane((unsigned)ev)) + 1;
            float ra_[4] = {0.f, 0.f, 0.f, 0.f}, ga_[4] = {0.f, 0.f, 0.f, 0.f};
#pragma unroll
            for (int r = 0; r < 4; ++r)
#pragma unroll
                for (int i = 0; i < 2; ++i)
#pragma unroll
                    for (int j = 0; j < 4; ++j) {
                        ra_[r] = dot2(xv[r][i][j], rv[i][j], ra_[r]);
                        ga_[r] = dot2(xv[r][i][j], gv[i][j], ga_[r]);
                    }
            const int rr = (g >> 1) | ((g & 1) << 1);
            unsigned k = f2bf(sum4(ra_));
            k ^= (k & 0x8000u) ? 0xFFFFu : 0x8000u;
            k = (k << 3) | (7 - 4 * hs - rr);
            auto r1 = __builtin_amdgcn_permlane16_swap(k, k, false, false);
            k = max(r1[0], r1[1]);
            auto r2 = __builtin_amdgcn_permlane32_swap(k, k, false, false);
            k = max(r2[0], r2[1]);
            u64 gq = (T << 16) | f2bf(1.f / (1.f + __expf(-rbf(sum4(ga_)))));
            if (hs) lds_write<unsigned>(lds0 + L_BEST, k | 0x80000000u);
            asm volatile("" : "+v"(gq), "+v"(k));
            __builtin_amdgcn_s_setprio(0);
            lds_wait8(PG, CLR0 + ph4);
            if (!hs) {
                lds_wait(lds0 + L_BEST, 1);
                const u64 rq = (T << 19) | max(k, lds_read<unsigned>(lds0 + L_BEST) & 0x7FFFFu);
                ST_MASK("global_store_dwordx2", 1ull, 0u, rq, ws + WS_RG + (mt * 16 + hp) * 8);
            }
            ST_MASK("global_store_dwordx2", 0x0001000100010001ull, (unsigned)(rr * 8), gq, ws + WS_GG + row0 * 8);
#pragma unroll
            for (int n = 0; n < 8; ++n) st_wt(rsrc, WS_O + ((mt * 2 + (n >> 2)) * 32 + 2 * hp + hs) * 4096 + (n & 3) * 1024 + l16, z4);
#pragma unroll
            for (int n = 0; n < 4; ++n) st_wt(rsrc, WS_KV + ((hp * 8 + hs * 4 + n) * 16 + mt) * 1024 + l16, z4);
            ST_MASK("global_store_dwordx4", 0xFFFFull, WS_Z + ((hp * 4 + hs * 2 + (srow & 1)) * 16 + mt) * 128 + sch * 16, z4, ws);
            waitv<0>();
            ST_MASK("global_store_dwordx2", 0xFFFFull, (unsigned)l * 8, T, ws + WS_CL + ((mt * 2 + hs) * 16 + hp) * 128);
            lds_wait8(lds0 + L_FLD + (l & 1) * 4, 1);
            rg_load();
            gu_w(0, GQ1);
            waitv<4 * GQ1>();
            select();
            flag(WR + L_FP);
            vx_init();
            vw_init();
            sfor<NF1, NF>([&](auto N) {
                constexpr int n = N;
                if constexpr (n == NF1 || F_N[n] != F_N[n - 1]) need(F_N[n]);
                fill(N);
            });
            gu_w(GQ1, 8);
            dn_w(0, 4);
            waitv<16>();
            gate_up();
            lds_wait(W4 + L_FKS, 1);
            wo_load(0, WOE, false);
            lds_wait(lds0 + L_FG + (l & 1) * 4, 1);
            gu_load();
            wo_load(WOE, WON, true);
            waitv<4 * (WON - WOE) + 1>();
            down();
            sq = lds_read<u32x4>(lds0 + L_SEL);
            xg_def();
            kf_def();
        }
        lds_wait0(WR + L_FQ);
#pragma unroll
        for (int i = 0; i < 4; ++i) ph[i >> 1][i & 1] = lds_read<u32x4>(AX + i * 1024);
    } else {
        __builtin_amdgcn_s_setprio(1);
        unsigned ra[4], xt[2];
#pragma unroll
        for (int u = 0; u < 4; ++u) ra[u] = lds0 + J_B[0] * 8192 + (8 * g + q) * 128 + ((u ^ (q >> 1) ^ ((g & 1) << 1)) * 32) + 8 * p;
        unsigned xa = lds0 + (32 * (w & 1) + l15) * 128 + ((g ^ (l15 >> 1)) * 16), fa = lds0 + L_FULL + (l & 3) * 4;
        xt[0] = xa + (hs ? X_U1[0] : X_U0[0]) * 8192;
        xt[1] = xt[0] ^ 64;
        asm volatile("s_mov_b32 m0, %4" : "+v"(xa), "+v"(fa), "+v"(xt[0]), "+v"(xt[1]) : "s"(lds0 + L_PROG + w * 256 - 124));
        f32x4 acc[3][4][2];
        bf16x8 xf[ND][2][2];
        s16x4 wh[PD + 1][2];
        unsigned fv = 0;
        bf16x8 kown[2];
        u64 bad = ~0ull;
        auto chk = [&](int c) AI {
            SB0();
            if (__builtin_expect(bad != 0ull, 0)) {
                unsigned c_;
                asm volatile("s_mov_b32 %1, 0x400000\n"
                             ".Lp%=:\n\tds_read_b32 %0, %2 offset:%3\n\ts_waitcnt lgkmcnt(0)\n\tv_cmp_gt_u32 vcc, %4, %0\n\ts_cbranch_vccz .Lf%=\n\t"
                             "s_sub_u32 %1, %1, 1\n\ts_cbranch_scc0 .Lp%=\n.Lf%=:"
                             : "=&v"(fv), "=&s"(c_) : "v"(fa), "n"(c > NM1 ? 32 : 0), "n"(c) : "memory", "vcc", "scc");
            }
            SB0();
        };
        auto kv_part = [&]() AI {
            bf16x8 ones;
#pragma unroll
            for (int j = 0; j < 8; ++j) ones[j] = (short)0x3f80;
            const unsigned pa = lds0 + (hs ? P_U[2] : P_U[0]) * 8192 + (4 * g + q) * 128 + (((8 * (rg & 1) + p) ^ fsw(4 * g + q)) << 3);
            const unsigned va = lds0 + L_A + hs * 2048 + l16;
            bf16x8 b0[4], b1[4], af[2][4];
            lds_wait0(lds0 + L_FV + sch * 4);
#pragma unroll
            for (int i = 0; i < 4; ++i) {
                af[0][i] = cat8(tr_read(pa + i * 4096), tr_read(pa + i * 4096 + 2048));
                b0[i] = lds_read128(va + i * 4096);
                af[1][i] = cat8(tr_read((pa ^ 32) + i * 4096), tr_read((pa ^ 32) + i * 4096 + 2048));
            }
#pragma unroll
            for (int i = 0; i < 4; ++i) b1[i] = lds_read128(va + i * 4096 + 1024);
            SB0();
            f32x4 k0[2] = {zero, zero}, k1[2] = {zero, zero}, zt[2] = {zero, zero};
            const unsigned kv_st = WS_KV + ((hp * 8 + rg * 2) * 16 + mt) * 1024 + l16;
#pragma unroll
            for (int i = 0; i < 4; ++i)
#pragma unroll
                for (int e = 0; e < 2; ++e) {
                    k0[e] = mfma16(af[e][i], b0[i], k0[e]);
                    zt[e] = mfma16(af[e][i], ones, zt[e]);
                }
            SB0();
#pragma unroll
            for (int i = 0; i < 4; ++i)
#pragma unroll
                for (int e = 0; e < 2; ++e) k1[e] = mfma16(af[e][i], b1[i], k1[e]);
            st_wt(rsrc, kv_st, nz4(__builtin_bit_cast(u32x4, pack44(k0[0], k0[1]))));
            const u32x4 zo = nz4((l & 1) ? zt[1] : zt[0]);
            ST_MASK("global_store_dwordx4", 0x0003000300030003ull, WS_Z + ((hp * 4 + rg) * 16 + mt) * 128 + (l & 1) * 64 + g * 16, zo, ws);
            st_wt(rsrc, kv_st + 16384, nz4(__builtin_bit_cast(u32x4, pack44(k1[0], k1[1]))));
            SB0();
            flag(WR + L_FKS);
        };
        auto report = [&](int r) AI {
            unsigned t, a;
            if (r > RLD) asm volatile("v_mov_b32 %0, %3\n\tv_mov_b32 %1, %2\n\tds_write_b32 %1, %0" : "=&v"(t), "=&v"(a) : "s"(WR + L_PRL), "n"(r) : "memory");
            else asm volatile("global_load_lds_dword %0, %1 offset:%2" ::"v"(v0), "s"(one), "n"(4 * r) : "memory");
        };
        auto rd1 = [&](auto F, auto H) AI {
            constexpr int f = F, h = H, n = f >> 2, j = f >> 3, c = h ? E_B[f] : E_A[f], a = c % 100;
            if constexpr (h == 0 && (f & 3) == 0 && Q_C[n] > 0) chk(Q_C[n]);
            wh[f % (PD + 1)][h] = tr_read(ra[f & 3] + (J_U[j] - J_B[j]) * 8192 + (n & 1) * 4096 + h * 512);
            SB0();
            if constexpr (c / 100 == 1) {
                constexpr int jn = (f + 3 - a) >> 3;
                asm volatile("v_add_u32 %0, %1, %0" : "+v"(ra[a]) : "n"((J_B[jn] - J_B[jn - 1]) * 8192));
            } else if constexpr (c / 50 == 4) {
                asm volatile("v_add_u32 %0, %1, %2" : "=v"(xt[0]) : "s"((hs ? X_U1[a] : X_U0[a]) * 8192), "v"(xa));
            } else if constexpr (c / 50 == 5) {
                asm volatile("v_xor_b32 %0, 64, %1" : "=v"(xt[1]) : "v"(xt[0]));
            } else if constexpr (c / 100 == 3) {
                xf[a % 25 % ND][a / 25 & 1][a / 50] = lds_read128(xt[a / 50] + (a / 25 & 1) * 2048);
            } else if constexpr (c / 100 == 4) {
                fv = *(volatile AS3 unsigned*)(size_t)(fa + (a > NM1 ? 32 : 0));
            } else if constexpr (c / 100 == 5) {
                bad = __builtin_amdgcn_ballot_w64(fv < (unsigned)a);
                asm volatile("" : "+s"(bad));
            } else if constexpr (c / 100 == 6) {
                report(a);
            }
        };
        sfor<0, PD>([&](auto F) { rd1(F, IC<0>{}); rd1(F, IC<1>{}); });
        constexpr int K0F = 4 * KD, K1F = 4 * KD + 1;
        sfor<0, 96>([&](auto N) {
            sfor<0, 4>([&](auto E) {
                constexpr int n = N, e = E, f = 4 * n + e, j = n >> 1, ks = n & 1, u = e, s = J_X[j], m = J_M[j];
                const bf16x8 wv = cat8(wh[f % (PD + 1)][0], wh[f % (PD + 1)][1]);
                sfor<0, 2>([&](auto RB) {
                    constexpr int rb = RB;
                    const bf16x8 xv = xf[s % ND][rb][ks];
                    if (j_first(j) && ks == 0) asm volatile("v_mfma_f32_16x16x32_bf16 %0, %1, %2, 0" : "=&v"(acc[m][u][rb]) : "v"(m == 1 ? xv : wv), "v"(m == 1 ? wv : xv));
                    else asm volatile("v_mfma_f32_16x16x32_bf16 %0, %1, %2, %0" : "+v"(acc[m][u][rb]) : "v"(m == 1 ? xv : wv), "v"(m == 1 ? wv : xv));
                    SB0();
                    if constexpr (f + PD < 384 && (f + PD < 4 * VD || f >= 4 * VD)) rd1(IC<f + PD>{}, RB);
                    if constexpr (rb == 0 && f == K0F) relane();
                    if constexpr (rb == 0 && (f == K0F || f == K1F)) asm volatile("s_nop 7");
                    SB0();
                    if constexpr (f == K0F) lds_write<bf16x8>(AX + (P_U[0] * 8192 - L_A) + rb * 1024, pack44(acc[0][0][rb], acc[0][1][rb]));
                    if constexpr (f == K1F) {
                        kown[rb] = pack44(acc[0][2][rb], acc[0][3][rb]);
                        asm volatile("" : "+v"(kown[rb]));
                    }
                    SB0();
                });
                if constexpr (f == K0F) flag(WR + L_FK0);
                if constexpr (f == 4 * VD - 1) {
                    bf16x8 ak[2];
                    f32x4 pk[2][2];
                    unsigned pw[4];
                    __builtin_amdgcn_s_setprio(2);
                    phi_w(ak, 1);
                    asm volatile("s_nop 7");
                    SB0();
#pragma unroll
                    for (int i = 0; i < 4; ++i) lds_write<bf16x8>(AX + i * 1024, pack44(acc[1][i][0], acc[1][i][1]));
                    SB0();
#pragma unroll
                    for (int i = 0; i < 4; ++i) pk[i >> 1][i & 1] = rbf4(mfma16(ak[i & 1], kown[i >> 1], zero));
                    phi_adr(pw, lds0 + P_U[2] * 8192);
                    phi_k(pk, pw, 0, []() {});
                    SB0();
                    flag(WR + L_FV);
                    kv_part();
                    __builtin_amdgcn_s_setprio(1);
                    fv = 0;
                    bad = ~0ull;
                    sfor<4 * VD, 4 * VD + PD>([&](auto F) { rd1(F, IC<0>{}); rd1(F, IC<1>{}); });
                }
            });
        });
        asm volatile("s_nop 7");
        SB0();
        __builtin_amdgcn_s_setprio(0);
        flag(WR + L_FQD);
        bf16x8 a0[2], a1[2];
        f32x4 pq[2][2][2];
        phi_w(a1, hs);
        phi_w(a0, hs ^ 1);
#pragma unroll
        for (int rb = 0; rb < 2; ++rb) {
            const bf16x8 b0 = pack44(acc[2][0][rb], acc[2][1][rb]), b1 = pack44(acc[2][2][rb], acc[2][3][rb]);
#pragma unroll
            for (int fb = 0; fb < 2; ++fb) {
                pq[1][rb][fb] = rbf4(mfma16(a1[fb], hs ? b1 : b0, zero));
                pq[0][rb][fb] = rbf4(mfma16(a0[fb], hs ? b0 : b1, zero));
            }
        }
        softmax2(pq[1], ph, []() {});
        lds_wait(W4 + L_FKS, 1);
#pragma unroll
        for (int i = 0; i < 4; ++i) lds_write<u32x4>(AX + i * 1024, ph[i >> 1][i & 1]);
        SB0();
        flag(WR + L_FQ);
        sq = lds_read<u32x4>(lds0 + L_SEL);
        softmax2(pq[0], ph, []() {});
        asm volatile("" : "+v"(ph[0][0]), "+v"(ph[0][1]), "+v"(ph[1][0]), "+v"(ph[1][1]));
        xg_def();
        kf_def();
    }
    relane();
    const char* cla = ws + WS_CL + ((rt * 2 + kp) * 16 + l15) * 128;
    const u64 T = (u64)sq[1] << 32 | sq[0];
    u64 cl;
    xg_load(w & 5);
    if ((w & 5) != 4) {
        lds_wait0(lds0 + L_FKF + oh * 4);
#pragma unroll
        for (int i = 0; i < 4; ++i) kf[i] = lds_read<u32x4>(lds0 + L_KF + oh * 4096 + i * 1024 + l16);
#pragma unroll
        for (int i = 0; i < 2; ++i) kf[4 + i] = lds_read<u32x4>(lds0 + L_KZ + oh * 128 + i * 64 + g * 16);
    }
    asm volatile("global_load_dwordx2 %0, %1, off sc1" : "=&v"(cl) : "v"(cla) : "memory");
    o_store(kf, ph, rsrc, WS_O + ((mt * 2 + hs) * 32 + 2 * hp + oh) * 4096 + (rg & 1) * 2048 + l16);
    flag(lds0 + L_FOS + w * 4);
    waitv<3>();
    flag(lds0 + L_FWO + w * 4);
    __builtin_amdgcn_s_setprio(0);
    if ((w & 5) == 5) {
        lds_wait(lds0 + L_FOS + sch * 4, 1);
        wo_load(WON, 16, true);
    }
    const u32x2 selv = {sq[2], sq[3]};

    const char* ob = ws + WS_O + (rt * 2 + (rg >> 1)) * 131072 + (rg & 1) * 2048;
    const unsigned ov0 = l16 + kp * 1024, ov1 = l16 + (kp ^ 1) * 1024;
    auto oa = [&](int i) { return ob + ((2 * i + kp + rot) & 31) * 4096; };
    u32x4 of[3][4];
    auto fetch1 = [&](int k, int j) AI { ld_async(of[k % 3][j], (j & 1) ? ov1 : ov0, oa(2 * k + (j >> 1))); };
    auto fix = [&](int k) AI { settle(of[k % 3], [&](int j) { return (j & 1) ? ov1 : ov0; }, [&](int j) { return oa(2 * k + (j >> 1)); }); };
    const bool own = (__builtin_amdgcn_readfirstlane(selv[0]) >> 4) == (unsigned)(rt * 8 + rg * 2 + kp);
    float gv_[2];
    f32x2 gb[2][4];
    auto g_mix = [&]() AI {
#pragma unroll
        for (int j = 0; j < 2; ++j) {
            gv_[j] = bf2f((u16)gg[j]);
            const float og = rbf(1.f - gv_[j]);
#pragma unroll
            for (int i = 0; i < 4; ++i) {
                const unsigned t = pack2(og * lo16(xr[j][i]), og * hi16(xr[j][i]));
                gb[j][i] = (f32x2){lo16(t), hi16(t)};
                asm volatile("" : "+v"(gb[j][i]));
            }
        }
    };
    asm volatile("" : "+v"(xr[0]), "+v"(xr[1]), "+v"(gg[0]), "+v"(gg[1]));
#pragma unroll 1
    for (unsigned s = 0; s < SPIN_CAP && __ballot((gg[0] >> 16) != (T & 0xFFFFFFFFFFFFull) || (gg[1] >> 16) != (T & 0xFFFFFFFFFFFFull)); ++s) {
        g_load();
        waitv<0>();
        asm volatile("" : "+v"(gg[0]), "+v"(gg[1]));
    }
    g_mix();
    if ((w & 5) != 5) __builtin_amdgcn_s_sleep(PDL);
    if ((w & 5) != 5) waitv<2>();
    else waitv<3 + 4 * (16 - WON)>();
    asm volatile("" : "+v"(cl));
#pragma unroll 1
    for (unsigned s = 0; s < SPIN_CAP && __ballot(cl != T); ++s)
        asm volatile("global_load_dwordx2 %0, %1, off sc1\n\ts_waitcnt vmcnt(0)" : "=&v"(cl) : "v"(cla) : "memory");
#pragma unroll
    for (int j = 0; j < 4; ++j) fetch1(0, j);
    f32x4 acc[2][4];
    unsigned wa[2][4];
#pragma unroll
    for (int i = 0; i < 8; ++i) {
        wa[i >> 2][i & 3] = ((lds0 + 4096 * kp + (4 * g + q) * 128 + ((((g & 1) << 1) | (q >> 1)) * 32) + 8 * p) ^ ((i & 3) << 5)) + (i >> 2) * 65536;
        asm volatile("" : "+v"(wa[i >> 2][i & 3]));
    }
    s16x4 wf[2][8][2];
    auto wrd = [&](int k, int i) AI {
        const int u = W_U[2 * k + (i >> 3)];
        wf[k & 1][i >> 1][i & 1] = tr_read(wa[u >> 3][(i >> 1) & 3] + (u & 7) * 8192 + (i & 1) * 2048);
    };
    u32x4 yp[14];
    const unsigned yo = srow * 4096 + cb * 256 + sch * 32;
    auto ya = [&](int i) { return ws + WS_Y + (i >> 1) * 32768 + (i & 1) * 16; };
    lds_wait(lds0 + L_FWD + ((l & 8) ? 16 + sch * 4 : (l & 1) * 4), 1);
#pragma unroll
    for (int i = 0; i < 16; ++i) wrd(0, i);
#pragma unroll
    for (int j = 0; j < 4; ++j) fetch1(1, j);
    waitv<4>();
    fix(0);
    sfor<0, 8>([&](auto K) {
        constexpr int k = K;
        unsigned m[4];
        if constexpr (2 * (k + 1) == WON) lds_wait(lds0 + L_FW2 + (l & 1) * 4, 1);
        if constexpr (k == 7) {
            if (own) {
#pragma unroll
                for (int i = 0; i < 14; ++i) ld_async(yp[i], yo, ya(i));
            }
        }
        SB0();
        sfor<0, 16>([&](auto I) {
            constexpr int i = I, j = i >> 2, nb = i & 3;
            const bf16x8 a = cat8(wf[k & 1][(j >> 1) * 4 + nb][0], wf[k & 1][(j >> 1) * 4 + nb][1]);
            if (k == 0 && j < 2) asm volatile("v_mfma_f32_16x16x32_bf16 %0, %1, %2, 0" : "=&v"(acc[j & 1][nb]) : "v"(a), "v"(of[k % 3][j]));
            else asm volatile("v_mfma_f32_16x16x32_bf16 %0, %1, %2, %0" : "+v"(acc[j & 1][nb]) : "v"(a), "v"(of[k % 3][j]));
            SB0();
            if constexpr (k < 7) wrd(k + 1, i);
            if constexpr (k < 6 && i < 4) fetch1(k + 2, i);
            if constexpr (k < 7 && i == 10) {
                waitv<(k < 6 ? 4 : 0)>();
#pragma unroll
                for (int n = 0; n < 4; ++n) pin(of[(k + 1) % 3][n]);
            }
            if constexpr (k < 7 && i > 10 && i <= 14) {
                m[i - 11] = min4(of[(k + 1) % 3][i - 11]);
                asm volatile("" : "+v"(m[i - 11]));
            }
            SB0();
        });
        if constexpr (k < 7) {
            if (__builtin_expect(__builtin_amdgcn_ballot_w64(min(min(m[0], m[1]), min(m[2], m[3])) == 0u) != 0ull, 0)) fix(k + 1);
        }
        SB0();
    });
    asm volatile("s_nop 7");
    SB0();
    const unsigned ea = lds0 + L_A + l16 + 4096 * w, eb = lds0 + L_A + 4096 * (w ^ 4);
    lds_write<u32x4>(ea, nz4(acc[1][0]));
#pragma unroll
    for (int nb = 1; nb < 4; ++nb) lds_write<f32x4>(ea + nb * 1024, acc[1][nb]);
    if (own) {
        waitv<0>();
        settle(yp, [&](int) { return yo; }, ya);
        f32x4 yv[2];
#pragma unroll
        for (int h = 0; h < 2; ++h) {
            yv[h] = __builtin_bit_cast(f32x4, yp[h]);
#pragma unroll
            for (int i = 1; i < 7; ++i) yv[h] += __builtin_bit_cast(f32x4, yp[2 * i + h]);
#pragma unroll
            for (int i = 0; i < 4; ++i) {
                float t = dpp_add<0x128>(yv[h][i]);
                t = swap16_add(t, t);
                yv[h][i] = rbf(rbf(swap32_add(t, t)) * __uint_as_float(selv[1]));
            }
        }
#pragma unroll
        for (int j = 0; j < 2; ++j) {
            const bool hit = row0 + 8 * j + srow == (int)selv[0];
#pragma unroll
            for (int i = 0; i < 4; ++i) {
                const unsigned t = pack2(lo16(xr[j][i]) + yv[i >> 1][2 * (i & 1)], hi16(xr[j][i]) + yv[i >> 1][2 * (i & 1) + 1]);
                xr[j][i] = hit ? t : xr[j][i];
            }
        }
        g_mix();
    }
    BAR();
#pragma unroll
    for (int nb = 0; nb < 4; ++nb) acc[0][nb] += lds_read<f32x4>(eb + l16 + nb * 1024);
#pragma unroll
    for (int nb = 0; nb < 4; ++nb) asm volatile("" : "+v"(acc[0][nb]));
#pragma unroll
    for (int nb = 0; nb < 4; ++nb) {
        const u32x2 a = {pack2(acc[0][nb][0], acc[0][nb][1]), pack2(acc[0][nb][2], acc[0][nb][3])};
        lds_write<u32x2>(eb + l15 * 128 + (((2 * nb + (g >> 1)) ^ (l & 7)) * 16) + (g & 1) * 8, a);
    }
#pragma unroll
    for (int j = 0; j < 2; ++j) {
        const u32x4 av = lds_read<u32x4>(eb + j * 1024 + srow * 128 + ((sch ^ srow) * 16));
        u32x4 o;
#pragma unroll
        for (int i = 0; i < 4; ++i) {
            const unsigned ga = pack2(gv_[j] * lo16(av[i]), gv_[j] * hi16(av[i]));
            o[i] = pack2(lo16(ga) + gb[j][i][0], hi16(ga) + gb[j][i][1]);
        }
        asm volatile("global_store_dwordx4 %0, %1, %2 sc1\n\ts_nop 1" ::"v"(xo), "v"(o), "s"(out + (size_t)(row0 + 8 * j) * DM + cb * 64) : "memory");
    }
}

extern "C" void kernel_launch(void* const* d_in, const int* in_sizes, int n_in, void* d_out, int out_size,
                              void* d_ws, size_t ws_size, hipStream_t stream) {
    fused_mix<<<256, 512, 0, stream>>>((const u16*)d_in[0], (const u16*)d_in[1], (const u16*)d_in[2], (const u16*)d_in[3],
                                       (const u16*)d_in[5], (const u16*)d_in[6], (const u16*)d_in[7], (const u16*)d_in[8],
                                       (const u16*)d_in[9], (char*)d_ws, (u16*)d_out);
}
```

```cpp
#include <hip/hip_runtime.h>
#include <hip/hip_bf16.h>
#include <stdint.h>
#include <type_traits>

typedef __attribute__((ext_vector_type(8))) short bf16x8;
typedef __attribute__((ext_vector_type(4))) short s16x4;
typedef __attribute__((ext_vector_type(4))) float f32x4;
typedef __attribute__((ext_vector_type(2))) float f32x2;
typedef __attribute__((ext_vector_type(2))) unsigned u32x2;
typedef __attribute__((ext_vector_type(4))) unsigned u32x4;
typedef __attribute__((ext_vector_type(2))) __bf16 bf16x2_t;
typedef unsigned short u16;
typedef unsigned long long u64;

#define DI __device__ __forceinline__
#define AS3 __attribute__((address_space(3)))

constexpr int DM = 1024;

DI float bf2f(u16 v) { return __uint_as_float(((unsigned)v) << 16); }
DI u16 f2bf(float f) { __bf16 b = (__bf16)f; return __builtin_bit_cast(u16, b); }
DI float rbf(float f) { return bf2f(f2bf(f)); }
DI unsigned pack2(float lo, float hi) {
    const f32x2 v = {lo, hi};
    return __builtin_bit_cast(unsigned, __builtin_convertvector(v, bf16x2_t));
}
DI float lo16(unsigned u) { return __uint_as_float(u << 16); }
DI float hi16(unsigned u) { return __uint_as_float(u & 0xffff0000u); }

DI s16x4 tr_read(unsigned a) { return __builtin_amdgcn_ds_read_tr16_b64_v4i16((AS3 s16x4*)(size_t)a); }
DI bf16x8 lds_read128(unsigned a) { return *(AS3 const bf16x8*)(size_t)a; }
template <class T> DI void lds_write(unsigned a, T v) { *(AS3 T*)(size_t)a = v; }
template <class T> DI T lds_read(unsigned a) { return *(AS3 const T*)(size_t)a; }
DI bf16x8 cat8(s16x4 a, s16x4 b) { return __builtin_shufflevector(a, b, 0, 1, 2, 3, 4, 5, 6, 7); }
DI f32x4 mfma16(bf16x8 a, bf16x8 b, f32x4 c) { return __builtin_amdgcn_mfma_f32_16x16x32_bf16(a, b, c, 0, 0, 0); }
DI bf16x8 pack44(const f32x4& a, const f32x4& b) {
    u32x4 r;
    r[0] = pack2(a[0], a[1]);
    r[1] = pack2(a[2], a[3]);
    r[2] = pack2(b[0], b[1]);
    r[3] = pack2(b[2], b[3]);
    return __builtin_bit_cast(bf16x8, r);
}
DI unsigned lds_of(const void* p) { return (unsigned)(size_t)(AS3 const char*)p; }

template <int CTRL>
DI unsigned dpp(unsigned x) { return __builtin_amdgcn_update_dpp(0u, x, CTRL, 0xf, 0xf, true); }
template <int CTRL>
DI float dpp_add(float x) { return x + __uint_as_float(dpp<CTRL>(__float_as_uint(x))); }
DI float swap32_add(float a, float b) {
    auto r = __builtin_amdgcn_permlane32_swap(__float_as_uint(a), __float_as_uint(b), false, false);
    return __uint_as_float(r[0]) + __uint_as_float(r[1]);
}
DI float swap16_add(float a, float b) {
    auto r = __builtin_amdgcn_permlane16_swap(__float_as_uint(a), __float_as_uint(b), false, false);
    return __uint_as_float(r[0]) + __uint_as_float(r[1]);
}
DI unsigned wave_umax(unsigned x) {
    x = max(x, dpp<0xB1>(x));
    x = max(x, dpp<0x4E>(x));
    x = max(x, dpp<0x141>(x));
    x = max(x, dpp<0x140>(x));
    auto r = __builtin_amdgcn_permlane16_swap(x, x, false, false);
    x = max(r[0], r[1]);
    auto s = __builtin_amdgcn_permlane32_swap(x, x, false, false);
    return max(s[0], s[1]);
}
constexpr unsigned WS_E = 0;
constexpr unsigned WS_RG = 256;
constexpr unsigned WS_GG = WS_RG + 2048;
constexpr unsigned WS_Z = 65536;
constexpr unsigned WS_KF = WS_Z + 131072;
constexpr unsigned WS_GU = WS_KF + 262144;
constexpr unsigned WS_Y = WS_GU + 262144;
constexpr unsigned WS_KV = WS_Y + 262144;
constexpr unsigned WS_O = WS_KV + (2u << 20);
constexpr unsigned WS_CL = WS_O + (4u << 20);
constexpr unsigned WS_END = WS_CL + 65536;

constexpr int NQ = 9, ND = 7, NX = 16, KD = 76, VD = 82, NF = 64, NF1 = 58, NF2 = 58, NP1 = 12, NP = 14, NM1 = 21, NM2 = 21, NM = 22, KFN = 100;
constexpr int J_S[48] = {0, 0, 0, 1, 1, 1, 2, 2, 2, 3, 3, 3, 4, 4, 4, 5, 5, 5, 6, 6, 6, 7, 7, 7, 8, 8, 8, 9, 9, 10, 10, 11, 11, 12, 12, 13, 14, 15, 13, 14, 15, 9, 10, 11, 12, 13, 14, 15};
constexpr int J_M[48] = {0, 1, 2, 0, 1, 2, 0, 1, 2, 0, 1, 2, 0, 1, 2, 0, 1, 2, 0, 1, 2, 0, 1, 2, 0, 1, 2, 0, 1, 0, 1, 0, 1, 0, 1, 0, 0, 0, 1, 1, 1, 2, 2, 2, 2, 2, 2, 2};
constexpr int J_X[48] = {0, 0, 0, 1, 1, 1, 2, 2, 2, 3, 3, 3, 4, 4, 4, 5, 5, 5, 6, 6, 6, 7, 7, 7, 8, 8, 8, 9, 9, 10, 10, 11, 11, 12, 12, 13, 14, 15, 13, 14, 15, 9, 10, 11, 12, 13, 14, 15};
constexpr int J_U[48] = {5, 6, 2, 0, 1, 15, 13, 11, 14, 9, 10, 4, 6, 2, 3, 1, 15, 16, 11, 14, 12, 10, 4, 8, 2, 3, 18, 15, 16, 11, 14, 9, 10, 5, 6, 18, 15, 13, 11, 14, 12, 7, 9, 10, 4, 8, 5, 6};
constexpr int J_B[48] = {0, 0, 0, 0, 0, 9, 9, 9, 9, 9, 9, 1, 1, 1, 1, 1, 10, 10, 10, 10, 10, 10, 2, 2, 2, 2, 11, 11, 11, 11, 11, 5, 5, 5, 5, 11, 11, 11, 11, 11, 11, 4, 4, 4, 4, 4, 4, 4};
constexpr int Q_C[96] = {1, 0, 2, 0, 3, 0, 4, 0, 5, 0, 6, 0, 7, 0, 0, 0, 0, 0, 8, 0, 0, 0, 0, 0, 9, 0, 0, 0, 0, 0, 10, 0, 0, 0, 0, 0, 11, 0, 0, 0, 0, 0, 12, 0, 0, 0, 0, 0, 13, 0, 0, 0, 0, 0, 14, 0, 0, 0, 0, 0, 15, 0, 0, 0, 0, 0, 16, 0, 0, 0, 0, 0, 17, 0, 0, 0, 18, 0, 19, 0, 20, 0, 21, 0, 22, 0, 0, 0, 0, 0, 0, 0, 0, 0, 0, 0};
constexpr int X_U0[16] = {4, 3, 16, 12, 8, 18, 17, 7, 5, 0, 17, 12, 4, 2, 0, 16};
constexpr int X_U1[16] = {8, 18, 17, 7, 5, 0, 13, 9, 6, 1, 13, 7, 8, 3, 1, 17};
constexpr int P_U[4] = {0, 1, 2, 3};
constexpr int W_U[16] = {15, 13, 11, 14, 12, 2, 3, 0, 1, 7, 9, 10, 4, 8, 5, 6};
constexpr int W_N[16] = {100, 100, 100, 100, 100, 101, 101, 101, 101, 13, 13, 13, 14, 14, 99, 99};
constexpr int F_J[64] = {-1, 0, 1, 2, -2, 3, 4, 5, -3, 6, 7, 8, -4, 9, 10, 11, -5, 12, 13, 14, -6, 15, 16, 17, -7, 18, 19, 20, -8, 21, 22, 23, -9, 24, 25, 26, -10, 27, 28, -11, 29, 30, -12, 31, 32, -13, 33, 34, -14, 35, -15, 36, -16, 37, 38, 39, 40, 41, 42, 43, 44, 45, 46, 47};
constexpr int F_N[64] = {0, 0, 0, 0, 0, 0, 0, 0, 0, 0, 0, 0, 0, 0, 0, 1, 1, 1, 1, 1, 2, 2, 2, 2, 3, 3, 3, 3, 4, 4, 4, 4, 5, 5, 5, 5, 6, 6, 6, 6, 7, 7, 7, 7, 8, 8, 8, 8, 9, 9, 9, 10, 10, 10, 11, 11, 11, 11, 12, 12, 12, 12, 12, 12};
constexpr int F_K[64] = {0, 1, 2, 3, 0, 4, 5, 6, 0, 0, 0, 7, 0, 0, 0, 8, 0, 0, 0, 9, 0, 0, 0, 10, 0, 0, 0, 11, 0, 0, 0, 12, 0, 0, 0, 13, 0, 0, 0, 0, 14, 0, 0, 0, 15, 0, 0, 0, 0, 16, 0, 0, 0, 17, 18, 19, 20, 21, 0, 0, 0, 0, 0, 22};
constexpr int E_A[384] = {300, 325, 350, 375, 0, 0, 0, 502, 0, 0, 0, 403, 0, 0, 0, 503, 201, 251, 0, 404, 0, 0, 0, 504, 301, 326, 351, 376, 0, 0, 0, 505, 601, 0, 0, 406, 0, 100, 101, 506, 102, 103, 202, 407, 252, 0, 0, 507, 302, 327, 352, 377, 0, 0, 0, 602, 0, 0, 0, 0, 0, 0, 0, 203, 253, 0, 0, 408, 0, 0, 0, 508, 303, 328, 353, 378, 0, 0, 0, 603, 0, 0, 0, 0, 0, 100, 101, 102, 103, 204, 254, 409, 0, 0, 0, 509, 304, 329, 354, 379, 0, 0, 0, 604, 0, 0, 0, 0, 0, 0, 0, 205, 255, 0, 0, 410, 0, 0, 0, 510, 305, 330, 355, 380, 0, 100, 101, 605, 102, 103, 0, 0, 0, 0, 0, 206, 256, 0, 0, 411, 0, 0, 0, 511, 306, 331, 356, 381, 0, 0, 0, 606, 0, 0, 0, 0, 0, 0, 0, 207, 257, 0, 0, 412, 0, 0, 0, 512, 307, 332, 357, 382, 0, 100, 101, 607, 102, 103, 0, 0, 0, 0, 0, 208, 258, 0, 0, 413, 0, 0, 0, 513, 308, 333, 358, 383, 0, 0, 0, 608, 0, 0, 0, 0, 0, 100, 101, 102, 103, 209, 259, 414, 0, 0, 0, 514, 309, 334, 359, 384, 0, 0, 0, 609, 210, 260, 0, 0, 0, 0, 0, 0, 310, 335, 360, 385, 0, 0, 0, 515, 610, 211, 261, 0, 0, 100, 101, 102, 311, 336, 361, 386, 0, 0, 0, 611, 212, 262, 0, 416, 0, 0, 0, 516, 312, 337, 362, 387, 0, 0, 0, 213, 263, 0, 0, 0, 0, 100, 101, 102, 313, 338, 363, 388, 214, 264, 0, 517, 314, 339, 364, 389, 215, 265, 0, 0, 315, 340, 365, 390, 0, 0, 0, 518, 0, 0, 0, 419, 0, 0, 0, 519, 0, 0, 0, 420, 0, 0, 0, 520, 0, 0, 0, 0, 0, 100, 101, 102, 103, 0, 0, 422, 0, 0, 0, 522, 0, 0, 0, 0, 0, 0, 0, 0, 0, 0, 0, 0, 0, 0, 0, 0, 0, 0, 0, 613, 0, 0, 0, 0, 0, 0, 0, 0, 0, 0, 0, 0, 0, 0, 0, 614, 0, 0, 0, 0, 0, 0, 0, 0, 0, 0, 0, 0};
constexpr int E_B[384] = {0, 0, 402, 0, 0, 0, 0, 0, 0, 0, 0, 0, 0, 0, 0, 0, 0, 0, 0, 0, 0, 0, 0, 0, 0, 0, 405, 0, 0, 0, 0, 0, 0, 0, 0, 0, 0, 0, 0, 0, 0, 0, 0, 0, 0, 0, 0, 0, 0, 0, 0, 0, 0, 0, 0, 0, 0, 0, 0, 0, 0, 0, 0, 0, 0, 0, 0, 0, 0, 0, 0, 0, 0, 0, 0, 0, 0, 0, 0, 0, 0, 0, 0, 0, 0, 0, 0, 0, 0, 0, 0, 0, 0, 0, 0, 0, 0, 0, 0, 0, 0, 0, 0, 0, 0, 0, 0, 0, 0, 0, 0, 0, 0, 0, 0, 0, 0, 0, 0, 0, 0, 0, 0, 0, 0, 0, 0, 0, 0, 0, 0, 0, 0, 0, 0, 0, 0, 0, 0, 0, 0, 0, 0, 0, 0, 0, 0, 0, 0, 0, 0, 0, 0, 0, 0, 0, 0, 0, 0, 0, 0, 0, 0, 0, 0, 0, 0, 0, 0, 0, 0, 0, 0, 0, 0, 0, 0, 0, 0, 0, 0, 0, 0, 0, 0, 0, 0, 0, 0, 0, 0, 0, 0, 0, 0, 0, 0, 0, 0, 0, 0, 0, 0, 0, 0, 0, 0, 0, 0, 0, 0, 0, 0, 0, 0, 0, 0, 0, 0, 0, 0, 0, 0, 0, 0, 0, 0, 0, 0, 0, 0, 0, 0, 0, 415, 0, 0, 0, 0, 0, 0, 0, 0, 0, 0, 0, 0, 0, 103, 0, 0, 0, 0, 0, 0, 0, 0, 0, 0, 0, 0, 0, 0, 0, 0, 0, 0, 0, 0, 0, 0, 0, 0, 0, 0, 0, 0, 0, 0, 0, 103, 0, 417, 0, 0, 0, 0, 612, 0, 0, 0, 0, 0, 0, 0, 0, 0, 0, 418, 0, 0, 0, 0, 0, 0, 0, 0, 0, 0, 0, 0, 0, 0, 0, 0, 0, 0, 0, 0, 0, 0, 0, 0, 0, 0, 0, 0, 0, 0, 0, 0, 0, 0, 0, 0, 0, 0, 0, 0, 0, 0, 0, 0, 0, 0, 0, 0, 0, 0, 0, 0, 0, 0, 0, 0, 0, 0, 0, 0, 0, 0, 0, 0, 0, 0, 0, 0, 0, 0, 0, 0, 0, 0, 0, 0, 0, 0, 0, 0, 0, 0, 0, 0, 0};
constexpr unsigned L_A = 16 * 8192;
constexpr unsigned L_KF = 18 * 8192;
constexpr unsigned L_SM = 19 * 8192;
constexpr unsigned L_FULL = L_SM + 256;
constexpr unsigned L_PROG = L_SM + 896;
constexpr unsigned L_PRL = L_SM + 336;
constexpr unsigned L_FW2 = L_SM + 512, L_FOS = L_SM + 480, L_FK0 = L_SM + 752, L_FV = L_SM + 560, L_FP = L_SM + 576, L_FKS = L_SM + 592;
constexpr unsigned L_FQD = L_SM + 608, L_FG = L_SM + 624, L_FKF = L_SM + 736, L_FQ = L_SM + 720, L_FWD = L_SM + 640, L_FWO = L_SM + 656;
constexpr unsigned L_FLD = L_SM + 688, L_BEST = L_SM + 696;
constexpr unsigned L_SEL = L_SM + 704;
constexpr unsigned L_XS = L_SM + 2048, L_KZ = L_SM + 3072, L_WPHI = L_SM + 4096;
static_assert(NP <= 31 && NM <= 63 && NF1 == NF2 && NX == 16, "");
static_assert(P_U[1] == P_U[0] + 1 && P_U[3] == P_U[2] + 1, "feature maps of a head lie side by side");
constexpr bool j_first(int j) {
    for (int i = 0; i < j; ++i)
        if (J_M[i] == J_M[j]) return false;
    return true;
}
constexpr unsigned SPIN_CAP = 1u << 16;
#define N8(a) a, a + 1, a + 2, a + 3, a + 4, a + 5, a + 6, a + 7
__device__ const unsigned k_num[64] = {N8(0), N8(8), N8(16), N8(24), N8(32), N8(40), N8(48), N8(56)};

constexpr int PD = 4;
constexpr int RLD = F_N[NF1 - 1];
constexpr int CLR0 = 2;
constexpr int GQ1 = 6;
constexpr int WOE = 4, WON = 10;
constexpr int GDL = 20, KFD = 4, PDL = 6;
constexpr unsigned CPF = 55552;
constexpr u64 KXB = 0xFEDBA9C768534210ull;
#define BAR() asm volatile("s_waitcnt lgkmcnt(0)\n\ts_barrier" ::: "memory")
template <int N>
DI void waitv() { asm volatile("s_waitcnt vmcnt(%0)" ::"n"(N) : "memory"); }

DI unsigned nz1(unsigned v) { return max(v, 1u); }
DI u32x4 nz4(u32x4 v) { return (u32x4){max(v[0], 1u), max(v[1], 1u), max(v[2], 1u), max(v[3], 1u)}; }
DI u32x4 nz4(f32x4 v) { return nz4(__builtin_bit_cast(u32x4, v)); }
DI unsigned min4(u32x4 v) { return min(min(v[0], v[1]), min(v[2], v[3])); }
DI void st_wt(__amdgpu_buffer_rsrc_t r, unsigned off, u32x4 v) { __builtin_amdgcn_raw_buffer_store_b128(v, r, off, 0, 16); }
DI void ld_async(u32x4& v, unsigned off, const char* base) {
    asm volatile("global_load_dwordx4 %0, %1, %2 sc1" : "=&v"(v) : "v"(off), "s"(base) : "memory");
}
#define ST_MASK(op, m, off, v, base) asm volatile("s_mov_b64 exec, %3\n\t" op " %0, %1, %2 sc1\n\ts_mov_b64 exec, -1\n\ts_nop 0" ::"v"(off), "v"(v), "s"(base), "s"((u64)(m)) : "memory")
DI void pin(u32x4& v) { asm volatile("" : "+v"(v)); }
template <int N, class OFF, class BASE>
DI void settle(u32x4 (&v)[N], OFF off, BASE base) {
#pragma unroll
    for (int i = 0; i < N; ++i) pin(v[i]);
#pragma unroll 1
    for (unsigned s = 0; s < SPIN_CAP; ++s) {
        unsigned m = min4(v[0]);
#pragma unroll
        for (int i = 1; i < N; ++i) m = min(m, min4(v[i]));
        if (__ballot(m == 0u) == 0ull) break;
#pragma unroll
        for (int i = 0; i < N; ++i) {
            u64 keep;
            asm volatile("s_mov_b64 %1, exec\n\ts_mov_b64 exec, %4\n\tglobal_load_dwordx4 %0, %2, %3 sc1\n\ts_mov_b64 exec, %1"
                         : "+&v"(v[i]), "=&s"(keep) : "v"(off(i)), "s"(base(i)), "s"((u64)__ballot(min4(v[i]) == 0u)) : "memory");
        }
        waitv<0>();
#pragma unroll
        for (int i = 0; i < N; ++i) pin(v[i]);
    }
}
DI void lds_wait(unsigned a, unsigned n) {
    unsigned v, c;
    asm volatile("s_mov_b32 %1, 0x400000\n"
                 ".Lp%=:\n\tds_read_b32 %0, %2\n\ts_waitcnt lgkmcnt(0)\n\tv_cmp_gt_u32 vcc, %3, %0\n\ts_cbranch_vccz .Lf%=\n\t"
                 "s_sleep 1\n\ts_sub_u32 %1, %1, 1\n\ts_cbranch_scc0 .Lp%=\n.Lf%=:"
                 : "=&v"(v), "=&s"(c) : "v"(a), "s"(n) : "memory", "vcc", "scc");
}
DI void lds_wait0(unsigned a) {
    unsigned v, c;
    asm volatile("s_mov_b32 %1, 0x1000000\n"
                 ".Lp%=:\n\tds_read_b32 %0, %2\n\ts_sub_u32 %1, %1, 1\n\ts_cbranch_scc1 .Lf%=\n\ts_waitcnt lgkmcnt(0)\n\tv_cmp_eq_u32 vcc, 0, %0\n\ts_cbranch_vccnz .Lp%=\n"
                 ".Lf%=:\n\ts_waitcnt lgkmcnt(0)"
                 : "=&v"(v), "=&s"(c) : "v"(a) : "memory", "vcc", "scc");
}
DI void lds_wait8(unsigned a, unsigned n) {
    unsigned v, c;
    asm volatile("s_mov_b32 %1, 0x100000\n"
                 ".Lp%=:\n\tds_read_b32 %0, %2\n\ts_waitcnt lgkmcnt(0)\n\tv_cmp_gt_u32 vcc, %3, %0\n\ts_cbranch_vccz .Lf%=\n\t"
                 "s_sleep 4\n\ts_sub_u32 %1, %1, 1\n\ts_cbranch_scc0 .Lp%=\n.Lf%=:"
                 : "=&v"(v), "=&s"(c) : "v"(a), "s"(n) : "memory", "vcc", "scc");
}
DI void lds_peek(unsigned& v, unsigned a) { asm volatile("ds_read_b32 %0, %1" : "=v"(v) : "v"(a) : "memory"); }
DI void lds_wait(unsigned& v, unsigned a, unsigned n) {
    unsigned c;
    asm volatile("s_mov_b32 %1, 0x400000\n"
                 ".Lp%=:\n\ts_waitcnt lgkmcnt(0)\n\tv_cmp_gt_u32 vcc, %3, %0\n\ts_cbranch_vccz .Lf%=\n\t"
                 "ds_read_b32 %0, %2\n\ts_sub_u32 %1, %1, 1\n\ts_cbranch_scc0 .Lp%=\n.Lf%=:"
                 : "+v"(v), "=&s"(c) : "v"(a), "s"(n) : "memory", "vcc", "scc");
}
DI float dot2(unsigned a, unsigned b, float c) {
    return __builtin_amdgcn_fdot2_f32_bf16(__builtin_bit_cast(bf16x2_t, a), __builtin_bit_cast(bf16x2_t, b), c, false);
}
DI float sum4(const float (&a)[4]) {
    float c = swap16_add(swap32_add(a[0], a[1]), swap32_add(a[2], a[3]));
    c = dpp_add<0xB1>(c);
    c = dpp_add<0x4E>(c);
    c = dpp_add<0x141>(c);
    return dpp_add<0x140>(c);
}

template <class OP, class POST>
DI void red4(float& a, float& b, float& c, float& d, OP op, POST post) {
    auto x = __builtin_amdgcn_permlane32_swap(__float_as_uint(a), __float_as_uint(b), false, false);
    auto y = __builtin_amdgcn_permlane32_swap(__float_as_uint(c), __float_as_uint(d), false, false);
    const float m = op(__uint_as_float(x[0]), __uint_as_float(x[1])), n = op(__uint_as_float(y[0]), __uint_as_float(y[1]));
    auto z = __builtin_amdgcn_permlane16_swap(__float_as_uint(m), __float_as_uint(n), false, false);
    const unsigned f = __float_as_uint(post(op(__uint_as_float(z[0]), __uint_as_float(z[1]))));
    auto u = __builtin_amdgcn_permlane16_swap(f, f, false, false);
    auto v = __builtin_amdgcn_permlane32_swap(u[0], u[0], false, false);
    auto t = __builtin_amdgcn_permlane32_swap(u[1], u[1], false, false);
    a = __uint_as_float(v[0]);
    b = __uint_as_float(v[1]);
    c = __uint_as_float(t[0]);
    d = __uint_as_float(t[1]);
}
DI float mx2(float a, float b) { return __builtin_elementwise_maximum(a, b); }
template <class HOOK>
DI void softmax2(const f32x4 (&v)[2][2], u32x4 (&ph)[2][2], HOOK hook) {
    constexpr float L2E = 1.4426950408889634f;
    float mx[2], mn[2], sp_[2], sn_[2];
#pragma unroll
    for (int b = 0; b < 2; ++b) {
        const f32x4 s = v[b][0], t = v[b][1];
        mx[b] = mx2(mx2(mx2(mx2(s[0], s[1]), s[2]), mx2(mx2(s[3], t[0]), t[1])), mx2(t[2], t[3]));
        mn[b] = mx2(mx2(mx2(mx2(-s[0], -s[1]), -s[2]), mx2(mx2(-s[3], -t[0]), -t[1])), mx2(-t[2], -t[3]));
    }
    red4(mx[0], mx[1], mn[0], mn[1], [](float a, float b) { return mx2(a, b); }, [](float a) { return a * -L2E; });
    f32x2 ep[2][4], en[2][4];
#pragma unroll
    for (int b = 0; b < 2; ++b) {
#pragma unroll
        for (int e = 0; e < 4; ++e) {
            const f32x2 u = {v[b][e >> 1][2 * (e & 1)], v[b][e >> 1][2 * (e & 1) + 1]};
            const f32x2 a = __builtin_elementwise_fma(u, (f32x2){L2E, L2E}, (f32x2){mx[b], mx[b]});
            const f32x2 c = __builtin_elementwise_fma(u, (f32x2){-L2E, -L2E}, (f32x2){mn[b], mn[b]});
            ep[b][e] = (f32x2){__builtin_amdgcn_exp2f(a[0]), __builtin_amdgcn_exp2f(a[1])};
            en[b][e] = (f32x2){__builtin_amdgcn_exp2f(c[0]), __builtin_amdgcn_exp2f(c[1])};
        }
        const f32x2 s = (ep[b][0] + ep[b][1]) + (ep[b][2] + ep[b][3]), t = (en[b][0] + en[b][1]) + (en[b][2] + en[b][3]);
        sp_[b] = s[0] + s[1];
        sn_[b] = t[0] + t[1];
    }
    asm volatile("" : "+v"(sp_[0]), "+v"(sn_[0]), "+v"(sp_[1]), "+v"(sn_[1]));
    hook();
    red4(sp_[0], sp_[1], sn_[0], sn_[1], [](float a, float b) { return a + b; }, [](float a) { return __builtin_amdgcn_rcpf(a); });
#pragma unroll
    for (int b = 0; b < 2; ++b)
#pragma unroll
        for (int e = 0; e < 4; ++e) {
            const f32x2 a = ep[b][e] * (f32x2){sp_[b], sp_[b]}, c = en[b][e] * (f32x2){sn_[b], sn_[b]};
            ph[b][0][e] = pack2(a[0], a[1]);
            ph[b][1][e] = pack2(c[0], c[1]);
        }
}
DI f32x4 rbf4(const f32x4& a) {
    const unsigned u = pack2(a[0], a[1]), v = pack2(a[2], a[3]);
    return (f32x4){lo16(u), hi16(u), lo16(v), hi16(v)};
}
DI int fsw(int row) { return (((row >> 1) & 3) << 2) | (((row >> 3) & 1) << 1) | (row & 1); }
template <class HOOK>
DI void phi_k(const f32x4 (&pp)[2][2], const unsigned (&pw)[4], unsigned off, HOOK hook) {
    u32x4 ph[2][2];
    softmax2(pp, ph, hook);
#pragma unroll
    for (int b = 0; b < 2; ++b)
#pragma unroll
        for (int kf = 0; kf < 2; ++kf)
#pragma unroll
            for (int fb = 0; fb < 2; ++fb) {
                const u32x2 a = {ph[b][kf][2 * fb], ph[b][kf][2 * fb + 1]};
                lds_write<u32x2>(pw[2 * kf + fb] + off + b * 2048, a);
            }
}

#define SB0() __builtin_amdgcn_sched_barrier(0)
template <int A, int B, class F>
DI void sfor(F f) {
    if constexpr (A < B) {
        f(std::integral_constant<int, A>{});
        sfor<A + 1, B>(f);
    }
}
template <int N> using IC = std::integral_constant<int, N>;
#define AI __attribute__((always_inline))

DI void o_store(const u32x4 (&kf)[6], const u32x4 (&ph)[2][2], __amdgpu_buffer_rsrc_t rsrc, unsigned o_st) {
    const f32x4 zero = {0.f, 0.f, 0.f, 0.f};
#pragma unroll
    for (int i = 0; i < 2; ++i) {
        const bf16x8 p0 = __builtin_bit_cast(bf16x8, ph[i][0]), p1 = __builtin_bit_cast(bf16x8, ph[i][1]);
        f32x4 dn = mfma16(__builtin_bit_cast(bf16x8, kf[4]), p0, zero);
        f32x4 n0 = mfma16(__builtin_bit_cast(bf16x8, kf[0]), p0, zero);
        f32x4 n1 = mfma16(__builtin_bit_cast(bf16x8, kf[1]), p0, zero);
        dn = mfma16(__builtin_bit_cast(bf16x8, kf[5]), p1, dn);
        n0 = mfma16(__builtin_bit_cast(bf16x8, kf[2]), p1, n0);
        n1 = mfma16(__builtin_bit_cast(bf16x8, kf[3]), p1, n1);
        const float rd_ = __builtin_amdgcn_rcpf(rbf(rbf(dn[0]) + 1e-6f));
        const u32x4 nb_ = __builtin_bit_cast(u32x4, pack44(n0, n1));
        u32x4 o;
#pragma unroll
        for (int j = 0; j < 4; ++j) o[j] = pack2(lo16(nb_[j]) * rd_, hi16(nb_[j]) * rd_);
        st_wt(rsrc, o_st + i * 1024, nz4(o));
    }
}


typedef __attribute__((ext_vector_type(16))) unsigned u32x16;
__global__ __launch_bounds__(512, 2) void fused_mix(const u16*, const u16*, const u16*, const u16*, const u16*, const u16*, const u16*, const u16*,
                                                    const u16*, char*, u16*) {
    __shared__ __attribute__((aligned(16))) char smem[163840];
    u32x4 ka;
    u32x16 kb_;
    u64 kz;
    asm volatile("s_load_dwordx4 %0, %3, 0x0\n\ts_load_dwordx16 %1, %3, 0x10\n\ts_load_dwordx2 %2, %3, 0x50"
                 : "=&s"(ka), "=&s"(kb_), "=&s"(kz) : "s"(__builtin_amdgcn_kernarg_segment_ptr()));
    SB0();
    const int tid = threadIdx.x;
    int l = tid & 63;
    const int w = __builtin_amdgcn_readfirstlane(tid >> 6) ^ 4;
    int bid = blockIdx.x;
    const unsigned lds0 = lds_of(smem);
    int g = l >> 4, l15 = l & 15, q = l15 >> 2, p = l & 3;
    int srow = l >> 3, sch = l & 7;
    unsigned l16 = l * 16;
    const int rg = w & 3, kp = w >> 2;
    const int hs = rg >> 1;
    {
        u64 pc;
        asm volatile("s_getpc_b64 %0\n\ts_mov_b32 m0, %2\n\ts_mov_b64 exec, 3\n\tglobal_load_lds_dword %1, %0\n\ts_mov_b64 exec, -1"
                     : "=&s"(pc) : "v"(min((unsigned)(((bid & ~7) + w) * 2 + l) * 128u, CPF)), "s"(lds0 + L_SM + 3584) : "memory");
    }
    lds_write<unsigned>(lds0 + L_SM + tid * 4, 0u);
    const int slot = bid >> 3;
    const int mt = (bid & 1) * 8 + (slot & 7);
    const int hp = ((bid >> 1) & 3) * 4 + (slot >> 3);
    unsigned vx[4], vw[4];
    auto vw_init = [&]() AI {
        int t = l;
        asm volatile("" : "+v"(t));
#pragma unroll
        for (int i = 0; i < 4; ++i) vw[i] = (unsigned)((8 * i + (t >> 3)) * 6144 + ((t & 7) ^ (((t >> 4) & 1) << 1) ^ ((i & 1) << 2)) * 16 + 4096 - i * 1024);
    };
    auto vx_init = [&]() AI {
        int t = l;
        asm volatile("" : "+v"(t));
#pragma unroll
        for (int i = 0; i < 4; ++i) vx[i] = (unsigned)((8 * i + (t >> 3)) * 2048 + (((t & 7) ^ ((4 * (i & 1) + (t >> 4)) & 7)) * 16) + 4096 - i * 1024);
    };
    vx_init();
    vw_init();
    asm volatile("" : "+v"(vx[0]), "+v"(vx[1]), "+v"(vx[2]), "+v"(vx[3]), "+v"(vw[0]), "+v"(vw[1]), "+v"(vw[2]), "+v"(vw[3]));
    unsigned xo0 = (mt * 128 + hs * 64) * 2048, wo0 = hp * 128 + hs * (32 * 6144);
    asm volatile("s_waitcnt lgkmcnt(0)" : "+s"(ka), "+s"(kb_), "+s"(kz), "+s"(xo0), "+s"(wo0) :: "memory");
    const u16* x = (const u16*)((u64)ka[1] << 32 | ka[0]);
    const u16* wqkv = (const u16*)((u64)ka[3] << 32 | ka[2]);
    auto arg = [&](int i) { return (char*)((u64)kb_[2 * i + 1] << 32 | kb_[2 * i]); };
    const u16 *wphi = (const u16*)arg(0), *wo = (const u16*)arg(1), *wr = (const u16*)arg(2), *wg = (const u16*)arg(3);
    const u16 *wu = (const u16*)arg(4), *wd = (const u16*)arg(5), *gw = (const u16*)arg(6);
    char* ws = arg(7);
    u16* out = (u16*)kz;
    const int sc = (3 - (int)((unsigned)(size_t)wqkv >> 7)) & 7;
    const unsigned* one = k_num;
    unsigned v0 = 0;
    asm volatile("" : "+s"(one), "+v"(v0));
    auto mark = [&](unsigned dst, int n = 1, int lanes = 1) AI {
        asm volatile("s_mov_b32 m0, %2\n\ts_mov_b64 exec, %4\n\tglobal_load_lds_dword %0, %1 offset:%3\n\ts_mov_b64 exec, -1"
                     ::"v"(v0), "s"(one), "s"(dst - 4 * n), "n"(4 * n), "n"(lanes) : "memory");
    };
    auto four = [&](const unsigned (&v)[4], const char* src, unsigned dst) AI {
        asm volatile("s_mov_b32 m0, %5\n\ts_nop 0\n\t"
                     "global_load_lds_dwordx4 %0, %4\n\tglobal_load_lds_dwordx4 %1, %4 offset:1024\n\t"
                     "global_load_lds_dwordx4 %2, %4 offset:2048\n\tglobal_load_lds_dwordx4 %3, %4 offset:3072"
                     ::"v"(v[0]), "v"(v[1]), "v"(v[2]), "v"(v[3]), "s"(src), "s"(dst) : "memory");
    };
    const int ks0 = (((slot + (slot >> 3)) & 3) | ((hp & 7) == sc ? slot & 4 : 0)) ^ ((bid & 1) * 8);
    const char* xb0 = (const char*)x + xo0 - 4096;
    const char* wb0 = (const char*)wqkv + wo0 - 4096;
    auto fill = [&](auto N) AI {
        constexpr int n = N, j = F_J[n] < 0 ? 0 : F_J[n], xi = F_J[n] < 0 ? -1 - F_J[n] : 0, s = F_J[n] < 0 ? xi : J_S[j], m = J_M[j];
        const int ks = ks0 ^ (int)((KXB >> (4 * s)) & 15);
        if constexpr (F_J[n] >= 0) {
            four(vw, wb0 + ks * (64 * 6144) + (m == 2 ? 0 : 2048 * (m + 1)), lds0 + J_U[j] * 8192 + hs * 4096);
        } else {
            const char* xs_ = xb0 + ks * 128;
            const unsigned d = lds0 + (hs ? X_U1[xi] : X_U0[xi]) * 8192;
            four(vx, xs_, d);
            four(vx, xs_ + 32 * 2048, d + 4096);
        }
        if constexpr (F_K[n] > 1) mark(lds0 + L_FULL + (n < NF1 ? 0 : 32) + hs * 8, F_K[n], 3);
    };
    static_assert(F_K[1] == 1, "");
    if (__builtin_expect((w & 5) == 4, 1)) sfor<0, 2>(fill);
    unsigned sd = 0, st;
    asm volatile("s_cmp_lg_u32 %2, 5\n\ts_cbranch_scc1 .La%=\n\t"
                 "s_min_u32 %1, %3, 23\n\ts_lshl_b32 %1, %1, 18\n\ts_load_dword %0, %5, %1\n\ts_load_dword %0, %6, 0x0\n\t"
                 "s_and_b32 %1, %1, 0x3c0000\n\ts_load_dword %0, %4, %1\n\ts_load_dword %0, %10, %1\n\t"
                 "s_load_dword %0, %7, 0x0\n\ts_load_dword %0, %8, 0x0\n\ts_load_dword %0, %9, 0x0\n.La%=:"
                 : "+s"(sd), "=&s"(st) : "s"(w), "s"(slot), "s"(x), "s"(wqkv), "s"(one), "s"(wr), "s"(gw), "s"(wphi), "s"(out) : "memory", "scc");
    asm volatile("s_cmp_lg_u32 %2, 7\n\ts_cbranch_scc1 .La%=\n\t"
                 "s_min_u32 %1, %3, 27\n\ts_lshl_b32 %1, %1, 18\n\ts_load_dword %0, %4, 0x0\n\ts_load_dword %0, %4, %1\n\t"
                 "s_load_dword %0, %5, %1\n\ts_load_dword %0, %6, %1\n\ts_load_dword %0, %7, %1\n\t"
                 "s_and_b32 %1, %1, 0x1c0000\n\ts_load_dword %0, %8, %1\n.La%=:\n\ts_barrier"
                 : "+s"(sd), "=&s"(st) : "s"(w), "s"(slot), "s"(ws), "s"(wg), "s"(wu), "s"(wd), "s"(wo) : "memory", "scc");
    if (__builtin_expect((w & 5) == 4, 1)) {
        mark(lds0 + L_FULL + hs * 8, 1, 3);
        sfor<2, 4>(fill);
    }
    asm volatile("" : "+s"(bid));
    const int xcd = bid & 7;
    const int rt = (xcd >> 1) * 4 + ((bid >> 3) & 3), cb = (xcd & 1) * 8 + (bid >> 5), rot = 8 * rt;
    const __amdgpu_buffer_rsrc_t rsrc = __builtin_amdgcn_make_buffer_rsrc(ws, 0, WS_END, 0x00020000);
    const u32x4 z4 = {0u, 0u, 0u, 0u};
    const f32x4 zero = {0.f, 0.f, 0.f, 0.f};
    unsigned AX = lds0 + L_A + rg * 4096 + l16;
    unsigned W4 = lds0 + p * 4;
    unsigned PG = lds0 + L_PROG + p * 256;
    const unsigned WR = lds0 + rg * 4;
    auto flag = [&](unsigned a) AI { asm volatile("ds_write_b32 %0, %0" ::"v"(a) : "memory"); };
    auto phi_w = [&](bf16x8 (&a)[2], int h) AI {
        const unsigned ad = lds0 + L_WPHI + h * 2048 + (4 * g + q) * 64 + (((p >> 1) ^ (2 * (g & 1))) * 16) + 8 * (p & 1);
        a[0] = cat8(tr_read(ad), tr_read(ad + 1024));
        a[1] = cat8(tr_read(ad ^ 32), tr_read((ad ^ 32) + 1024));
    };
    u32x4 fw[32], fd[16], gu[2];
    unsigned sel_[4][2];
    u32x4 ph[2][2], sq;
    const int hp2 = ((bid >> 1) & 3) * 4 + (bid >> 6);
    const bool ffn = (hp2 & 7) != sc;
    const int ph4 = ((bid >> 3) + (bid >> 6)) & 3;
    const int kq = mt >> 2;
    const int cs = (ffn ? hp2 - (hp2 > sc) - (hp2 > sc + 8) : 14 + (hp2 >> 3)) * 4 + ((ph4 + 3 - (int)((unsigned)(size_t)wg >> 7)) & 3);
    auto spread = [&](const u32x4& v, int i) -> bf16x8 {
        const unsigned s = v[i >> 1];
        u32x4 a;
#pragma unroll
        for (int d = 0; d < 4; ++d) a[d] = __builtin_amdgcn_perm(s, s, sel_[d][i & 1]);
        return __builtin_bit_cast(bf16x8, a);
    };
    auto fold_store = [&](const f32x4& a, unsigned off) {
        f32x4 v;
#pragma unroll
        for (int i = 0; i < 4; ++i) {
            const unsigned u = __float_as_uint(a[i]);
            const float r = __uint_as_float(__builtin_amdgcn_update_dpp(u, u, 0x128, 0xc, 0xf, false));
            v[i] = swap32_add(r, r);
        }
        const u32x4 o = nz4(v);
        ST_MASK("global_store_dwordx4", 0x00FF00FFull, off + l15 * 32 + g * 16, o, ws);
    };
    auto sel_init = [&]() AI {
#pragma unroll
        for (int d = 0; d < 4; ++d)
#pragma unroll
            for (int hf = 0; hf < 2; ++hf) {
                const unsigned b = hf ? 0x0302u : 0x0100u;
                sel_[d][hf] = (sch >> 1) != d ? 0x0c0c0c0cu : (l & 1) ? (b << 16) | 0x0c0cu : 0x0c0c0000u | b;
            }
    };
    auto gua = [&](int i) { return ws + WS_GU + cs * 4096 + i * 1024; };
    auto gu_load = [&]() AI {
#pragma unroll
        for (int i = 0; i < 2; ++i) ld_async(gu[i], l16, gua(i));
    };
    auto down = [&]() AI {
#pragma unroll
        for (int i = 0; i < 16; ++i) pin(fd[i]);
        settle(gu, [&](int) { return l16; }, gua);
        const f32x4 v = __builtin_bit_cast(f32x4, gu[0]) + __builtin_bit_cast(f32x4, gu[1]);
        auto r = __builtin_amdgcn_permlane16_swap(__float_as_uint(swap32_add(v[0], v[2])), __float_as_uint(swap32_add(v[1], v[3])), false, false);
        const float gt = rbf(__uint_as_float(r[0])), up = rbf(__uint_as_float(r[1]));
        const unsigned ha = lds0 + L_XS + hs * 512;
        lds_write<u16>(ha + l15 * 8 + g * 2, f2bf(rbf(gt / (1.f + __expf(-gt))) * up));
        const u32x4 hv = lds_read<u32x4>(ha + srow * 16);
        f32x4 y0 = zero, y1 = zero;
#pragma unroll
        for (int i = 0; i < 8; ++i) {
            const bf16x8 a = spread(hv, i);
            y0 = mfma16(a, __builtin_bit_cast(bf16x8, fd[i]), y0);
            y1 = mfma16(a, __builtin_bit_cast(bf16x8, fd[8 + i]), y1);
        }
        fold_store(y0, WS_Y + cs * 4096 + kq * 1024 + hs * 512);
        fold_store(y1, WS_Y + cs * 4096 + kq * 1024 + hs * 512 + 256);
    };
    auto need = [&](int c) AI {
        if (c == 101) lds_wait(W4 + L_FKS, 1);
        else if (c == 100) lds_wait(W4 + L_FV, 1);
        else if (c == 99) lds_wait(W4 + L_FQD, 1);
        else if (c > 0) lds_wait(c > RLD ? W4 + L_PRL : PG, c);
    };
    auto wo_load = [&](int n0, int n1, bool mk) AI {
        unsigned vo[4];
#pragma unroll
        for (int i = 0; i < 4; ++i) vo[i] = (unsigned)((8 * i + srow) * 2048 + ((sch ^ ((g & 3) << 1)) * 16) + 4096 - i * 1024);
        const char* wob = (const char*)wo + cb * 128 - 4096;
#pragma unroll
        for (int n = n0; n < n1; ++n) {
            if (n < WON && (n == 0 || W_N[n] != W_N[n - 1])) need(W_N[n]);
            four(vo, wob + ((2 * n + hs + rot) & 31) * 65536, lds0 + W_U[n] * 8192 + hs * 4096);
        }
        if (mk) mark(lds0 + (n0 >= WON ? L_FW2 : L_FWD) + hs * 4);
    };
    auto phi_adr = [&](unsigned (&pw)[4], unsigned base) AI {
#pragma unroll
        for (int i = 0; i < 4; ++i) pw[i] = (base + rg * 4096 + l15 * 128 + ((g ^ fsw(l15)) << 3)) ^ (i << 5);
    };
    const int row0 = rt * 128 + rg * 32 + kp * 16;
    u32x4 xr[2];
    u64 gg[2];
    unsigned xo = srow * 2048 + sch * 16;
    auto g_load = [&]() AI {
        asm volatile("global_load_dwordx2 %0, %2, %3 sc1\n\tglobal_load_dwordx2 %1, %2, %3 offset:64 sc1"
                     : "=&v"(gg[0]), "=&v"(gg[1]) : "v"((unsigned)srow * 8), "s"(ws + WS_GG + row0 * 8) : "memory");
    };
    auto xg_load = [&](int skip) AI {
        asm volatile("s_cmp_eq_u32 %4, 4\n\ts_cbranch_scc1 .Lk%=\n\t"
                     "global_load_dwordx4 %0, %5, %6\n\tglobal_load_dwordx4 %1, %5, %7\n\t"
                     "global_load_dwordx2 %2, %8, %9 sc1\n\tglobal_load_dwordx2 %3, %8, %9 offset:64 sc1\n.Lk%=:"
                     : "+v"(xr[0]), "+v"(xr[1]), "+v"(gg[0]), "+v"(gg[1])
                     : "s"(skip), "v"(xo), "s"(x + (size_t)row0 * DM + cb * 64), "s"(x + (size_t)(row0 + 8) * DM + cb * 64),
                       "v"((unsigned)srow * 8), "s"(ws + WS_GG + row0 * 8) : "memory", "scc");
    };
    auto xg_def = [&]() AI { asm volatile("" : "=v"(xr[0]), "=v"(xr[1]), "=v"(gg[0]), "=v"(gg[1])); };
    auto relane = [&]() AI {
        unsigned z = 0;
        asm volatile("" : "+v"(z));
        l = __builtin_amdgcn_mbcnt_hi(~0u, __builtin_amdgcn_mbcnt_lo(~0u, z));
        g = l >> 4, l15 = l & 15, q = l15 >> 2, p = l & 3, srow = l >> 3, sch = l & 7, l16 = l * 16;
        AX = lds0 + L_A + rg * 4096 + l16, W4 = lds0 + p * 4, PG = lds0 + L_PROG + p * 256, xo = srow * 2048 + sch * 16;
    };
    u32x4 kf[6];
    auto fd_def = [&]() AI {
#pragma unroll
        for (int i = 0; i < 16; ++i) asm volatile("" : "=v"(fd[i]));
        asm volatile("" : "=v"(gu[0]), "=v"(gu[1]));
    };
    auto kf_def = [&]() AI { asm volatile("" : "=v"(kf[0]), "=v"(kf[1]), "=v"(kf[2]), "=v"(kf[3]), "=v"(kf[4]), "=v"(kf[5])); };
    const int oh = hs ^ kp ^ 1;

    if (__builtin_expect(w >= 4, 1)) {
        const bool loader = !(w & 1);
        const unsigned kf_kv = WS_KF + (hp * 2 + (mt >> 3)) * 8192 + (mt & 7) * 512;
        const unsigned kf_z = WS_KF + (hp * 2 + (mt >> 3)) * 8192 + 4096 + (mt & 7) * 128;
        const unsigned fgo = ffn ? (unsigned)((256 * kq + 16 * srow) * 7168 + cs * 128 + sch * 16) : 0u;
        const char* fsrc = (const char*)(hs ? wu : wg);
        auto gu_w = [&](int q0, int q1) AI {
#pragma unroll
            for (int i = q0; i < q1; ++i) {
                unsigned t1, t2, t3;
                asm volatile("v_add_u32 %4, 0x1c00, %7\n\tv_add_u32 %5, 0x3800, %7\n\tv_add_u32 %6, 0x5400, %7\n\t"
                             "global_load_dwordx4 %0, %7, %8 nt\n\tglobal_load_dwordx4 %1, %4, %8 nt\n\t"
                             "global_load_dwordx4 %2, %5, %8 nt\n\tglobal_load_dwordx4 %3, %6, %8 nt"
                             : "=&v"(fw[4 * i]), "=&v"(fw[4 * i + 1]), "=&v"(fw[4 * i + 2]), "=&v"(fw[4 * i + 3]), "=&v"(t1), "=&v"(t2), "=&v"(t3)
                             : "v"(fgo), "s"(fsrc + (128 * (i >> 2) + 4 * (i & 3)) * 7168) : "memory");
            }
        };
        const unsigned fdo = ffn ? (unsigned)((64 * cs + 8 * srow) * 2048 + 512 * kq + 256 * hs + sch * 16) : 0u;
        auto dn_w = [&](int q0, int q1) AI {
#pragma unroll
            for (int i = q0; i < q1; ++i)
                asm volatile("global_load_dwordx4 %0, %4, %5 offset:-4096 nt\n\tglobal_load_dwordx4 %1, %4, %5 offset:-2048 nt\n\t"
                             "global_load_dwordx4 %2, %4, %5 nt\n\tglobal_load_dwordx4 %3, %4, %5 offset:2048 nt"
                             : "=&v"(fd[4 * i]), "=&v"(fd[4 * i + 1]), "=&v"(fd[4 * i + 2]), "=&v"(fd[4 * i + 3])
                             : "v"(fdo), "s"((const char*)wd + 4096 + (i & 1) * 8192 + (i >> 1) * 128) : "memory");
        };
        auto gate_up = [&]() AI {
            sel_init();
#pragma unroll
            for (int i = 0; i < 32; ++i) pin(fw[i]);
            u32x4 xs[4];
#pragma unroll
            for (int i = 0; i < 4; ++i) xs[i] = lds_read<u32x4>(lds0 + L_XS + hs * 512 + (i >> 1) * 256 + srow * 32 + (i & 1) * 16);
            f32x4 a = zero;
#pragma unroll
            for (int i = 0; i < 32; ++i) a = mfma16(spread(xs[i >> 3], i & 7), __builtin_bit_cast(bf16x8, fw[i]), a);
            fold_store(a, WS_GU + cs * 4096 + kq * 512 + hs * 256);
            SB0();
        };
        u64 T;
        u32x4 sv[2];
        auto rg_load = [&]() AI {
            asm volatile("global_load_dwordx4 %0, %2, %3 sc1\n\tglobal_load_dwordx4 %1, %2, %3 offset:16 sc1"
                         : "=&v"(sv[0]), "=&v"(sv[1]) : "v"(l * 32), "s"(ws + WS_RG) : "memory");
        };
        auto select = [&]() AI {
            const unsigned tl = (unsigned)T & 0x1FFFu, thi = (unsigned)(T >> 13);
#pragma unroll 1
            for (unsigned s = 0; s < SPIN_CAP; ++s) {
                pin(sv[0]);
                pin(sv[1]);
                const unsigned bad = ((sv[0][0] >> 19) ^ tl) | (sv[0][1] ^ thi) | ((sv[0][2] >> 19) ^ tl) | (sv[0][3] ^ thi) |
                                     ((sv[1][0] >> 19) ^ tl) | (sv[1][1] ^ thi) | ((sv[1][2] >> 19) ^ tl) | (sv[1][3] ^ thi);
                if (__all(bad == 0u)) break;
                __builtin_amdgcn_s_sleep(16);
                rg_load();
                waitv<0>();
            }
            pin(sv[0]);
            pin(sv[1]);
            unsigned k2 = 0;
#pragma unroll
            for (int j = 0; j < 4; ++j) {
                const unsigned v = sv[j >> 1][(j & 1) * 2] & 0x7FFFFu;
                k2 = max(k2, ((v >> 3) << 11) | ((255 - (4 * l + j)) << 3) | (v & 7));
            }
            const unsigned key = __builtin_amdgcn_readfirstlane(wave_umax(k2));
            asm volatile("s_mov_b32 m0, %2\n\ts_mov_b64 exec, %3\n\tglobal_load_lds_dwordx4 %0, %1\n\ts_mov_b64 exec, -1"
                         ::"v"((unsigned)(512 * kq) + l16), "s"(x + (size_t)(2047 - (int)(key & 2047u)) * DM), "s"(lds0 + L_XS + hs * 512), "s"(0xFFFFFFFFull) : "memory");
            unsigned rb = key >> 11;
            rb ^= (rb & 0x8000u) ? 0x8000u : 0xFFFFu;
            const float rsel = rbf(1.f / (1.f + __expf(-bf2f((u16)rb))));
            lds_write<u32x4>(lds0 + L_SEL, (u32x4){(unsigned)T, (unsigned)(T >> 32), 2047u - (key & 2047u), __float_as_uint(rsel)});
            if (bid == 0 && hs) ST_MASK("global_store_dwordx2", 1ull, 0u, T, ws + WS_E);
        };
        auto k_soft = [&]() AI {
            bf16x8 a0[2], kb[2];
            f32x4 pk[2][2][2];
            unsigned pw[4];
            phi_w(a0, 0);
            phi_adr(pw, lds0 + P_U[0] * 8192);
            asm volatile("" : "+v"(pw[0]), "+v"(pw[1]), "+v"(pw[2]), "+v"(pw[3]));
            auto logits = [&](int h) AI {
                lds_wait(WR + L_FK0 + 4 * h, 1);
#pragma unroll
                for (int i = 0; i < 2; ++i) kb[i] = lds_read128(AX + (P_U[0] * 8192 - L_A) + h * 4096 + i * 1024);
#pragma unroll
                for (int i = 0; i < 4; ++i) pk[h][i >> 1][i & 1] = mfma16(a0[i & 1], kb[i >> 1], zero);
            };
            auto round = [&](int h) AI {
#pragma unroll
                for (int i = 0; i < 4; ++i) pk[h][i >> 1][i & 1] = rbf4(pk[h][i >> 1][i & 1]);
            };
            logits(0);
            round(0);
            SB0();
            phi_k(pk[0], pw, 0, [&]() AI {
                logits(1);
                SB0();
            });
            SB0();
            round(1);
            phi_k(pk[1], pw, 4096, []() {});
            SB0();
            flag(WR + L_FP);
        };
        auto q_soft = [&]() AI {
            bf16x8 a1[2];
            f32x4 pq[2][2];
            phi_w(a1, hs);
            lds_wait0(WR + L_FQ);
#pragma unroll
            for (int rb = 0; rb < 2; ++rb) {
                const bf16x8 b = lds_read128(AX + rb * 1024);
#pragma unroll
                for (int fb = 0; fb < 2; ++fb) pq[rb][fb] = rbf4(mfma16(a1[fb], b, zero));
            }
            softmax2(pq, ph, []() {});
            asm volatile("" : "+v"(ph[0][0]), "+v"(ph[0][1]), "+v"(ph[1][0]), "+v"(ph[1][1]));
        };
        if (__builtin_expect(loader, 1)) {
            vx_init();
            vw_init();
            unsigned pv = 0;
            sfor<4, NF1>([&](auto N) {
                constexpr int n = N;
                if constexpr (F_N[n] > F_N[n - 1]) {
                    __builtin_amdgcn_s_setprio(3);
                    lds_wait(pv, PG, F_N[n]);
                    if (F_N[n] < RLD) lds_peek(pv, PG);
                    __builtin_amdgcn_s_setprio(0);
                }
                if constexpr (n == 12) {
                    asm volatile("s_mov_b32 m0, %2\n\ts_nop 0\n\tglobal_load_lds_dwordx4 %0, %1\n\tglobal_load_lds_dwordx4 %0, %1 offset:1024"
                                 ::"v"((unsigned)((l >> 2) * 64 + (((l & 3) ^ (2 * ((l >> 4) & 1))) * 16))),
                                   "s"(wphi + (hp * 2 + hs) * 1024), "s"(lds0 + L_WPHI + hs * 2048) : "memory");
                }
                fill(N);
            });
            flag(lds0 + L_FLD + hs * 4);
            __builtin_amdgcn_s_setprio(2);
            const char* cla = ws + WS_CL + ((l15 * 2 + (mt >> 3)) * 16 + hp) * 128;
            u64 cl;
            asm volatile("global_load_dwordx2 %0, %1, off sc1" : "=&v"(cl) : "v"(cla) : "memory");
            k_soft();
            lds_wait(W4 + L_FP, 1);
            sq = lds_read<u32x4>(lds0 + L_SEL);
            waitv<0>();
            asm volatile("" : "+v"(cl));
            {
                u32x4 kp_[6];
                const char* kb = ws + WS_KV + (hp * 8 + (mt >> 1)) * 16384 + (mt & 1) * 512 + hs * 256;
                const bool zw = !((mt & 3) | hs);
                const char* zb = zw ? ws + WS_Z + (hp * 4 + (mt >> 2)) * 2048 : kb;
                const unsigned kvo = g * 1024 + l15 * 16, zo = zw ? l16 : 0u;
                auto ko = [&](int i) { return i < 4 ? kvo : zo; };
                auto ka = [&](int i) { return i < 4 ? kb + i * 4096 : zb + (i - 4) * 1024; };
                const u64 T = (u64)sq[1] << 32 | sq[0];
#pragma unroll 1
                for (unsigned s = 0; s < SPIN_CAP && __ballot(cl != T); ++s)
                    asm volatile("global_load_dwordx2 %0, %1, off sc1\n\ts_waitcnt vmcnt(0)" : "=&v"(cl) : "v"(cla) : "memory");
                u32x4 a01;
#pragma unroll
                for (int i = 0; i < 4; ++i) a01[i] = (l15 >> 1) != i ? 0u : (l & 1) ? 0x3f800000u : 0x3f80u;
                unsigned po = kf_kv + hs * 256 + l15 * 16 + (g & 1) * 8;
                pin(a01);
                asm volatile("" : "+v"(po));
                lds_wait0(W4 + L_FKS);
                __builtin_amdgcn_s_sleep(GDL);
#pragma unroll
                for (int i = 0; i < 6; ++i) ld_async(kp_[i], ko(i), ka(i));
                xg_def();
                xg_load(w + 1);
                SB0();
                waitv<0>();
                settle(kp_, ko, ka);
                f32x4 d = zero;
#pragma unroll
                for (int i = 0; i < 4; ++i) d = mfma16(__builtin_bit_cast(bf16x8, a01), __builtin_bit_cast(bf16x8, kp_[i]), d);
                const u32x2 o = {nz1(pack2(d[0], d[1])), nz1(pack2(d[2], d[3]))};
                ST_MASK("global_store_dwordx2", 0xFFFFFFFFull, po, o, ws);
                if (zw) {
                    f32x4 z = __builtin_bit_cast(f32x4, kp_[4]) + __builtin_bit_cast(f32x4, kp_[5]);
#pragma unroll
                    for (int i = 0; i < 4; ++i) {
                        float t = dpp_add<0x128>(z[i]);
                        t = swap16_add(t, t);
                        z[i] = swap32_add(t, t);
                    }
                    const u32x2 oz = {nz1(pack2(z[0], z[1])), nz1(pack2(z[2], z[3]))};
                    ST_MASK("global_store_dwordx2", 0xFFFFull, kf_z + (l & 8) * 8 + p * 16 + (sch >> 2) * 8, oz, ws);
                }
                flag(lds0 + L_FG + hs * 4);
            }
            const char* kfb = ws + WS_KF + (hp * 2 + hs) * 8192;
            auto fo = [&](int i) { return i < 4 ? l16 : (unsigned)g * 16; };
            auto fb = [&](int i) { return i < 4 ? kfb + i * 1024 : kfb + 4096 + (i - 4) * 512; };
            need(KFN);
            q_soft();
            SB0();
            __builtin_amdgcn_s_sleep(KFD);
#pragma unroll
            for (int i = 0; i < 6; ++i) ld_async(kf[i], fo(i), fb(i));
            SB0();
            waitv<0>();
            settle(kf, fo, fb);
            asm volatile("" : "+v"(xr[0]), "+v"(xr[1]), "+v"(gg[0]), "+v"(gg[1]));
#pragma unroll
            for (int i = 0; i < 4; ++i) lds_write<u32x4>(lds0 + L_KF + hs * 4096 + i * 1024 + l16, kf[i]);
#pragma unroll
            for (int i = 0; i < 2; ++i) lds_write<u32x4>(lds0 + L_KZ + hs * 128 + i * 64 + g * 16, kf[4 + i]);
            SB0();
            flag(lds0 + L_FKF + hs * 4);
            fd_def();
        } else {
            const int row0 = (mt * 16 + hp) * 8 + 4 * hs;
            __builtin_amdgcn_s_setprio(2);
            asm volatile("s_waitcnt lgkmcnt(0)" : "+s"(sd));
            u32x4 rv[2], gv[2], xv[4][2];
            u64 ev;
            asm volatile("global_load_dwordx2 %4, %5, %9 sc1\n\t"
                         "global_store_dwordx4 %10, %11, %9 sc1\n\t"
                         "global_store_dwordx2 %12, %13, %9 sc1\n\t"
                         "global_load_dwordx4 %0, %6, %7\n\tglobal_load_dwordx4 %1, %6, %7 offset:1024\n\t"
                         "global_load_dwordx4 %2, %6, %8\n\tglobal_load_dwordx4 %3, %6, %8 offset:1024"
                         : "=&v"(rv[0]), "=&v"(rv[1]), "=&v"(gv[0]), "=&v"(gv[1]), "=&v"(ev)
                         : "v"(0u), "v"(l16), "s"(wr), "s"(gw), "s"(ws),
                           "v"(l < 32 ? WS_Y + cs * 4096 + kq * 1024 + hs * 512 + l16 : WS_GU + cs * 4096 + kq * 512 + hs * 256 + l15 * 16), "v"(z4),
                           "v"(hs ? kf_z + l15 * 8 : kf_kv + l * 8), "v"(0ull)
                         : "memory");
#pragma unroll
            for (int r = 0; r < 2; ++r)
                asm volatile("global_load_dwordx4 %0, %4, %5\n\tglobal_load_dwordx4 %1, %4, %5 offset:1024\n\t"
                             "global_load_dwordx4 %2, %4, %5 offset:2048\n\tglobal_load_dwordx4 %3, %4, %5 offset:3072"
                             : "=&v"(xv[2 * r][0]), "=&v"(xv[2 * r][1]), "=&v"(xv[2 * r + 1][0]), "=&v"(xv[2 * r + 1][1])
                             : "v"(l16), "s"(x + (size_t)(row0 + 2 * r) * DM) : "memory");
            SB0();
            waitv<0>();
            asm volatile("" : "+v"(rv[0]), "+v"(rv[1]), "+v"(gv[0]), "+v"(gv[1]), "+v"(ev));
#pragma unroll
            for (int r = 0; r < 4; ++r) asm volatile("" : "+v"(xv[r][0]), "+v"(xv[r][1]));
            T = ((u64)__builtin_amdgcn_readfirstlane((unsigned)(ev >> 32)) << 32 | __builtin_amdgcn_readfirstlane((unsigned)ev)) + 1;
            float ra_[4] = {0.f, 0.f, 0.f, 0.f}, ga_[4] = {0.f, 0.f, 0.f, 0.f};
#pragma unroll
            for (int r = 0; r < 4; ++r)
#pragma unroll
                for (int i = 0; i < 2; ++i)
#pragma unroll
                    for (int j = 0; j < 4; ++j) {
                        ra_[r] = dot2(xv[r][i][j], rv[i][j], ra_[r]);
                        ga_[r] = dot2(xv[r][i][j], gv[i][j], ga_[r]);
                    }
            const int rr = (g >> 1) | ((g & 1) << 1);
            unsigned k = f2bf(sum4(ra_));
            k ^= (k & 0x8000u) ? 0xFFFFu : 0x8000u;
            k = (k << 3) | (7 - 4 * hs - rr);
            auto r1 = __builtin_amdgcn_permlane16_swap(k, k, false, false);
            k = max(r1[0], r1[1]);
            auto r2 = __builtin_amdgcn_permlane32_swap(k, k, false, false);
            k = max(r2[0], r2[1]);
            u64 gq = (T << 16) | f2bf(1.f / (1.f + __expf(-rbf(sum4(ga_)))));
            if (hs) lds_write<unsigned>(lds0 + L_BEST, k | 0x80000000u);
            asm volatile("" : "+v"(gq), "+v"(k));
            __builtin_amdgcn_s_setprio(0);
            lds_wait8(PG, CLR0 + ph4);
            if (!hs) {
                lds_wait(lds0 + L_BEST, 1);
                const u64 rq = (T << 19) | max(k, lds_read<unsigned>(lds0 + L_BEST) & 0x7FFFFu);
                ST_MASK("global_store_dwordx2", 1ull, 0u, rq, ws + WS_RG + (mt * 16 + hp) * 8);
            }
            ST_MASK("global_store_dwordx2", 0x0001000100010001ull, (unsigned)(rr * 8), gq, ws + WS_GG + row0 * 8);
#pragma unroll
            for (int n = 0; n < 8; ++n) st_wt(rsrc, WS_O + ((mt * 2 + (n >> 2)) * 32 + 2 * hp + hs) * 4096 + (n & 3) * 1024 + l16, z4);
#pragma unroll
            for (int n = 0; n < 4; ++n) st_wt(rsrc, WS_KV + ((hp * 8 + hs * 4 + n) * 16 + mt) * 1024 + l16, z4);
            ST_MASK("global_store_dwordx4", 0xFFFFull, WS_Z + ((hp * 4 + hs * 2 + (srow & 1)) * 16 + mt) * 128 + sch * 16, z4, ws);
            waitv<0>();
            ST_MASK("global_store_dwordx2", 0xFFFFull, (unsigned)l * 8, T, ws + WS_CL + ((mt * 2 + hs) * 16 + hp) * 128);
            lds_wait8(lds0 + L_FLD + (l & 1) * 4, 1);
            rg_load();
            gu_w(0, GQ1);
            waitv<4 * GQ1>();
            select();
            flag(WR + L_FP);
            vx_init();
            vw_init();
            sfor<NF1, NF>([&](auto N) {
                constexpr int n = N;
                if constexpr (n == NF1 || F_N[n] != F_N[n - 1]) need(F_N[n]);
                fill(N);
            });
            gu_w(GQ1, 8);
            dn_w(0, 4);
            waitv<16>();
            __builtin_amdgcn_s_setprio(3);
            gate_up();
            __builtin_amdgcn_s_setprio(0);
            lds_wait(W4 + L_FKS, 1);
            wo_load(0, WOE, false);
            lds_wait(lds0 + L_FG + (l & 1) * 4, 1);
            gu_load();
            wo_load(WOE, WON, true);
            __builtin_amdgcn_s_setprio(2);
            q_soft();
            waitv<4 * (WON - WOE) + 1>();
#pragma unroll
            for (int i = 0; i < 16; ++i) pin(fd[i]);
            pin(gu[0]);
            pin(gu[1]);
            sq = lds_read<u32x4>(lds0 + L_SEL);
            xg_def();
            kf_def();
        }
    } else {
        __builtin_amdgcn_s_setprio(1);
        unsigned ra[4], xt[2];
#pragma unroll
        for (int u = 0; u < 4; ++u) ra[u] = lds0 + J_B[0] * 8192 + (8 * g + q) * 128 + ((u ^ (q >> 1) ^ ((g & 1) << 1)) * 32) + 8 * p;
        unsigned xa = lds0 + (32 * (w & 1) + l15) * 128 + ((g ^ (l15 >> 1)) * 16), fa = lds0 + L_FULL + (l & 3) * 4;
        xt[0] = xa + (hs ? X_U1[0] : X_U0[0]) * 8192;
        xt[1] = xt[0] ^ 64;
        asm volatile("s_mov_b32 m0, %4" : "+v"(xa), "+v"(fa), "+v"(xt[0]), "+v"(xt[1]) : "s"(lds0 + L_PROG + w * 256 - 124));
        f32x4 acc[3][4][2];
        bf16x8 xf[ND][2][2];
        s16x4 wh[PD + 1][2];
        unsigned fv = 0;
        bf16x8 kown[2];
        u64 bad = ~0ull;
        auto chk = [&](int c) AI {
            SB0();
            if (__builtin_expect(bad != 0ull, 0)) {
                unsigned c_;
                asm volatile("s_mov_b32 %1, 0x400000\n"
                             ".Lp%=:\n\tds_read_b32 %0, %2 offset:%3\n\ts_waitcnt lgkmcnt(0)\n\tv_cmp_gt_u32 vcc, %4, %0\n\ts_cbranch_vccz .Lf%=\n\t"
                             "s_sub_u32 %1, %1, 1\n\ts_cbranch_scc0 .Lp%=\n.Lf%=:"
                             : "=&v"(fv), "=&s"(c_) : "v"(fa), "n"(c > NM1 ? 32 : 0), "n"(c) : "memory", "vcc", "scc");
            }
            SB0();
        };
        auto kv_part = [&]() AI {
            bf16x8 ones;
#pragma unroll
            for (int j = 0; j < 8; ++j) ones[j] = (short)0x3f80;
            const unsigned pa = lds0 + (hs ? P_U[2] : P_U[0]) * 8192 + (4 * g + q) * 128 + (((8 * (rg & 1) + p) ^ fsw(4 * g + q)) << 3);
            const unsigned va = lds0 + L_A + hs * 2048 + l16;
            bf16x8 b0[4], b1[4], af[2][4];
            lds_wait0(lds0 + L_FV + sch * 4);
#pragma unroll
            for (int i = 0; i < 4; ++i) {
                af[0][i] = cat8(tr_read(pa + i * 4096), tr_read(pa + i * 4096 + 2048));
                b0[i] = lds_read128(va + i * 4096);
                af[1][i] = cat8(tr_read((pa ^ 32) + i * 4096), tr_read((pa ^ 32) + i * 4096 + 2048));
            }
#pragma unroll
            for (int i = 0; i < 4; ++i) b1[i] = lds_read128(va + i * 4096 + 1024);
            SB0();
            f32x4 k0[2] = {zero, zero}, k1[2] = {zero, zero}, zt[2] = {zero, zero};
            const unsigned kv_st = WS_KV + ((hp * 8 + rg * 2) * 16 + mt) * 1024 + l16;
#pragma unroll
            for (int i = 0; i < 4; ++i)
#pragma unroll
                for (int e = 0; e < 2; ++e) {
                    k0[e] = mfma16(af[e][i], b0[i], k0[e]);
                    zt[e] = mfma16(af[e][i], ones, zt[e]);
                }
            SB0();
#pragma unroll
            for (int i = 0; i < 4; ++i)
#pragma unroll
                for (int e = 0; e < 2; ++e) k1[e] = mfma16(af[e][i], b1[i], k1[e]);
            st_wt(rsrc, kv_st, nz4(__builtin_bit_cast(u32x4, pack44(k0[0], k0[1]))));
            const u32x4 zo = nz4((l & 1) ? zt[1] : zt[0]);
            ST_MASK("global_store_dwordx4", 0x0003000300030003ull, WS_Z + ((hp * 4 + rg) * 16 + mt) * 128 + (l & 1) * 64 + g * 16, zo, ws);
            st_wt(rsrc, kv_st + 16384, nz4(__builtin_bit_cast(u32x4, pack44(k1[0], k1[1]))));
            SB0();
            flag(WR + L_FKS);
        };
        auto report = [&](int r) AI {
            unsigned t, a;
            if (r > RLD) asm volatile("v_mov_b32 %0, %3\n\tv_mov_b32 %1, %2\n\tds_write_b32 %1, %0" : "=&v"(t), "=&v"(a) : "s"(WR + L_PRL), "n"(r) : "memory");
            else asm volatile("global_load_lds_dword %0, %1 offset:%2" ::"v"(v0), "s"(one), "n"(4 * r) : "memory");
        };
        auto rd1 = [&](auto F, auto H) AI {
            constexpr int f = F, h = H, n = f >> 2, j = f >> 3, c = h ? E_B[f] : E_A[f], a = c % 100;
            if constexpr (h == 0 && (f & 3) == 0 && Q_C[n] > 0) chk(Q_C[n]);
            wh[f % (PD + 1)][h] = tr_read(ra[f & 3] + (J_U[j] - J_B[j]) * 8192 + (n & 1) * 4096 + h * 512);
            SB0();
            if constexpr (c / 100 == 1) {
                constexpr int jn = (f + 3 - a) >> 3;
                asm volatile("v_add_u32 %0, %1, %0" : "+v"(ra[a]) : "n"((J_B[jn] - J_B[jn - 1]) * 8192));
            } else if constexpr (c / 50 == 4) {
                asm volatile("v_add_u32 %0, %1, %2" : "=v"(xt[0]) : "s"((hs ? X_U1[a] : X_U0[a]) * 8192), "v"(xa));
            } else if constexpr (c / 50 == 5) {
                asm volatile("v_xor_b32 %0, 64, %1" : "=v"(xt[1]) : "v"(xt[0]));
            } else if constexpr (c / 100 == 3) {
                xf[a % 25 % ND][a / 25 & 1][a / 50] = lds_read128(xt[a / 50] + (a / 25 & 1) * 2048);
            } else if constexpr (c / 100 == 4) {
                fv = *(volatile AS3 unsigned*)(size_t)(fa + (a > NM1 ? 32 : 0));
            } else if constexpr (c / 100 == 5) {
                bad = __builtin_amdgcn_ballot_w64(fv < (unsigned)a);
                asm volatile("" : "+s"(bad));
            } else if constexpr (c / 100 == 6) {
                report(a);
            }
        };
        sfor<0, PD>([&](auto F) { rd1(F, IC<0>{}); rd1(F, IC<1>{}); });
        constexpr int K0F = 4 * KD, K1F = 4 * KD + 1;
        sfor<0, 96>([&](auto N) {
            sfor<0, 4>([&](auto E) {
                constexpr int n = N, e = E, f = 4 * n + e, j = n >> 1, ks = n & 1, u = e, s = J_X[j], m = J_M[j];
                const bf16x8 wv = cat8(wh[f % (PD + 1)][0], wh[f % (PD + 1)][1]);
                sfor<0, 2>([&](auto RB) {
                    constexpr int rb = RB;
                    const bf16x8 xv = xf[s % ND][rb][ks];
                    if (j_first(j) && ks == 0) asm volatile("v_mfma_f32_16x16x32_bf16 %0, %1, %2, 0" : "=&v"(acc[m][u][rb]) : "v"(m == 1 ? xv : wv), "v"(m == 1 ? wv : xv));
                    else asm volatile("v_mfma_f32_16x16x32_bf16 %0, %1, %2, %0" : "+v"(acc[m][u][rb]) : "v"(m == 1 ? xv : wv), "v"(m == 1 ? wv : xv));
                    SB0();
                    if constexpr (f + PD < 384 && (f + PD < 4 * VD || f >= 4 * VD)) rd1(IC<f + PD>{}, RB);
                    if constexpr (rb == 0 && f == K0F) relane();
                    if constexpr (rb == 0 && (f == K0F || f == K1F)) asm volatile("s_nop 7");
                    SB0();
                    if constexpr (f == K0F) lds_write<bf16x8>(AX + (P_U[0] * 8192 - L_A) + rb * 1024, pack44(acc[0][0][rb], acc[0][1][rb]));
                    if constexpr (f == K1F) {
                        kown[rb] = pack44(acc[0][2][rb], acc[0][3][rb]);
                        asm volatile("" : "+v"(kown[rb]));
                    }
                    SB0();
                });
                if constexpr (f == K0F) flag(WR + L_FK0);
                if constexpr (f == 4 * VD - 1) {
                    bf16x8 ak[2];
                    f32x4 pk[2][2];
                    unsigned pw[4];
                    __builtin_amdgcn_s_setprio(2);
                    phi_w(ak, 1);
                    asm volatile("s_nop 7");
                    SB0();
#pragma unroll
                    for (int i = 0; i < 4; ++i) lds_write<bf16x8>(AX + i * 1024, pack44(acc[1][i][0], acc[1][i][1]));
                    SB0();
#pragma unroll
                    for (int i = 0; i < 4; ++i) pk[i >> 1][i & 1] = rbf4(mfma16(ak[i & 1], kown[i >> 1], zero));
                    phi_adr(pw, lds0 + P_U[2] * 8192);
                    phi_k(pk, pw, 0, []() {});
                    SB0();
                    flag(WR + L_FV);
                    kv_part();
                    __builtin_amdgcn_s_setprio(1);
                    fv = 0;
                    bad = ~0ull;
                    sfor<4 * VD, 4 * VD + PD>([&](auto F) { rd1(F, IC<0>{}); rd1(F, IC<1>{}); });
                }
                if constexpr (f == 23) __builtin_amdgcn_s_sleep(8);
            });
        });
        asm volatile("s_nop 7");
        SB0();
        __builtin_amdgcn_s_setprio(0);
        flag(WR + L_FQD);
        bf16x8 a0[2];
        f32x4 pq[2][2];
        phi_w(a0, hs ^ 1);
        lds_wait(W4 + L_FKS, 1);
#pragma unroll
        for (int rb = 0; rb < 2; ++rb) {
            const bf16x8 b0 = pack44(acc[2][0][rb], acc[2][1][rb]), b1 = pack44(acc[2][2][rb], acc[2][3][rb]);
            lds_write<bf16x8>(AX + rb * 1024, hs ? b1 : b0);
#pragma unroll
            for (int fb = 0; fb < 2; ++fb) pq[rb][fb] = mfma16(a0[fb], hs ? b0 : b1, zero);
        }
        SB0();
        flag(WR + L_FQ);
        sq = lds_read<u32x4>(lds0 + L_SEL);
#pragma unroll
        for (int i = 0; i < 4; ++i) pq[i >> 1][i & 1] = rbf4(pq[i >> 1][i & 1]);
        softmax2(pq, ph, []() {});
        asm volatile("" : "+v"(ph[0][0]), "+v"(ph[0][1]), "+v"(ph[1][0]), "+v"(ph[1][1]));
        xg_def();
        kf_def();
        fd_def();
    }
    relane();
    const char* cla = ws + WS_CL + ((rt * 2 + kp) * 16 + l15) * 128;
    const u64 T = (u64)sq[1] << 32 | sq[0];
    u64 cl;
    xg_load(w & 5);
    if ((w & 5) != 4) {
        lds_wait0(lds0 + L_FKF + oh * 4);
#pragma unroll
        for (int i = 0; i < 4; ++i) kf[i] = lds_read<u32x4>(lds0 + L_KF + oh * 4096 + i * 1024 + l16);
#pragma unroll
        for (int i = 0; i < 2; ++i) kf[4 + i] = lds_read<u32x4>(lds0 + L_KZ + oh * 128 + i * 64 + g * 16);
    }
    asm volatile("global_load_dwordx2 %0, %1, off sc1" : "=&v"(cl) : "v"(cla) : "memory");
    o_store(kf, ph, rsrc, WS_O + ((mt * 2 + hs) * 32 + 2 * hp + oh) * 4096 + (rg & 1) * 2048 + l16);
    flag(lds0 + L_FOS + w * 4);
    waitv<3>();
    __builtin_amdgcn_s_setprio(0);
    if ((w & 5) == 4) {
        lds_wait(lds0 + L_FOS + sch * 4, 1);
        wo_load(WON, 16, true);
    }
    if ((w & 5) == 5) {
        sel_init();
        down();
    }
    flag(lds0 + L_FWO + w * 4);
    const u32x2 selv = {sq[2], sq[3]};

    const char* ob = ws + WS_O + (rt * 2 + (rg >> 1)) * 131072 + (rg & 1) * 2048;
    const unsigned ov0 = l16 + kp * 1024, ov1 = l16 + (kp ^ 1) * 1024;
    auto oa = [&](int i) { return ob + ((2 * i + kp + rot) & 31) * 4096; };
    u32x4 of[3][4];
    auto fetch1 = [&](int k, int j) AI { ld_async(of[k % 3][j], (j & 1) ? ov1 : ov0, oa(2 * k + (j >> 1))); };
    auto fix = [&](int k) AI { settle(of[k % 3], [&](int j) { return (j & 1) ? ov1 : ov0; }, [&](int j) { return oa(2 * k + (j >> 1)); }); };
    const bool own = (__builtin_amdgcn_readfirstlane(selv[0]) >> 4) == (unsigned)(rt * 8 + rg * 2 + kp);
    float gv_[2];
    f32x2 gb[2][4];
    auto g_mix = [&]() AI {
#pragma unroll
        for (int j = 0; j < 2; ++j) {
            gv_[j] = bf2f((u16)gg[j]);
            const float og = rbf(1.f - gv_[j]);
#pragma unroll
            for (int i = 0; i < 4; ++i) {
                const unsigned t = pack2(og * lo16(xr[j][i]), og * hi16(xr[j][i]));
                gb[j][i] = (f32x2){lo16(t), hi16(t)};
                asm volatile("" : "+v"(gb[j][i]));
            }
        }
    };
    asm volatile("" : "+v"(xr[0]), "+v"(xr[1]), "+v"(gg[0]), "+v"(gg[1]));
#pragma unroll 1
    for (unsigned s = 0; s < SPIN_CAP && __ballot((gg[0] >> 16) != (T & 0xFFFFFFFFFFFFull) || (gg[1] >> 16) != (T & 0xFFFFFFFFFFFFull)); ++s) {
        g_load();
        waitv<0>();
        asm volatile("" : "+v"(gg[0]), "+v"(gg[1]));
    }
    g_mix();
    if (!kp) {
        __builtin_amdgcn_s_sleep(PDL);
        waitv<2>();
    } else if (w & 1) waitv<4>();
    else waitv<3 + 4 * (16 - WON)>();
    asm volatile("" : "+v"(cl));
#pragma unroll 1
    for (unsigned s = 0; s < SPIN_CAP && __ballot(cl != T); ++s)
        asm volatile("global_load_dwordx2 %0, %1, off sc1\n\ts_waitcnt vmcnt(0)" : "=&v"(cl) : "v"(cla) : "memory");
#pragma unroll
    for (int j = 0; j < 4; ++j) fetch1(0, j);
    f32x4 acc[2][4];
    unsigned wa[2][4];
#pragma unroll
    for (int i = 0; i < 8; ++i) {
        wa[i >> 2][i & 3] = ((lds0 + 4096 * kp + (4 * g + q) * 128 + ((((g & 1) << 1) | (q >> 1)) * 32) + 8 * p) ^ ((i & 3) << 5)) + (i >> 2) * 65536;
        asm volatile("" : "+v"(wa[i >> 2][i & 3]));
    }
    s16x4 wf[2][8][2];
    auto wrd = [&](int k, int i) AI {
        const int u = W_U[2 * k + (i >> 3)];
        wf[k & 1][i >> 1][i & 1] = tr_read(wa[u >> 3][(i >> 1) & 3] + (u & 7) * 8192 + (i & 1) * 2048);
    };
    u32x4 yp[14];
    const unsigned yo = srow * 4096 + cb * 256 + sch * 32;
    auto ya = [&](int i) { return ws + WS_Y + (i >> 1) * 32768 + (i & 1) * 16; };
    lds_wait(lds0 + L_FWD + (l & 1) * 4, 1);
#pragma unroll
    for (int i = 0; i < 16; ++i) wrd(0, i);
#pragma unroll
    for (int j = 0; j < 4; ++j) fetch1(1, j);
    waitv<4>();
    fix(0);
    sfor<0, 8>([&](auto K) {
        constexpr int k = K;
        unsigned m[4];
        if constexpr (2 * (k + 1) == WON) lds_wait(lds0 + ((l & 8) ? L_FWO + sch * 4 : L_FW2 + (l & 1) * 4), 1);
        if constexpr (k == 7) {
            if (own) {
#pragma unroll
                for (int i = 0; i < 14; ++i) ld_async(yp[i], yo, ya(i));
            }
        }
        SB0();
        sfor<0, 16>([&](auto I) {
            constexpr int i = I, j = i >> 2, nb = i & 3;
            const bf16x8 a = cat8(wf[k & 1][(j >> 1) * 4 + nb][0], wf[k & 1][(j >> 1) * 4 + nb][1]);
            if (k == 0 && j < 2) asm volatile("v_mfma_f32_16x16x32_bf16 %0, %1, %2, 0" : "=&v"(acc[j & 1][nb]) : "v"(a), "v"(of[k % 3][j]));
            else asm volatile("v_mfma_f32_16x16x32_bf16 %0, %1, %2, %0" : "+v"(acc[j & 1][nb]) : "v"(a), "v"(of[k % 3][j]));
            SB0();
            if constexpr (k < 7) wrd(k + 1, i);
            if constexpr (k < 6 && i < 4) fetch1(k + 2, i);
            if constexpr (k < 7 && i == 10) {
                waitv<(k < 6 ? 4 : 0)>();
#pragma unroll
                for (int n = 0; n < 4; ++n) pin(of[(k + 1) % 3][n]);
            }
            if constexpr (k < 7 && i > 10 && i <= 14) {
                m[i - 11] = min4(of[(k + 1) % 3][i - 11]);
                asm volatile("" : "+v"(m[i - 11]));
            }
            SB0();
        });
        if constexpr (k < 7) {
            if (__builtin_expect(__builtin_amdgcn_ballot_w64(min(min(m[0], m[1]), min(m[2], m[3])) == 0u) != 0ull, 0)) fix(k + 1);
        }
        SB0();
    });
    asm volatile("s_nop 7");
    SB0();
    const unsigned ea = lds0 + L_A + l16 + 4096 * w, eb = lds0 + L_A + 4096 * (w ^ 4);
    lds_write<u32x4>(ea, nz4(acc[1][0]));
#pragma unroll
    for (int nb = 1; nb < 4; ++nb) lds_write<f32x4>(ea + nb * 1024, acc[1][nb]);
    if (own) {
        waitv<0>();
        settle(yp, [&](int) { return yo; }, ya);
        f32x4 yv[2];
#pragma unroll
        for (int h = 0; h < 2; ++h) {
            yv[h] = __builtin_bit_cast(f32x4, yp[h]);
#pragma unroll
            for (int i = 1; i < 7; ++i) yv[h] += __builtin_bit_cast(f32x4, yp[2 * i + h]);
#pragma unroll
            for (int i = 0; i < 4; ++i) {
                float t = dpp_add<0x128>(yv[h][i]);
                t = swap16_add(t, t);
                yv[h][i] = rbf(rbf(swap32_add(t, t)) * __uint_as_float(selv[1]));
            }
        }
#pragma unroll
        for (int j = 0; j < 2; ++j) {
            const bool hit = row0 + 8 * j + srow == (int)selv[0];
#pragma unroll
            for (int i = 0; i < 4; ++i) {
                const unsigned t = pack2(lo16(xr[j][i]) + yv[i >> 1][2 * (i & 1)], hi16(xr[j][i]) + yv[i >> 1][2 * (i & 1) + 1]);
                xr[j][i] = hit ? t : xr[j][i];
            }
        }
        g_mix();
    }
    BAR();
#pragma unroll
    for (int nb = 0; nb < 4; ++nb) acc[0][nb] += lds_read<f32x4>(eb + l16 + nb * 1024);
#pragma unroll
    for (int nb = 0; nb < 4; ++nb) asm volatile("" : "+v"(acc[0][nb]));
#pragma unroll
    for (int nb = 0; nb < 4; ++nb) {
        const u32x2 a = {pack2(acc[0][nb][0], acc[0][nb][1]), pack2(acc[0][nb][2], acc[0][nb][3])};
        lds_write<u32x2>(eb + l15 * 128 + (((2 * nb + (g >> 1)) ^ (l & 7)) * 16) + (g & 1) * 8, a);
    }
#pragma unroll
    for (int j = 0; j < 2; ++j) {
        const u32x4 av = lds_read<u32x4>(eb + j * 1024 + srow * 128 + ((sch ^ srow) * 16));
        u32x4 o;
#pragma unroll
        for (int i = 0; i < 4; ++i) {
            const unsigned ga = pack2(gv_[j] * lo16(av[i]), gv_[j] * hi16(av[i]));
            o[i] = pack2(lo16(ga) + gb[j][i][0], hi16(ga) + gb[j][i][1]);
        }
        asm volatile("global_store_dwordx4 %0, %1, %2 sc1\n\ts_nop 1" ::"v"(xo), "v"(o), "s"(out + (size_t)(row0 + 8 * j) * DM + cb * 64) : "memory");
    }
}

extern "C" void kernel_launch(void* const* d_in, const int* in_sizes, int n_in, void* d_out, int out_size,
                              void* d_ws, size_t ws_size, hipStream_t stream) {
    fused_mix<<<256, 512, 0, stream>>>((const u16*)d_in[0], (const u16*)d_in[1], (const u16*)d_in[2], (const u16*)d_in[3],
                                       (const u16*)d_in[5], (const u16*)d_in[6], (const u16*)d_in[7], (const u16*)d_in[8],
                                       (const u16*)d_in[9], (char*)d_ws, (u16*)d_out);
}
```

```cpp
#include <hip/hip_runtime.h>
#include <hip/hip_bf16.h>
#include <stdint.h>
#include <type_traits>

typedef __attribute__((ext_vector_type(8))) short bf16x8;
typedef __attribute__((ext_vector_type(4))) short s16x4;
typedef __attribute__((ext_vector_type(4))) float f32x4;
typedef __attribute__((ext_vector_type(2))) float f32x2;
typedef __attribute__((ext_vector_type(2))) unsigned u32x2;
typedef __attribute__((ext_vector_type(4))) unsigned u32x4;
typedef __attribute__((ext_vector_type(2))) __bf16 bf16x2_t;
typedef unsigned short u16;
typedef unsigned long long u64;

#define DI __device__ __forceinline__
#define AS3 __attribute__((address_space(3)))

constexpr int DM = 1024;

DI float bf2f(u16 v) { return __uint_as_float(((unsigned)v) << 16); }
DI u16 f2bf(float f) { __bf16 b = (__bf16)f; return __builtin_bit_cast(u16, b); }
DI float rbf(float f) { return bf2f(f2bf(f)); }
DI unsigned pack2(float lo, float hi) {
    const f32x2 v = {lo, hi};
    return __builtin_bit_cast(unsigned, __builtin_convertvector(v, bf16x2_t));
}
DI float lo16(unsigned u) { return __uint_as_float(u << 16); }
DI float hi16(unsigned u) { return __uint_as_float(u & 0xffff0000u); }

DI s16x4 tr_read(unsigned a) { return __builtin_amdgcn_ds_read_tr16_b64_v4i16((AS3 s16x4*)(size_t)a); }
DI bf16x8 lds_read128(unsigned a) { return *(AS3 const bf16x8*)(size_t)a; }
template <class T> DI void lds_write(unsigned a, T v) { *(AS3 T*)(size_t)a = v; }
template <class T> DI T lds_read(unsigned a) { return *(AS3 const T*)(size_t)a; }
DI bf16x8 cat8(s16x4 a, s16x4 b) { return __builtin_shufflevector(a, b, 0, 1, 2, 3, 4, 5, 6, 7); }
DI f32x4 mfma16(bf16x8 a, bf16x8 b, f32x4 c) { return __builtin_amdgcn_mfma_f32_16x16x32_bf16(a, b, c, 0, 0, 0); }
DI bf16x8 pack44(const f32x4& a, const f32x4& b) {
    u32x4 r;
    r[0] = pack2(a[0], a[1]);
    r[1] = pack2(a[2], a[3]);
    r[2] = pack2(b[0], b[1]);
    r[3] = pack2(b[2], b[3]);
    return __builtin_bit_cast(bf16x8, r);
}
DI unsigned lds_of(const void* p) { return (unsigned)(size_t)(AS3 const char*)p; }

template <int CTRL>
DI unsigned dpp(unsigned x) { return __builtin_amdgcn_update_dpp(0u, x, CTRL, 0xf, 0xf, true); }
template <int CTRL>
DI float dpp_add(float x) { return x + __uint_as_float(dpp<CTRL>(__float_as_uint(x))); }
DI float swap32_add(float a, float b) {
    auto r = __builtin_amdgcn_permlane32_swap(__float_as_uint(a), __float_as_uint(b), false, false);
    return __uint_as_float(r[0]) + __uint_as_float(r[1]);
}
DI float swap16_add(float a, float b) {
    auto r = __builtin_amdgcn_permlane16_swap(__float_as_uint(a), __float_as_uint(b), false, false);
    return __uint_as_float(r[0]) + __uint_as_float(r[1]);
}
DI unsigned wave_umax(unsigned x) {
    x = max(x, dpp<0xB1>(x));
    x = max(x, dpp<0x4E>(x));
    x = max(x, dpp<0x141>(x));
    x = max(x, dpp<0x140>(x));
    auto r = __builtin_amdgcn_permlane16_swap(x, x, false, false);
    x = max(r[0], r[1]);
    auto s = __builtin_amdgcn_permlane32_swap(x, x, false, false);
    return max(s[0], s[1]);
}
constexpr unsigned WS_E = 0;
constexpr unsigned WS_RG = 256;
constexpr unsigned WS_GG = WS_RG + 2048;
constexpr unsigned WS_Z = 65536;
constexpr unsigned WS_KF = WS_Z + 131072;
constexpr unsigned WS_GU = WS_KF + 262144;
constexpr unsigned WS_Y = WS_GU + 262144;
constexpr unsigned WS_KV = WS_Y + 262144;
constexpr unsigned WS_O = WS_KV + (2u << 20);
constexpr unsigned WS_CL = WS_O + (4u << 20);
constexpr unsigned WS_END = WS_CL + 65536;

constexpr int NQ = 9, ND = 7, NX = 16, KD = 76, VD = 82, NF = 64, NF1 = 58, NF2 = 58, NP1 = 12, NP = 14, NM1 = 21, NM2 = 21, NM = 22, KFN = 100;
constexpr int J_S[48] = {0, 0, 0, 1, 1, 1, 2, 2, 2, 3, 3, 3, 4, 4, 4, 5, 5, 5, 6, 6, 6, 7, 7, 7, 8, 8, 8, 9, 9, 10, 10, 11, 11, 12, 12, 13, 14, 15, 13, 14, 15, 9, 10, 11, 12, 13, 14, 15};
constexpr int J_M[48] = {0, 1, 2, 0, 1, 2, 0, 1, 2, 0, 1, 2, 0, 1, 2, 0, 1, 2, 0, 1, 2, 0, 1, 2, 0, 1, 2, 0, 1, 0, 1, 0, 1, 0, 1, 0, 0, 0, 1, 1, 1, 2, 2, 2, 2, 2, 2, 2};
constexpr int J_X[48] = {0, 0, 0, 1, 1, 1, 2, 2, 2, 3, 3, 3, 4, 4, 4, 5, 5, 5, 6, 6, 6, 7, 7, 7, 8, 8, 8, 9, 9, 10, 10, 11, 11, 12, 12, 13, 14, 15, 13, 14, 15, 9, 10, 11, 12, 13, 14, 15};
constexpr int J_U[48] = {5, 6, 2, 0, 1, 15, 13, 11, 14, 9, 10, 4, 6, 2, 3, 1, 15, 16, 11, 14, 12, 10, 4, 8, 2, 3, 18, 15, 16, 11, 14, 9, 10, 5, 6, 18, 15, 13, 11, 14, 12, 7, 9, 10, 4, 8, 5, 6};
constexpr int J_B[48] = {0, 0, 0, 0, 0, 9, 9, 9, 9, 9, 9, 1, 1, 1, 1, 1, 10, 10, 10, 10, 10, 10, 2, 2, 2, 2, 11, 11, 11, 11, 11, 5, 5, 5, 5, 11, 11, 11, 11, 11, 11, 4, 4, 4, 4, 4, 4, 4};
constexpr int Q_C[96] = {1, 0, 2, 0, 3, 0, 4, 0, 5, 0, 6, 0, 7, 0, 0, 0, 0, 0, 8, 0, 0, 0, 0, 0, 9, 0, 0, 0, 0, 0, 10, 0, 0, 0, 0, 0, 11, 0, 0, 0, 0, 0, 12, 0, 0, 0, 0, 0, 13, 0, 0, 0, 0, 0, 14, 0, 0, 0, 0, 0, 15, 0, 0, 0, 0, 0, 16, 0, 0, 0, 0, 0, 17, 0, 0, 0, 18, 0, 19, 0, 20, 0, 21, 0, 22, 0, 0, 0, 0, 0, 0, 0, 0, 0, 0, 0};
constexpr int X_U0[16] = {4, 3, 16, 12, 8, 18, 17, 7, 5, 0, 17, 12, 4, 2, 0, 16};
constexpr int X_U1[16] = {8, 18, 17, 7, 5, 0, 13, 9, 6, 1, 13, 7, 8, 3, 1, 17};
constexpr int P_U[4] = {0, 1, 2, 3};
constexpr int W_U[16] = {15, 13, 11, 14, 12, 2, 3, 0, 1, 7, 9, 10, 4, 8, 5, 6};
constexpr int W_N[16] = {100, 100, 100, 100, 100, 101, 101, 101, 101, 13, 13, 13, 14, 14, 99, 99};
constexpr int F_J[64] = {-1, 0, 1, 2, -2, 3, 4, 5, -3, 6, 7, 8, -4, 9, 10, 11, -5, 12, 13, 14, -6, 15, 16, 17, -7, 18, 19, 20, -8, 21, 22, 23, -9, 24, 25, 26, -10, 27, 28, -11, 29, 30, -12, 31, 32, -13, 33, 34, -14, 35, -15, 36, -16, 37, 38, 39, 40, 41, 42, 43, 44, 45, 46, 47};
constexpr int F_N[64] = {0, 0, 0, 0, 0, 0, 0, 0, 0, 0, 0, 0, 0, 0, 0, 1, 1, 1, 1, 1, 2, 2, 2, 2, 3, 3, 3, 3, 4, 4, 4, 4, 5, 5, 5, 5, 6, 6, 6, 6, 7, 7, 7, 7, 8, 8, 8, 8, 9, 9, 9, 10, 10, 10, 11, 11, 11, 11, 12, 12, 12, 12, 12, 12};
constexpr int F_K[64] = {0, 1, 2, 3, 0, 4, 5, 6, 0, 0, 0, 7, 0, 0, 0, 8, 0, 0, 0, 9, 0, 0, 0, 10, 0, 0, 0, 11, 0, 0, 0, 12, 0, 0, 0, 13, 0, 0, 0, 0, 14, 0, 0, 0, 15, 0, 0, 0, 0, 16, 0, 0, 0, 17, 18, 19, 20, 21, 0, 0, 0, 0, 0, 22};
constexpr int E_A[384] = {300, 325, 350, 375, 0, 0, 0, 502, 0, 0, 0, 403, 0, 0, 0, 503, 201, 251, 0, 404, 0, 0, 0, 504, 301, 326, 351, 376, 0, 0, 0, 505, 601, 0, 0, 406, 0, 100, 101, 506, 102, 103, 202, 407, 252, 0, 0, 507, 302, 327, 352, 377, 0, 0, 0, 602, 0, 0, 0, 0, 0, 0, 0, 203, 253, 0, 0, 408, 0, 0, 0, 508, 303, 328, 353, 378, 0, 0, 0, 603, 0, 0, 0, 0, 0, 100, 101, 102, 103, 204, 254, 409, 0, 0, 0, 509, 304, 329, 354, 379, 0, 0, 0, 604, 0, 0, 0, 0, 0, 0, 0, 205, 255, 0, 0, 410, 0, 0, 0, 510, 305, 330, 355, 380, 0, 100, 101, 605, 102, 103, 0, 0, 0, 0, 0, 206, 256, 0, 0, 411, 0, 0, 0, 511, 306, 331, 356, 381, 0, 0, 0, 606, 0, 0, 0, 0, 0, 0, 0, 207, 257, 0, 0, 412, 0, 0, 0, 512, 307, 332, 357, 382, 0, 100, 101, 607, 102, 103, 0, 0, 0, 0, 0, 208, 258, 0, 0, 413, 0, 0, 0, 513, 308, 333, 358, 383, 0, 0, 0, 608, 0, 0, 0, 0, 0, 100, 101, 102, 103, 209, 259, 414, 0, 0, 0, 514, 309, 334, 359, 384, 0, 0, 0, 609, 210, 260, 0, 0, 0, 0, 0, 0, 310, 335, 360, 385, 0, 0, 0, 515, 610, 211, 261, 0, 0, 100, 101, 102, 311, 336, 361, 386, 0, 0, 0, 611, 212, 262, 0, 416, 0, 0, 0, 516, 312, 337, 362, 387, 0, 0, 0, 213, 263, 0, 0, 0, 0, 100, 101, 102, 313, 338, 363, 388, 214, 264, 0, 517, 314, 339, 364, 389, 215, 265, 0, 0, 315, 340, 365, 390, 0, 0, 0, 518, 0, 0, 0, 419, 0, 0, 0, 519, 0, 0, 0, 420, 0, 0, 0, 520, 0, 0, 0, 0, 0, 100, 101, 102, 103, 0, 0, 422, 0, 0, 0, 522, 0, 0, 0, 0, 0, 0, 0, 0, 0, 0, 0, 0, 0, 0, 0, 0, 0, 0, 0, 613, 0, 0, 0, 0, 0, 0, 0, 0, 0, 0, 0, 0, 0, 0, 0, 614, 0, 0, 0, 0, 0, 0, 0, 0, 0, 0, 0, 0};
constexpr int E_B[384] = {0, 0, 402, 0, 0, 0, 0, 0, 0, 0, 0, 0, 0, 0, 0, 0, 0, 0, 0, 0, 0, 0, 0, 0, 0, 0, 405, 0, 0, 0, 0, 0, 0, 0, 0, 0, 0, 0, 0, 0, 0, 0, 0, 0, 0, 0, 0, 0, 0, 0, 0, 0, 0, 0, 0, 0, 0, 0, 0, 0, 0, 0, 0, 0, 0, 0, 0, 0, 0, 0, 0, 0, 0, 0, 0, 0, 0, 0, 0, 0, 0, 0, 0, 0, 0, 0, 0, 0, 0, 0, 0, 0, 0, 0, 0, 0, 0, 0, 0, 0, 0, 0, 0, 0, 0, 0, 0, 0, 0, 0, 0, 0, 0, 0, 0, 0, 0, 0, 0, 0, 0, 0, 0, 0, 0, 0, 0, 0, 0, 0, 0, 0, 0, 0, 0, 0, 0, 0, 0, 0, 0, 0, 0, 0, 0, 0, 0, 0, 0, 0, 0, 0, 0, 0, 0, 0, 0, 0, 0, 0, 0, 0, 0, 0, 0, 0, 0, 0, 0, 0, 0, 0, 0, 0, 0, 0, 0, 0, 0, 0, 0, 0, 0, 0, 0, 0, 0, 0, 0, 0, 0, 0, 0, 0, 0, 0, 0, 0, 0, 0, 0, 0, 0, 0, 0, 0, 0, 0, 0, 0, 0, 0, 0, 0, 0, 0, 0, 0, 0, 0, 0, 0, 0, 0, 0, 0, 0, 0, 0, 0, 0, 0, 0, 0, 415, 0, 0, 0, 0, 0, 0, 0, 0, 0, 0, 0, 0, 0, 103, 0, 0, 0, 0, 0, 0, 0, 0, 0, 0, 0, 0, 0, 0, 0, 0, 0, 0, 0, 0, 0, 0, 0, 0, 0, 0, 0, 0, 0, 0, 0, 103, 0, 417, 0, 0, 0, 0, 612, 0, 0, 0, 0, 0, 0, 0, 0, 0, 0, 418, 0, 0, 0, 0, 0, 0, 0, 0, 0, 0, 0, 0, 0, 0, 0, 0, 0, 0, 0, 0, 0, 0, 0, 0, 0, 0, 0, 0, 0, 0, 0, 0, 0, 0, 0, 0, 0, 0, 0, 0, 0, 0, 0, 0, 0, 0, 0, 0, 0, 0, 0, 0, 0, 0, 0, 0, 0, 0, 0, 0, 0, 0, 0, 0, 0, 0, 0, 0, 0, 0, 0, 0, 0, 0, 0, 0, 0, 0, 0, 0, 0, 0, 0, 0, 0};
constexpr unsigned L_A = 16 * 8192;
constexpr unsigned L_KF = 18 * 8192;
constexpr unsigned L_SM = 19 * 8192;
constexpr unsigned L_FULL = L_SM + 256;
constexpr unsigned L_PROG = L_SM + 896;
constexpr unsigned L_PRL = L_SM + 336;
constexpr unsigned L_FW2 = L_SM + 512, L_FOS = L_SM + 480, L_FK0 = L_SM + 752, L_FV = L_SM + 560, L_FP = L_SM + 576, L_FKS = L_SM + 592;
constexpr unsigned L_FQD = L_SM + 608, L_FG = L_SM + 624, L_FKF = L_SM + 736, L_FQ = L_SM + 720, L_FWD = L_SM + 640, L_FWO = L_SM + 656;
constexpr unsigned L_FLD = L_SM + 688, L_BEST = L_SM + 696;
constexpr unsigned L_SEL = L_SM + 704;
constexpr unsigned L_XS = L_SM + 2048, L_KZ = L_SM + 3072, L_WPHI = L_SM + 4096;
static_assert(NP <= 31 && NM <= 63 && NF1 == NF2 && NX == 16, "");
static_assert(P_U[1] == P_U[0] + 1 && P_U[3] == P_U[2] + 1, "feature maps of a head lie side by side");
constexpr bool j_first(int j) {
    for (int i = 0; i < j; ++i)
        if (J_M[i] == J_M[j]) return false;
    return true;
}
constexpr unsigned SPIN_CAP = 1u << 16;
#define N8(a) a, a + 1, a + 2, a + 3, a + 4, a + 5, a + 6, a + 7
__device__ const unsigned k_num[64] = {N8(0), N8(8), N8(16), N8(24), N8(32), N8(40), N8(48), N8(56)};

constexpr int PD = 4;
constexpr int RLD = F_N[NF1 - 1];
constexpr int CLR0 = 2;
constexpr int GQ1 = 6;
constexpr int WOE = 4, WON = 10;
constexpr int GDL = 20, KFD = 4, PDL = 6;
constexpr unsigned CPF = 55552;
constexpr u64 KXB = 0xFEDBA9C768534210ull;
#define BAR() asm volatile("s_waitcnt lgkmcnt(0)\n\ts_barrier" ::: "memory")
template <int N>
DI void waitv() { asm volatile("s_waitcnt vmcnt(%0)" ::"n"(N) : "memory"); }

DI unsigned nz1(unsigned v) { return max(v, 1u); }
DI u32x4 nz4(u32x4 v) { return (u32x4){max(v[0], 1u), max(v[1], 1u), max(v[2], 1u), max(v[3], 1u)}; }
DI u32x4 nz4(f32x4 v) { return nz4(__builtin_bit_cast(u32x4, v)); }
DI unsigned min4(u32x4 v) { return min(min(v[0], v[1]), min(v[2], v[3])); }
DI void st_wt(__amdgpu_buffer_rsrc_t r, unsigned off, u32x4 v) { __builtin_amdgcn_raw_buffer_store_b128(v, r, off, 0, 16); }
DI void ld_async(u32x4& v, unsigned off, const char* base) {
    asm volatile("global_load_dwordx4 %0, %1, %2 sc1" : "=&v"(v) : "v"(off), "s"(base) : "memory");
}
#define ST_MASK(op, m, off, v, base) asm volatile("s_mov_b64 exec, %3\n\t" op " %0, %1, %2 sc1\n\ts_mov_b64 exec, -1\n\ts_nop 0" ::"v"(off), "v"(v), "s"(base), "s"((u64)(m)) : "memory")
DI void pin(u32x4& v) { asm volatile("" : "+v"(v)); }
template <int N, class OFF, class BASE>
DI void settle(u32x4 (&v)[N], OFF off, BASE base) {
#pragma unroll
    for (int i = 0; i < N; ++i) pin(v[i]);
#pragma unroll 1
    for (unsigned s = 0; s < SPIN_CAP; ++s) {
        unsigned m = min4(v[0]);
#pragma unroll
        for (int i = 1; i < N; ++i) m = min(m, min4(v[i]));
        if (__ballot(m == 0u) == 0ull) break;
#pragma unroll
        for (int i = 0; i < N; ++i) {
            u64 keep;
            asm volatile("s_mov_b64 %1, exec\n\ts_mov_b64 exec, %4\n\tglobal_load_dwordx4 %0, %2, %3 sc1\n\ts_mov_b64 exec, %1"
                         : "+&v"(v[i]), "=&s"(keep) : "v"(off(i)), "s"(base(i)), "s"((u64)__ballot(min4(v[i]) == 0u)) : "memory");
        }
        waitv<0>();
#pragma unroll
        for (int i = 0; i < N; ++i) pin(v[i]);
    }
}
DI void lds_wait(unsigned a, unsigned n) {
    unsigned v, c;
    asm volatile("s_mov_b32 %1, 0x400000\n"
                 ".Lp%=:\n\tds_read_b32 %0, %2\n\ts_waitcnt lgkmcnt(0)\n\tv_cmp_gt_u32 vcc, %3, %0\n\ts_cbranch_vccz .Lf%=\n\t"
                 "s_sleep 1\n\ts_sub_u32 %1, %1, 1\n\ts_cbranch_scc0 .Lp%=\n.Lf%=:"
                 : "=&v"(v), "=&s"(c) : "v"(a), "s"(n) : "memory", "vcc", "scc");
}
DI void lds_wait0(unsigned a) {
    unsigned v, c;
    asm volatile("s_mov_b32 %1, 0x1000000\n"
                 ".Lp%=:\n\tds_read_b32 %0, %2\n\ts_sub_u32 %1, %1, 1\n\ts_cbranch_scc1 .Lf%=\n\ts_waitcnt lgkmcnt(0)\n\tv_cmp_eq_u32 vcc, 0, %0\n\ts_cbranch_vccnz .Lp%=\n"
                 ".Lf%=:\n\ts_waitcnt lgkmcnt(0)"
                 : "=&v"(v), "=&s"(c) : "v"(a) : "memory", "vcc", "scc");
}
DI void lds_wait8(unsigned a, unsigned n) {
    unsigned v, c;
    asm volatile("s_mov_b32 %1, 0x100000\n"
                 ".Lp%=:\n\tds_read_b32 %0, %2\n\ts_waitcnt lgkmcnt(0)\n\tv_cmp_gt_u32 vcc, %3, %0\n\ts_cbranch_vccz .Lf%=\n\t"
                 "s_sleep 4\n\ts_sub_u32 %1, %1, 1\n\ts_cbranch_scc0 .Lp%=\n.Lf%=:"
                 : "=&v"(v), "=&s"(c) : "v"(a), "s"(n) : "memory", "vcc", "scc");
}
DI void lds_peek(unsigned& v, unsigned a) { asm volatile("ds_read_b32 %0, %1" : "=v"(v) : "v"(a) : "memory"); }
DI void lds_wait(unsigned& v, unsigned a, unsigned n) {
    unsigned c;
    asm volatile("s_mov_b32 %1, 0x400000\n"
                 ".Lp%=:\n\ts_waitcnt lgkmcnt(0)\n\tv_cmp_gt_u32 vcc, %3, %0\n\ts_cbranch_vccz .Lf%=\n\t"
                 "ds_read_b32 %0, %2\n\ts_sub_u32 %1, %1, 1\n\ts_cbranch_scc0 .Lp%=\n.Lf%=:"
                 : "+v"(v), "=&s"(c) : "v"(a), "s"(n) : "memory", "vcc", "scc");
}
DI float dot2(unsigned a, unsigned b, float c) {
    return __builtin_amdgcn_fdot2_f32_bf16(__builtin_bit_cast(bf16x2_t, a), __builtin_bit_cast(bf16x2_t, b), c, false);
}
DI float sum4(const float (&a)[4]) {
    float c = swap16_add(swap32_add(a[0], a[1]), swap32_add(a[2], a[3]));
    c = dpp_add<0xB1>(c);
    c = dpp_add<0x4E>(c);
    c = dpp_add<0x141>(c);
    return dpp_add<0x140>(c);
}

template <class OP, class POST>
DI void red4(float& a, float& b, float& c, float& d, OP op, POST post) {
    auto x = __builtin_amdgcn_permlane32_swap(__float_as_uint(a), __float_as_uint(b), false, false);
    auto y = __builtin_amdgcn_permlane32_swap(__float_as_uint(c), __float_as_uint(d), false, false);
    const float m = op(__uint_as_float(x[0]), __uint_as_float(x[1])), n = op(__uint_as_float(y[0]), __uint_as_float(y[1]));
    auto z = __builtin_amdgcn_permlane16_swap(__float_as_uint(m), __float_as_uint(n), false, false);
    const unsigned f = __float_as_uint(post(op(__uint_as_float(z[0]), __uint_as_float(z[1]))));
    auto u = __builtin_amdgcn_permlane16_swap(f, f, false, false);
    auto v = __builtin_amdgcn_permlane32_swap(u[0], u[0], false, false);
    auto t = __builtin_amdgcn_permlane32_swap(u[1], u[1], false, false);
    a = __uint_as_float(v[0]);
    b = __uint_as_float(v[1]);
    c = __uint_as_float(t[0]);
    d = __uint_as_float(t[1]);
}
DI float mx2(float a, float b) { return __builtin_elementwise_maximum(a, b); }
template <class HOOK>
DI void softmax2(const f32x4 (&v)[2][2], u32x4 (&ph)[2][2], HOOK hook) {
    constexpr float L2E = 1.4426950408889634f;
    float mx[2], mn[2], sp_[2], sn_[2];
#pragma unroll
    for (int b = 0; b < 2; ++b) {
        const f32x4 s = v[b][0], t = v[b][1];
        mx[b] = mx2(mx2(mx2(mx2(s[0], s[1]), s[2]), mx2(mx2(s[3], t[0]), t[1])), mx2(t[2], t[3]));
        mn[b] = mx2(mx2(mx2(mx2(-s[0], -s[1]), -s[2]), mx2(mx2(-s[3], -t[0]), -t[1])), mx2(-t[2], -t[3]));
    }
    red4(mx[0], mx[1], mn[0], mn[1], [](float a, float b) { return mx2(a, b); }, [](float a) { return a * -L2E; });
    f32x2 ep[2][4], en[2][4];
#pragma unroll
    for (int b = 0; b < 2; ++b) {
#pragma unroll
        for (int e = 0; e < 4; ++e) {
            const f32x2 u = {v[b][e >> 1][2 * (e & 1)], v[b][e >> 1][2 * (e & 1) + 1]};
            const f32x2 a = __builtin_elementwise_fma(u, (f32x2){L2E, L2E}, (f32x2){mx[b], mx[b]});
            const f32x2 c = __builtin_elementwise_fma(u, (f32x2){-L2E, -L2E}, (f32x2){mn[b], mn[b]});
            ep[b][e] = (f32x2){__builtin_amdgcn_exp2f(a[0]), __builtin_amdgcn_exp2f(a[1])};
            en[b][e] = (f32x2){__builtin_amdgcn_exp2f(c[0]), __builtin_amdgcn_exp2f(c[1])};
        }
        const f32x2 s = (ep[b][0] + ep[b][1]) + (ep[b][2] + ep[b][3]), t = (en[b][0] + en[b][1]) + (en[b][2] + en[b][3]);
        sp_[b] = s[0] + s[1];
        sn_[b] = t[0] + t[1];
    }
    asm volatile("" : "+v"(sp_[0]), "+v"(sn_[0]), "+v"(sp_[1]), "+v"(sn_[1]));
    hook();
    red4(sp_[0], sp_[1], sn_[0], sn_[1], [](float a, float b) { return a + b; }, [](float a) { return __builtin_amdgcn_rcpf(a); });
#pragma unroll
    for (int b = 0; b < 2; ++b)
#pragma unroll
        for (int e = 0; e < 4; ++e) {
            const f32x2 a = ep[b][e] * (f32x2){sp_[b], sp_[b]}, c = en[b][e] * (f32x2){sn_[b], sn_[b]};
            ph[b][0][e] = pack2(a[0], a[1]);
            ph[b][1][e] = pack2(c[0], c[1]);
        }
}
DI f32x4 rbf4(const f32x4& a) {
    const unsigned u = pack2(a[0], a[1]), v = pack2(a[2], a[3]);
    return (f32x4){lo16(u), hi16(u), lo16(v), hi16(v)};
}
DI int fsw(int row) { return (((row >> 1) & 3) << 2) | (((row >> 3) & 1) << 1) | (row & 1); }
template <class HOOK>
DI void phi_k(const f32x4 (&pp)[2][2], const unsigned (&pw)[4], unsigned off, HOOK hook) {
    u32x4 ph[2][2];
    softmax2(pp, ph, hook);
#pragma unroll
    for (int b = 0; b < 2; ++b)
#pragma unroll
        for (int kf = 0; kf < 2; ++kf)
#pragma unroll
            for (int fb = 0; fb < 2; ++fb) {
                const u32x2 a = {ph[b][kf][2 * fb], ph[b][kf][2 * fb + 1]};
                lds_write<u32x2>(pw[2 * kf + fb] + off + b * 2048, a);
            }
}

#define SB0() __builtin_amdgcn_sched_barrier(0)
template <int A, int B, class F>
DI void sfor(F f) {
    if constexpr (A < B) {
        f(std::integral_constant<int, A>{});
        sfor<A + 1, B>(f);
    }
}
template <int N> using IC = std::integral_constant<int, N>;
#define AI __attribute__((always_inline))

DI void o_store(const u32x4 (&kf)[6], const u32x4 (&ph)[2][2], __amdgpu_buffer_rsrc_t rsrc, unsigned o_st) {
    const f32x4 zero = {0.f, 0.f, 0.f, 0.f};
    auto b8 = [](const u32x4& a) { return __builtin_bit_cast(bf16x8, a); };
    f32x4 dn[2], nm[2][2];
#pragma unroll
    for (int i = 0; i < 2; ++i) dn[i] = mfma16(b8(kf[4]), b8(ph[i][0]), zero);
#pragma unroll
    for (int i = 0; i < 2; ++i) dn[i] = mfma16(b8(kf[5]), b8(ph[i][1]), dn[i]);
    SB0();
#pragma unroll
    for (int i = 0; i < 4; ++i) nm[i & 1][i >> 1] = mfma16(b8(kf[i >> 1]), b8(ph[i & 1][0]), zero);
    SB0();
    float rd_[2];
#pragma unroll
    for (int i = 0; i < 4; ++i) nm[i >> 1][i & 1] = mfma16(b8(kf[2 + (i & 1)]), b8(ph[i >> 1][1]), nm[i >> 1][i & 1]);
#pragma unroll
    for (int i = 0; i < 2; ++i) rd_[i] = __builtin_amdgcn_rcpf(rbf(rbf(dn[i][0]) + 1e-6f));
    SB0();
#pragma unroll
    for (int i = 0; i < 2; ++i) {
        u32x4 o = __builtin_bit_cast(u32x4, pack44(nm[i][0], nm[i][1]));
        f32x2 t[4];
        SB0();
#pragma unroll
        for (int j = 0; j < 4; ++j) t[j] = (f32x2){lo16(o[j]), hi16(o[j])};
        SB0();
#pragma unroll
        for (int j = 0; j < 4; ++j) t[j] *= (f32x2){rd_[i], rd_[i]};
        SB0();
#pragma unroll
        for (int j = 0; j < 4; ++j) o[j] = pack2(t[j][0], t[j][1]);
        SB0();
        st_wt(rsrc, o_st + i * 1024, nz4(o));
        SB0();
    }
}


typedef __attribute__((ext_vector_type(16))) unsigned u32x16;
__global__ __launch_bounds__(512, 2) void fused_mix(const u16*, const u16*, const u16*, const u16*, const u16*, const u16*, const u16*, const u16*,
                                                    const u16*, char*, u16*) {
    __shared__ __attribute__((aligned(16))) char smem[163840];
    u32x4 ka;
    u32x16 kb_;
    u64 kz;
    asm volatile("s_load_dwordx4 %0, %3, 0x0\n\ts_load_dwordx16 %1, %3, 0x10\n\ts_load_dwordx2 %2, %3, 0x50"
                 : "=&s"(ka), "=&s"(kb_), "=&s"(kz) : "s"(__builtin_amdgcn_kernarg_segment_ptr()));
    SB0();
    const int tid = threadIdx.x;
    int l = tid & 63;
    const int w = __builtin_amdgcn_readfirstlane(tid >> 6) ^ 4;
    int bid = blockIdx.x;
    const unsigned lds0 = lds_of(smem);
    int g = l >> 4, l15 = l & 15, q = l15 >> 2, p = l & 3;
    int srow = l >> 3, sch = l & 7;
    unsigned l16 = l * 16;
    const int rg = w & 3, kp = w >> 2;
    const int hs = rg >> 1;
    {
        u64 pc;
        asm volatile("s_getpc_b64 %0\n\ts_mov_b32 m0, %2\n\ts_mov_b64 exec, 3\n\tglobal_load_lds_dword %1, %0\n\ts_mov_b64 exec, -1"
                     : "=&s"(pc) : "v"(min((unsigned)(((bid & ~7) + w) * 2 + l) * 128u, CPF)), "s"(lds0 + L_SM + 3584) : "memory");
    }
    lds_write<unsigned>(lds0 + L_SM + tid * 4, 0u);
    const int slot = bid >> 3;
    const int mt = (bid & 1) * 8 + (slot & 7);
    const int hp = ((bid >> 1) & 3) * 4 + (slot >> 3);
    unsigned vx[4], vw[4];
    auto vw_init = [&]() AI {
        int t = l;
        asm volatile("" : "+v"(t));
#pragma unroll
        for (int i = 0; i < 4; ++i) vw[i] = (unsigned)((8 * i + (t >> 3)) * 6144 + ((t & 7) ^ (((t >> 4) & 1) << 1) ^ ((i & 1) << 2)) * 16 + 4096 - i * 1024);
    };
    auto vx_init = [&]() AI {
        int t = l;
        asm volatile("" : "+v"(t));
#pragma unroll
        for (int i = 0; i < 4; ++i) vx[i] = (unsigned)((8 * i + (t >> 3)) * 2048 + (((t & 7) ^ ((4 * (i & 1) + (t >> 4)) & 7)) * 16) + 4096 - i * 1024);
    };
    vx_init();
    vw_init();
    asm volatile("" : "+v"(vx[0]), "+v"(vx[1]), "+v"(vx[2]), "+v"(vx[3]), "+v"(vw[0]), "+v"(vw[1]), "+v"(vw[2]), "+v"(vw[3]));
    unsigned xo0 = (mt * 128 + hs * 64) * 2048, wo0 = hp * 128 + hs * (32 * 6144);
    asm volatile("s_waitcnt lgkmcnt(0)" : "+s"(ka), "+s"(kb_), "+s"(kz), "+s"(xo0), "+s"(wo0) :: "memory");
    const u16* x = (const u16*)((u64)ka[1] << 32 | ka[0]);
    const u16* wqkv = (const u16*)((u64)ka[3] << 32 | ka[2]);
    auto arg = [&](int i) { return (char*)((u64)kb_[2 * i + 1] << 32 | kb_[2 * i]); };
    const u16 *wphi = (const u16*)arg(0), *wo = (const u16*)arg(1), *wr = (const u16*)arg(2), *wg = (const u16*)arg(3);
    const u16 *wu = (const u16*)arg(4), *wd = (const u16*)arg(5), *gw = (const u16*)arg(6);
    char* ws = arg(7);
    u16* out = (u16*)kz;
    const int sc = (3 - (int)((unsigned)(size_t)wqkv >> 7)) & 7;
    const unsigned* one = k_num;
    unsigned v0 = 0;
    asm volatile("" : "+s"(one), "+v"(v0));
    auto mark = [&](unsigned dst, int n = 1, int lanes = 1) AI {
        asm volatile("s_mov_b32 m0, %2\n\ts_mov_b64 exec, %4\n\tglobal_load_lds_dword %0, %1 offset:%3\n\ts_mov_b64 exec, -1"
                     ::"v"(v0), "s"(one), "s"(dst - 4 * n), "n"(4 * n), "n"(lanes) : "memory");
    };
    auto four = [&](const unsigned (&v)[4], const char* src, unsigned dst) AI {
        asm volatile("s_mov_b32 m0, %5\n\ts_nop 0\n\t"
                     "global_load_lds_dwordx4 %0, %4\n\tglobal_load_lds_dwordx4 %1, %4 offset:1024\n\t"
                     "global_load_lds_dwordx4 %2, %4 offset:2048\n\tglobal_load_lds_dwordx4 %3, %4 offset:3072"
                     ::"v"(v[0]), "v"(v[1]), "v"(v[2]), "v"(v[3]), "s"(src), "s"(dst) : "memory");
    };
    const int ks0 = (((slot + (slot >> 3)) & 3) | ((hp & 7) == sc ? slot & 4 : 0)) ^ ((bid & 1) * 8);
    const char* xb0 = (const char*)x + xo0 - 4096;
    const char* wb0 = (const char*)wqkv + wo0 - 4096;
    auto fill = [&](auto N) AI {
        constexpr int n = N, j = F_J[n] < 0 ? 0 : F_J[n], xi = F_J[n] < 0 ? -1 - F_J[n] : 0, s = F_J[n] < 0 ? xi : J_S[j], m = J_M[j];
        const int ks = ks0 ^ (int)((KXB >> (4 * s)) & 15);
        if constexpr (F_J[n] >= 0) {
            four(vw, wb0 + ks * (64 * 6144) + (m == 2 ? 0 : 2048 * (m + 1)), lds0 + J_U[j] * 8192 + hs * 4096);
        } else {
            const char* xs_ = xb0 + ks * 128;
            const unsigned d = lds0 + (hs ? X_U1[xi] : X_U0[xi]) * 8192;
            four(vx, xs_, d);
            four(vx, xs_ + 32 * 2048, d + 4096);
        }
        if constexpr (F_K[n] > 1) mark(lds0 + L_FULL + (n < NF1 ? 0 : 32) + hs * 8, F_K[n], 3);
    };
    static_assert(F_K[1] == 1, "");
    if (__builtin_expect((w & 5) == 4, 1)) sfor<0, 2>(fill);
    unsigned sd = 0, st;
    asm volatile("s_cmp_lg_u32 %2, 5\n\ts_cbranch_scc1 .La%=\n\t"
                 "s_min_u32 %1, %3, 23\n\ts_lshl_b32 %1, %1, 18\n\ts_load_dword %0, %5, %1\n\ts_load_dword %0, %6, 0x0\n\t"
                 "s_and_b32 %1, %1, 0x3c0000\n\ts_load_dword %0, %4, %1\n\ts_load_dword %0, %10, %1\n\t"
                 "s_load_dword %0, %7, 0x0\n\ts_load_dword %0, %8, 0x0\n\ts_load_dword %0, %9, 0x0\n.La%=:"
                 : "+s"(sd), "=&s"(st) : "s"(w), "s"(slot), "s"(x), "s"(wqkv), "s"(one), "s"(wr), "s"(gw), "s"(wphi), "s"(out) : "memory", "scc");
    asm volatile("s_cmp_lg_u32 %2, 7\n\ts_cbranch_scc1 .La%=\n\t"
                 "s_min_u32 %1, %3, 27\n\ts_lshl_b32 %1, %1, 18\n\ts_load_dword %0, %4, 0x0\n\ts_load_dword %0, %4, %1\n\t"
                 "s_load_dword %0, %5, %1\n\ts_load_dword %0, %6, %1\n\ts_load_dword %0, %7, %1\n\t"
                 "s_and_b32 %1, %1, 0x1c0000\n\ts_load_dword %0, %8, %1\n.La%=:\n\ts_barrier"
                 : "+s"(sd), "=&s"(st) : "s"(w), "s"(slot), "s"(ws), "s"(wg), "s"(wu), "s"(wd), "s"(wo) : "memory", "scc");
    if (__builtin_expect((w & 5) == 4, 1)) {
        mark(lds0 + L_FULL + hs * 8, 1, 3);
        sfor<2, 4>(fill);
    }
    asm volatile("" : "+s"(bid));
    const int xcd = bid & 7;
    const int rt = (xcd >> 1) * 4 + ((bid >> 3) & 3), cb = (xcd & 1) * 8 + (bid >> 5), rot = 8 * rt;
    const __amdgpu_buffer_rsrc_t rsrc = __builtin_amdgcn_make_buffer_rsrc(ws, 0, WS_END, 0x00020000);
    const u32x4 z4 = {0u, 0u, 0u, 0u};
    const f32x4 zero = {0.f, 0.f, 0.f, 0.f};
    unsigned AX = lds0 + L_A + rg * 4096 + l16;
    unsigned W4 = lds0 + p * 4;
    unsigned PG = lds0 + L_PROG + p * 256;
    const unsigned WR = lds0 + rg * 4;
    auto flag = [&](unsigned a) AI { asm volatile("ds_write_b32 %0, %0" ::"v"(a) : "memory"); };
    auto phi_w = [&](bf16x8 (&a)[2], int h) AI {
        const unsigned ad = lds0 + L_WPHI + h * 2048 + (4 * g + q) * 64 + (((p >> 1) ^ (2 * (g & 1))) * 16) + 8 * (p & 1);
        a[0] = cat8(tr_read(ad), tr_read(ad + 1024));
        a[1] = cat8(tr_read(ad ^ 32), tr_read((ad ^ 32) + 1024));
    };
    u32x4 fw[32], fd[16], gu[2];
    unsigned sel_[4][2];
    u32x4 ph[2][2], sq;
    const int hp2 = ((bid >> 1) & 3) * 4 + (bid >> 6);
    const bool ffn = (hp2 & 7) != sc;
    const int ph4 = ((bid >> 3) + (bid >> 6)) & 3;
    const int kq = mt >> 2;
    const int cs = (ffn ? hp2 - (hp2 > sc) - (hp2 > sc + 8) : 14 + (hp2 >> 3)) * 4 + ((ph4 + 3 - (int)((unsigned)(size_t)wg >> 7)) & 3);
    auto spread = [&](const u32x4& v, int i) -> bf16x8 {
        const unsigned s = v[i >> 1];
        u32x4 a;
#pragma unroll
        for (int d = 0; d < 4; ++d) a[d] = __builtin_amdgcn_perm(s, s, sel_[d][i & 1]);
        return __builtin_bit_cast(bf16x8, a);
    };
    auto fold_store = [&](const f32x4& a, unsigned off) {
        f32x4 v;
#pragma unroll
        for (int i = 0; i < 4; ++i) {
            const unsigned u = __float_as_uint(a[i]);
            const float r = __uint_as_float(__builtin_amdgcn_update_dpp(u, u, 0x128, 0xc, 0xf, false));
            v[i] = swap32_add(r, r);
        }
        const u32x4 o = nz4(v);
        ST_MASK("global_store_dwordx4", 0x00FF00FFull, off + l15 * 32 + g * 16, o, ws);
    };
    auto sel_init = [&]() AI {
#pragma unroll
        for (int d = 0; d < 4; ++d)
#pragma unroll
            for (int hf = 0; hf < 2; ++hf) {
                const unsigned b = hf ? 0x0302u : 0x0100u;
                sel_[d][hf] = (sch >> 1) != d ? 0x0c0c0c0cu : (l & 1) ? (b << 16) | 0x0c0cu : 0x0c0c0000u | b;
            }
    };
    auto gua = [&](int i) { return ws + WS_GU + cs * 4096 + i * 1024; };
    auto gu_load = [&]() AI {
#pragma unroll
        for (int i = 0; i < 2; ++i) ld_async(gu[i], l16, gua(i));
    };
    auto down = [&]() AI {
#pragma unroll
        for (int i = 0; i < 16; ++i) pin(fd[i]);
        settle(gu, [&](int) { return l16; }, gua);
        const f32x4 v = __builtin_bit_cast(f32x4, gu[0]) + __builtin_bit_cast(f32x4, gu[1]);
        auto r = __builtin_amdgcn_permlane16_swap(__float_as_uint(swap32_add(v[0], v[2])), __float_as_uint(swap32_add(v[1], v[3])), false, false);
        const float gt = rbf(__uint_as_float(r[0])), up = rbf(__uint_as_float(r[1]));
        const unsigned ha = lds0 + L_XS + hs * 512;
        lds_write<u16>(ha + l15 * 8 + g * 2, f2bf(rbf(gt / (1.f + __expf(-gt))) * up));
        const u32x4 hv = lds_read<u32x4>(ha + srow * 16);
        f32x4 y0 = zero, y1 = zero;
#pragma unroll
        for (int i = 0; i < 8; ++i) {
            const bf16x8 a = spread(hv, i);
            y0 = mfma16(a, __builtin_bit_cast(bf16x8, fd[i]), y0);
            y1 = mfma16(a, __builtin_bit_cast(bf16x8, fd[8 + i]), y1);
        }
        fold_store(y0, WS_Y + cs * 4096 + kq * 1024 + hs * 512);
        fold_store(y1, WS_Y + cs * 4096 + kq * 1024 + hs * 512 + 256);
    };
    auto need = [&](int c) AI {
        if (c == 101) lds_wait(W4 + L_FKS, 1);
        else if (c == 100) lds_wait(W4 + L_FV, 1);
        else if (c == 99) lds_wait(W4 + L_FQD, 1);
        else if (c > 0) lds_wait(c > RLD ? W4 + L_PRL : PG, c);
    };
    auto wo_load = [&](int n0, int n1, bool mk) AI {
        unsigned vo[4];
#pragma unroll
        for (int i = 0; i < 4; ++i) vo[i] = (unsigned)((8 * i + srow) * 2048 + ((sch ^ ((g & 3) << 1)) * 16) + 4096 - i * 1024);
        const char* wob = (const char*)wo + cb * 128 - 4096;
#pragma unroll
        for (int n = n0; n < n1; ++n) {
            if (n < WON && (n == 0 || W_N[n] != W_N[n - 1])) need(W_N[n]);
            four(vo, wob + ((2 * n + hs + rot) & 31) * 65536, lds0 + W_U[n] * 8192 + hs * 4096);
        }
        if (mk) mark(lds0 + (n0 >= WON ? L_FW2 : L_FWD) + hs * 4);
    };
    auto phi_adr = [&](unsigned (&pw)[4], unsigned base) AI {
#pragma unroll
        for (int i = 0; i < 4; ++i) pw[i] = (base + rg * 4096 + l15 * 128 + ((g ^ fsw(l15)) << 3)) ^ (i << 5);
    };
    const int row0 = rt * 128 + rg * 32 + kp * 16;
    u32x4 xr[2];
    u64 gg[2];
    unsigned xo = srow * 2048 + sch * 16;
    auto g_load = [&]() AI {
        asm volatile("global_load_dwordx2 %0, %2, %3 sc1\n\tglobal_load_dwordx2 %1, %2, %3 offset:64 sc1"
                     : "=&v"(gg[0]), "=&v"(gg[1]) : "v"((unsigned)srow * 8), "s"(ws + WS_GG + row0 * 8) : "memory");
    };
    auto xg_load = [&](int skip) AI {
        asm volatile("s_cmp_eq_u32 %4, 4\n\ts_cbranch_scc1 .Lk%=\n\t"
                     "global_load_dwordx4 %0, %5, %6\n\tglobal_load_dwordx4 %1, %5, %7\n\t"
                     "global_load_dwordx2 %2, %8, %9 sc1\n\tglobal_load_dwordx2 %3, %8, %9 offset:64 sc1\n.Lk%=:"
                     : "+v"(xr[0]), "+v"(xr[1]), "+v"(gg[0]), "+v"(gg[1])
                     : "s"(skip), "v"(xo), "s"(x + (size_t)row0 * DM + cb * 64), "s"(x + (size_t)(row0 + 8) * DM + cb * 64),
                       "v"((unsigned)srow * 8), "s"(ws + WS_GG + row0 * 8) : "memory", "scc");
    };
    auto xg_def = [&]() AI { asm volatile("" : "=v"(xr[0]), "=v"(xr[1]), "=v"(gg[0]), "=v"(gg[1])); };
    auto relane = [&]() AI {
        unsigned z = 0;
        asm volatile("" : "+v"(z));
        l = __builtin_amdgcn_mbcnt_hi(~0u, __builtin_amdgcn_mbcnt_lo(~0u, z));
        g = l >> 4, l15 = l & 15, q = l15 >> 2, p = l & 3, srow = l >> 3, sch = l & 7, l16 = l * 16;
        AX = lds0 + L_A + rg * 4096 + l16, W4 = lds0 + p * 4, PG = lds0 + L_PROG + p * 256, xo = srow * 2048 + sch * 16;
    };
    u32x4 kf[6];
    auto fd_def = [&]() AI {
#pragma unroll
        for (int i = 0; i < 16; ++i) asm volatile("" : "=v"(fd[i]));
        asm volatile("" : "=v"(gu[0]), "=v"(gu[1]));
    };
    auto kf_def = [&]() AI { asm volatile("" : "=v"(kf[0]), "=v"(kf[1]), "=v"(kf[2]), "=v"(kf[3]), "=v"(kf[4]), "=v"(kf[5])); };
    const int oh = hs ^ kp ^ 1;

    if (__builtin_expect(w >= 4, 1)) {
        const bool loader = !(w & 1);
        const unsigned kf_kv = WS_KF + (hp * 2 + (mt >> 3)) * 8192 + (mt & 7) * 512;
        const unsigned kf_z = WS_KF + (hp * 2 + (mt >> 3)) * 8192 + 4096 + (mt & 7) * 128;
        const unsigned fgo = ffn ? (unsigned)((256 * kq + 16 * srow) * 7168 + cs * 128 + sch * 16) : 0u;
        const char* fsrc = (const char*)(hs ? wu : wg);
        auto gu_w = [&](int q0, int q1) AI {
#pragma unroll
            for (int i = q0; i < q1; ++i) {
                unsigned t1, t2, t3;
                asm volatile("v_add_u32 %4, 0x1c00, %7\n\tv_add_u32 %5, 0x3800, %7\n\tv_add_u32 %6, 0x5400, %7\n\t"
                             "global_load_dwordx4 %0, %7, %8 nt\n\tglobal_load_dwordx4 %1, %4, %8 nt\n\t"
                             "global_load_dwordx4 %2, %5, %8 nt\n\tglobal_load_dwordx4 %3, %6, %8 nt"
                             : "=&v"(fw[4 * i]), "=&v"(fw[4 * i + 1]), "=&v"(fw[4 * i + 2]), "=&v"(fw[4 * i + 3]), "=&v"(t1), "=&v"(t2), "=&v"(t3)
                             : "v"(fgo), "s"(fsrc + (128 * (i >> 2) + 4 * (i & 3)) * 7168) : "memory");
            }
        };
        const unsigned fdo = ffn ? (unsigned)((64 * cs + 8 * srow) * 2048 + 512 * kq + 256 * hs + sch * 16) : 0u;
        auto dn_w = [&](int q0, int q1) AI {
#pragma unroll
            for (int i = q0; i < q1; ++i)
                asm volatile("global_load_dwordx4 %0, %4, %5 offset:-4096 nt\n\tglobal_load_dwordx4 %1, %4, %5 offset:-2048 nt\n\t"
                             "global_load_dwordx4 %2, %4, %5 nt\n\tglobal_load_dwordx4 %3, %4, %5 offset:2048 nt"
                             : "=&v"(fd[4 * i]), "=&v"(fd[4 * i + 1]), "=&v"(fd[4 * i + 2]), "=&v"(fd[4 * i + 3])
                             : "v"(fdo), "s"((const char*)wd + 4096 + (i & 1) * 8192 + (i >> 1) * 128) : "memory");
        };
        auto gate_up = [&]() AI {
            sel_init();
#pragma unroll
            for (int i = 0; i < 32; ++i) pin(fw[i]);
            u32x4 xs[4];
#pragma unroll
            for (int i = 0; i < 4; ++i) xs[i] = lds_read<u32x4>(lds0 + L_XS + hs * 512 + (i >> 1) * 256 + srow * 32 + (i & 1) * 16);
            f32x4 a = zero;
#pragma unroll
            for (int i = 0; i < 32; ++i) a = mfma16(spread(xs[i >> 3], i & 7), __builtin_bit_cast(bf16x8, fw[i]), a);
            fold_store(a, WS_GU + cs * 4096 + kq * 512 + hs * 256);
            SB0();
        };
        u64 T;
        u32x4 sv[2];
        auto rg_load = [&]() AI {
            asm volatile("global_load_dwordx4 %0, %2, %3 sc1\n\tglobal_load_dwordx4 %1, %2, %3 offset:16 sc1"
                         : "=&v"(sv[0]), "=&v"(sv[1]) : "v"(l * 32), "s"(ws + WS_RG) : "memory");
        };
        auto select = [&]() AI {
            const unsigned tl = (unsigned)T & 0x1FFFu, thi = (unsigned)(T >> 13);
#pragma unroll 1
            for (unsigned s = 0; s < SPIN_CAP; ++s) {
                pin(sv[0]);
                pin(sv[1]);
                const unsigned bad = ((sv[0][0] >> 19) ^ tl) | (sv[0][1] ^ thi) | ((sv[0][2] >> 19) ^ tl) | (sv[0][3] ^ thi) |
                                     ((sv[1][0] >> 19) ^ tl) | (sv[1][1] ^ thi) | ((sv[1][2] >> 19) ^ tl) | (sv[1][3] ^ thi);
                if (__all(bad == 0u)) break;
                __builtin_amdgcn_s_sleep(16);
                rg_load();
                waitv<0>();
            }
            pin(sv[0]);
            pin(sv[1]);
            unsigned k2 = 0;
#pragma unroll
            for (int j = 0; j < 4; ++j) {
                const unsigned v = sv[j >> 1][(j & 1) * 2] & 0x7FFFFu;
                k2 = max(k2, ((v >> 3) << 11) | ((255 - (4 * l + j)) << 3) | (v & 7));
            }
            const unsigned key = __builtin_amdgcn_readfirstlane(wave_umax(k2));
            asm volatile("s_mov_b32 m0, %2\n\ts_mov_b64 exec, %3\n\tglobal_load_lds_dwordx4 %0, %1\n\ts_mov_b64 exec, -1"
                         ::"v"((unsigned)(512 * kq) + l16), "s"(x + (size_t)(2047 - (int)(key & 2047u)) * DM), "s"(lds0 + L_XS + hs * 512), "s"(0xFFFFFFFFull) : "memory");
            unsigned rb = key >> 11;
            rb ^= (rb & 0x8000u) ? 0x8000u : 0xFFFFu;
            const float rsel = rbf(1.f / (1.f + __expf(-bf2f((u16)rb))));
            lds_write<u32x4>(lds0 + L_SEL, (u32x4){(unsigned)T, (unsigned)(T >> 32), 2047u - (key & 2047u), __float_as_uint(rsel)});
            if (bid == 0 && hs) ST_MASK("global_store_dwordx2", 1ull, 0u, T, ws + WS_E);
        };
        auto k_soft = [&]() AI {
            bf16x8 a0[2], kb[2];
            f32x4 pk[2][2][2];
            unsigned pw[4];
            phi_w(a0, 0);
            phi_adr(pw, lds0 + P_U[0] * 8192);
            asm volatile("" : "+v"(pw[0]), "+v"(pw[1]), "+v"(pw[2]), "+v"(pw[3]));
            auto logits = [&](int h) AI {
                lds_wait(WR + L_FK0 + 4 * h, 1);
#pragma unroll
                for (int i = 0; i < 2; ++i) kb[i] = lds_read128(AX + (P_U[0] * 8192 - L_A) + h * 4096 + i * 1024);
#pragma unroll
                for (int i = 0; i < 4; ++i) pk[h][i >> 1][i & 1] = mfma16(a0[i & 1], kb[i >> 1], zero);
            };
            auto round = [&](int h) AI {
#pragma unroll
                for (int i = 0; i < 4; ++i) pk[h][i >> 1][i & 1] = rbf4(pk[h][i >> 1][i & 1]);
            };
            logits(0);
            round(0);
            SB0();
            phi_k(pk[0], pw, 0, [&]() AI {
                logits(1);
                SB0();
            });
            SB0();
            round(1);
            phi_k(pk[1], pw, 4096, []() {});
            SB0();
            flag(WR + L_FP);
        };
        auto q_soft = [&]() AI {
            bf16x8 a1[2];
            f32x4 pq[2][2];
            phi_w(a1, hs);
            lds_wait0(WR + L_FQ);
#pragma unroll
            for (int rb = 0; rb < 2; ++rb) {
                const bf16x8 b = lds_read128(AX + rb * 1024);
#pragma unroll
                for (int fb = 0; fb < 2; ++fb) pq[rb][fb] = rbf4(mfma16(a1[fb], b, zero));
            }
            softmax2(pq, ph, []() {});
            asm volatile("" : "+v"(ph[0][0]), "+v"(ph[0][1]), "+v"(ph[1][0]), "+v"(ph[1][1]));
        };
        if (__builtin_expect(loader, 1)) {
            vx_init();
            vw_init();
            unsigned pv = 0;
            sfor<4, NF1>([&](auto N) {
                constexpr int n = N;
                if constexpr (F_N[n] > F_N[n - 1]) {
                    __builtin_amdgcn_s_setprio(3);
                    lds_wait(pv, PG, F_N[n]);
                    if (F_N[n] < RLD) lds_peek(pv, PG);
                    __builtin_amdgcn_s_setprio(0);
                }
                if constexpr (n == 12) {
                    asm volatile("s_mov_b32 m0, %2\n\ts_nop 0\n\tglobal_load_lds_dwordx4 %0, %1\n\tglobal_load_lds_dwordx4 %0, %1 offset:1024"
                                 ::"v"((unsigned)((l >> 2) * 64 + (((l & 3) ^ (2 * ((l >> 4) & 1))) * 16))),
                                   "s"(wphi + (hp * 2 + hs) * 1024), "s"(lds0 + L_WPHI + hs * 2048) : "memory");
                }
                fill(N);
            });
            flag(lds0 + L_FLD + hs * 4);
            __builtin_amdgcn_s_setprio(2);
            const char* cla = ws + WS_CL + ((l15 * 2 + (mt >> 3)) * 16 + hp) * 128;
            u64 cl;
            asm volatile("global_load_dwordx2 %0, %1, off sc1" : "=&v"(cl) : "v"(cla) : "memory");
            k_soft();
            lds_wait(W4 + L_FP, 1);
            sq = lds_read<u32x4>(lds0 + L_SEL);
            waitv<0>();
            asm volatile("" : "+v"(cl));
            {
                u32x4 kp_[6];
                const char* kb = ws + WS_KV + (hp * 8 + (mt >> 1)) * 16384 + (mt & 1) * 512 + hs * 256;
                const bool zw = !((mt & 3) | hs);
                const char* zb = zw ? ws + WS_Z + (hp * 4 + (mt >> 2)) * 2048 : kb;
                const unsigned kvo = g * 1024 + l15 * 16, zo = zw ? l16 : 0u;
                auto ko = [&](int i) { return i < 4 ? kvo : zo; };
                auto ka = [&](int i) { return i < 4 ? kb + i * 4096 : zb + (i - 4) * 1024; };
                const u64 T = (u64)sq[1] << 32 | sq[0];
#pragma unroll 1
                for (unsigned s = 0; s < SPIN_CAP && __ballot(cl != T); ++s)
                    asm volatile("global_load_dwordx2 %0, %1, off sc1\n\ts_waitcnt vmcnt(0)" : "=&v"(cl) : "v"(cla) : "memory");
                u32x4 a01;
#pragma unroll
                for (int i = 0; i < 4; ++i) a01[i] = (l15 >> 1) != i ? 0u : (l & 1) ? 0x3f800000u : 0x3f80u;
                unsigned po = kf_kv + hs * 256 + l15 * 16 + (g & 1) * 8;
                pin(a01);
                asm volatile("" : "+v"(po));
                lds_wait0(W4 + L_FKS);
                __builtin_amdgcn_s_sleep(GDL);
#pragma unroll
                for (int i = 0; i < 6; ++i) ld_async(kp_[i], ko(i), ka(i));
                xg_def();
                xg_load(w + 1);
                SB0();
                waitv<0>();
                settle(kp_, ko, ka);
                f32x4 d = zero;
#pragma unroll
                for (int i = 0; i < 4; ++i) d = mfma16(__builtin_bit_cast(bf16x8, a01), __builtin_bit_cast(bf16x8, kp_[i]), d);
                const u32x2 o = {nz1(pack2(d[0], d[1])), nz1(pack2(d[2], d[3]))};
                ST_MASK("global_store_dwordx2", 0xFFFFFFFFull, po, o, ws);
                if (zw) {
                    f32x4 z = __builtin_bit_cast(f32x4, kp_[4]) + __builtin_bit_cast(f32x4, kp_[5]);
#pragma unroll
                    for (int i = 0; i < 4; ++i) {
                        float t = dpp_add<0x128>(z[i]);
                        t = swap16_add(t, t);
                        z[i] = swap32_add(t, t);
                    }
                    const u32x2 oz = {nz1(pack2(z[0], z[1])), nz1(pack2(z[2], z[3]))};
                    ST_MASK("global_store_dwordx2", 0xFFFFull, kf_z + (l & 8) * 8 + p * 16 + (sch >> 2) * 8, oz, ws);
                }
                flag(lds0 + L_FG + hs * 4);
            }
            const char* kfb = ws + WS_KF + (hp * 2 + hs) * 8192;
            auto fo = [&](int i) { return i < 4 ? l16 : (unsigned)g * 16; };
            auto fb = [&](int i) { return i < 4 ? kfb + i * 1024 : kfb + 4096 + (i - 4) * 512; };
            need(KFN);
            unsigned sa0 = lds0 + L_KF + hs * 4096 + l16, sa1 = lds0 + L_KZ + hs * 128 + g * 16, sf = lds0 + L_FKF + hs * 4;
            asm volatile("" : "+v"(sa0), "+v"(sa1), "+v"(sf));
            q_soft();
            SB0();
            __builtin_amdgcn_s_sleep(KFD);
#pragma unroll
            for (int i = 0; i < 6; ++i) ld_async(kf[i], fo(i), fb(i));
            SB0();
            waitv<0>();
            settle(kf, fo, fb);
            asm volatile("" : "+v"(xr[0]), "+v"(xr[1]), "+v"(gg[0]), "+v"(gg[1]));
#pragma unroll
            for (int i = 0; i < 4; ++i) lds_write<u32x4>(sa0 + i * 1024, kf[i]);
#pragma unroll
            for (int i = 0; i < 2; ++i) lds_write<u32x4>(sa1 + i * 64, kf[4 + i]);
            SB0();
            flag(sf);
            fd_def();
        } else {
            const int row0 = (mt * 16 + hp) * 8 + 4 * hs;
            __builtin_amdgcn_s_setprio(2);
            asm volatile("s_waitcnt lgkmcnt(0)" : "+s"(sd));
            u32x4 rv[2], gv[2], xv[4][2];
            u64 ev;
            asm volatile("global_load_dwordx2 %4, %5, %9 sc1\n\t"
                         "global_store_dwordx4 %10, %11, %9 sc1\n\t"
                         "global_store_dwordx2 %12, %13, %9 sc1\n\t"
                         "global_load_dwordx4 %0, %6, %7\n\tglobal_load_dwordx4 %1, %6, %7 offset:1024\n\t"
                         "global_load_dwordx4 %2, %6, %8\n\tglobal_load_dwordx4 %3, %6, %8 offset:1024"
                         : "=&v"(rv[0]), "=&v"(rv[1]), "=&v"(gv[0]), "=&v"(gv[1]), "=&v"(ev)
                         : "v"(0u), "v"(l16), "s"(wr), "s"(gw), "s"(ws),
                           "v"(l < 32 ? WS_Y + cs * 4096 + kq * 1024 + hs * 512 + l16 : WS_GU + cs * 4096 + kq * 512 + hs * 256 + l15 * 16), "v"(z4),
                           "v"(hs ? kf_z + l15 * 8 : kf_kv + l * 8), "v"(0ull)
                         : "memory");
#pragma unroll
            for (int r = 0; r < 2; ++r)
                asm volatile("global_load_dwordx4 %0, %4, %5\n\tglobal_load_dwordx4 %1, %4, %5 offset:1024\n\t"
                             "global_load_dwordx4 %2, %4, %5 offset:2048\n\tglobal_load_dwordx4 %3, %4, %5 offset:3072"
                             : "=&v"(xv[2 * r][0]), "=&v"(xv[2 * r][1]), "=&v"(xv[2 * r + 1][0]), "=&v"(xv[2 * r + 1][1])
                             : "v"(l16), "s"(x + (size_t)(row0 + 2 * r) * DM) : "memory");
            SB0();
            waitv<0>();
            asm volatile("" : "+v"(rv[0]), "+v"(rv[1]), "+v"(gv[0]), "+v"(gv[1]), "+v"(ev));
#pragma unroll
            for (int r = 0; r < 4; ++r) asm volatile("" : "+v"(xv[r][0]), "+v"(xv[r][1]));
            T = ((u64)__builtin_amdgcn_readfirstlane((unsigned)(ev >> 32)) << 32 | __builtin_amdgcn_readfirstlane((unsigned)ev)) + 1;
            float ra_[4] = {0.f, 0.f, 0.f, 0.f}, ga_[4] = {0.f, 0.f, 0.f, 0.f};
#pragma unroll
            for (int r = 0; r < 4; ++r)
#pragma unroll
                for (int i = 0; i < 2; ++i)
#pragma unroll
                    for (int j = 0; j < 4; ++j) {
                        ra_[r] = dot2(xv[r][i][j], rv[i][j], ra_[r]);
                        ga_[r] = dot2(xv[r][i][j], gv[i][j], ga_[r]);
                    }
            const int rr = (g >> 1) | ((g & 1) << 1);
            unsigned k = f2bf(sum4(ra_));
            k ^= (k & 0x8000u) ? 0xFFFFu : 0x8000u;
            k = (k << 3) | (7 - 4 * hs - rr);
            auto r1 = __builtin_amdgcn_permlane16_swap(k, k, false, false);
            k = max(r1[0], r1[1]);
            auto r2 = __builtin_amdgcn_permlane32_swap(k, k, false, false);
            k = max(r2[0], r2[1]);
            u64 gq = (T << 16) | f2bf(1.f / (1.f + __expf(-rbf(sum4(ga_)))));
            if (hs) lds_write<unsigned>(lds0 + L_BEST, k | 0x80000000u);
            asm volatile("" : "+v"(gq), "+v"(k));
            __builtin_amdgcn_s_setprio(0);
            lds_wait8(PG, CLR0 + ph4);
            if (!hs) {
                lds_wait(lds0 + L_BEST, 1);
                const u64 rq = (T << 19) | max(k, lds_read<unsigned>(lds0 + L_BEST) & 0x7FFFFu);
                ST_MASK("global_store_dwordx2", 1ull, 0u, rq, ws + WS_RG + (mt * 16 + hp) * 8);
            }
            ST_MASK("global_store_dwordx2", 0x0001000100010001ull, (unsigned)(rr * 8), gq, ws + WS_GG + row0 * 8);
#pragma unroll
            for (int n = 0; n < 8; ++n) st_wt(rsrc, WS_O + ((mt * 2 + (n >> 2)) * 32 + 2 * hp + hs) * 4096 + (n & 3) * 1024 + l16, z4);
#pragma unroll
            for (int n = 0; n < 4; ++n) st_wt(rsrc, WS_KV + ((hp * 8 + hs * 4 + n) * 16 + mt) * 1024 + l16, z4);
            ST_MASK("global_store_dwordx4", 0xFFFFull, WS_Z + ((hp * 4 + hs * 2 + (srow & 1)) * 16 + mt) * 128 + sch * 16, z4, ws);
            waitv<0>();
            ST_MASK("global_store_dwordx2", 0xFFFFull, (unsigned)l * 8, T, ws + WS_CL + ((mt * 2 + hs) * 16 + hp) * 128);
            lds_wait8(lds0 + L_FLD + (l & 1) * 4, 1);
            rg_load();
            gu_w(0, GQ1);
            waitv<4 * GQ1>();
            select();
            flag(WR + L_FP);
            vx_init();
            vw_init();
            sfor<NF1, NF>([&](auto N) {
                constexpr int n = N;
                if constexpr (n == NF1 || F_N[n] != F_N[n - 1]) need(F_N[n]);
                fill(N);
            });
            gu_w(GQ1, 8);
            dn_w(0, 4);
            waitv<16>();
            __builtin_amdgcn_s_setprio(3);
            gate_up();
            __builtin_amdgcn_s_setprio(0);
            lds_wait(W4 + L_FKS, 1);
            wo_load(0, WOE, false);
            lds_wait(lds0 + L_FG + (l & 1) * 4, 1);
            gu_load();
            wo_load(WOE, WON, true);
            __builtin_amdgcn_s_setprio(2);
            q_soft();
            waitv<4 * (WON - WOE) + 1>();
#pragma unroll
            for (int i = 0; i < 16; ++i) pin(fd[i]);
            pin(gu[0]);
            pin(gu[1]);
            sq = lds_read<u32x4>(lds0 + L_SEL);
            xg_def();
            kf_def();
        }
    } else {
        __builtin_amdgcn_s_setprio(1);
        unsigned ra[4], xt[2];
#pragma unroll
        for (int u = 0; u < 4; ++u) ra[u] = lds0 + J_B[0] * 8192 + (8 * g + q) * 128 + ((u ^ (q >> 1) ^ ((g & 1) << 1)) * 32) + 8 * p;
        unsigned xa = lds0 + (32 * (w & 1) + l15) * 128 + ((g ^ (l15 >> 1)) * 16), fa = lds0 + L_FULL + (l & 3) * 4;
        xt[0] = xa + (hs ? X_U1[0] : X_U0[0]) * 8192;
        xt[1] = xt[0] ^ 64;
        asm volatile("s_mov_b32 m0, %4" : "+v"(xa), "+v"(fa), "+v"(xt[0]), "+v"(xt[1]) : "s"(lds0 + L_PROG + w * 256 - 124));
        f32x4 acc[3][4][2];
        bf16x8 xf[ND][2][2];
        s16x4 wh[PD + 1][2];
        unsigned fv = 0;
        bf16x8 kown[2];
        u64 bad = ~0ull;
        auto chk = [&](int c) AI {
            SB0();
            if (__builtin_expect(bad != 0ull, 0)) {
                unsigned c_;
                asm volatile("s_mov_b32 %1, 0x400000\n"
                             ".Lp%=:\n\tds_read_b32 %0, %2 offset:%3\n\ts_waitcnt lgkmcnt(0)\n\tv_cmp_gt_u32 vcc, %4, %0\n\ts_cbranch_vccz .Lf%=\n\t"
                             "s_sub_u32 %1, %1, 1\n\ts_cbranch_scc0 .Lp%=\n.Lf%=:"
                             : "=&v"(fv), "=&s"(c_) : "v"(fa), "n"(c > NM1 ? 32 : 0), "n"(c) : "memory", "vcc", "scc");
            }
            SB0();
        };
        auto kv_part = [&]() AI {
            bf16x8 ones;
#pragma unroll
            for (int j = 0; j < 8; ++j) ones[j] = (short)0x3f80;
            unsigned pa = lds0 + (hs ? P_U[2] : P_U[0]) * 8192 + (4 * g + q) * 128 + (((8 * (rg & 1) + p) ^ fsw(4 * g + q)) << 3);
            unsigned pb = pa ^ 32, va = lds0 + L_A + hs * 2048 + l16, kv_st = WS_KV + ((hp * 8 + rg * 2) * 16 + mt) * 1024 + l16;
            unsigned kv_s1 = kv_st + 16384, z_st = WS_Z + ((hp * 4 + rg) * 16 + mt) * 128 + (l & 1) * 64 + g * 16, fk = WR + L_FKS;
            asm volatile("" : "+v"(pa), "+v"(pb), "+v"(va), "+v"(kv_st), "+v"(kv_s1), "+v"(z_st), "+v"(fk), "+v"(ones));
            bf16x8 b0[4], b1[4], af[2][4];
            lds_wait0(lds0 + L_FV + sch * 4);
#pragma unroll
            for (int i = 0; i < 4; ++i) {
                af[0][i] = cat8(tr_read(pa + i * 4096), tr_read(pa + i * 4096 + 2048));
                b0[i] = lds_read128(va + i * 4096);
                SB0();
                af[1][i] = cat8(tr_read(pb + i * 4096), tr_read(pb + i * 4096 + 2048));
                SB0();
            }
#pragma unroll
            for (int i = 0; i < 4; ++i) b1[i] = lds_read128(va + i * 4096 + 1024);
            SB0();
            f32x4 k0[2] = {zero, zero}, k1[2] = {zero, zero}, zt[2] = {zero, zero};
#pragma unroll
            for (int i = 0; i < 4; ++i)
#pragma unroll
                for (int e = 0; e < 2; ++e) {
                    k0[e] = mfma16(af[e][i], b0[i], k0[e]);
                    zt[e] = mfma16(af[e][i], ones, zt[e]);
                }
            SB0();
#pragma unroll
            for (int i = 0; i < 4; ++i)
#pragma unroll
                for (int e = 0; e < 2; ++e) k1[e] = mfma16(af[e][i], b1[i], k1[e]);
            st_wt(rsrc, kv_st, nz4(__builtin_bit_cast(u32x4, pack44(k0[0], k0[1]))));
            const u32x4 zo = nz4((l & 1) ? zt[1] : zt[0]);
            ST_MASK("global_store_dwordx4", 0x0003000300030003ull, z_st, zo, ws);
            st_wt(rsrc, kv_s1, nz4(__builtin_bit_cast(u32x4, pack44(k1[0], k1[1]))));
            SB0();
            flag(fk);
        };
        auto report = [&](int r) AI {
            unsigned t, a;
            if (r > RLD) asm volatile("v_mov_b32 %0, %3\n\tv_mov_b32 %1, %2\n\tds_write_b32 %1, %0" : "=&v"(t), "=&v"(a) : "s"(WR + L_PRL), "n"(r) : "memory");
            else asm volatile("global_load_lds_dword %0, %1 offset:%2" ::"v"(v0), "s"(one), "n"(4 * r) : "memory");
        };
        auto rd1 = [&](auto F, auto H) AI {
            constexpr int f = F, h = H, n = f >> 2, j = f >> 3, c = h ? E_B[f] : E_A[f], a = c % 100;
            if constexpr (h == 0 && (f & 3) == 0 && Q_C[n] > 0) chk(Q_C[n]);
            wh[f % (PD + 1)][h] = tr_read(ra[f & 3] + (J_U[j] - J_B[j]) * 8192 + (n & 1) * 4096 + h * 512);
            SB0();
            if constexpr (c / 100 == 1) {
                constexpr int jn = (f + 3 - a) >> 3;
                asm volatile("v_add_u32 %0, %1, %0" : "+v"(ra[a]) : "n"((J_B[jn] - J_B[jn - 1]) * 8192));
            } else if constexpr (c / 50 == 4) {
                asm volatile("v_add_u32 %0, %1, %2" : "=v"(xt[0]) : "s"((hs ? X_U1[a] : X_U0[a]) * 8192), "v"(xa));
            } else if constexpr (c / 50 == 5) {
                asm volatile("v_xor_b32 %0, 64, %1" : "=v"(xt[1]) : "v"(xt[0]));
            } else if constexpr (c / 100 == 3) {
                xf[a % 25 % ND][a / 25 & 1][a / 50] = lds_read128(xt[a / 50] + (a / 25 & 1) * 2048);
            } else if constexpr (c / 100 == 4) {
                fv = *(volatile AS3 unsigned*)(size_t)(fa + (a > NM1 ? 32 : 0));
            } else if constexpr (c / 100 == 5) {
                bad = __builtin_amdgcn_ballot_w64(fv < (unsigned)a);
                asm volatile("" : "+s"(bad));
            } else if constexpr (c / 100 == 6) {
                report(a);
            }
        };
        sfor<0, PD>([&](auto F) { rd1(F, IC<0>{}); rd1(F, IC<1>{}); });
        constexpr int K0F = 4 * KD, K1F = 4 * KD + 1;
        sfor<0, 96>([&](auto N) {
            sfor<0, 4>([&](auto E) {
                constexpr int n = N, e = E, f = 4 * n + e, j = n >> 1, ks = n & 1, u = e, s = J_X[j], m = J_M[j];
                const bf16x8 wv = cat8(wh[f % (PD + 1)][0], wh[f % (PD + 1)][1]);
                sfor<0, 2>([&](auto RB) {
                    constexpr int rb = RB;
                    const bf16x8 xv = xf[s % ND][rb][ks];
                    if (j_first(j) && ks == 0) asm volatile("v_mfma_f32_16x16x32_bf16 %0, %1, %2, 0" : "=&v"(acc[m][u][rb]) : "v"(m == 1 ? xv : wv), "v"(m == 1 ? wv : xv));
                    else asm volatile("v_mfma_f32_16x16x32_bf16 %0, %1, %2, %0" : "+v"(acc[m][u][rb]) : "v"(m == 1 ? xv : wv), "v"(m == 1 ? wv : xv));
                    SB0();
                    if constexpr (f + PD < 384 && (f + PD < 4 * VD || f >= 4 * VD)) rd1(IC<f + PD>{}, RB);
                    if constexpr (rb == 0 && f == K0F) relane();
                    if constexpr (rb == 0 && (f == K0F || f == K1F)) asm volatile("s_nop 7");
                    SB0();
                    if constexpr (f == K0F) lds_write<bf16x8>(AX + (P_U[0] * 8192 - L_A) + rb * 1024, pack44(acc[0][0][rb], acc[0][1][rb]));
                    if constexpr (f == K1F) {
                        kown[rb] = pack44(acc[0][2][rb], acc[0][3][rb]);
                        asm volatile("" : "+v"(kown[rb]));
                    }
                    SB0();
                });
                if constexpr (f == K0F) flag(WR + L_FK0);
                if constexpr (f == 4 * VD - 1) {
                    bf16x8 ak[2];
                    f32x4 pk[2][2];
                    unsigned pw[4];
                    __builtin_amdgcn_s_setprio(2);
                    phi_w(ak, 1);
                    asm volatile("s_nop 7");
                    SB0();
#pragma unroll
                    for (int i = 0; i < 4; ++i) lds_write<bf16x8>(AX + i * 1024, pack44(acc[1][i][0], acc[1][i][1]));
                    SB0();
#pragma unroll
                    for (int i = 0; i < 4; ++i) pk[i >> 1][i & 1] = rbf4(mfma16(ak[i & 1], kown[i >> 1], zero));
                    phi_adr(pw, lds0 + P_U[2] * 8192);
                    phi_k(pk, pw, 0, []() {});
                    SB0();
                    flag(WR + L_FV);
                    kv_part();
                    __builtin_amdgcn_s_setprio(1);
                    fv = 0;
                    bad = ~0ull;
                    sfor<4 * VD, 4 * VD + PD>([&](auto F) { rd1(F, IC<0>{}); rd1(F, IC<1>{}); });
                }
                if constexpr (f >= 24 && f < 56 && (f & 7) == 7) __builtin_amdgcn_s_sleep(4);
            });
        });
        asm volatile("s_nop 7");
        SB0();
        __builtin_amdgcn_s_setprio(0);
        flag(WR + L_FQD);
        bf16x8 a0[2];
        f32x4 pq[2][2];
        phi_w(a0, hs ^ 1);
        lds_wait(W4 + L_FKS, 1);
#pragma unroll
        for (int rb = 0; rb < 2; ++rb) {
            const bf16x8 b0 = pack44(acc[2][0][rb], acc[2][1][rb]), b1 = pack44(acc[2][2][rb], acc[2][3][rb]);
            lds_write<bf16x8>(AX + rb * 1024, hs ? b1 : b0);
#pragma unroll
            for (int fb = 0; fb < 2; ++fb) pq[rb][fb] = mfma16(a0[fb], hs ? b0 : b1, zero);
        }
        SB0();
        flag(WR + L_FQ);
        sq = lds_read<u32x4>(lds0 + L_SEL);
#pragma unroll
        for (int i = 0; i < 4; ++i) pq[i >> 1][i & 1] = rbf4(pq[i >> 1][i & 1]);
        softmax2(pq, ph, []() {});
        asm volatile("" : "+v"(ph[0][0]), "+v"(ph[0][1]), "+v"(ph[1][0]), "+v"(ph[1][1]));
        xg_def();
        kf_def();
        fd_def();
    }
    relane();
    const char* cla = ws + WS_CL + ((rt * 2 + kp) * 16 + l15) * 128;
    const u64 T = (u64)sq[1] << 32 | sq[0];
    u64 cl;
    xg_load(w & 5);
    unsigned ka0 = lds0 + L_KF + oh * 4096 + l16, ka1 = lds0 + L_KZ + oh * 128 + g * 16;
    unsigned o_st = WS_O + ((mt * 2 + hs) * 32 + 2 * hp + oh) * 4096 + (rg & 1) * 2048 + l16;
    asm volatile("" : "+v"(ka0), "+v"(ka1), "+v"(o_st), "+v"(cla));
    if ((w & 5) != 4) {
        lds_wait0(lds0 + L_FKF + oh * 4);
#pragma unroll
        for (int i = 0; i < 2; ++i) kf[4 + i] = lds_read<u32x4>(ka1 + i * 64);
        SB0();
#pragma unroll
        for (int i = 0; i < 4; ++i) kf[i] = lds_read<u32x4>(ka0 + i * 1024);
    }
    asm volatile("global_load_dwordx2 %0, %1, off sc1" : "=&v"(cl) : "v"(cla) : "memory");
    SB0();
    o_store(kf, ph, rsrc, o_st);
    flag(lds0 + L_FOS + w * 4);
    waitv<3>();
    __builtin_amdgcn_s_setprio(0);
    if ((w & 5) == 4) {
        lds_wait(lds0 + L_FOS + sch * 4, 1);
        wo_load(WON, 16, true);
    }
    if ((w & 5) == 5) {
        sel_init();
        down();
    }
    flag(lds0 + L_FWO + w * 4);
    const u32x2 selv = {sq[2], sq[3]};

    const char* ob = ws + WS_O + (rt * 2 + (rg >> 1)) * 131072 + (rg & 1) * 2048;
    const unsigned ov0 = l16 + kp * 1024, ov1 = l16 + (kp ^ 1) * 1024;
    auto oa = [&](int i) { return ob + ((2 * i + kp + rot) & 31) * 4096; };
    u32x4 of[3][4];
    auto fetch1 = [&](int k, int j) AI { ld_async(of[k % 3][j], (j & 1) ? ov1 : ov0, oa(2 * k + (j >> 1))); };
    auto fix = [&](int k) AI { settle(of[k % 3], [&](int j) { return (j & 1) ? ov1 : ov0; }, [&](int j) { return oa(2 * k + (j >> 1)); }); };
    const bool own = (__builtin_amdgcn_readfirstlane(selv[0]) >> 4) == (unsigned)(rt * 8 + rg * 2 + kp);
    float gv_[2];
    f32x2 gb[2][4];
    auto g_mix = [&]() AI {
#pragma unroll
        for (int j = 0; j < 2; ++j) {
            gv_[j] = bf2f((u16)gg[j]);
            const float og = rbf(1.f - gv_[j]);
#pragma unroll
            for (int i = 0; i < 4; ++i) {
                const unsigned t = pack2(og * lo16(xr[j][i]), og * hi16(xr[j][i]));
                gb[j][i] = (f32x2){lo16(t), hi16(t)};
                asm volatile("" : "+v"(gb[j][i]));
            }
        }
    };
    asm volatile("" : "+v"(xr[0]), "+v"(xr[1]), "+v"(gg[0]), "+v"(gg[1]));
#pragma unroll 1
    for (unsigned s = 0; s < SPIN_CAP && __ballot((gg[0] >> 16) != (T & 0xFFFFFFFFFFFFull) || (gg[1] >> 16) != (T & 0xFFFFFFFFFFFFull)); ++s) {
        g_load();
        waitv<0>();
        asm volatile("" : "+v"(gg[0]), "+v"(gg[1]));
    }
    g_mix();
    if (!kp) {
        __builtin_amdgcn_s_sleep(PDL);
        waitv<2>();
    } else if (w & 1) waitv<4>();
    else waitv<3 + 4 * (16 - WON)>();
    asm volatile("" : "+v"(cl));
#pragma unroll 1
    for (unsigned s = 0; s < SPIN_CAP && __ballot(cl != T); ++s)
        asm volatile("global_load_dwordx2 %0, %1, off sc1\n\ts_waitcnt vmcnt(0)" : "=&v"(cl) : "v"(cla) : "memory");
#pragma unroll
    for (int j = 0; j < 4; ++j) fetch1(0, j);
    f32x4 acc[2][4];
    unsigned wa[2][4];
#pragma unroll
    for (int i = 0; i < 8; ++i) {
        wa[i >> 2][i & 3] = ((lds0 + 4096 * kp + (4 * g + q) * 128 + ((((g & 1) << 1) | (q >> 1)) * 32) + 8 * p) ^ ((i & 3) << 5)) + (i >> 2) * 65536;
        asm volatile("" : "+v"(wa[i >> 2][i & 3]));
    }
    s16x4 wf[2][8][2];
    auto wrd = [&](int k, int i) AI {
        const int u = W_U[2 * k + (i >> 3)];
        wf[k & 1][i >> 1][i & 1] = tr_read(wa[u >> 3][(i >> 1) & 3] + (u & 7) * 8192 + (i & 1) * 2048);
    };
    u32x4 yp[14];
    const unsigned yo = srow * 4096 + cb * 256 + sch * 32;
    auto ya = [&](int i) { return ws + WS_Y + (i >> 1) * 32768 + (i & 1) * 16; };
    lds_wait(lds0 + L_FWD + (l & 1) * 4, 1);
#pragma unroll
    for (int i = 0; i < 16; ++i) wrd(0, i);
#pragma unroll
    for (int j = 0; j < 4; ++j) fetch1(1, j);
    const unsigned ea = lds0 + L_A + l16 + 4096 * w, eb = lds0 + L_A + 4096 * (w ^ 4);
    unsigned er = eb + l16, et = eb + srow * 128 + ((sch ^ srow) * 16), ew[4];
#pragma unroll
    for (int nb = 0; nb < 4; ++nb) ew[nb] = eb + l15 * 128 + (((2 * nb + (g >> 1)) ^ (l & 7)) * 16) + (g & 1) * 8;
    u16 *o0 = out + (size_t)row0 * DM + cb * 64, *o1 = o0 + 8 * DM;
    asm volatile("" : "+v"(er), "+v"(et), "+v"(ew[0]), "+v"(ew[1]), "+v"(ew[2]), "+v"(ew[3]), "+s"(o0), "+s"(o1));
    waitv<4>();
    fix(0);
    sfor<0, 8>([&](auto K) {
        constexpr int k = K;
        unsigned m[4];
        if constexpr (2 * (k + 1) == WON) lds_wait(lds0 + ((l & 8) ? L_FWO + sch * 4 : L_FW2 + (l & 1) * 4), 1);
        if constexpr (k == 7) {
            if (own) {
#pragma unroll
                for (int i = 0; i < 14; ++i) ld_async(yp[i], yo, ya(i));
            }
        }
        SB0();
        sfor<0, 16>([&](auto I) {
            constexpr int i = I, j = k == 7 ? (0x2031 >> (i & 12)) & 3 : i >> 2, nb = i & 3;
            const bf16x8 a = cat8(wf[k & 1][(j >> 1) * 4 + nb][0], wf[k & 1][(j >> 1) * 4 + nb][1]);
            if (k == 0 && j < 2) asm volatile("v_mfma_f32_16x16x32_bf16 %0, %1, %2, 0" : "=&v"(acc[j & 1][nb]) : "v"(a), "v"(of[k % 3][j]));
            else asm volatile("v_mfma_f32_16x16x32_bf16 %0, %1, %2, %0" : "+v"(acc[j & 1][nb]) : "v"(a), "v"(of[k % 3][j]));
            SB0();
            if constexpr (k == 7 && i >= 12) {
                if constexpr (i == 12) lds_write<u32x4>(ea, nz4(acc[1][0]));
                else lds_write<f32x4>(ea + (i - 12) * 1024, acc[1][i - 12]);
            }
            if constexpr (k < 7) wrd(k + 1, i);
            if constexpr (k < 6 && i < 4) fetch1(k + 2, i);
            if constexpr (k < 7 && i == 10) {
                waitv<(k < 6 ? 4 : 0)>();
#pragma unroll
                for (int n = 0; n < 4; ++n) pin(of[(k + 1) % 3][n]);
            }
            if constexpr (k < 7 && i > 10 && i <= 14) {
                m[i - 11] = min4(of[(k + 1) % 3][i - 11]);
                asm volatile("" : "+v"(m[i - 11]));
            }
            SB0();
        });
        if constexpr (k < 7) {
            if (__builtin_expect(__builtin_amdgcn_ballot_w64(min(min(m[0], m[1]), min(m[2], m[3])) == 0u) != 0ull, 0)) fix(k + 1);
        }
        SB0();
    });
    asm volatile("s_nop 7");
    SB0();
    if (own) {
        waitv<0>();
        settle(yp, [&](int) { return yo; }, ya);
        f32x4 yv[2];
#pragma unroll
        for (int h = 0; h < 2; ++h) {
            yv[h] = __builtin_bit_cast(f32x4, yp[h]);
#pragma unroll
            for (int i = 1; i < 7; ++i) yv[h] += __builtin_bit_cast(f32x4, yp[2 * i + h]);
#pragma unroll
            for (int i = 0; i < 4; ++i) {
                float t = dpp_add<0x128>(yv[h][i]);
                t = swap16_add(t, t);
                yv[h][i] = rbf(rbf(swap32_add(t, t)) * __uint_as_float(selv[1]));
            }
        }
#pragma unroll
        for (int j = 0; j < 2; ++j) {
            const bool hit = row0 + 8 * j + srow == (int)selv[0];
#pragma unroll
            for (int i = 0; i < 4; ++i) {
                const unsigned t = pack2(lo16(xr[j][i]) + yv[i >> 1][2 * (i & 1)], hi16(xr[j][i]) + yv[i >> 1][2 * (i & 1) + 1]);
                xr[j][i] = hit ? t : xr[j][i];
            }
        }
        g_mix();
    }
    BAR();
    f32x4 pa[4];
#pragma unroll
    for (int nb = 0; nb < 4; ++nb) pa[nb] = lds_read<f32x4>(er + nb * 1024);
    SB0();
#pragma unroll
    for (int nb = 0; nb < 4; ++nb) acc[0][nb] += pa[nb];
#pragma unroll
    for (int nb = 0; nb < 4; ++nb) asm volatile("" : "+v"(acc[0][nb]));
#pragma unroll
    for (int nb = 0; nb < 4; ++nb) lds_write<u32x2>(ew[nb], (u32x2){pack2(acc[0][nb][0], acc[0][nb][1]), pack2(acc[0][nb][2], acc[0][nb][3])});
    u32x4 av[2];
    f32x2 t[8];
#pragma unroll
    for (int j = 0; j < 2; ++j) av[j] = lds_read<u32x4>(et + j * 1024);
    SB0();
#pragma unroll
    for (int i = 0; i < 8; ++i) t[i] = (f32x2){lo16(av[i >> 2][i & 3]), hi16(av[i >> 2][i & 3])};
    SB0();
#pragma unroll
    for (int i = 0; i < 8; ++i) t[i] *= (f32x2){gv_[i >> 2], gv_[i >> 2]};
    SB0();
#pragma unroll
    for (int i = 0; i < 8; ++i) av[i >> 2][i & 3] = pack2(t[i][0], t[i][1]);
    SB0();
#pragma unroll
    for (int i = 0; i < 8; ++i) t[i] = (f32x2){lo16(av[i >> 2][i & 3]), hi16(av[i >> 2][i & 3])};
    SB0();
#pragma unroll
    for (int i = 0; i < 8; ++i) t[i] += gb[i >> 2][i & 3];
    SB0();
#pragma unroll
    for (int j = 0; j < 2; ++j) {
#pragma unroll
        for (int i = 0; i < 4; ++i) av[j][i] = pack2(t[4 * j + i][0], t[4 * j + i][1]);
        asm volatile("global_store_dwordx4 %0, %1, %2 sc1\n\ts_nop 1" ::"v"(xo), "v"(av[j]), "s"(j ? o1 : o0) : "memory");
    }
}

extern "C" void kernel_launch(void* const* d_in, const int* in_sizes, int n_in, void* d_out, int out_size,
                              void* d_ws, size_t ws_size, hipStream_t stream) {
    fused_mix<<<256, 512, 0, stream>>>((const u16*)d_in[0], (const u16*)d_in[1], (const u16*)d_in[2], (const u16*)d_in[3],
                                       (const u16*)d_in[5], (const u16*)d_in[6], (const u16*)d_in[7], (const u16*)d_in[8],
                                       (const u16*)d_in[9], (char*)d_ws, (u16*)d_out);
}
```

```cpp
#include <hip/hip_runtime.h>
#include <hip/hip_bf16.h>
#include <stdint.h>
#include <type_traits>

typedef __attribute__((ext_vector_type(8))) short bf16x8;
typedef __attribute__((ext_vector_type(4))) short s16x4;
typedef __attribute__((ext_vector_type(4))) float f32x4;
typedef __attribute__((ext_vector_type(2))) float f32x2;
typedef __attribute__((ext_vector_type(2))) unsigned u32x2;
typedef __attribute__((ext_vector_type(4))) unsigned u32x4;
typedef __attribute__((ext_vector_type(2))) __bf16 bf16x2_t;
typedef unsigned short u16;
typedef unsigned long long u64;

#define DI __device__ __forceinline__
#define AS3 __attribute__((address_space(3)))

constexpr int DM = 1024;

DI float bf2f(u16 v) { return __uint_as_float(((unsigned)v) << 16); }
DI u16 f2bf(float f) { __bf16 b = (__bf16)f; return __builtin_bit_cast(u16, b); }
DI float rbf(float f) { return bf2f(f2bf(f)); }
DI unsigned pack2(float lo, float hi) {
    const f32x2 v = {lo, hi};
    return __builtin_bit_cast(unsigned, __builtin_convertvector(v, bf16x2_t));
}
DI float lo16(unsigned u) { return __uint_as_float(u << 16); }
DI float hi16(unsigned u) { return __uint_as_float(u & 0xffff0000u); }

DI s16x4 tr_read(unsigned a) { return __builtin_amdgcn_ds_read_tr16_b64_v4i16((AS3 s16x4*)(size_t)a); }
DI bf16x8 lds_read128(unsigned a) { return *(AS3 const bf16x8*)(size_t)a; }
template <class T> DI void lds_write(unsigned a, T v) { *(AS3 T*)(size_t)a = v; }
template <class T> DI T lds_read(unsigned a) { return *(AS3 const T*)(size_t)a; }
DI bf16x8 cat8(s16x4 a, s16x4 b) { return __builtin_shufflevector(a, b, 0, 1, 2, 3, 4, 5, 6, 7); }
DI f32x4 mfma16(bf16x8 a, bf16x8 b, f32x4 c) { return __builtin_amdgcn_mfma_f32_16x16x32_bf16(a, b, c, 0, 0, 0); }
DI bf16x8 pack44(const f32x4& a, const f32x4& b) {
    u32x4 r;
    r[0] = pack2(a[0], a[1]);
    r[1] = pack2(a[2], a[3]);
    r[2] = pack2(b[0], b[1]);
    r[3] = pack2(b[2], b[3]);
    return __builtin_bit_cast(bf16x8, r);
}
DI unsigned lds_of(const void* p) { return (unsigned)(size_t)(AS3 const char*)p; }

template <int CTRL>
DI unsigned dpp(unsigned x) { return __builtin_amdgcn_update_dpp(0u, x, CTRL, 0xf, 0xf, true); }
template <int CTRL>
DI float dpp_add(float x) { return x + __uint_as_float(dpp<CTRL>(__float_as_uint(x))); }
DI float swap32_add(float a, float b) {
    auto r = __builtin_amdgcn_permlane32_swap(__float_as_uint(a), __float_as_uint(b), false, false);
    return __uint_as_float(r[0]) + __uint_as_float(r[1]);
}
DI float swap16_add(float a, float b) {
    auto r = __builtin_amdgcn_permlane16_swap(__float_as_uint(a), __float_as_uint(b), false, false);
    return __uint_as_float(r[0]) + __uint_as_float(r[1]);
}
DI unsigned wave_umax(unsigned x) {
    x = max(x, dpp<0xB1>(x));
    x = max(x, dpp<0x4E>(x));
    x = max(x, dpp<0x141>(x));
    x = max(x, dpp<0x140>(x));
    auto r = __builtin_amdgcn_permlane16_swap(x, x, false, false);
    x = max(r[0], r[1]);
    auto s = __builtin_amdgcn_permlane32_swap(x, x, false, false);
    return max(s[0], s[1]);
}
constexpr unsigned WS_E = 0;
constexpr unsigned WS_RG = 256;
constexpr unsigned WS_GG = WS_RG + 2048;
constexpr unsigned WS_Z = 65536;
constexpr unsigned WS_KF = WS_Z + 131072;
constexpr unsigned WS_GU = WS_KF + 262144;
constexpr unsigned WS_Y = WS_GU + 262144;
constexpr unsigned WS_KV = WS_Y + 262144;
constexpr unsigned WS_O = WS_KV + (2u << 20);
constexpr unsigned WS_CL = WS_O + (4u << 20);
constexpr unsigned WS_END = WS_CL + 65536;

constexpr int NQ = 9, ND = 7, NX = 16, KD = 76, VD = 82, NF = 64, NF1 = 58, NF2 = 58, NP1 = 14, NP = 16, NM1 = 23, NM2 = 23, NM = 24, KFN = 100;
constexpr int J_S[48] = {0, 0, 0, 1, 1, 1, 2, 2, 2, 3, 3, 3, 4, 4, 4, 5, 5, 5, 6, 6, 6, 7, 7, 7, 8, 8, 8, 9, 9, 10, 10, 11, 11, 12, 12, 13, 14, 15, 13, 14, 15, 9, 10, 11, 12, 13, 14, 15};
constexpr int J_M[48] = {0, 1, 2, 0, 1, 2, 0, 1, 2, 0, 1, 2, 0, 1, 2, 0, 1, 2, 0, 1, 2, 0, 1, 2, 0, 1, 2, 0, 1, 0, 1, 0, 1, 0, 1, 0, 0, 0, 1, 1, 1, 2, 2, 2, 2, 2, 2, 2};
constexpr int J_X[48] = {0, 0, 0, 1, 1, 1, 2, 2, 2, 3, 3, 3, 4, 4, 4, 5, 5, 5, 6, 6, 6, 7, 7, 7, 8, 8, 8, 9, 9, 10, 10, 11, 11, 12, 12, 13, 14, 15, 13, 14, 15, 9, 10, 11, 12, 13, 14, 15};
constexpr int J_U[48] = {2, 6, 5, 0, 1, 15, 17, 14, 12, 4, 10, 9, 6, 5, 3, 18, 15, 16, 13, 12, 11, 7, 9, 8, 2, 3, 18, 15, 16, 13, 12, 10, 7, 6, 5, 18, 15, 14, 13, 12, 11, 4, 10, 7, 9, 8, 6, 5};
constexpr int J_B[48] = {0, 0, 0, 0, 0, 12, 12, 12, 12, 3, 3, 3, 3, 3, 3, 11, 11, 11, 11, 11, 11, 2, 2, 2, 2, 2, 12, 12, 12, 12, 12, 5, 5, 5, 5, 11, 11, 11, 11, 11, 11, 4, 4, 4, 4, 4, 4, 4};
constexpr int Q_C[96] = {1, 0, 2, 0, 3, 0, 4, 0, 5, 0, 6, 0, 7, 0, 0, 0, 0, 0, 8, 0, 0, 0, 9, 0, 0, 0, 0, 0, 10, 0, 0, 0, 0, 0, 0, 0, 11, 0, 0, 0, 0, 0, 12, 0, 0, 0, 0, 0, 13, 0, 0, 0, 0, 0, 14, 0, 0, 0, 15, 0, 16, 0, 0, 0, 0, 0, 17, 0, 0, 0, 0, 0, 18, 0, 19, 0, 20, 0, 21, 0, 22, 0, 23, 0, 24, 0, 0, 0, 0, 0, 0, 0, 0, 0, 0, 0};
constexpr int X_U0[16] = {9, 3, 16, 11, 8, 0, 17, 4, 6, 0, 17, 11, 9, 2, 0, 16};
constexpr int X_U1[16] = {8, 18, 13, 7, 2, 1, 14, 10, 5, 1, 14, 4, 8, 3, 1, 17};
constexpr int P_U[4] = {0, 1, 2, 3};
constexpr int W_U[16] = {15, 13, 14, 12, 11, 2, 3, 0, 1, 7, 4, 10, 9, 8, 6, 5};
constexpr int W_N[16] = {100, 100, 100, 100, 100, 101, 101, 101, 101, 15, 15, 15, 16, 16, 99, 99};
constexpr int F_J[64] = {-1, 0, 1, 2, -2, 3, 4, 5, -3, 6, 7, 8, -4, 9, 10, 11, -5, 12, 13, 14, 15, -6, 16, 17, 18, -7, 19, 20, 21, -8, 22, 23, 24, -9, 25, 26, -10, 27, 28, -11, 29, 30, -12, 31, 32, -13, 33, 34, -14, 35, -15, 36, -16, 37, 38, 39, 40, 41, 42, 43, 44, 45, 46, 47};
constexpr int F_N[64] = {0, 0, 0, 0, 0, 0, 0, 0, 0, 0, 0, 0, 0, 0, 0, 1, 1, 1, 1, 1, 1, 2, 2, 2, 2, 3, 3, 3, 3, 4, 4, 4, 4, 5, 5, 5, 5, 6, 6, 6, 7, 7, 8, 8, 9, 9, 9, 9, 10, 10, 10, 11, 12, 12, 13, 13, 13, 13, 13, 14, 14, 14, 14, 14};
constexpr int F_K[64] = {0, 1, 2, 3, 0, 4, 5, 6, 0, 0, 0, 7, 0, 0, 8, 0, 0, 0, 9, 0, 0, 0, 0, 10, 0, 0, 0, 11, 0, 0, 0, 12, 0, 0, 0, 13, 0, 0, 14, 0, 15, 0, 0, 0, 16, 0, 0, 0, 0, 17, 0, 18, 0, 19, 20, 21, 22, 23, 0, 0, 0, 0, 0, 24};
constexpr int E_A[384] = {300, 325, 350, 375, 0, 0, 0, 502, 0, 0, 0, 403, 0, 0, 0, 503, 201, 251, 0, 404, 0, 0, 0, 504, 301, 326, 351, 376, 0, 0, 0, 505, 601, 0, 0, 406, 0, 100, 101, 506, 102, 103, 202, 407, 252, 0, 0, 507, 302, 327, 352, 377, 0, 0, 0, 602, 0, 0, 0, 0, 0, 0, 0, 203, 253, 0, 0, 408, 0, 100, 101, 508, 303, 328, 353, 378, 0, 0, 0, 603, 0, 0, 0, 409, 0, 0, 0, 509, 204, 254, 0, 0, 0, 0, 0, 0, 304, 329, 354, 379, 0, 0, 0, 604, 0, 0, 0, 410, 0, 0, 0, 510, 205, 255, 0, 0, 0, 100, 101, 102, 305, 330, 355, 380, 0, 0, 0, 0, 0, 0, 0, 605, 0, 0, 0, 206, 256, 0, 0, 411, 0, 0, 0, 511, 306, 331, 356, 381, 0, 0, 0, 606, 0, 0, 0, 0, 0, 0, 0, 207, 257, 0, 0, 412, 607, 100, 101, 512, 307, 332, 357, 382, 0, 0, 0, 608, 0, 0, 0, 0, 0, 0, 0, 208, 258, 0, 0, 413, 0, 0, 0, 513, 308, 333, 358, 383, 0, 0, 0, 609, 0, 0, 0, 0, 0, 100, 101, 102, 103, 209, 259, 414, 0, 0, 0, 514, 309, 334, 359, 384, 0, 0, 0, 610, 210, 260, 0, 415, 0, 0, 0, 515, 310, 335, 360, 385, 0, 0, 0, 516, 612, 211, 261, 0, 0, 100, 101, 102, 311, 336, 361, 386, 0, 0, 0, 212, 262, 0, 0, 417, 613, 0, 0, 517, 312, 337, 362, 387, 0, 0, 0, 213, 263, 0, 0, 0, 0, 100, 101, 102, 313, 338, 363, 388, 214, 264, 0, 518, 314, 339, 364, 389, 215, 265, 0, 519, 315, 340, 365, 390, 0, 0, 0, 520, 0, 0, 0, 421, 0, 0, 0, 521, 0, 0, 0, 422, 0, 0, 0, 522, 0, 0, 0, 0, 0, 100, 101, 102, 103, 0, 0, 424, 0, 0, 0, 524, 0, 0, 0, 0, 0, 0, 0, 0, 0, 0, 0, 0, 0, 0, 0, 0, 0, 0, 0, 615, 0, 0, 0, 0, 0, 0, 0, 0, 0, 0, 0, 0, 0, 0, 0, 616, 0, 0, 0, 0, 0, 0, 0, 0, 0, 0, 0, 0};
constexpr int E_B[384] = {0, 0, 402, 0, 0, 0, 0, 0, 0, 0, 0, 0, 0, 0, 0, 0, 0, 0, 0, 0, 0, 0, 0, 0, 0, 0, 405, 0, 0, 0, 0, 0, 0, 0, 0, 0, 0, 0, 0, 0, 0, 0, 0, 0, 0, 0, 0, 0, 0, 0, 0, 0, 0, 0, 0, 0, 0, 0, 0, 0, 0, 0, 0, 0, 0, 0, 0, 0, 0, 0, 0, 102, 103, 0, 0, 0, 0, 0, 0, 0, 0, 0, 0, 0, 0, 0, 0, 0, 0, 0, 0, 0, 0, 0, 0, 0, 0, 0, 0, 0, 0, 0, 0, 0, 0, 0, 0, 0, 0, 0, 0, 0, 0, 0, 0, 0, 0, 0, 0, 0, 103, 0, 0, 0, 0, 0, 0, 0, 0, 0, 0, 0, 0, 0, 0, 0, 0, 0, 0, 0, 0, 0, 0, 0, 0, 0, 0, 0, 0, 0, 0, 0, 0, 0, 0, 0, 0, 0, 0, 0, 0, 0, 0, 0, 0, 0, 0, 102, 103, 0, 0, 0, 0, 0, 0, 0, 0, 0, 0, 0, 0, 0, 0, 0, 0, 0, 0, 0, 0, 0, 0, 0, 0, 0, 0, 0, 0, 0, 0, 0, 0, 0, 0, 0, 0, 0, 0, 0, 0, 0, 0, 0, 0, 0, 0, 0, 0, 0, 0, 0, 0, 0, 0, 0, 0, 0, 0, 0, 0, 0, 0, 611, 0, 0, 416, 0, 0, 0, 0, 0, 0, 0, 0, 0, 0, 0, 0, 0, 103, 0, 0, 0, 0, 0, 0, 0, 0, 0, 0, 0, 0, 0, 0, 0, 0, 0, 0, 0, 0, 0, 0, 0, 0, 0, 0, 0, 0, 0, 0, 0, 103, 0, 418, 0, 0, 0, 0, 614, 0, 0, 419, 0, 0, 0, 0, 0, 0, 0, 420, 0, 0, 0, 0, 0, 0, 0, 0, 0, 0, 0, 0, 0, 0, 0, 0, 0, 0, 0, 0, 0, 0, 0, 0, 0, 0, 0, 0, 0, 0, 0, 0, 0, 0, 0, 0, 0, 0, 0, 0, 0, 0, 0, 0, 0, 0, 0, 0, 0, 0, 0, 0, 0, 0, 0, 0, 0, 0, 0, 0, 0, 0, 0, 0, 0, 0, 0, 0, 0, 0, 0, 0, 0, 0, 0, 0, 0, 0, 0, 0, 0, 0, 0, 0, 0};
constexpr unsigned L_A = 16 * 8192;
constexpr unsigned L_KF = 18 * 8192;
constexpr unsigned L_SM = 19 * 8192;
constexpr unsigned L_FULL = L_SM + 256;
constexpr unsigned L_PROG = L_SM + 896;
constexpr unsigned L_PRL = L_SM + 336;
constexpr unsigned L_FW2 = L_SM + 512, L_FOS = L_SM + 480, L_FK0 = L_SM + 752, L_FV = L_SM + 560, L_FP = L_SM + 576, L_FKS = L_SM + 592;
constexpr unsigned L_FQD = L_SM + 608, L_FG = L_SM + 624, L_FKF = L_SM + 736, L_FQ = L_SM + 720, L_FWD = L_SM + 640, L_FWO = L_SM + 656;
constexpr unsigned L_FLD = L_SM + 688, L_BEST = L_SM + 696;
constexpr unsigned L_SEL = L_SM + 704;
constexpr unsigned L_XS = L_SM + 2048, L_KZ = L_SM + 3072, L_WPHI = L_SM + 4096;
constexpr unsigned L_G = L_SM + 3328;
static_assert(NP <= 31 && NM <= 63 && NF1 == NF2 && NX == 16, "");
static_assert(P_U[1] == P_U[0] + 1 && P_U[3] == P_U[2] + 1, "feature maps of a head lie side by side");
constexpr bool j_first(int j) {
    for (int i = 0; i < j; ++i)
        if (J_M[i] == J_M[j]) return false;
    return true;
}
constexpr unsigned SPIN_CAP = 1u << 16;
#define N8(a) a, a + 1, a + 2, a + 3, a + 4, a + 5, a + 6, a + 7
__device__ const unsigned k_num[64] = {N8(0), N8(8), N8(16), N8(24), N8(32), N8(40), N8(48), N8(56)};

constexpr int PD = 4;
constexpr int PACE = 16;
constexpr int RLD = F_N[NF1 - 1];
constexpr int CLR0 = 2;
constexpr int GQ1 = 6;
constexpr int WOE = 2, WOM = 7, WON = 10;
constexpr int GDL = 20, KFD = 4, PDL = 9, PD1 = 2;
constexpr unsigned CPF = 55552;
constexpr u64 KXB = 0xFEDBA9C768534210ull;
#define BAR() asm volatile("s_waitcnt lgkmcnt(0)\n\ts_barrier" ::: "memory")
template <int N>
DI void waitv() { asm volatile("s_waitcnt vmcnt(%0)" ::"n"(N) : "memory"); }

DI unsigned nz1(unsigned v) { return max(v, 1u); }
DI u32x4 nz4(u32x4 v) { return (u32x4){max(v[0], 1u), max(v[1], 1u), max(v[2], 1u), max(v[3], 1u)}; }
DI u32x4 nz4(f32x4 v) { return nz4(__builtin_bit_cast(u32x4, v)); }
DI unsigned min4(u32x4 v) { return min(min(v[0], v[1]), min(v[2], v[3])); }
DI void st_wt(__amdgpu_buffer_rsrc_t r, unsigned off, u32x4 v) { __builtin_amdgcn_raw_buffer_store_b128(v, r, off, 0, 16); }
DI void ld_async(u32x4& v, unsigned off, const char* base) {
    asm volatile("global_load_dwordx4 %0, %1, %2 sc1" : "=&v"(v) : "v"(off), "s"(base) : "memory");
}
#define ST_MASK(op, m, off, v, base) asm volatile("s_mov_b64 exec, %3\n\t" op " %0, %1, %2 sc1\n\ts_mov_b64 exec, -1\n\ts_nop 0" ::"v"(off), "v"(v), "s"(base), "s"((u64)(m)) : "memory")
DI void pin(u32x4& v) { asm volatile("" : "+v"(v)); }
template <int N, class OFF, class BASE>
DI void settle(u32x4 (&v)[N], OFF off, BASE base) {
#pragma unroll
    for (int i = 0; i < N; ++i) pin(v[i]);
#pragma unroll 1
    for (unsigned s = 0; s < SPIN_CAP; ++s) {
        unsigned m = min4(v[0]);
#pragma unroll
        for (int i = 1; i < N; ++i) m = min(m, min4(v[i]));
        if (__ballot(m == 0u) == 0ull) break;
#pragma unroll
        for (int i = 0; i < N; ++i) {
            u64 keep;
            asm volatile("s_mov_b64 %1, exec\n\ts_mov_b64 exec, %4\n\tglobal_load_dwordx4 %0, %2, %3 sc1\n\ts_mov_b64 exec, %1"
                         : "+&v"(v[i]), "=&s"(keep) : "v"(off(i)), "s"(base(i)), "s"((u64)__ballot(min4(v[i]) == 0u)) : "memory");
        }
        waitv<0>();
#pragma unroll
        for (int i = 0; i < N; ++i) pin(v[i]);
    }
}
DI void lds_wait(unsigned a, unsigned n) {
    unsigned v, c;
    asm volatile("s_mov_b32 %1, 0x400000\n"
                 ".Lp%=:\n\tds_read_b32 %0, %2\n\ts_waitcnt lgkmcnt(0)\n\tv_cmp_gt_u32 vcc, %3, %0\n\ts_cbranch_vccz .Lf%=\n\t"
                 "s_sleep 1\n\ts_sub_u32 %1, %1, 1\n\ts_cbranch_scc0 .Lp%=\n.Lf%=:"
                 : "=&v"(v), "=&s"(c) : "v"(a), "s"(n) : "memory", "vcc", "scc");
}
DI void lds_wait0(unsigned a) {
    unsigned v, c;
    asm volatile("s_mov_b32 %1, 0x1000000\n"
                 ".Lp%=:\n\tds_read_b32 %0, %2\n\ts_sub_u32 %1, %1, 1\n\ts_cbranch_scc1 .Lf%=\n\ts_waitcnt lgkmcnt(0)\n\tv_cmp_eq_u32 vcc, 0, %0\n\ts_cbranch_vccnz .Lp%=\n"
                 ".Lf%=:\n\ts_waitcnt lgkmcnt(0)"
                 : "=&v"(v), "=&s"(c) : "v"(a) : "memory", "vcc", "scc");
}
DI void lds_wait8(unsigned a, unsigned n) {
    unsigned v, c;
    asm volatile("s_mov_b32 %1, 0x100000\n"
                 ".Lp%=:\n\tds_read_b32 %0, %2\n\ts_waitcnt lgkmcnt(0)\n\tv_cmp_gt_u32 vcc, %3, %0\n\ts_cbranch_vccz .Lf%=\n\t"
                 "s_sleep 4\n\ts_sub_u32 %1, %1, 1\n\ts_cbranch_scc0 .Lp%=\n.Lf%=:"
                 : "=&v"(v), "=&s"(c) : "v"(a), "s"(n) : "memory", "vcc", "scc");
}
DI void lds_peek(unsigned& v, unsigned a) { asm volatile("ds_read_b32 %0, %1" : "=v"(v) : "v"(a) : "memory"); }
DI void lds_wait(unsigned& v, unsigned a, unsigned n) {
    unsigned c;
    asm volatile("s_mov_b32 %1, 0x400000\n"
                 ".Lp%=:\n\ts_waitcnt lgkmcnt(0)\n\tv_cmp_gt_u32 vcc, %3, %0\n\ts_cbranch_vccz .Lf%=\n\t"
                 "ds_read_b32 %0, %2\n\ts_sub_u32 %1, %1, 1\n\ts_cbranch_scc0 .Lp%=\n.Lf%=:"
                 : "+v"(v), "=&s"(c) : "v"(a), "s"(n) : "memory", "vcc", "scc");
}
DI float dot2(unsigned a, unsigned b, float c) {
    return __builtin_amdgcn_fdot2_f32_bf16(__builtin_bit_cast(bf16x2_t, a), __builtin_bit_cast(bf16x2_t, b), c, false);
}
DI float sum4(const float (&a)[4]) {
    float c = swap16_add(swap32_add(a[0], a[1]), swap32_add(a[2], a[3]));
    c = dpp_add<0xB1>(c);
    c = dpp_add<0x4E>(c);
    c = dpp_add<0x141>(c);
    return dpp_add<0x140>(c);
}

template <class OP, class POST>
DI void red4(float& a, float& b, float& c, float& d, OP op, POST post) {
    auto x = __builtin_amdgcn_permlane32_swap(__float_as_uint(a), __float_as_uint(b), false, false);
    auto y = __builtin_amdgcn_permlane32_swap(__float_as_uint(c), __float_as_uint(d), false, false);
    const float m = op(__uint_as_float(x[0]), __uint_as_float(x[1])), n = op(__uint_as_float(y[0]), __uint_as_float(y[1]));
    auto z = __builtin_amdgcn_permlane16_swap(__float_as_uint(m), __float_as_uint(n), false, false);
    const unsigned f = __float_as_uint(post(op(__uint_as_float(z[0]), __uint_as_float(z[1]))));
    auto u = __builtin_amdgcn_permlane16_swap(f, f, false, false);
    auto v = __builtin_amdgcn_permlane32_swap(u[0], u[0], false, false);
    auto t = __builtin_amdgcn_permlane32_swap(u[1], u[1], false, false);
    a = __uint_as_float(v[0]);
    b = __uint_as_float(v[1]);
    c = __uint_as_float(t[0]);
    d = __uint_as_float(t[1]);
}
DI float mx2(float a, float b) { return __builtin_elementwise_maximum(a, b); }
template <class HOOK>
DI void softmax2(const f32x4 (&v)[2][2], u32x4 (&ph)[2][2], HOOK hook) {
    constexpr float L2E = 1.4426950408889634f;
    float mx[2], mn[2], sp_[2], sn_[2];
#pragma unroll
    for (int b = 0; b < 2; ++b) {
        const f32x4 s = v[b][0], t = v[b][1];
        mx[b] = mx2(mx2(mx2(mx2(s[0], s[1]), s[2]), mx2(mx2(s[3], t[0]), t[1])), mx2(t[2], t[3]));
        mn[b] = mx2(mx2(mx2(mx2(-s[0], -s[1]), -s[2]), mx2(mx2(-s[3], -t[0]), -t[1])), mx2(-t[2], -t[3]));
    }
    red4(mx[0], mx[1], mn[0], mn[1], [](float a, float b) { return mx2(a, b); }, [](float a) { return a * -L2E; });
    f32x2 ep[2][4], en[2][4];
#pragma unroll
    for (int b = 0; b < 2; ++b) {
#pragma unroll
        for (int e = 0; e < 4; ++e) {
            const f32x2 u = {v[b][e >> 1][2 * (e & 1)], v[b][e >> 1][2 * (e & 1) + 1]};
            const f32x2 a = __builtin_elementwise_fma(u, (f32x2){L2E, L2E}, (f32x2){mx[b], mx[b]});
            const f32x2 c = __builtin_elementwise_fma(u, (f32x2){-L2E, -L2E}, (f32x2){mn[b], mn[b]});
            ep[b][e] = (f32x2){__builtin_amdgcn_exp2f(a[0]), __builtin_amdgcn_exp2f(a[1])};
            en[b][e] = (f32x2){__builtin_amdgcn_exp2f(c[0]), __builtin_amdgcn_exp2f(c[1])};
        }
        const f32x2 s = (ep[b][0] + ep[b][1]) + (ep[b][2] + ep[b][3]), t = (en[b][0] + en[b][1]) + (en[b][2] + en[b][3]);
        sp_[b] = s[0] + s[1];
        sn_[b] = t[0] + t[1];
    }
    asm volatile("" : "+v"(sp_[0]), "+v"(sn_[0]), "+v"(sp_[1]), "+v"(sn_[1]));
    hook();
    red4(sp_[0], sp_[1], sn_[0], sn_[1], [](float a, float b) { return a + b; }, [](float a) { return __builtin_amdgcn_rcpf(a); });
#pragma unroll
    for (int b = 0; b < 2; ++b)
#pragma unroll
        for (int e = 0; e < 4; ++e) {
            const f32x2 a = ep[b][e] * (f32x2){sp_[b], sp_[b]}, c = en[b][e] * (f32x2){sn_[b], sn_[b]};
            ph[b][0][e] = pack2(a[0], a[1]);
            ph[b][1][e] = pack2(c[0], c[1]);
        }
}
DI f32x4 rbf4(const f32x4& a) {
    const unsigned u = pack2(a[0], a[1]), v = pack2(a[2], a[3]);
    return (f32x4){lo16(u), hi16(u), lo16(v), hi16(v)};
}
DI int fsw(int row) { return (((row >> 1) & 3) << 2) | (((row >> 3) & 1) << 1) | (row & 1); }
template <class HOOK>
DI void phi_k(const f32x4 (&pp)[2][2], const unsigned (&pw)[4], unsigned off, HOOK hook) {
    u32x4 ph[2][2];
    softmax2(pp, ph, hook);
#pragma unroll
    for (int b = 0; b < 2; ++b)
#pragma unroll
        for (int kf = 0; kf < 2; ++kf)
#pragma unroll
            for (int fb = 0; fb < 2; ++fb) {
                const u32x2 a = {ph[b][kf][2 * fb], ph[b][kf][2 * fb + 1]};
                lds_write<u32x2>(pw[2 * kf + fb] + off + b * 2048, a);
            }
}

#define SB0() __builtin_amdgcn_sched_barrier(0)
template <int A, int B, class F>
DI void sfor(F f) {
    if constexpr (A < B) {
        f(std::integral_constant<int, A>{});
        sfor<A + 1, B>(f);
    }
}
template <int N> using IC = std::integral_constant<int, N>;
#define AI __attribute__((always_inline))

DI void o_store(const u32x4 (&kf)[6], const u32x4 (&ph)[2][2], const float (&gt)[2], __amdgpu_buffer_rsrc_t rsrc, unsigned o_st) {
    const f32x4 zero = {0.f, 0.f, 0.f, 0.f};
    auto b8 = [](const u32x4& a) { return __builtin_bit_cast(bf16x8, a); };
    f32x4 dn[2], nm[2][2];
#pragma unroll
    for (int i = 0; i < 2; ++i) dn[i] = mfma16(b8(kf[4]), b8(ph[i][0]), zero);
#pragma unroll
    for (int i = 0; i < 2; ++i) dn[i] = mfma16(b8(kf[5]), b8(ph[i][1]), dn[i]);
    SB0();
#pragma unroll
    for (int i = 0; i < 4; ++i) nm[i & 1][i >> 1] = mfma16(b8(kf[i >> 1]), b8(ph[i & 1][0]), zero);
    SB0();
    float rd_[2];
#pragma unroll
    for (int i = 0; i < 4; ++i) nm[i >> 1][i & 1] = mfma16(b8(kf[2 + (i & 1)]), b8(ph[i >> 1][1]), nm[i >> 1][i & 1]);
#pragma unroll
    for (int i = 0; i < 2; ++i) rd_[i] = __builtin_amdgcn_rcpf(rbf(rbf(dn[i][0]) + 1e-6f)) * gt[i];
    SB0();
#pragma unroll
    for (int i = 0; i < 2; ++i) {
        u32x4 o;
        f32x2 t[4];
#pragma unroll
        for (int j = 0; j < 4; ++j) t[j] = (f32x2){nm[i][j >> 1][2 * (j & 1)], nm[i][j >> 1][2 * (j & 1) + 1]} * (f32x2){rd_[i], rd_[i]};
        SB0();
#pragma unroll
        for (int j = 0; j < 4; ++j) o[j] = pack2(t[j][0], t[j][1]);
        SB0();
        st_wt(rsrc, o_st + i * 1024, nz4(o));
        SB0();
    }
}


typedef __attribute__((ext_vector_type(16))) unsigned u32x16;
__global__ __launch_bounds__(512, 2) void fused_mix(const u16*, const u16*, const u16*, const u16*, const u16*, const u16*, const u16*, const u16*,
                                                    const u16*, char*, u16*) {
    __shared__ __attribute__((aligned(16))) char smem[163840];
    u32x4 ka;
    u32x16 kb_;
    u64 kz;
    asm volatile("s_load_dwordx4 %0, %3, 0x0\n\ts_load_dwordx16 %1, %3, 0x10\n\ts_load_dwordx2 %2, %3, 0x50"
                 : "=&s"(ka), "=&s"(kb_), "=&s"(kz) : "s"(__builtin_amdgcn_kernarg_segment_ptr()));
    SB0();
    const int tid = threadIdx.x;
    int l = tid & 63;
    const int w = __builtin_amdgcn_readfirstlane(tid >> 6) ^ 4;
    int bid = blockIdx.x;
    const unsigned lds0 = lds_of(smem);
    int g = l >> 4, l15 = l & 15, q = l15 >> 2, p = l & 3;
    int srow = l >> 3, sch = l & 7;
    unsigned l16 = l * 16;
    const int rg = w & 3, kp = w >> 2;
    const int hs = rg >> 1;
    {
        u64 pc;
        asm volatile("s_getpc_b64 %0\n\ts_mov_b32 m0, %2\n\ts_mov_b64 exec, 3\n\tglobal_load_lds_dword %1, %0\n\ts_mov_b64 exec, -1"
                     : "=&s"(pc) : "v"(min((unsigned)(((bid & ~7) + w) * 2 + l) * 128u, CPF)), "s"(lds0 + L_SM + 3584) : "memory");
    }
    lds_write<unsigned>(lds0 + L_SM + tid * 4, 0u);
    const int slot = bid >> 3;
    const int mt = (bid & 1) * 8 + (slot & 7);
    const int hp = ((bid >> 1) & 3) * 4 + (slot >> 3);
    unsigned vx[4], vw[4];
    auto vw_init = [&]() AI {
        int t = l;
        asm volatile("" : "+v"(t));
#pragma unroll
        for (int i = 0; i < 4; ++i) vw[i] = (unsigned)((8 * i + (t >> 3)) * 6144 + ((t & 7) ^ (((t >> 4) & 1) << 1) ^ ((i & 1) << 2)) * 16 + 4096 - i * 1024);
    };
    auto vx_init = [&]() AI {
        int t = l;
        asm volatile("" : "+v"(t));
#pragma unroll
        for (int i = 0; i < 4; ++i) vx[i] = (unsigned)((8 * i + (t >> 3)) * 2048 + (((t & 7) ^ ((4 * (i & 1) + (t >> 4)) & 7)) * 16) + 4096 - i * 1024);
    };
    vx_init();
    vw_init();
    asm volatile("" : "+v"(vx[0]), "+v"(vx[1]), "+v"(vx[2]), "+v"(vx[3]), "+v"(vw[0]), "+v"(vw[1]), "+v"(vw[2]), "+v"(vw[3]));
    int ks0 = ((slot + (slot >> 3)) & 3) ^ ((bid & 1) * 8), role = w & 5;
    long long xo0 = (mt * 128 + hs * 64) * 2048 - 4096, wo0 = hp * 128 + hs * (32 * 6144) - 4096;
    long long xo1 = xo0 + ks0 * 128, wo1 = wo0 + ks0 * (64 * 6144) + 2048;
    unsigned d0 = lds0 + (hs ? X_U1[0] : X_U0[0]) * 8192, d1 = lds0 + J_U[0] * 8192 + hs * 4096;
    const unsigned* one = k_num;
    asm volatile("s_waitcnt lgkmcnt(0)" : "+s"(ka), "+s"(kb_), "+s"(kz), "+s"(xo0), "+s"(wo0), "+s"(xo1), "+s"(wo1), "+s"(ks0), "+s"(role),
                 "+s"(d0), "+s"(d1), "+s"(one) :: "memory");
    const u16* x = (const u16*)((u64)ka[1] << 32 | ka[0]);
    const u16* wqkv = (const u16*)((u64)ka[3] << 32 | ka[2]);
    auto arg = [&](int i) { return (char*)((u64)kb_[2 * i + 1] << 32 | kb_[2 * i]); };
    const u16 *wphi = (const u16*)arg(0), *wo = (const u16*)arg(1), *wr = (const u16*)arg(2), *wg = (const u16*)arg(3);
    const u16 *wu = (const u16*)arg(4), *wd = (const u16*)arg(5), *gw = (const u16*)arg(6);
    char* ws = arg(7);
    u16* out = (u16*)kz;
    unsigned v0 = 0;
    asm volatile("" : "+v"(v0));
    auto mark = [&](unsigned dst, int n = 1, int lanes = 1) AI {
        asm volatile("s_mov_b32 m0, %2\n\ts_mov_b64 exec, %4\n\tglobal_load_lds_dword %0, %1 offset:%3\n\ts_mov_b64 exec, -1"
                     ::"v"(v0), "s"(one), "s"(dst - 4 * n), "n"(4 * n), "n"(lanes) : "memory");
    };
    auto four = [&](const unsigned (&v)[4], const char* src, unsigned dst) AI {
        asm volatile("s_mov_b32 m0, %5\n\ts_nop 0\n\t"
                     "global_load_lds_dwordx4 %0, %4\n\tglobal_load_lds_dwordx4 %1, %4 offset:1024\n\t"
                     "global_load_lds_dwordx4 %2, %4 offset:2048\n\tglobal_load_lds_dwordx4 %3, %4 offset:3072"
                     ::"v"(v[0]), "v"(v[1]), "v"(v[2]), "v"(v[3]), "s"(src), "s"(dst) : "memory");
    };
    static_assert(F_J[0] == -1 && F_J[1] == 0 && F_K[1] == 1 && (KXB & 15) == 0, "");
    if (__builtin_expect(role == 4, 1)) {
        four(vx, (const char*)x + xo1, d0);
        four(vx, (const char*)x + xo1 + 32 * 2048, d0 + 4096);
        four(vw, (const char*)wqkv + wo1, d1);
    }
    asm volatile("" : "+s"(xo0), "+s"(wo0));
    const char* xb0 = (const char*)x + xo0;
    const char* wb0 = (const char*)wqkv + wo0;
    auto fill = [&](auto N) AI {
        constexpr int n = N, j = F_J[n] < 0 ? 0 : F_J[n], xi = F_J[n] < 0 ? -1 - F_J[n] : 0, s = F_J[n] < 0 ? xi : J_S[j], m = J_M[j];
        const int ks = ks0 ^ (int)((KXB >> (4 * s)) & 15);
        if constexpr (F_N[n] > 0 && n < NF1) waitv<PACE>();
        if constexpr (F_J[n] >= 0) {
            four(vw, wb0 + ks * (64 * 6144) + (m == 2 ? 0 : 2048 * (m + 1)), lds0 + J_U[j] * 8192 + hs * 4096);
        } else {
            const char* xs_ = xb0 + ks * 128;
            const unsigned d = lds0 + (hs ? X_U1[xi] : X_U0[xi]) * 8192;
            four(vx, xs_, d);
            if constexpr (F_N[n] > 0) waitv<PACE>();
            four(vx, xs_ + 32 * 2048, d + 4096);
        }
        if constexpr (F_K[n] > 1) mark(lds0 + L_FULL + (n < NF1 ? 0 : 32) + hs * 8, F_K[n], 3);
    };
    unsigned sd = 0, st;
    asm volatile("s_cmp_lg_u32 %2, 5\n\ts_cbranch_scc1 .La%=\n\t"
                 "s_min_u32 %1, %3, 23\n\ts_lshl_b32 %1, %1, 18\n\ts_load_dword %0, %5, %1\n\ts_load_dword %0, %6, 0x0\n\t"
                 "s_and_b32 %1, %1, 0x3c0000\n\ts_load_dword %0, %4, %1\n\ts_load_dword %0, %10, %1\n\t"
                 "s_load_dword %0, %7, 0x0\n\ts_load_dword %0, %8, 0x0\n\ts_load_dword %0, %9, 0x0\n.La%=:"
                 : "+s"(sd), "=&s"(st) : "s"(w), "s"(slot), "s"(x), "s"(wqkv), "s"(one), "s"(wr), "s"(gw), "s"(wphi), "s"(out) : "memory", "scc");
    asm volatile("s_cmp_lg_u32 %2, 7\n\ts_cbranch_scc1 .La%=\n\t"
                 "s_min_u32 %1, %3, 27\n\ts_lshl_b32 %1, %1, 18\n\ts_load_dword %0, %4, 0x0\n\ts_load_dword %0, %4, %1\n\t"
                 "s_load_dword %0, %5, %1\n\ts_load_dword %0, %6, %1\n\ts_load_dword %0, %7, %1\n\t"
                 "s_and_b32 %1, %1, 0x1c0000\n\ts_load_dword %0, %8, %1\n.La%=:\n\ts_barrier"
                 : "+s"(sd), "=&s"(st) : "s"(w), "s"(slot), "s"(ws), "s"(wg), "s"(wu), "s"(wd), "s"(wo) : "memory", "scc");
    if (__builtin_expect((w & 5) == 4, 1)) {
        mark(lds0 + L_FULL + hs * 8, 1, 3);
        sfor<2, 4>(fill);
    }
    asm volatile("" : "+s"(bid));
    const int xcd = bid & 7;
    const int sc = (3 - (int)((unsigned)(size_t)wqkv >> 7)) & 7;
    const int rt = (xcd >> 1) * 4 + ((bid >> 3) & 3), cb = (xcd & 1) * 8 + (bid >> 5), rot = 8 * rt;
    const __amdgpu_buffer_rsrc_t rsrc = __builtin_amdgcn_make_buffer_rsrc(ws, 0, WS_END, 0x00020000);
    const u32x4 z4 = {0u, 0u, 0u, 0u};
    const f32x4 zero = {0.f, 0.f, 0.f, 0.f};
    unsigned AX = lds0 + L_A + rg * 4096 + l16;
    unsigned W4 = lds0 + p * 4;
    unsigned PG = lds0 + L_PROG + p * 256;
    const unsigned WR = lds0 + rg * 4;
    auto flag = [&](unsigned a) AI { asm volatile("ds_write_b32 %0, %0" ::"v"(a) : "memory"); };
    auto phi_w = [&](bf16x8 (&a)[2], int h) AI {
        const unsigned ad = lds0 + L_WPHI + h * 2048 + (4 * g + q) * 64 + (((p >> 1) ^ (2 * (g & 1))) * 16) + 8 * (p & 1);
        a[0] = cat8(tr_read(ad), tr_read(ad + 1024));
        a[1] = cat8(tr_read(ad ^ 32), tr_read((ad ^ 32) + 1024));
    };
    u32x4 fw[32], fd[16], gu[2];
    unsigned sel_[4][2];
    u32x4 ph[2][2], sq;
    const int hp2 = ((bid >> 1) & 3) * 4 + (bid >> 6);
    const bool ffn = (hp2 & 7) != sc;
    const int ph4 = ((bid >> 3) + (bid >> 6)) & 3;
    const int kq = mt >> 2;
    const int cs = (ffn ? hp2 - (hp2 > sc) - (hp2 > sc + 8) : 14 + (hp2 >> 3)) * 4 + ((ph4 + 3 - (int)((unsigned)(size_t)wg >> 7)) & 3);
    auto spread = [&](const u32x4& v, int i) -> bf16x8 {
        const unsigned s = v[i >> 1];
        u32x4 a;
#pragma unroll
        for (int d = 0; d < 4; ++d) a[d] = __builtin_amdgcn_perm(s, s, sel_[d][i & 1]);
        return __builtin_bit_cast(bf16x8, a);
    };
    auto fold_store = [&](const f32x4& a, unsigned off) {
        f32x4 v;
#pragma unroll
        for (int i = 0; i < 4; ++i) {
            const unsigned u = __float_as_uint(a[i]);
            const float r = __uint_as_float(__builtin_amdgcn_update_dpp(u, u, 0x128, 0xc, 0xf, false));
            v[i] = swap32_add(r, r);
        }
        const u32x4 o = nz4(v);
        ST_MASK("global_store_dwordx4", 0x00FF00FFull, off + l15 * 32 + g * 16, o, ws);
    };
    auto sel_init = [&]() AI {
#pragma unroll
        for (int d = 0; d < 4; ++d)
#pragma unroll
            for (int hf = 0; hf < 2; ++hf) {
                const unsigned b = hf ? 0x0302u : 0x0100u;
                sel_[d][hf] = (sch >> 1) != d ? 0x0c0c0c0cu : (l & 1) ? (b << 16) | 0x0c0cu : 0x0c0c0000u | b;
            }
    };
    auto gua = [&](int i) { return ws + WS_GU + cs * 4096 + i * 1024; };
    auto gu_load = [&]() AI {
#pragma unroll
        for (int i = 0; i < 2; ++i) ld_async(gu[i], l16, gua(i));
    };
    auto down = [&]() AI {
#pragma unroll
        for (int i = 0; i < 16; ++i) pin(fd[i]);
        settle(gu, [&](int) { return l16; }, gua);
        const f32x4 v = __builtin_bit_cast(f32x4, gu[0]) + __builtin_bit_cast(f32x4, gu[1]);
        auto r = __builtin_amdgcn_permlane16_swap(__float_as_uint(swap32_add(v[0], v[2])), __float_as_uint(swap32_add(v[1], v[3])), false, false);
        const float gt = rbf(__uint_as_float(r[0])), up = rbf(__uint_as_float(r[1]));
        const unsigned ha = lds0 + L_XS + hs * 512;
        lds_write<u16>(ha + l15 * 8 + g * 2, f2bf(rbf(gt / (1.f + __expf(-gt))) * up));
        const u32x4 hv = lds_read<u32x4>(ha + srow * 16);
        f32x4 y0 = zero, y1 = zero;
#pragma unroll
        for (int i = 0; i < 8; ++i) {
            const bf16x8 a = spread(hv, i);
            y0 = mfma16(a, __builtin_bit_cast(bf16x8, fd[i]), y0);
            y1 = mfma16(a, __builtin_bit_cast(bf16x8, fd[8 + i]), y1);
        }
        fold_store(y0, WS_Y + cs * 4096 + kq * 1024 + hs * 512);
        fold_store(y1, WS_Y + cs * 4096 + kq * 1024 + hs * 512 + 256);
    };
    auto need = [&](int c) AI {
        if (c == 101) lds_wait(W4 + L_FKS, 1);
        else if (c == 100) lds_wait(W4 + L_FV, 1);
        else if (c == 99) lds_wait(W4 + L_FQD, 1);
        else if (c > 0) lds_wait(c > RLD ? W4 + L_PRL : PG, c);
    };
    auto wo_load = [&](int n0, int n1, bool mk) AI {
        unsigned vo[4];
#pragma unroll
        for (int i = 0; i < 4; ++i) vo[i] = (unsigned)((8 * i + srow) * 2048 + ((sch ^ ((g & 3) << 1)) * 16) + 4096 - i * 1024);
        const char* wob = (const char*)wo + cb * 128 - 4096;
#pragma unroll
        for (int n = n0; n < n1; ++n) {
            if (n < WON && (n == 0 || W_N[n] != W_N[n - 1])) need(W_N[n]);
            four(vo, wob + ((2 * n + hs + rot) & 31) * 65536, lds0 + W_U[n] * 8192 + hs * 4096);
        }
        if (mk) mark(lds0 + (n1 > WON ? L_FW2 : L_FWD) + hs * 4);
    };
    auto phi_adr = [&](unsigned (&pw)[4], unsigned base) AI {
#pragma unroll
        for (int i = 0; i < 4; ++i) pw[i] = (base + rg * 4096 + l15 * 128 + ((g ^ fsw(l15)) << 3)) ^ (i << 5);
    };
    const int row0 = rt * 128 + rg * 32 + kp * 16;
    u32x4 xr[2];
    u64 gg;
    unsigned xo = srow * 2048 + sch * 16;
    auto g_load = [&]() AI {
        asm volatile("global_load_dwordx2 %0, %1, %2 sc1" : "=&v"(gg) : "v"((unsigned)l15 * 8), "s"(ws + WS_GG + row0 * 8) : "memory");
    };
    auto xg_load = [&]() AI {
        asm volatile("global_load_dwordx4 %0, %3, %4\n\tglobal_load_dwordx4 %1, %3, %4 offset:64\n\tglobal_load_dwordx2 %2, %5, %6 sc1"
                     : "=&v"(xr[0]), "=&v"(xr[1]), "=&v"(gg)
                     : "v"((unsigned)((l >> 2) * 2048 + p * 16)), "s"(x + (size_t)row0 * DM + cb * 64),
                       "v"((unsigned)l15 * 8), "s"(ws + WS_GG + row0 * 8) : "memory");
    };
    f32x4 a0_[4];
    float og;
    auto x_init = [&](bool staged) AI {
        if (staged) asm volatile("" : "+v"(gg));
        else asm volatile("" : "+v"(xr[0]), "+v"(xr[1]), "+v"(gg));
#pragma unroll 1
        for (unsigned s = 0; s < SPIN_CAP && __ballot((gg >> 16) != (((u64)sq[1] << 32 | sq[0]) & 0xFFFFFFFFFFFFull)); ++s) {
            g_load();
            waitv<0>();
            asm volatile("" : "+v"(gg));
        }
        og = rbf(1.f - bf2f((u16)gg));
        if (staged) {
#pragma unroll
            for (int nb = 0; nb < 4; ++nb) {
                const u32x2 u = lds_read<u32x2>(lds0 + L_A + 2048 + rg * 4096 + l15 * 128 + (((2 * nb + (g >> 1)) ^ sch) * 16) + (g & 1) * 8);
                a0_[nb] = (f32x4){lo16(u[0]), hi16(u[0]), lo16(u[1]), hi16(u[1])} * og;
            }
            asm volatile("" : "+v"(a0_[0]), "+v"(a0_[1]), "+v"(a0_[2]), "+v"(a0_[3]), "+v"(og));
            SB0();
            return;
        }
        unsigned t[8];
#pragma unroll
        for (int i = 0; i < 8; ++i) t[i] = __builtin_amdgcn_ds_bpermute(l15 * 16 + (g >> 1) * 4 + (g & 1) * 8, xr[i >> 2][i & 3]);
#pragma unroll
        for (int j = 0; j < 2; ++j) {
            auto u = __builtin_amdgcn_permlane16_swap(t[4 * j], t[4 * j + 2], false, false);
            auto v = __builtin_amdgcn_permlane16_swap(t[4 * j + 1], t[4 * j + 3], false, false);
#pragma unroll
            for (int b = 0; b < 2; ++b) a0_[2 * j + b] = (f32x4){lo16(u[b]), hi16(u[b]), lo16(v[b]), hi16(v[b])} * og;
        }
        asm volatile("" : "+v"(a0_[0]), "+v"(a0_[1]), "+v"(a0_[2]), "+v"(a0_[3]), "+v"(og));
        SB0();
    };
    auto xg_def = [&]() AI { asm volatile("" : "=v"(xr[0]), "=v"(xr[1]), "=v"(gg)); };
    auto relane = [&]() AI {
        unsigned z = 0;
        asm volatile("" : "+v"(z));
        l = __builtin_amdgcn_mbcnt_hi(~0u, __builtin_amdgcn_mbcnt_lo(~0u, z));
        g = l >> 4, l15 = l & 15, q = l15 >> 2, p = l & 3, srow = l >> 3, sch = l & 7, l16 = l * 16;
        AX = lds0 + L_A + rg * 4096 + l16, W4 = lds0 + p * 4, PG = lds0 + L_PROG + p * 256, xo = srow * 2048 + sch * 16;
    };
    u32x4 kf[6];
    auto fd_def = [&]() AI {
#pragma unroll
        for (int i = 0; i < 16; ++i) asm volatile("" : "=v"(fd[i]));
        asm volatile("" : "=v"(gu[0]), "=v"(gu[1]));
    };
    auto kf_def = [&]() AI { asm volatile("" : "=v"(kf[0]), "=v"(kf[1]), "=v"(kf[2]), "=v"(kf[3]), "=v"(kf[4]), "=v"(kf[5])); };
    const int oh = hs ^ kp ^ 1;

    if (__builtin_expect(w >= 4, 1)) {
        const bool loader = !(w & 1);
        const unsigned kf_kv = WS_KF + (hp * 2 + (mt >> 3)) * 8192 + (mt & 7) * 512;
        const unsigned kf_z = WS_KF + (hp * 2 + (mt >> 3)) * 8192 + 4096 + (mt & 7) * 128;
        const unsigned fgo = ffn ? (unsigned)((256 * kq + 16 * srow) * 7168 + cs * 128 + sch * 16) : 0u;
        const char* fsrc = (const char*)(hs ? wu : wg);
        auto gu_w = [&](int q0, int q1) AI {
#pragma unroll
            for (int i = q0; i < q1; ++i) {
                unsigned t1, t2, t3;
                asm volatile("v_add_u32 %4, 0x1c00, %7\n\tv_add_u32 %5, 0x3800, %7\n\tv_add_u32 %6, 0x5400, %7\n\t"
                             "global_load_dwordx4 %0, %7, %8 nt\n\tglobal_load_dwordx4 %1, %4, %8 nt\n\t"
                             "global_load_dwordx4 %2, %5, %8 nt\n\tglobal_load_dwordx4 %3, %6, %8 nt"
                             : "=&v"(fw[4 * i]), "=&v"(fw[4 * i + 1]), "=&v"(fw[4 * i + 2]), "=&v"(fw[4 * i + 3]), "=&v"(t1), "=&v"(t2), "=&v"(t3)
                             : "v"(fgo), "s"(fsrc + (128 * (i >> 2) + 4 * (i & 3)) * 7168) : "memory");
            }
        };
        const unsigned fdo = ffn ? (unsigned)((64 * cs + 8 * srow) * 2048 + 512 * kq + 256 * hs + sch * 16) : 0u;
        auto dn_w = [&](int q0, int q1) AI {
#pragma unroll
            for (int i = q0; i < q1; ++i)
                asm volatile("global_load_dwordx4 %0, %4, %5 offset:-4096 nt\n\tglobal_load_dwordx4 %1, %4, %5 offset:-2048 nt\n\t"
                             "global_load_dwordx4 %2, %4, %5 nt\n\tglobal_load_dwordx4 %3, %4, %5 offset:2048 nt"
                             : "=&v"(fd[4 * i]), "=&v"(fd[4 * i + 1]), "=&v"(fd[4 * i + 2]), "=&v"(fd[4 * i + 3])
                             : "v"(fdo), "s"((const char*)wd + 4096 + (i & 1) * 8192 + (i >> 1) * 128) : "memory");
        };
        auto gate_up = [&]() AI {
            sel_init();
            u32x4 xs[4];
            waitv<4 * (NF - NF1) + 1 + 4 * (8 - GQ1) + 16>();
#pragma unroll
            for (int i = 0; i < 4; ++i) xs[i] = lds_read<u32x4>(lds0 + L_XS + hs * 512 + (i >> 1) * 256 + srow * 32 + (i & 1) * 16);
            f32x4 a;
            bf16x8 t[2];
            t[0] = spread(xs[0], 0);
            sfor<0, 32>([&](auto I) {
                constexpr int i = I;
                SB0();
                if constexpr (i < 31) t[~i & 1] = spread(xs[(i + 1) >> 3], (i + 1) & 7);
                else asm volatile("s_nop 1");
                asm volatile("" : "+v"(t[~i & 1]));
                if constexpr (i == 4 * GQ1) waitv<16>();
                SB0();
                u32x4& f = fw[i];
                bf16x8& u = t[i & 1];
                f32x4& c = a;
                pin(f);
                if constexpr (i == 0) asm volatile("v_mfma_f32_16x16x32_bf16 %0, %1, %2, 0" : "=&v"(c) : "v"(u), "v"(f));
                else asm volatile("v_mfma_f32_16x16x32_bf16 %0, %1, %2, %0" : "+v"(c) : "v"(u), "v"(f));
            });
            asm volatile("s_nop 7" : "+v"(a));
            SB0();
            fold_store(a, WS_GU + cs * 4096 + kq * 512 + hs * 256);
            SB0();
        };
        u64 T;
        u32x4 sv[2];
        u32x2 sg;
        auto rg_load = [&]() AI {
            asm volatile("global_load_dwordx4 %0, %3, %4 sc1\n\tglobal_load_dwordx4 %1, %3, %4 offset:16 sc1\n\tglobal_load_dwordx2 %2, %5, %6 sc1"
                         : "=&v"(sv[0]), "=&v"(sv[1]), "=&v"(sg) : "v"(l * 32), "s"(ws + WS_RG), "v"(l * 8), "s"(ws + WS_GG + (mt * 2 + hs) * 512) : "memory");
        };
        auto select = [&]() AI {
            const unsigned tl = (unsigned)T & 0x1FFFu, thi = (unsigned)(T >> 13);
#pragma unroll 1
            for (unsigned s = 0; s < SPIN_CAP; ++s) {
                pin(sv[0]);
                pin(sv[1]);
                asm volatile("" : "+v"(sg));
                const unsigned bad = ((sg[0] >> 16) ^ ((unsigned)T & 0xFFFFu)) | (sg[1] ^ (unsigned)(T >> 16)) | ((sv[0][0] >> 19) ^ tl) | (sv[0][1] ^ thi) | ((sv[0][2] >> 19) ^ tl) | (sv[0][3] ^ thi) |
                                     ((sv[1][0] >> 19) ^ tl) | (sv[1][1] ^ thi) | ((sv[1][2] >> 19) ^ tl) | (sv[1][3] ^ thi);
                if (__all(bad == 0u)) break;
                __builtin_amdgcn_s_sleep(16);
                rg_load();
                waitv<0>();
            }
            pin(sv[0]);
            pin(sv[1]);
            asm volatile("" : "+v"(sg));
            lds_write<unsigned>(lds0 + L_G + hs * 512 + l * 4, sg[0] << 16);
            unsigned k2 = 0;
#pragma unroll
            for (int j = 0; j < 4; ++j) {
                const unsigned v = sv[j >> 1][(j & 1) * 2] & 0x7FFFFu;
                k2 = max(k2, ((v >> 3) << 11) | ((255 - (4 * l + j)) << 3) | (v & 7));
            }
            const unsigned key = __builtin_amdgcn_readfirstlane(wave_umax(k2));
            asm volatile("s_mov_b32 m0, %2\n\ts_mov_b64 exec, %3\n\tglobal_load_lds_dwordx4 %0, %1\n\ts_mov_b64 exec, -1"
                         ::"v"((unsigned)(512 * kq) + l16), "s"(x + (size_t)(2047 - (int)(key & 2047u)) * DM), "s"(lds0 + L_XS + hs * 512), "s"(0xFFFFFFFFull) : "memory");
            unsigned rb = key >> 11;
            rb ^= (rb & 0x8000u) ? 0x8000u : 0xFFFFu;
            const float rsel = rbf(1.f / (1.f + __expf(-bf2f((u16)rb))));
            lds_write<u32x4>(lds0 + L_SEL, (u32x4){(unsigned)T, (unsigned)(T >> 32), 2047u - (key & 2047u), __float_as_uint(rsel)});
            if (bid == 0 && hs) ST_MASK("global_store_dwordx2", 1ull, 0u, T, ws + WS_E);
        };
        auto k_soft = [&]() AI {
            bf16x8 a0[2], kb[2];
            f32x4 pk[2][2][2];
            unsigned pw[4];
            phi_w(a0, 0);
            phi_adr(pw, lds0 + P_U[0] * 8192);
            asm volatile("" : "+v"(pw[0]), "+v"(pw[1]), "+v"(pw[2]), "+v"(pw[3]));
            auto logits = [&](int h) AI {
                lds_wait(WR + L_FK0 + 4 * h, 1);
#pragma unroll
                for (int i = 0; i < 2; ++i) kb[i] = lds_read128(AX + (P_U[0] * 8192 - L_A) + h * 4096 + i * 1024);
#pragma unroll
                for (int i = 0; i < 4; ++i) pk[h][i >> 1][i & 1] = mfma16(a0[i & 1], kb[i >> 1], zero);
            };
            auto round = [&](int h) AI {
#pragma unroll
                for (int i = 0; i < 4; ++i) pk[h][i >> 1][i & 1] = rbf4(pk[h][i >> 1][i & 1]);
            };
            logits(0);
            round(0);
            SB0();
            phi_k(pk[0], pw, 0, [&]() AI {
                logits(1);
                SB0();
            });
            SB0();
            round(1);
            phi_k(pk[1], pw, 4096, []() {});
            SB0();
            flag(WR + L_FP);
        };
        auto q_soft = [&](auto hook) AI {
            bf16x8 a1[2];
            f32x4 pq[2][2];
            phi_w(a1, hs);
            lds_wait0(WR + L_FQ);
#pragma unroll
            for (int rb = 0; rb < 2; ++rb) {
                const bf16x8 b = lds_read128(AX + rb * 1024);
#pragma unroll
                for (int fb = 0; fb < 2; ++fb) pq[rb][fb] = rbf4(mfma16(a1[fb], b, zero));
            }
            softmax2(pq, ph, hook);
            asm volatile("" : "+v"(ph[0][0]), "+v"(ph[0][1]), "+v"(ph[1][0]), "+v"(ph[1][1]));
        };
        if (__builtin_expect(loader, 1)) {
            vx_init();
            vw_init();
            unsigned pv = 0;
            sfor<4, NF1>([&](auto N) {
                constexpr int n = N;
                if constexpr (F_N[n] > F_N[n - 1]) {
                    __builtin_amdgcn_s_setprio(3);
                    lds_wait(pv, PG, F_N[n]);
                    if (F_N[n] < RLD) lds_peek(pv, PG);
                    __builtin_amdgcn_s_setprio(0);
                }
                if constexpr (n == 12) {
                    asm volatile("s_mov_b32 m0, %2\n\ts_nop 0\n\tglobal_load_lds_dwordx4 %0, %1\n\tglobal_load_lds_dwordx4 %0, %1 offset:1024"
                                 ::"v"((unsigned)((l >> 2) * 64 + (((l & 3) ^ (2 * ((l >> 4) & 1))) * 16))),
                                   "s"(wphi + (hp * 2 + hs) * 1024), "s"(lds0 + L_WPHI + hs * 2048) : "memory");
                }
                fill(N);
            });
            flag(lds0 + L_FLD + hs * 4);
            __builtin_amdgcn_s_setprio(2);
            const char* cla = ws + WS_CL + ((l15 * 2 + (mt >> 3)) * 16 + hp) * 128;
            u64 cl;
            asm volatile("global_load_dwordx2 %0, %1, off sc1" : "=&v"(cl) : "v"(cla) : "memory");
            k_soft();
            lds_wait(W4 + L_FP, 1);
            sq = lds_read<u32x4>(lds0 + L_SEL);
            waitv<0>();
            asm volatile("" : "+v"(cl));
            {
                u32x4 kp_[6];
                const char* kb = ws + WS_KV + (hp * 8 + (mt >> 1)) * 16384 + (mt & 1) * 512 + hs * 256;
                const bool zw = !((mt & 3) | hs);
                const char* zb = zw ? ws + WS_Z + (hp * 4 + (mt >> 2)) * 2048 : kb;
                const unsigned kvo = g * 1024 + l15 * 16, zo = zw ? l16 : 0u;
                auto ko = [&](int i) { return i < 4 ? kvo : zo; };
                auto ka = [&](int i) { return i < 4 ? kb + i * 4096 : zb + (i - 4) * 1024; };
                const u64 T = (u64)sq[1] << 32 | sq[0];
#pragma unroll 1
                for (unsigned s = 0; s < SPIN_CAP && __ballot(cl != T); ++s)
                    asm volatile("global_load_dwordx2 %0, %1, off sc1\n\ts_waitcnt vmcnt(0)" : "=&v"(cl) : "v"(cla) : "memory");
                u32x4 a01;
#pragma unroll
                for (int i = 0; i < 4; ++i) a01[i] = (l15 >> 1) != i ? 0u : (l & 1) ? 0x3f800000u : 0x3f80u;
                unsigned po = kf_kv + hs * 256 + l15 * 16 + (g & 1) * 8;
                pin(a01);
                asm volatile("" : "+v"(po));
                lds_wait0(W4 + L_FKS);
                __builtin_amdgcn_s_sleep(GDL);
#pragma unroll
                for (int i = 0; i < 6; ++i) ld_async(kp_[i], ko(i), ka(i));
                xg_load();
                SB0();
                waitv<0>();
                settle(kp_, ko, ka);
                f32x4 d = zero;
#pragma unroll
                for (int i = 0; i < 4; ++i) d = mfma16(__builtin_bit_cast(bf16x8, a01), __builtin_bit_cast(bf16x8, kp_[i]), d);
                const u32x2 o = {nz1(pack2(d[0], d[1])), nz1(pack2(d[2], d[3]))};
                ST_MASK("global_store_dwordx2", 0xFFFFFFFFull, po, o, ws);
                if (zw) {
                    f32x4 z = __builtin_bit_cast(f32x4, kp_[4]) + __builtin_bit_cast(f32x4, kp_[5]);
#pragma unroll
                    for (int i = 0; i < 4; ++i) {
                        float t = dpp_add<0x128>(z[i]);
                        t = swap16_add(t, t);
                        z[i] = swap32_add(t, t);
                    }
                    const u32x2 oz = {nz1(pack2(z[0], z[1])), nz1(pack2(z[2], z[3]))};
                    ST_MASK("global_store_dwordx2", 0xFFFFull, kf_z + (l & 8) * 8 + p * 16 + (sch >> 2) * 8, oz, ws);
                }
                flag(lds0 + L_FG + hs * 4);
            }
            const char* kfb = ws + WS_KF + (hp * 2 + hs) * 8192;
            auto fo = [&](int i) { return i < 4 ? l16 : (unsigned)g * 16; };
            auto fb = [&](int i) { return i < 4 ? kfb + i * 1024 : kfb + 4096 + (i - 4) * 512; };
            need(KFN);
            unsigned sa0 = lds0 + L_KF + hs * 4096 + l16, sa1 = lds0 + L_KZ + hs * 128 + g * 16, sf = lds0 + L_FKF + hs * 4;
            asm volatile("" : "+v"(sa0), "+v"(sa1), "+v"(sf));
            q_soft([]() {});
            SB0();
            __builtin_amdgcn_s_sleep(KFD);
#pragma unroll
            for (int i = 0; i < 6; ++i) ld_async(kf[i], fo(i), fb(i));
            SB0();
            x_init(false);
            waitv<0>();
            settle(kf, fo, fb);
#pragma unroll
            for (int i = 0; i < 4; ++i) lds_write<u32x4>(sa0 + i * 1024, kf[i]);
#pragma unroll
            for (int i = 0; i < 2; ++i) lds_write<u32x4>(sa1 + i * 64, kf[4 + i]);
            SB0();
            flag(sf);
            fd_def();
            xg_def();
        } else {
            const int row0 = (mt * 16 + hp) * 8 + 4 * hs;
            __builtin_amdgcn_s_setprio(2);
            asm volatile("s_waitcnt lgkmcnt(0)" : "+s"(sd));
            lds_wait(lds0 + L_FULL + p * 4, 3);
            u32x4 rv[2], gv[2], xv[4][2];
            u64 ev;
            asm volatile("global_load_dwordx2 %4, %5, %9 sc1\n\t"
                         "global_store_dwordx4 %10, %11, %9 sc1\n\t"
                         "global_store_dwordx2 %12, %13, %9 sc1\n\t"
                         "global_load_dwordx4 %0, %6, %7\n\tglobal_load_dwordx4 %1, %6, %7 offset:1024\n\t"
                         "global_load_dwordx4 %2, %6, %8\n\tglobal_load_dwordx4 %3, %6, %8 offset:1024"
                         : "=&v"(rv[0]), "=&v"(rv[1]), "=&v"(gv[0]), "=&v"(gv[1]), "=&v"(ev)
                         : "v"(0u), "v"(l16), "s"(wr), "s"(gw), "s"(ws),
                           "v"(l < 32 ? WS_Y + cs * 4096 + kq * 1024 + hs * 512 + l16 : WS_GU + cs * 4096 + kq * 512 + hs * 256 + l15 * 16), "v"(z4),
                           "v"(hs ? kf_z + l15 * 8 : kf_kv + l * 8), "v"(0ull)
                         : "memory");
#pragma unroll
            for (int r = 0; r < 2; ++r)
                asm volatile("global_load_dwordx4 %0, %4, %5\n\tglobal_load_dwordx4 %1, %4, %5 offset:1024\n\t"
                             "global_load_dwordx4 %2, %4, %5 offset:2048\n\tglobal_load_dwordx4 %3, %4, %5 offset:3072"
                             : "=&v"(xv[2 * r][0]), "=&v"(xv[2 * r][1]), "=&v"(xv[2 * r + 1][0]), "=&v"(xv[2 * r + 1][1])
                             : "v"(l16), "s"(x + (size_t)(row0 + 2 * r) * DM) : "memory");
            SB0();
            waitv<0>();
            asm volatile("" : "+v"(rv[0]), "+v"(rv[1]), "+v"(gv[0]), "+v"(gv[1]), "+v"(ev));
#pragma unroll
            for (int r = 0; r < 4; ++r) asm volatile("" : "+v"(xv[r][0]), "+v"(xv[r][1]));
            T = ((u64)__builtin_amdgcn_readfirstlane((unsigned)(ev >> 32)) << 32 | __builtin_amdgcn_readfirstlane((unsigned)ev)) + 1;
            float ra_[4] = {0.f, 0.f, 0.f, 0.f}, ga_[4] = {0.f, 0.f, 0.f, 0.f};
#pragma unroll
            for (int r = 0; r < 4; ++r)
#pragma unroll
                for (int i = 0; i < 2; ++i)
#pragma unroll
                    for (int j = 0; j < 4; ++j) {
                        ra_[r] = dot2(xv[r][i][j], rv[i][j], ra_[r]);
                        ga_[r] = dot2(xv[r][i][j], gv[i][j], ga_[r]);
                    }
            const int rr = (g >> 1) | ((g & 1) << 1);
            unsigned k = f2bf(sum4(ra_));
            k ^= (k & 0x8000u) ? 0xFFFFu : 0x8000u;
            k = (k << 3) | (7 - 4 * hs - rr);
            auto r1 = __builtin_amdgcn_permlane16_swap(k, k, false, false);
            k = max(r1[0], r1[1]);
            auto r2 = __builtin_amdgcn_permlane32_swap(k, k, false, false);
            k = max(r2[0], r2[1]);
            u64 gq = (T << 16) | f2bf(1.f / (1.f + __expf(-rbf(sum4(ga_)))));
            if (hs) lds_write<unsigned>(lds0 + L_BEST, k | 0x80000000u);
            asm volatile("" : "+v"(gq), "+v"(k));
            __builtin_amdgcn_s_setprio(0);
            lds_wait8(PG, CLR0 + ph4);
            if (!hs) {
                lds_wait(lds0 + L_BEST, 1);
                const u64 rq = (T << 19) | max(k, lds_read<unsigned>(lds0 + L_BEST) & 0x7FFFFu);
                ST_MASK("global_store_dwordx2", 1ull, 0u, rq, ws + WS_RG + (mt * 16 + hp) * 8);
            }
            ST_MASK("global_store_dwordx2", 0x0001000100010001ull, (unsigned)(rr * 8), gq, ws + WS_GG + row0 * 8);
#pragma unroll
            for (int n = 0; n < 8; ++n) st_wt(rsrc, WS_O + ((mt * 2 + (n >> 2)) * 32 + 2 * hp + hs) * 4096 + (n & 3) * 1024 + l16, z4);
#pragma unroll
            for (int n = 0; n < 4; ++n) st_wt(rsrc, WS_KV + ((hp * 8 + hs * 4 + n) * 16 + mt) * 1024 + l16, z4);
            ST_MASK("global_store_dwordx4", 0xFFFFull, WS_Z + ((hp * 4 + hs * 2 + (srow & 1)) * 16 + mt) * 128 + sch * 16, z4, ws);
            waitv<0>();
            ST_MASK("global_store_dwordx2", 0xFFFFull, (unsigned)l * 8, T, ws + WS_CL + ((mt * 2 + hs) * 16 + hp) * 128);
            lds_wait8(lds0 + L_FLD + (l & 1) * 4, 1);
            rg_load();
            gu_w(0, GQ1);
            waitv<4 * GQ1>();
            select();
            flag(WR + L_FP);
            vx_init();
            vw_init();
            sfor<NF1, NF>([&](auto N) {
                constexpr int n = N;
                if constexpr (n == NF1 || F_N[n] != F_N[n - 1]) need(F_N[n]);
                fill(N);
            });
            gu_w(GQ1, 8);
            dn_w(0, 4);
            __builtin_amdgcn_s_setprio(3);
            gate_up();
            __builtin_amdgcn_s_setprio(0);
            xg_load();
            lds_wait(W4 + L_FKS, 1);
            wo_load(0, WOE, false);
            sq = lds_read<u32x4>(lds0 + L_SEL);
            waitv<4 * WOE>();
            x_init(false);
            lds_wait(lds0 + L_FG + (l & 1) * 4, 1);
            gu_load();
            wo_load(WOE, WOM, false);
            __builtin_amdgcn_s_setprio(2);
            q_soft([&]() AI {
                wo_load(WOM, WON, true);
                SB0();
            });
            waitv<4 * (WON - WOE) + 1>();
#pragma unroll
            for (int i = 0; i < 16; ++i) pin(fd[i]);
            pin(gu[0]);
            pin(gu[1]);
            xg_def();
            kf_def();
        }
    } else {
        __builtin_amdgcn_s_setprio(1);
        unsigned ra[4], xt[2];
#pragma unroll
        for (int u = 0; u < 4; ++u) ra[u] = lds0 + J_B[0] * 8192 + (8 * g + q) * 128 + ((u ^ (q >> 1) ^ ((g & 1) << 1)) * 32) + 8 * p;
        unsigned xa = lds0 + (32 * (w & 1) + l15) * 128 + ((g ^ (l15 >> 1)) * 16), fa = lds0 + L_FULL + (l & 3) * 4;
        xt[0] = xa + (hs ? X_U1[0] : X_U0[0]) * 8192;
        xt[1] = xt[0] ^ 64;
        asm volatile("s_mov_b32 m0, %4" : "+v"(xa), "+v"(fa), "+v"(xt[0]), "+v"(xt[1]) : "s"(lds0 + L_PROG + w * 256 - 124));
        f32x4 acc[3][4][2];
        bf16x8 xf[ND][2][2];
        s16x4 wh[PD + 1][2];
        unsigned fv = 0;
        bf16x8 kown[2];
        u64 bad = ~0ull;
        auto chk = [&](int c) AI {
            SB0();
            if (__builtin_expect(bad != 0ull, 0)) {
                unsigned c_;
                asm volatile("s_mov_b32 %1, 0x400000\n"
                             ".Lp%=:\n\tds_read_b32 %0, %2 offset:%3\n\ts_waitcnt lgkmcnt(0)\n\tv_cmp_gt_u32 vcc, %4, %0\n\ts_cbranch_vccz .Lf%=\n\t"
                             "s_sub_u32 %1, %1, 1\n\ts_cbranch_scc0 .Lp%=\n.Lf%=:"
                             : "=&v"(fv), "=&s"(c_) : "v"(fa), "n"(c > NM1 ? 32 : 0), "n"(c) : "memory", "vcc", "scc");
            }
            SB0();
        };
        auto kv_part = [&]() AI {
            bf16x8 ones;
#pragma unroll
            for (int j = 0; j < 8; ++j) ones[j] = (short)0x3f80;
            unsigned pa = lds0 + (hs ? P_U[2] : P_U[0]) * 8192 + (4 * g + q) * 128 + (((8 * (rg & 1) + p) ^ fsw(4 * g + q)) << 3);
            unsigned pb = pa ^ 32, va = lds0 + L_A + hs * 2048 + l16, kv_st = WS_KV + ((hp * 8 + rg * 2) * 16 + mt) * 1024 + l16;
            unsigned kv_s1 = kv_st + 16384, z_st = WS_Z + ((hp * 4 + rg) * 16 + mt) * 128 + (l & 1) * 64 + g * 16, fk = WR + L_FKS;
            asm volatile("" : "+v"(pa), "+v"(pb), "+v"(va), "+v"(kv_st), "+v"(kv_s1), "+v"(z_st), "+v"(fk), "+v"(ones));
            bf16x8 b0[4], b1[4], af[2][4];
            lds_wait0(lds0 + L_FV + sch * 4);
#pragma unroll
            for (int i = 0; i < 4; ++i) {
                af[0][i] = cat8(tr_read(pa + i * 4096), tr_read(pa + i * 4096 + 2048));
                b0[i] = lds_read128(va + i * 4096);
                SB0();
                af[1][i] = cat8(tr_read(pb + i * 4096), tr_read(pb + i * 4096 + 2048));
                SB0();
            }
#pragma unroll
            for (int i = 0; i < 4; ++i) b1[i] = lds_read128(va + i * 4096 + 1024);
            SB0();
            f32x4 k0[2] = {zero, zero}, k1[2] = {zero, zero}, zt[2] = {zero, zero};
#pragma unroll
            for (int i = 0; i < 4; ++i)
#pragma unroll
                for (int e = 0; e < 2; ++e) {
                    k0[e] = mfma16(af[e][i], b0[i], k0[e]);
                    zt[e] = mfma16(af[e][i], ones, zt[e]);
                }
            SB0();
#pragma unroll
            for (int i = 0; i < 4; ++i)
#pragma unroll
                for (int e = 0; e < 2; ++e) k1[e] = mfma16(af[e][i], b1[i], k1[e]);
            st_wt(rsrc, kv_st, nz4(__builtin_bit_cast(u32x4, pack44(k0[0], k0[1]))));
            const u32x4 zo = nz4((l & 1) ? zt[1] : zt[0]);
            ST_MASK("global_store_dwordx4", 0x0003000300030003ull, z_st, zo, ws);
            st_wt(rsrc, kv_s1, nz4(__builtin_bit_cast(u32x4, pack44(k1[0], k1[1]))));
            SB0();
            flag(fk);
        };
        auto report = [&](int r) AI {
            unsigned t, a;
            if (r > RLD) asm volatile("v_mov_b32 %0, %3\n\tv_mov_b32 %1, %2\n\tds_write_b32 %1, %0" : "=&v"(t), "=&v"(a) : "s"(WR + L_PRL), "n"(r) : "memory");
            else asm volatile("global_load_lds_dword %0, %1 offset:%2" ::"v"(v0), "s"(one), "n"(4 * r) : "memory");
        };
        auto rd1 = [&](auto F, auto H) AI {
            constexpr int f = F, h = H, n = f >> 2, j = f >> 3, c = h ? E_B[f] : E_A[f], a = c % 100;
            if constexpr (h == 0 && (f & 3) == 0 && Q_C[n] > 0) chk(Q_C[n]);
            wh[f % (PD + 1)][h] = tr_read(ra[f & 3] + (J_U[j] - J_B[j]) * 8192 + (n & 1) * 4096 + h * 512);
            SB0();
            if constexpr (c / 100 == 1) {
                constexpr int jn = (f + 3 - a) >> 3;
                asm volatile("v_add_u32 %0, %1, %0" : "+v"(ra[a]) : "n"((J_B[jn] - J_B[jn - 1]) * 8192));
            } else if constexpr (c / 50 == 4) {
                asm volatile("v_add_u32 %0, %1, %2" : "=v"(xt[0]) : "s"((hs ? X_U1[a] : X_U0[a]) * 8192), "v"(xa));
            } else if constexpr (c / 50 == 5) {
                asm volatile("v_xor_b32 %0, 64, %1" : "=v"(xt[1]) : "v"(xt[0]));
            } else if constexpr (c / 100 == 3) {
                xf[a % 25 % ND][a / 25 & 1][a / 50] = lds_read128(xt[a / 50] + (a / 25 & 1) * 2048);
            } else if constexpr (c / 100 == 4) {
                fv = *(volatile AS3 unsigned*)(size_t)(fa + (a > NM1 ? 32 : 0));
            } else if constexpr (c / 100 == 5) {
                bad = __builtin_amdgcn_ballot_w64(fv < (unsigned)a);
                asm volatile("" : "+s"(bad));
            } else if constexpr (c / 100 == 6) {
                report(a);
            }
        };
        sfor<0, PD>([&](auto F) { rd1(F, IC<0>{}); rd1(F, IC<1>{}); });
        constexpr int K0F = 4 * KD, K1F = 4 * KD + 1;
        sfor<0, 96>([&](auto N) {
            sfor<0, 4>([&](auto E) {
                constexpr int n = N, e = E, f = 4 * n + e, j = n >> 1, ks = n & 1, u = e, s = J_X[j], m = J_M[j];
                const bf16x8 wv = cat8(wh[f % (PD + 1)][0], wh[f % (PD + 1)][1]);
                sfor<0, 2>([&](auto RB) {
                    constexpr int rb = RB;
                    const bf16x8 xv = xf[s % ND][rb][ks];
                    if (j_first(j) && ks == 0) asm volatile("v_mfma_f32_16x16x32_bf16 %0, %1, %2, 0" : "=&v"(acc[m][u][rb]) : "v"(m == 1 ? xv : wv), "v"(m == 1 ? wv : xv));
                    else asm volatile("v_mfma_f32_16x16x32_bf16 %0, %1, %2, %0" : "+v"(acc[m][u][rb]) : "v"(m == 1 ? xv : wv), "v"(m == 1 ? wv : xv));
                    SB0();
                    if constexpr (f + PD < 384 && (f + PD < 4 * VD || f >= 4 * VD)) rd1(IC<f + PD>{}, RB);
                    if constexpr (rb == 0 && f == K0F) relane();
                    if constexpr (rb == 0 && (f == K0F || f == K1F)) asm volatile("s_nop 7");
                    SB0();
                    if constexpr (f == K0F) lds_write<bf16x8>(AX + (P_U[0] * 8192 - L_A) + rb * 1024, pack44(acc[0][0][rb], acc[0][1][rb]));
                    if constexpr (f == K1F) {
                        kown[rb] = pack44(acc[0][2][rb], acc[0][3][rb]);
                        asm volatile("" : "+v"(kown[rb]));
                    }
                    SB0();
                });
                if constexpr (f == K0F) flag(WR + L_FK0);
                if constexpr (f == 4 * VD - 1) {
                    bf16x8 ak[2];
                    f32x4 pk[2][2];
                    unsigned pw[4];
                    __builtin_amdgcn_s_setprio(2);
                    phi_w(ak, 1);
                    asm volatile("s_nop 7");
                    SB0();
#pragma unroll
                    for (int i = 0; i < 4; ++i) lds_write<bf16x8>(AX + i * 1024, pack44(acc[1][i][0], acc[1][i][1]));
                    SB0();
#pragma unroll
                    for (int i = 0; i < 4; ++i) pk[i >> 1][i & 1] = rbf4(mfma16(ak[i & 1], kown[i >> 1], zero));
                    phi_adr(pw, lds0 + P_U[2] * 8192);
                    phi_k(pk, pw, 0, []() {});
                    SB0();
                    flag(WR + L_FV);
                    kv_part();
                    __builtin_amdgcn_s_setprio(1);
                    fv = 0;
                    bad = ~0ull;
                    sfor<4 * VD, 4 * VD + PD>([&](auto F) { rd1(F, IC<0>{}); rd1(F, IC<1>{}); });
                }
                if constexpr (f >= 24 && f < 56 && (f & 7) == 7) __builtin_amdgcn_s_sleep(5);
            });
        });
        asm volatile("s_nop 7");
        SB0();
        __builtin_amdgcn_s_setprio(0);
        flag(WR + L_FQD);
        bf16x8 a0[2];
        f32x4 pq[2][2];
        phi_w(a0, hs ^ 1);
        lds_wait(W4 + L_FKS, 1);
#pragma unroll
        for (int rb = 0; rb < 2; ++rb) {
            const bf16x8 b0 = pack44(acc[2][0][rb], acc[2][1][rb]), b1 = pack44(acc[2][2][rb], acc[2][3][rb]);
            lds_write<bf16x8>(AX + rb * 1024, hs ? b1 : b0);
#pragma unroll
            for (int fb = 0; fb < 2; ++fb) pq[rb][fb] = mfma16(a0[fb], hs ? b0 : b1, zero);
        }
        SB0();
        flag(WR + L_FQ);
        sq = lds_read<u32x4>(lds0 + L_SEL);
#pragma unroll
        for (int i = 0; i < 4; ++i) pq[i >> 1][i & 1] = rbf4(pq[i >> 1][i & 1]);
        softmax2(pq, ph, []() {});
        asm volatile("" : "+v"(ph[0][0]), "+v"(ph[0][1]), "+v"(ph[1][0]), "+v"(ph[1][1]));
        asm volatile("s_mov_b32 m0, %3\n\ts_nop 0\n\tglobal_load_lds_dwordx4 %0, %1\n\tglobal_load_lds_dwordx4 %0, %2 offset:1024"
                     ::"v"((unsigned)(srow * 2048 + (sch ^ srow) * 16)), "s"(x + (size_t)row0 * DM + cb * 64),
                       "s"(x + (size_t)(row0 + 8) * DM + cb * 64 - 512), "s"(lds0 + L_A + 2048 + rg * 4096) : "memory");
        g_load();
        asm volatile("" : "=v"(xr[0]), "=v"(xr[1]));
        asm volatile("" : "=v"(a0_[0]), "=v"(a0_[1]), "=v"(a0_[2]), "=v"(a0_[3]), "=v"(og));
        kf_def();
        fd_def();
    }
    relane();
    const char* cla = ws + WS_CL + ((rt * 2 + kp) * 16 + l15) * 128;
    const u64 T = (u64)sq[1] << 32 | sq[0];
    u64 cl;
    unsigned ka0 = lds0 + L_KF + oh * 4096 + l16, ka1 = lds0 + L_KZ + oh * 128 + g * 16;
    unsigned o_st = WS_O + ((mt * 2 + hs) * 32 + 2 * hp + oh) * 4096 + (rg & 1) * 2048 + l16;
    float gt[2];
#pragma unroll
    for (int i = 0; i < 2; ++i) gt[i] = lds_read<float>(lds0 + L_G + hs * 512 + (rg & 1) * 128 + i * 64 + l15 * 4);
    asm volatile("" : "+v"(ka0), "+v"(ka1), "+v"(o_st), "+v"(cla), "+v"(gt[0]), "+v"(gt[1]));
    if ((w & 5) != 4) {
        lds_wait0(lds0 + L_FKF + oh * 4);
#pragma unroll
        for (int i = 0; i < 2; ++i) kf[4 + i] = lds_read<u32x4>(ka1 + i * 64);
        SB0();
#pragma unroll
        for (int i = 0; i < 4; ++i) kf[i] = lds_read<u32x4>(ka0 + i * 1024);
    }
    asm volatile("global_load_dwordx2 %0, %1, off sc1" : "=&v"(cl) : "v"(cla) : "memory");
    SB0();
    o_store(kf, ph, gt, rsrc, o_st);
    flag(lds0 + L_FOS + w * 4);
    waitv<3>();
    __builtin_amdgcn_s_setprio(0);
    if ((w & 5) == 4) {
        lds_wait(lds0 + L_FOS + sch * 4, 1);
        wo_load(WON, 16, true);
    }
    if ((w & 5) == 5) {
        sel_init();
        down();
    }
    flag(lds0 + L_FWO + w * 4);
    const u32x2 selv = {sq[2], sq[3]};

    const char* ob = ws + WS_O + (rt * 2 + (rg >> 1)) * 131072 + (rg & 1) * 2048;
    const unsigned ov0 = l16 + kp * 1024, ov1 = l16 + (kp ^ 1) * 1024;
    auto oa = [&](int i) { return ob + ((2 * i + kp + rot) & 31) * 4096; };
    u32x4 of[3][4];
    auto fetch1 = [&](int k, int j) AI { ld_async(of[k % 3][j], (j & 1) ? ov1 : ov0, oa(2 * k + (j >> 1))); };
    auto fix = [&](int k) AI { settle(of[k % 3], [&](int j) { return (j & 1) ? ov1 : ov0; }, [&](int j) { return oa(2 * k + (j >> 1)); }); };
    const bool own = (__builtin_amdgcn_readfirstlane(selv[0]) >> 4) == (unsigned)(rt * 8 + rg * 2 + kp);
    if (!kp) {
        x_init(true);
        __builtin_amdgcn_s_sleep(PDL);
        waitv<2>();
    } else {
        __builtin_amdgcn_s_sleep(PD1);
        if (w & 1) waitv<4>();
        else waitv<3 + 4 * (16 - WON)>();
    }
    f32x4 acc[2][4];
#pragma unroll
    for (int nb = 0; nb < 4; ++nb) acc[0][nb] = a0_[nb];
    const float ys = __uint_as_float(__builtin_amdgcn_readlane(__float_as_uint(og), __builtin_amdgcn_readfirstlane(selv[0]) & 15));
    asm volatile("" : "+v"(cl));
#pragma unroll 1
    for (unsigned s = 0; s < SPIN_CAP && __ballot(cl != T); ++s)
        asm volatile("global_load_dwordx2 %0, %1, off sc1\n\ts_waitcnt vmcnt(0)" : "=&v"(cl) : "v"(cla) : "memory");
#pragma unroll
    for (int j = 0; j < 4; ++j) fetch1(0, j);
    unsigned wa[2][4];
#pragma unroll
    for (int i = 0; i < 8; ++i) {
        wa[i >> 2][i & 3] = ((lds0 + 4096 * kp + (4 * g + q) * 128 + ((((g & 1) << 1) | (q >> 1)) * 32) + 8 * p) ^ ((i & 3) << 5)) + (i >> 2) * 65536;
        asm volatile("" : "+v"(wa[i >> 2][i & 3]));
    }
    s16x4 wf[2][8][2];
    auto wrd = [&](int k, int i) AI {
        const int u = W_U[2 * k + (i >> 3)];
        wf[k & 1][i >> 1][i & 1] = tr_read(wa[u >> 3][(i >> 1) & 3] + (u & 7) * 8192 + (i & 1) * 2048);
    };
    u32x4 yp[14];
    const unsigned yo = srow * 4096 + cb * 256 + sch * 32;
    auto ya = [&](int i) { return ws + WS_Y + (i >> 1) * 32768 + (i & 1) * 16; };
    lds_wait(lds0 + L_FWD + (l & 1) * 4, 1);
#pragma unroll
    for (int i = 0; i < 16; ++i) wrd(0, i);
#pragma unroll
    for (int j = 0; j < 4; ++j) fetch1(1, j);
    const unsigned ea = lds0 + L_A + l16 + 4096 * w, eb = lds0 + L_A + 4096 * (w ^ 4);
    unsigned er = eb + l16, et = eb + srow * 128 + ((sch ^ srow) * 16), ew[4];
#pragma unroll
    for (int nb = 0; nb < 4; ++nb) ew[nb] = eb + l15 * 128 + (((2 * nb + (g >> 1)) ^ (l & 7)) * 16) + (g & 1) * 8;
    u16 *o0 = out + (size_t)row0 * DM + cb * 64, *o1 = o0 + 8 * DM;
    asm volatile("" : "+v"(er), "+v"(et), "+v"(ew[0]), "+v"(ew[1]), "+v"(ew[2]), "+v"(ew[3]), "+s"(o0), "+s"(o1));
    waitv<4>();
    fix(0);
    sfor<0, 8>([&](auto K) {
        constexpr int k = K;
        unsigned m[4];
        if constexpr (2 * (k + 1) == WON) lds_wait(lds0 + ((l & 8) ? L_FWO + sch * 4 : L_FW2 + (l & 1) * 4), 1);
        if constexpr (k == 7) {
            if (own) {
#pragma unroll
                for (int i = 0; i < 14; ++i) ld_async(yp[i], yo, ya(i));
            }
        }
        SB0();
        sfor<0, 16>([&](auto I) {
            constexpr int i = I, j = k == 7 ? (0x2031 >> (i & 12)) & 3 : i >> 2, nb = i & 3;
            const bf16x8 a = cat8(wf[k & 1][(j >> 1) * 4 + nb][0], wf[k & 1][(j >> 1) * 4 + nb][1]);
            if (k == 0 && j == 1) asm volatile("v_mfma_f32_16x16x32_bf16 %0, %1, %2, 0" : "=&v"(acc[j & 1][nb]) : "v"(a), "v"(of[k % 3][j]));
            else asm volatile("v_mfma_f32_16x16x32_bf16 %0, %1, %2, %0" : "+v"(acc[j & 1][nb]) : "v"(a), "v"(of[k % 3][j]));
            SB0();
            if constexpr (k == 7 && i >= 12) {
                if constexpr (i == 12) lds_write<u32x4>(ea, nz4(acc[1][0]));
                else lds_write<f32x4>(ea + (i - 12) * 1024, acc[1][i - 12]);
            }
            if constexpr (k < 7) wrd(k + 1, i);
            if constexpr (k < 6 && i < 4) fetch1(k + 2, i);
            if constexpr (k < 7 && i == 10) {
                waitv<(k < 6 ? 4 : 0)>();
#pragma unroll
                for (int n = 0; n < 4; ++n) pin(of[(k + 1) % 3][n]);
            }
            if constexpr (k < 7 && i > 10 && i <= 14) {
                m[i - 11] = min4(of[(k + 1) % 3][i - 11]);
                asm volatile("" : "+v"(m[i - 11]));
            }
            SB0();
        });
        if constexpr (k < 7) {
            if (__builtin_expect(__builtin_amdgcn_ballot_w64(min(min(m[0], m[1]), min(m[2], m[3])) == 0u) != 0ull, 0)) fix(k + 1);
        }
        SB0();
    });
    asm volatile("s_nop 7");
    SB0();
    BAR();
    f32x4 pa[4];
#pragma unroll
    for (int nb = 0; nb < 4; ++nb) pa[nb] = lds_read<f32x4>(er + nb * 1024);
    SB0();
#pragma unroll
    for (int nb = 0; nb < 4; ++nb) acc[0][nb] += pa[nb];
#pragma unroll
    for (int nb = 0; nb < 4; ++nb) asm volatile("" : "+v"(acc[0][nb]));
#pragma unroll
    for (int nb = 0; nb < 4; ++nb) lds_write<u32x2>(ew[nb], (u32x2){pack2(acc[0][nb][0], acc[0][nb][1]), pack2(acc[0][nb][2], acc[0][nb][3])});
    u32x4 av[2];
#pragma unroll
    for (int j = 0; j < 2; ++j) av[j] = lds_read<u32x4>(et + j * 1024);
    SB0();
    if (own) {
        waitv<0>();
        settle(yp, [&](int) { return yo; }, ya);
        f32x4 yv[2];
#pragma unroll
        for (int h = 0; h < 2; ++h) {
            yv[h] = __builtin_bit_cast(f32x4, yp[h]);
#pragma unroll
            for (int i = 1; i < 7; ++i) yv[h] += __builtin_bit_cast(f32x4, yp[2 * i + h]);
#pragma unroll
            for (int i = 0; i < 4; ++i) {
                float t = dpp_add<0x128>(yv[h][i]);
                t = swap16_add(t, t);
                yv[h][i] = rbf(rbf(swap32_add(t, t)) * __uint_as_float(selv[1])) * ys;
            }
        }
#pragma unroll
        for (int j = 0; j < 2; ++j) {
            const bool hit = row0 + 8 * j + srow == (int)selv[0];
#pragma unroll
            for (int i = 0; i < 4; ++i) {
                const unsigned t = pack2(lo16(av[j][i]) + yv[i >> 1][2 * (i & 1)], hi16(av[j][i]) + yv[i >> 1][2 * (i & 1) + 1]);
                av[j][i] = hit ? t : av[j][i];
            }
        }
    }
#pragma unroll
    for (int j = 0; j < 2; ++j) asm volatile("global_store_dwordx4 %0, %1, %2 sc1\n\ts_nop 1" ::"v"(xo), "v"(av[j]), "s"(j ? o1 : o0) : "memory");
}

extern "C" void kernel_launch(void* const* d_in, const int* in_sizes, int n_in, void* d_out, int out_size,
                              void* d_ws, size_t ws_size, hipStream_t stream) {
    fused_mix<<<256, 512, 0, stream>>>((const u16*)d_in[0], (const u16*)d_in[1], (const u16*)d_in[2], (const u16*)d_in[3],
                                       (const u16*)d_in[5], (const u16*)d_in[6], (const u16*)d_in[7], (const u16*)d_in[8],
                                       (const u16*)d_in[9], (char*)d_ws, (u16*)d_out);
}
```

```cpp
#include <hip/hip_runtime.h>
#include <hip/hip_bf16.h>
#include <stdint.h>
#include <type_traits>

typedef __attribute__((ext_vector_type(8))) short bf16x8;
typedef __attribute__((ext_vector_type(4))) short s16x4;
typedef __attribute__((ext_vector_type(4))) float f32x4;
typedef __attribute__((ext_vector_type(2))) float f32x2;
typedef __attribute__((ext_vector_type(2))) unsigned u32x2;
typedef __attribute__((ext_vector_type(4))) unsigned u32x4;
typedef __attribute__((ext_vector_type(2))) __bf16 bf16x2_t;
typedef unsigned short u16;
typedef unsigned long long u64;

#define DI __device__ __forceinline__
#define AS3 __attribute__((address_space(3)))

constexpr int DM = 1024;

DI float bf2f(u16 v) { return __uint_as_float(((unsigned)v) << 16); }
DI u16 f2bf(float f) { __bf16 b = (__bf16)f; return __builtin_bit_cast(u16, b); }
DI float rbf(float f) { return bf2f(f2bf(f)); }
DI unsigned pack2(float lo, float hi) {
    const f32x2 v = {lo, hi};
    return __builtin_bit_cast(unsigned, __builtin_convertvector(v, bf16x2_t));
}
DI float lo16(unsigned u) { return __uint_as_float(u << 16); }
DI float hi16(unsigned u) { return __uint_as_float(u & 0xffff0000u); }

DI s16x4 tr_read(unsigned a) { return __builtin_amdgcn_ds_read_tr16_b64_v4i16((AS3 s16x4*)(size_t)a); }
DI bf16x8 lds_read128(unsigned a) { return *(AS3 const bf16x8*)(size_t)a; }
template <class T> DI void lds_write(unsigned a, T v) { *(AS3 T*)(size_t)a = v; }
template <class T> DI T lds_read(unsigned a) { return *(AS3 const T*)(size_t)a; }
DI bf16x8 cat8(s16x4 a, s16x4 b) { return __builtin_shufflevector(a, b, 0, 1, 2, 3, 4, 5, 6, 7); }
DI f32x4 mfma16(bf16x8 a, bf16x8 b, f32x4 c) { return __builtin_amdgcn_mfma_f32_16x16x32_bf16(a, b, c, 0, 0, 0); }
DI bf16x8 pack44(const f32x4& a, const f32x4& b) {
    u32x4 r;
    r[0] = pack2(a[0], a[1]);
    r[1] = pack2(a[2], a[3]);
    r[2] = pack2(b[0], b[1]);
    r[3] = pack2(b[2], b[3]);
    return __builtin_bit_cast(bf16x8, r);
}
DI unsigned lds_of(const void* p) { return (unsigned)(size_t)(AS3 const char*)p; }

template <int CTRL>
DI unsigned dpp(unsigned x) { return __builtin_amdgcn_update_dpp(0u, x, CTRL, 0xf, 0xf, true); }
template <int CTRL>
DI float dpp_add(float x) { return x + __uint_as_float(dpp<CTRL>(__float_as_uint(x))); }
DI float swap32_add(float a, float b) {
    auto r = __builtin_amdgcn_permlane32_swap(__float_as_uint(a), __float_as_uint(b), false, false);
    return __uint_as_float(r[0]) + __uint_as_float(r[1]);
}
DI float swap16_add(float a, float b) {
    auto r = __builtin_amdgcn_permlane16_swap(__float_as_uint(a), __float_as_uint(b), false, false);
    return __uint_as_float(r[0]) + __uint_as_float(r[1]);
}
DI unsigned wave_umax(unsigned x) {
    x = max(x, dpp<0xB1>(x));
    x = max(x, dpp<0x4E>(x));
    x = max(x, dpp<0x141>(x));
    x = max(x, dpp<0x140>(x));
    auto r = __builtin_amdgcn_permlane16_swap(x, x, false, false);
    x = max(r[0], r[1]);
    auto s = __builtin_amdgcn_permlane32_swap(x, x, false, false);
    return max(s[0], s[1]);
}
constexpr unsigned WS_E = 0;
constexpr unsigned WS_RG = 256;
constexpr unsigned WS_GG = WS_RG + 2048;
constexpr unsigned WS_Z = 65536;
constexpr unsigned WS_KF = WS_Z + 131072;
constexpr unsigned WS_GU = WS_KF + 262144;
constexpr unsigned WS_Y = WS_GU + 262144;
constexpr unsigned WS_KV = WS_Y + 262144;
constexpr unsigned WS_O = WS_KV + (2u << 20);
constexpr unsigned WS_CL = WS_O + (4u << 20);
constexpr unsigned WS_END = WS_CL + 65536;

constexpr int NQ = 9, ND = 7, NX = 16, KD = 76, VD = 82, NF = 64, NF1 = 58, NF2 = 58, NP1 = 14, NP = 16, NM1 = 23, NM2 = 23, NM = 24, KFN = 100;
constexpr int J_S[48] = {0, 0, 0, 1, 1, 1, 2, 2, 2, 3, 3, 3, 4, 4, 4, 5, 5, 5, 6, 6, 6, 7, 7, 7, 8, 8, 8, 9, 9, 10, 10, 11, 11, 12, 12, 13, 14, 15, 13, 14, 15, 9, 10, 11, 12, 13, 14, 15};
constexpr int J_M[48] = {0, 1, 2, 0, 1, 2, 0, 1, 2, 0, 1, 2, 0, 1, 2, 0, 1, 2, 0, 1, 2, 0, 1, 2, 0, 1, 2, 0, 1, 0, 1, 0, 1, 0, 1, 0, 0, 0, 1, 1, 1, 2, 2, 2, 2, 2, 2, 2};
constexpr int J_X[48] = {0, 0, 0, 1, 1, 1, 2, 2, 2, 3, 3, 3, 4, 4, 4, 5, 5, 5, 6, 6, 6, 7, 7, 7, 8, 8, 8, 9, 9, 10, 10, 11, 11, 12, 12, 13, 14, 15, 13, 14, 15, 9, 10, 11, 12, 13, 14, 15};
constexpr int J_U[48] = {2, 6, 5, 0, 1, 15, 17, 14, 12, 4, 10, 9, 6, 5, 3, 18, 15, 16, 13, 12, 11, 7, 9, 8, 2, 3, 18, 15, 16, 13, 12, 10, 7, 6, 5, 18, 15, 14, 13, 12, 11, 4, 10, 7, 9, 8, 6, 5};
constexpr int J_B[48] = {0, 0, 0, 0, 0, 12, 12, 12, 12, 3, 3, 3, 3, 3, 3, 11, 11, 11, 11, 11, 11, 2, 2, 2, 2, 2, 12, 12, 12, 12, 12, 5, 5, 5, 5, 11, 11, 11, 11, 11, 11, 4, 4, 4, 4, 4, 4, 4};
constexpr int Q_C[96] = {1, 0, 2, 0, 3, 0, 4, 0, 5, 0, 6, 0, 7, 0, 0, 0, 0, 0, 8, 0, 0, 0, 9, 0, 0, 0, 0, 0, 10, 0, 0, 0, 0, 0, 0, 0, 11, 0, 0, 0, 0, 0, 12, 0, 0, 0, 0, 0, 13, 0, 0, 0, 0, 0, 14, 0, 0, 0, 15, 0, 16, 0, 0, 0, 0, 0, 17, 0, 0, 0, 0, 0, 18, 0, 19, 0, 20, 0, 21, 0, 22, 0, 23, 0, 24, 0, 0, 0, 0, 0, 0, 0, 0, 0, 0, 0};
constexpr int X_U0[16] = {9, 3, 16, 11, 8, 0, 17, 4, 6, 0, 17, 11, 9, 2, 0, 16};
constexpr int X_U1[16] = {8, 18, 13, 7, 2, 1, 14, 10, 5, 1, 14, 4, 8, 3, 1, 17};
constexpr int P_U[4] = {0, 1, 2, 3};
constexpr int W_U[16] = {15, 13, 14, 12, 11, 2, 3, 0, 1, 7, 4, 10, 9, 8, 6, 5};
constexpr int W_N[16] = {100, 100, 100, 100, 100, 101, 101, 101, 101, 15, 15, 15, 16, 16, 99, 99};
constexpr int F_J[64] = {-1, 0, 1, 2, -2, 3, 4, 5, -3, 6, 7, 8, -4, 9, 10, 11, -5, 12, 13, 14, 15, -6, 16, 17, 18, -7, 19, 20, 21, -8, 22, 23, 24, -9, 25, 26, -10, 27, 28, -11, 29, 30, -12, 31, 32, -13, 33, 34, -14, 35, -15, 36, -16, 37, 38, 39, 40, 41, 42, 43, 44, 45, 46, 47};
constexpr int F_N[64] = {0, 0, 0, 0, 0, 0, 0, 0, 0, 0, 0, 0, 0, 0, 0, 1, 1, 1, 1, 1, 1, 2, 2, 2, 2, 3, 3, 3, 3, 4, 4, 4, 4, 5, 5, 5, 5, 6, 6, 6, 7, 7, 8, 8, 9, 9, 9, 9, 10, 10, 10, 11, 12, 12, 13, 13, 13, 13, 13, 14, 14, 14, 14, 14};
constexpr int F_K[64] = {0, 1, 2, 3, 0, 4, 5, 6, 0, 0, 0, 7, 0, 0, 8, 0, 0, 0, 9, 0, 0, 0, 0, 10, 0, 0, 0, 11, 0, 0, 0, 12, 0, 0, 0, 13, 0, 0, 14, 0, 15, 0, 0, 0, 16, 0, 0, 0, 0, 17, 0, 18, 0, 19, 20, 21, 22, 23, 0, 0, 0, 0, 0, 24};
constexpr int E_A[384] = {300, 325, 350, 375, 0, 0, 0, 502, 0, 0, 0, 403, 0, 0, 0, 503, 201, 251, 0, 404, 0, 0, 0, 504, 301, 326, 351, 376, 0, 0, 0, 505, 601, 0, 0, 406, 0, 100, 101, 506, 102, 103, 202, 407, 252, 0, 0, 507, 302, 327, 352, 377, 0, 0, 0, 602, 0, 0, 0, 0, 0, 0, 0, 203, 253, 0, 0, 408, 0, 100, 101, 508, 303, 328, 353, 378, 0, 0, 0, 603, 0, 0, 0, 409, 0, 0, 0, 509, 204, 254, 0, 0, 0, 0, 0, 0, 304, 329, 354, 379, 0, 0, 0, 604, 0, 0, 0, 410, 0, 0, 0, 510, 205, 255, 0, 0, 0, 100, 101, 102, 305, 330, 355, 380, 0, 0, 0, 0, 0, 0, 0, 605, 0, 0, 0, 206, 256, 0, 0, 411, 0, 0, 0, 511, 306, 331, 356, 381, 0, 0, 0, 606, 0, 0, 0, 0, 0, 0, 0, 207, 257, 0, 0, 412, 607, 100, 101, 512, 307, 332, 357, 382, 0, 0, 0, 608, 0, 0, 0, 0, 0, 0, 0, 208, 258, 0, 0, 413, 0, 0, 0, 513, 308, 333, 358, 383, 0, 0, 0, 609, 0, 0, 0, 0, 0, 100, 101, 102, 103, 209, 259, 414, 0, 0, 0, 514, 309, 334, 359, 384, 0, 0, 0, 610, 210, 260, 0, 415, 0, 0, 0, 515, 310, 335, 360, 385, 0, 0, 0, 516, 612, 211, 261, 0, 0, 100, 101, 102, 311, 336, 361, 386, 0, 0, 0, 212, 262, 0, 0, 417, 613, 0, 0, 517, 312, 337, 362, 387, 0, 0, 0, 213, 263, 0, 0, 0, 0, 100, 101, 102, 313, 338, 363, 388, 214, 264, 0, 518, 314, 339, 364, 389, 215, 265, 0, 519, 315, 340, 365, 390, 0, 0, 0, 520, 0, 0, 0, 421, 0, 0, 0, 521, 0, 0, 0, 422, 0, 0, 0, 522, 0, 0, 0, 0, 0, 100, 101, 102, 103, 0, 0, 424, 0, 0, 0, 524, 0, 0, 0, 0, 0, 0, 0, 0, 0, 0, 0, 0, 0, 0, 0, 0, 0, 0, 0, 615, 0, 0, 0, 0, 0, 0, 0, 0, 0, 0, 0, 0, 0, 0, 0, 616, 0, 0, 0, 0, 0, 0, 0, 0, 0, 0, 0, 0};
constexpr int E_B[384] = {0, 0, 402, 0, 0, 0, 0, 0, 0, 0, 0, 0, 0, 0, 0, 0, 0, 0, 0, 0, 0, 0, 0, 0, 0, 0, 405, 0, 0, 0, 0, 0, 0, 0, 0, 0, 0, 0, 0, 0, 0, 0, 0, 0, 0, 0, 0, 0, 0, 0, 0, 0, 0, 0, 0, 0, 0, 0, 0, 0, 0, 0, 0, 0, 0, 0, 0, 0, 0, 0, 0, 102, 103, 0, 0, 0, 0, 0, 0, 0, 0, 0, 0, 0, 0, 0, 0, 0, 0, 0, 0, 0, 0, 0, 0, 0, 0, 0, 0, 0, 0, 0, 0, 0, 0, 0, 0, 0, 0, 0, 0, 0, 0, 0, 0, 0, 0, 0, 0, 0, 103, 0, 0, 0, 0, 0, 0, 0, 0, 0, 0, 0, 0, 0, 0, 0, 0, 0, 0, 0, 0, 0, 0, 0, 0, 0, 0, 0, 0, 0, 0, 0, 0, 0, 0, 0, 0, 0, 0, 0, 0, 0, 0, 0, 0, 0, 0, 102, 103, 0, 0, 0, 0, 0, 0, 0, 0, 0, 0, 0, 0, 0, 0, 0, 0, 0, 0, 0, 0, 0, 0, 0, 0, 0, 0, 0, 0, 0, 0, 0, 0, 0, 0, 0, 0, 0, 0, 0, 0, 0, 0, 0, 0, 0, 0, 0, 0, 0, 0, 0, 0, 0, 0, 0, 0, 0, 0, 0, 0, 0, 0, 611, 0, 0, 416, 0, 0, 0, 0, 0, 0, 0, 0, 0, 0, 0, 0, 0, 103, 0, 0, 0, 0, 0, 0, 0, 0, 0, 0, 0, 0, 0, 0, 0, 0, 0, 0, 0, 0, 0, 0, 0, 0, 0, 0, 0, 0, 0, 0, 0, 103, 0, 418, 0, 0, 0, 0, 614, 0, 0, 419, 0, 0, 0, 0, 0, 0, 0, 420, 0, 0, 0, 0, 0, 0, 0, 0, 0, 0, 0, 0, 0, 0, 0, 0, 0, 0, 0, 0, 0, 0, 0, 0, 0, 0, 0, 0, 0, 0, 0, 0, 0, 0, 0, 0, 0, 0, 0, 0, 0, 0, 0, 0, 0, 0, 0, 0, 0, 0, 0, 0, 0, 0, 0, 0, 0, 0, 0, 0, 0, 0, 0, 0, 0, 0, 0, 0, 0, 0, 0, 0, 0, 0, 0, 0, 0, 0, 0, 0, 0, 0, 0, 0, 0};
constexpr unsigned L_A = 16 * 8192;
constexpr unsigned L_KF = 18 * 8192;
constexpr unsigned L_SM = 19 * 8192;
constexpr unsigned L_FULL = L_SM + 256;
constexpr unsigned L_PROG = L_SM + 896;
constexpr unsigned L_PRL = L_SM + 336;
constexpr unsigned L_FW2 = L_SM + 512, L_FOS = L_SM + 480, L_FK0 = L_SM + 752, L_FV = L_SM + 560, L_FP = L_SM + 576, L_FKS = L_SM + 592;
constexpr unsigned L_FQD = L_SM + 608, L_FG = L_SM + 624, L_FKF = L_SM + 736, L_FQ = L_SM + 720, L_FWD = L_SM + 640, L_FWO = L_SM + 656;
constexpr unsigned L_FLD = L_SM + 688, L_BEST = L_SM + 696;
constexpr unsigned L_SEL = L_SM + 704;
constexpr unsigned L_XS = L_SM + 2048, L_KZ = L_SM + 3072, L_WPHI = L_SM + 4096;
constexpr unsigned L_G = L_SM + 3328;
static_assert(NP <= 31 && NM <= 63 && NF1 == NF2 && NX == 16, "");
static_assert(P_U[1] == P_U[0] + 1 && P_U[3] == P_U[2] + 1, "feature maps of a head lie side by side");
constexpr bool j_first(int j) {
    for (int i = 0; i < j; ++i)
        if (J_M[i] == J_M[j]) return false;
    return true;
}
constexpr unsigned SPIN_CAP = 1u << 16;
#define N8(a) a, a + 1, a + 2, a + 3, a + 4, a + 5, a + 6, a + 7
__device__ const unsigned k_num[64] = {N8(0), N8(8), N8(16), N8(24), N8(32), N8(40), N8(48), N8(56)};

constexpr int PD = 4;
constexpr int PACE = 16;
constexpr int RLD = F_N[NF1 - 1];
constexpr int CLR0 = 2;
constexpr int GQ1 = 6;
constexpr int WOE = 2, WOM = 7, WON = 10;
constexpr int GDL = 20, KFD = 4, PDL = 9, PD1 = 2;
constexpr unsigned CPF = 55552;
constexpr u64 KXB = 0xFEDBA9C768534210ull;
#define BAR() asm volatile("s_waitcnt lgkmcnt(0)\n\ts_barrier" ::: "memory")
template <int N>
DI void waitv() { asm volatile("s_waitcnt vmcnt(%0)" ::"n"(N) : "memory"); }

DI unsigned nz1(unsigned v) { return max(v, 1u); }
DI u32x4 nz4(u32x4 v) { return (u32x4){max(v[0], 1u), max(v[1], 1u), max(v[2], 1u), max(v[3], 1u)}; }
DI u32x4 nz4(f32x4 v) { return nz4(__builtin_bit_cast(u32x4, v)); }
DI void st_wt(__amdgpu_buffer_rsrc_t r, unsigned off, u32x4 v) { __builtin_amdgcn_raw_buffer_store_b128(v, r, off, 0, 16); }
DI void ld_async(u32x4& v, unsigned off, const char* base) {
    asm volatile("global_load_dwordx4 %0, %1, %2 sc1" : "=&v"(v) : "v"(off), "s"(base) : "memory");
}
#define ST_MASK(op, m, off, v, base) asm volatile("s_mov_b64 exec, %3\n\t" op " %0, %1, %2 sc1\n\ts_mov_b64 exec, -1\n\ts_nop 0" ::"v"(off), "v"(v), "s"(base), "s"((u64)(m)) : "memory")
DI void pin(u32x4& v) { asm volatile("" : "+v"(v)); }
template <int N>
DI unsigned least(const u32x4 (&v)[N]) {
    unsigned a = min(v[0][0], v[0][1]), b = min(v[0][2], v[0][3]);
#pragma unroll
    for (int i = 1; i < N; ++i) a = min(min(a, v[i][0]), v[i][1]), b = min(min(b, v[i][2]), v[i][3]);
    return min(a, b);
}
template <int N, class OFF, class BASE>
DI void again(u32x4 (&v)[N], OFF off, BASE base) {
    unsigned s = SPIN_CAP;
    do {
#pragma unroll
        for (int i = 0; i < N; ++i) {
            unsigned t;
            asm volatile("v_min3_u32 %1, %4, %5, %6\n\tv_min_u32 %1, %1, %7\n\tv_cmp_eq_u32 vcc, 0, %1\n\ts_mov_b64 exec, vcc\n\t"
                         "global_load_dwordx4 %0, %2, %3 sc1\n\ts_mov_b64 exec, -1"
                         : "+v"(v[i]), "=&v"(t) : "v"(off(i)), "s"(base(i)), "v"(v[i][0]), "v"(v[i][1]), "v"(v[i][2]), "v"(v[i][3]) : "memory", "vcc");
        }
        waitv<0>();
#pragma unroll
        for (int i = 0; i < N; ++i) pin(v[i]);
    } while (__ballot(least(v) == 0u) != 0ull && --s);
}
template <int N, class OFF, class BASE>
DI void settle(u32x4 (&v)[N], OFF off, BASE base) {
#pragma unroll
    for (int i = 0; i < N; ++i) pin(v[i]);
    if (__builtin_expect(__ballot(least(v) == 0u) != 0ull, 0)) again(v, off, base);
}
DI void lds_wait(unsigned a, unsigned n) {
    unsigned v, c;
    asm volatile("s_mov_b32 %1, 0x400000\n"
                 ".Lp%=:\n\tds_read_b32 %0, %2\n\ts_waitcnt lgkmcnt(0)\n\tv_cmp_gt_u32 vcc, %3, %0\n\ts_cbranch_vccz .Lf%=\n\t"
                 "s_sleep 1\n\ts_sub_u32 %1, %1, 1\n\ts_cbranch_scc0 .Lp%=\n.Lf%=:"
                 : "=&v"(v), "=&s"(c) : "v"(a), "s"(n) : "memory", "vcc", "scc");
}
DI void lds_wait0(unsigned a) {
    unsigned v, c;
    asm volatile("s_mov_b32 %1, 0x1000000\n"
                 ".Lp%=:\n\tds_read_b32 %0, %2\n\ts_sub_u32 %1, %1, 1\n\ts_cbranch_scc1 .Lf%=\n\ts_waitcnt lgkmcnt(0)\n\tv_cmp_eq_u32 vcc, 0, %0\n\ts_cbranch_vccnz .Lp%=\n"
                 ".Lf%=:\n\ts_waitcnt lgkmcnt(0)"
                 : "=&v"(v), "=&s"(c) : "v"(a) : "memory", "vcc", "scc");
}
DI void lds_wait8(unsigned a, unsigned n) {
    unsigned v, c;
    asm volatile("s_mov_b32 %1, 0x100000\n"
                 ".Lp%=:\n\tds_read_b32 %0, %2\n\ts_waitcnt lgkmcnt(0)\n\tv_cmp_gt_u32 vcc, %3, %0\n\ts_cbranch_vccz .Lf%=\n\t"
                 "s_sleep 4\n\ts_sub_u32 %1, %1, 1\n\ts_cbranch_scc0 .Lp%=\n.Lf%=:"
                 : "=&v"(v), "=&s"(c) : "v"(a), "s"(n) : "memory", "vcc", "scc");
}
DI void lds_peek(unsigned& v, unsigned a) { asm volatile("ds_read_b32 %0, %1" : "=v"(v) : "v"(a) : "memory"); }
DI void lds_wait(unsigned& v, unsigned a, unsigned n) {
    unsigned c;
    asm volatile("s_mov_b32 %1, 0x400000\n"
                 ".Lp%=:\n\ts_waitcnt lgkmcnt(0)\n\tv_cmp_gt_u32 vcc, %3, %0\n\ts_cbranch_vccz .Lf%=\n\t"
                 "ds_read_b32 %0, %2\n\ts_sub_u32 %1, %1, 1\n\ts_cbranch_scc0 .Lp%=\n.Lf%=:"
                 : "+v"(v), "=&s"(c) : "v"(a), "s"(n) : "memory", "vcc", "scc");
}
DI float dot2(unsigned a, unsigned b, float c) {
    return __builtin_amdgcn_fdot2_f32_bf16(__builtin_bit_cast(bf16x2_t, a), __builtin_bit_cast(bf16x2_t, b), c, false);
}
DI float sum4(const float (&a)[4]) {
    float c = swap16_add(swap32_add(a[0], a[1]), swap32_add(a[2], a[3]));
    c = dpp_add<0xB1>(c);
    c = dpp_add<0x4E>(c);
    c = dpp_add<0x141>(c);
    return dpp_add<0x140>(c);
}

template <class OP, class POST>
DI void red4(float& a, float& b, float& c, float& d, OP op, POST post) {
    auto x = __builtin_amdgcn_permlane32_swap(__float_as_uint(a), __float_as_uint(b), false, false);
    auto y = __builtin_amdgcn_permlane32_swap(__float_as_uint(c), __float_as_uint(d), false, false);
    const float m = op(__uint_as_float(x[0]), __uint_as_float(x[1])), n = op(__uint_as_float(y[0]), __uint_as_float(y[1]));
    auto z = __builtin_amdgcn_permlane16_swap(__float_as_uint(m), __float_as_uint(n), false, false);
    const unsigned f = __float_as_uint(post(op(__uint_as_float(z[0]), __uint_as_float(z[1]))));
    auto u = __builtin_amdgcn_permlane16_swap(f, f, false, false);
    auto v = __builtin_amdgcn_permlane32_swap(u[0], u[0], false, false);
    auto t = __builtin_amdgcn_permlane32_swap(u[1], u[1], false, false);
    a = __uint_as_float(v[0]);
    b = __uint_as_float(v[1]);
    c = __uint_as_float(t[0]);
    d = __uint_as_float(t[1]);
}
DI float mx2(float a, float b) { return __builtin_elementwise_maximum(a, b); }
template <class HOOK>
DI void softmax2(const f32x4 (&v)[2][2], u32x4 (&ph)[2][2], HOOK hook) {
    constexpr float L2E = 1.4426950408889634f;
    float mx[2], mn[2], sp_[2], sn_[2];
#pragma unroll
    for (int b = 0; b < 2; ++b) {
        const f32x4 s = v[b][0], t = v[b][1];
        mx[b] = mx2(mx2(mx2(mx2(s[0], s[1]), s[2]), mx2(mx2(s[3], t[0]), t[1])), mx2(t[2], t[3]));
        mn[b] = mx2(mx2(mx2(mx2(-s[0], -s[1]), -s[2]), mx2(mx2(-s[3], -t[0]), -t[1])), mx2(-t[2], -t[3]));
    }
    red4(mx[0], mx[1], mn[0], mn[1], [](float a, float b) { return mx2(a, b); }, [](float a) { return a * -L2E; });
    f32x2 ep[2][4], en[2][4];
#pragma unroll
    for (int b = 0; b < 2; ++b) {
#pragma unroll
        for (int e = 0; e < 4; ++e) {
            const f32x2 u = {v[b][e >> 1][2 * (e & 1)], v[b][e >> 1][2 * (e & 1) + 1]};
            const f32x2 a = __builtin_elementwise_fma(u, (f32x2){L2E, L2E}, (f32x2){mx[b], mx[b]});
            const f32x2 c = __builtin_elementwise_fma(u, (f32x2){-L2E, -L2E}, (f32x2){mn[b], mn[b]});
            ep[b][e] = (f32x2){__builtin_amdgcn_exp2f(a[0]), __builtin_amdgcn_exp2f(a[1])};
            en[b][e] = (f32x2){__builtin_amdgcn_exp2f(c[0]), __builtin_amdgcn_exp2f(c[1])};
        }
        const f32x2 s = (ep[b][0] + ep[b][1]) + (ep[b][2] + ep[b][3]), t = (en[b][0] + en[b][1]) + (en[b][2] + en[b][3]);
        sp_[b] = s[0] + s[1];
        sn_[b] = t[0] + t[1];
    }
    asm volatile("" : "+v"(sp_[0]), "+v"(sn_[0]), "+v"(sp_[1]), "+v"(sn_[1]));
    hook();
    red4(sp_[0], sp_[1], sn_[0], sn_[1], [](float a, float b) { return a + b; }, [](float a) { return __builtin_amdgcn_rcpf(a); });
#pragma unroll
    for (int b = 0; b < 2; ++b)
#pragma unroll
        for (int e = 0; e < 4; ++e) {
            const f32x2 a = ep[b][e] * (f32x2){sp_[b], sp_[b]}, c = en[b][e] * (f32x2){sn_[b], sn_[b]};
            ph[b][0][e] = pack2(a[0], a[1]);
            ph[b][1][e] = pack2(c[0], c[1]);
        }
}
DI f32x4 rbf4(const f32x4& a) {
    const unsigned u = pack2(a[0], a[1]), v = pack2(a[2], a[3]);
    return (f32x4){lo16(u), hi16(u), lo16(v), hi16(v)};
}
DI int fsw(int row) { return (((row >> 1) & 3) << 2) | (((row >> 3) & 1) << 1) | (row & 1); }
template <class HOOK>
DI void phi_k(const f32x4 (&pp)[2][2], const unsigned (&pw)[4], unsigned off, HOOK hook) {
    u32x4 ph[2][2];
    softmax2(pp, ph, hook);
#pragma unroll
    for (int b = 0; b < 2; ++b)
#pragma unroll
        for (int kf = 0; kf < 2; ++kf)
#pragma unroll
            for (int fb = 0; fb < 2; ++fb) {
                const u32x2 a = {ph[b][kf][2 * fb], ph[b][kf][2 * fb + 1]};
                lds_write<u32x2>(pw[2 * kf + fb] + off + b * 2048, a);
            }
}

#define SB0() __builtin_amdgcn_sched_barrier(0)
template <int A, int B, class F>
DI void sfor(F f) {
    if constexpr (A < B) {
        f(std::integral_constant<int, A>{});
        sfor<A + 1, B>(f);
    }
}
template <int N> using IC = std::integral_constant<int, N>;
#define AI __attribute__((always_inline))

DI void o_store(const u32x4 (&kf)[6], const u32x4 (&ph)[2][2], const float (&gt)[2], __amdgpu_buffer_rsrc_t rsrc, unsigned o_st) {
    const f32x4 zero = {0.f, 0.f, 0.f, 0.f};
    auto b8 = [](const u32x4& a) { return __builtin_bit_cast(bf16x8, a); };
    f32x4 dn[2], nm[2][2];
#pragma unroll
    for (int i = 0; i < 2; ++i) dn[i] = mfma16(b8(kf[4]), b8(ph[i][0]), zero);
#pragma unroll
    for (int i = 0; i < 2; ++i) dn[i] = mfma16(b8(kf[5]), b8(ph[i][1]), dn[i]);
    SB0();
#pragma unroll
    for (int i = 0; i < 4; ++i) nm[i & 1][i >> 1] = mfma16(b8(kf[i >> 1]), b8(ph[i & 1][0]), zero);
    SB0();
    float rd_[2];
#pragma unroll
    for (int i = 0; i < 4; ++i) nm[i >> 1][i & 1] = mfma16(b8(kf[2 + (i & 1)]), b8(ph[i >> 1][1]), nm[i >> 1][i & 1]);
#pragma unroll
    for (int i = 0; i < 2; ++i) rd_[i] = __builtin_amdgcn_rcpf(rbf(rbf(dn[i][0]) + 1e-6f)) * gt[i];
    SB0();
#pragma unroll
    for (int i = 0; i < 2; ++i) {
        u32x4 o;
        f32x2 t[4];
#pragma unroll
        for (int j = 0; j < 4; ++j) t[j] = (f32x2){nm[i][j >> 1][2 * (j & 1)], nm[i][j >> 1][2 * (j & 1) + 1]} * (f32x2){rd_[i], rd_[i]};
        SB0();
#pragma unroll
        for (int j = 0; j < 4; ++j) o[j] = pack2(t[j][0], t[j][1]);
        SB0();
        st_wt(rsrc, o_st + i * 1024, nz4(o));
        SB0();
    }
}


typedef __attribute__((ext_vector_type(16))) unsigned u32x16;
__global__ __launch_bounds__(512, 2) void fused_mix(const u16*, const u16*, const u16*, const u16*, const u16*, const u16*, const u16*, const u16*,
                                                    const u16*, char*, u16*) {
    __shared__ __attribute__((aligned(16))) char smem[163840];
    u32x4 ka;
    u32x16 kb_;
    u64 kz;
    asm volatile("s_load_dwordx4 %0, %3, 0x0\n\ts_load_dwordx16 %1, %3, 0x10\n\ts_load_dwordx2 %2, %3, 0x50"
                 : "=&s"(ka), "=&s"(kb_), "=&s"(kz) : "s"(__builtin_amdgcn_kernarg_segment_ptr()));
    SB0();
    const int tid = threadIdx.x;
    int l = tid & 63;
    const int w = __builtin_amdgcn_readfirstlane(tid >> 6) ^ 4;
    int bid = blockIdx.x;
    const unsigned lds0 = lds_of(smem);
    int g = l >> 4, l15 = l & 15, q = l15 >> 2, p = l & 3;
    int srow = l >> 3, sch = l & 7;
    unsigned l16 = l * 16;
    const int rg = w & 3, kp = w >> 2;
    const int hs = rg >> 1;
    {
        u64 pc;
        asm volatile("s_getpc_b64 %0\n\ts_mov_b32 m0, %2\n\ts_mov_b64 exec, 3\n\tglobal_load_lds_dword %1, %0\n\ts_mov_b64 exec, -1"
                     : "=&s"(pc) : "v"(min((unsigned)(((bid & ~7) + w) * 2 + l) * 128u, CPF)), "s"(lds0 + L_SM + 3584) : "memory");
    }
    lds_write<unsigned>(lds0 + L_SM + tid * 4, 0u);
    const int slot = bid >> 3;
    const int mt = (bid & 1) * 8 + (slot & 7);
    const int hp = ((bid >> 1) & 3) * 4 + (slot >> 3);
    unsigned vx[4], vw[4];
    auto vw_init = [&]() AI {
        int t = l;
        asm volatile("" : "+v"(t));
#pragma unroll
        for (int i = 0; i < 4; ++i) vw[i] = (unsigned)((8 * i + (t >> 3)) * 6144 + ((t & 7) ^ (((t >> 4) & 1) << 1) ^ ((i & 1) << 2)) * 16 + 4096 - i * 1024);
    };
    auto vx_init = [&]() AI {
        int t = l;
        asm volatile("" : "+v"(t));
#pragma unroll
        for (int i = 0; i < 4; ++i) vx[i] = (unsigned)((8 * i + (t >> 3)) * 2048 + (((t & 7) ^ ((4 * (i & 1) + (t >> 4)) & 7)) * 16) + 4096 - i * 1024);
    };
    vx_init();
    vw_init();
    asm volatile("" : "+v"(vx[0]), "+v"(vx[1]), "+v"(vx[2]), "+v"(vx[3]), "+v"(vw[0]), "+v"(vw[1]), "+v"(vw[2]), "+v"(vw[3]));
    int ks0 = ((slot + (slot >> 3)) & 3) ^ ((bid & 1) * 8), role = w & 5;
    long long xo0 = (mt * 128 + hs * 64) * 2048 - 4096, wo0 = hp * 128 + hs * (32 * 6144) - 4096;
    long long xo1 = xo0 + ks0 * 128, wo1 = wo0 + ks0 * (64 * 6144) + 2048;
    unsigned d0 = lds0 + (hs ? X_U1[0] : X_U0[0]) * 8192, d1 = lds0 + J_U[0] * 8192 + hs * 4096;
    const unsigned* one = k_num;
    asm volatile("s_waitcnt lgkmcnt(0)" : "+s"(ka), "+s"(kb_), "+s"(kz), "+s"(xo0), "+s"(wo0), "+s"(xo1), "+s"(wo1), "+s"(ks0), "+s"(role),
                 "+s"(d0), "+s"(d1), "+s"(one) :: "memory");
    const u16* x = (const u16*)((u64)ka[1] << 32 | ka[0]);
    const u16* wqkv = (const u16*)((u64)ka[3] << 32 | ka[2]);
    auto arg = [&](int i) { return (char*)((u64)kb_[2 * i + 1] << 32 | kb_[2 * i]); };
    const u16 *wphi = (const u16*)arg(0), *wo = (const u16*)arg(1), *wr = (const u16*)arg(2), *wg = (const u16*)arg(3);
    const u16 *wu = (const u16*)arg(4), *wd = (const u16*)arg(5), *gw = (const u16*)arg(6);
    char* ws = arg(7);
    u16* out = (u16*)kz;
    unsigned v0 = 0;
    asm volatile("" : "+v"(v0));
    auto mark = [&](unsigned dst, int n = 1, int lanes = 1) AI {
        asm volatile("s_mov_b32 m0, %2\n\ts_mov_b64 exec, %4\n\tglobal_load_lds_dword %0, %1 offset:%3\n\ts_mov_b64 exec, -1"
                     ::"v"(v0), "s"(one), "s"(dst - 4 * n), "n"(4 * n), "n"(lanes) : "memory");
    };
    auto four = [&](const unsigned (&v)[4], const char* src, unsigned dst) AI {
        asm volatile("s_mov_b32 m0, %5\n\ts_nop 0\n\t"
                     "global_load_lds_dwordx4 %0, %4\n\tglobal_load_lds_dwordx4 %1, %4 offset:1024\n\t"
                     "global_load_lds_dwordx4 %2, %4 offset:2048\n\tglobal_load_lds_dwordx4 %3, %4 offset:3072"
                     ::"v"(v[0]), "v"(v[1]), "v"(v[2]), "v"(v[3]), "s"(src), "s"(dst) : "memory");
    };
    static_assert(F_J[0] == -1 && F_J[1] == 0 && F_K[1] == 1 && (KXB & 15) == 0, "");
    if (__builtin_expect(role == 4, 1)) {
        four(vx, (const char*)x + xo1, d0);
        four(vx, (const char*)x + xo1 + 32 * 2048, d0 + 4096);
        four(vw, (const char*)wqkv + wo1, d1);
    }
    asm volatile("" : "+s"(xo0), "+s"(wo0));
    const char* xb0 = (const char*)x + xo0;
    const char* wb0 = (const char*)wqkv + wo0;
    auto fill = [&](auto N) AI {
        constexpr int n = N, j = F_J[n] < 0 ? 0 : F_J[n], xi = F_J[n] < 0 ? -1 - F_J[n] : 0, s = F_J[n] < 0 ? xi : J_S[j], m = J_M[j];
        const int ks = ks0 ^ (int)((KXB >> (4 * s)) & 15);
        if constexpr (F_N[n] > 0 && n < NF1) waitv<PACE>();
        if constexpr (F_J[n] >= 0) {
            four(vw, wb0 + ks * (64 * 6144) + (m == 2 ? 0 : 2048 * (m + 1)), lds0 + J_U[j] * 8192 + hs * 4096);
        } else {
            const char* xs_ = xb0 + ks * 128;
            const unsigned d = lds0 + (hs ? X_U1[xi] : X_U0[xi]) * 8192;
            four(vx, xs_, d);
            if constexpr (F_N[n] > 0) waitv<PACE>();
            four(vx, xs_ + 32 * 2048, d + 4096);
        }
        if constexpr (F_K[n] > 1) mark(lds0 + L_FULL + (n < NF1 ? 0 : 32) + hs * 8, F_K[n], 3);
    };
    unsigned sd = 0, st;
    asm volatile("s_cmp_lg_u32 %2, 5\n\ts_cbranch_scc1 .La%=\n\t"
                 "s_min_u32 %1, %3, 23\n\ts_lshl_b32 %1, %1, 18\n\ts_load_dword %0, %5, %1\n\ts_load_dword %0, %6, 0x0\n\t"
                 "s_and_b32 %1, %1, 0x3c0000\n\ts_load_dword %0, %4, %1\n\ts_load_dword %0, %10, %1\n\t"
                 "s_load_dword %0, %7, 0x0\n\ts_load_dword %0, %8, 0x0\n\ts_load_dword %0, %9, 0x0\n.La%=:"
                 : "+s"(sd), "=&s"(st) : "s"(w), "s"(slot), "s"(x), "s"(wqkv), "s"(one), "s"(wr), "s"(gw), "s"(wphi), "s"(out) : "memory", "scc");
    asm volatile("s_cmp_lg_u32 %2, 7\n\ts_cbranch_scc1 .La%=\n\t"
                 "s_min_u32 %1, %3, 27\n\ts_lshl_b32 %1, %1, 18\n\ts_load_dword %0, %4, 0x0\n\ts_load_dword %0, %4, %1\n\t"
                 "s_load_dword %0, %5, %1\n\ts_load_dword %0, %6, %1\n\ts_load_dword %0, %7, %1\n\t"
                 "s_and_b32 %1, %1, 0x1c0000\n\ts_load_dword %0, %8, %1\n.La%=:\n\ts_barrier"
                 : "+s"(sd), "=&s"(st) : "s"(w), "s"(slot), "s"(ws), "s"(wg), "s"(wu), "s"(wd), "s"(wo) : "memory", "scc");
    if (__builtin_expect((w & 5) == 4, 1)) {
        mark(lds0 + L_FULL + hs * 8, 1, 3);
        sfor<2, 4>(fill);
    }
    asm volatile("" : "+s"(bid));
    const int xcd = bid & 7;
    const int sc = (3 - (int)((unsigned)(size_t)wqkv >> 7)) & 7;
    const int rt = (xcd >> 1) * 4 + ((bid >> 3) & 3), cb = (xcd & 1) * 8 + (bid >> 5), rot = 8 * rt;
    const __amdgpu_buffer_rsrc_t rsrc = __builtin_amdgcn_make_buffer_rsrc(ws, 0, WS_END, 0x00020000);
    const u32x4 z4 = {0u, 0u, 0u, 0u};
    const f32x4 zero = {0.f, 0.f, 0.f, 0.f};
    unsigned AX = lds0 + L_A + rg * 4096 + l16;
    unsigned W4 = lds0 + p * 4;
    unsigned PG = lds0 + L_PROG + p * 256;
    const unsigned WR = lds0 + rg * 4;
    auto flag = [&](unsigned a) AI { asm volatile("ds_write_b32 %0, %0" ::"v"(a) : "memory"); };
    auto phi_w = [&](bf16x8 (&a)[2], int h) AI {
        const unsigned ad = lds0 + L_WPHI + h * 2048 + (4 * g + q) * 64 + (((p >> 1) ^ (2 * (g & 1))) * 16) + 8 * (p & 1);
        a[0] = cat8(tr_read(ad), tr_read(ad + 1024));
        a[1] = cat8(tr_read(ad ^ 32), tr_read((ad ^ 32) + 1024));
    };
    u32x4 fw[32], fd[16], gu[2];
    unsigned sel_[4][2];
    u32x4 ph[2][2], sq;
    const int hp2 = ((bid >> 1) & 3) * 4 + (bid >> 6);
    const bool ffn = (hp2 & 7) != sc;
    const int ph4 = ((bid >> 3) + (bid >> 6)) & 3;
    const int kq = mt >> 2;
    const int cs = (ffn ? hp2 - (hp2 > sc) - (hp2 > sc + 8) : 14 + (hp2 >> 3)) * 4 + ((ph4 + 3 - (int)((unsigned)(size_t)wg >> 7)) & 3);
    const int c3 = (3 - (int)((unsigned)(size_t)wg >> 7)) & 7;
    const int cd = cs >= 56 ? (cs < 63 ? 8 * (cs - 56) + c3 : cs) : (cs & 7) == c3 ? 56 + (cs >> 3) : cs;
    auto spread = [&](const u32x4& v, int i) -> bf16x8 {
        const unsigned s = v[i >> 1];
        u32x4 a;
#pragma unroll
        for (int d = 0; d < 4; ++d) a[d] = __builtin_amdgcn_perm(s, s, sel_[d][i & 1]);
        return __builtin_bit_cast(bf16x8, a);
    };
    auto fold_store = [&](const f32x4& a, unsigned off) {
        f32x4 v;
#pragma unroll
        for (int i = 0; i < 4; ++i) {
            const unsigned u = __float_as_uint(a[i]);
            const float r = __uint_as_float(__builtin_amdgcn_update_dpp(u, u, 0x128, 0xc, 0xf, false));
            v[i] = swap32_add(r, r);
        }
        const u32x4 o = nz4(v);
        ST_MASK("global_store_dwordx4", 0x00FF00FFull, off + l15 * 32 + g * 16, o, ws);
    };
    auto sel_init = [&]() AI {
#pragma unroll
        for (int d = 0; d < 4; ++d)
#pragma unroll
            for (int hf = 0; hf < 2; ++hf) {
                const unsigned b = hf ? 0x0302u : 0x0100u;
                sel_[d][hf] = (sch >> 1) != d ? 0x0c0c0c0cu : (l & 1) ? (b << 16) | 0x0c0cu : 0x0c0c0000u | b;
            }
    };
    auto gua = [&](int i) { return ws + WS_GU + cd * 4096 + i * 1024; };
    auto gu_load = [&]() AI {
#pragma unroll
        for (int i = 0; i < 2; ++i) ld_async(gu[i], l16, gua(i));
    };
    auto down = [&]() AI {
#pragma unroll
        for (int i = 0; i < 16; ++i) pin(fd[i]);
        settle(gu, [&](int) { return l16; }, gua);
        const f32x4 v = __builtin_bit_cast(f32x4, gu[0]) + __builtin_bit_cast(f32x4, gu[1]);
        auto r = __builtin_amdgcn_permlane16_swap(__float_as_uint(swap32_add(v[0], v[2])), __float_as_uint(swap32_add(v[1], v[3])), false, false);
        const float gt = rbf(__uint_as_float(r[0])), up = rbf(__uint_as_float(r[1]));
        const unsigned ha = lds0 + L_XS + hs * 512;
        lds_write<u16>(ha + l15 * 8 + g * 2, f2bf(rbf(gt / (1.f + __expf(-gt))) * up));
        const u32x4 hv = lds_read<u32x4>(ha + srow * 16);
        f32x4 y0 = zero, y1 = zero;
#pragma unroll
        for (int i = 0; i < 8; ++i) {
            const bf16x8 a = spread(hv, i);
            y0 = mfma16(a, __builtin_bit_cast(bf16x8, fd[i]), y0);
            y1 = mfma16(a, __builtin_bit_cast(bf16x8, fd[8 + i]), y1);
        }
        fold_store(y0, WS_Y + cd * 4096 + kq * 1024 + hs * 512);
        fold_store(y1, WS_Y + cd * 4096 + kq * 1024 + hs * 512 + 256);
    };
    auto need = [&](int c) AI {
        if (c == 101) lds_wait(W4 + L_FKS, 1);
        else if (c == 100) lds_wait(W4 + L_FV, 1);
        else if (c == 99) lds_wait(W4 + L_FQD, 1);
        else if (c > 0) lds_wait(c > RLD ? W4 + L_PRL : PG, c);
    };
    auto wo_load = [&](int n0, int n1, bool mk) AI {
        unsigned vo[4];
#pragma unroll
        for (int i = 0; i < 4; ++i) vo[i] = (unsigned)((8 * i + srow) * 2048 + ((sch ^ ((g & 3) << 1)) * 16) + 4096 - i * 1024);
        const char* wob = (const char*)wo + cb * 128 - 4096;
#pragma unroll
        for (int n = n0; n < n1; ++n) {
            if (n < WON && (n == 0 || W_N[n] != W_N[n - 1])) need(W_N[n]);
            four(vo, wob + ((2 * n + hs + rot) & 31) * 65536, lds0 + W_U[n] * 8192 + hs * 4096);
        }
        if (mk) mark(lds0 + (n1 > WON ? L_FW2 : L_FWD) + hs * 4);
    };
    auto phi_adr = [&](unsigned (&pw)[4], unsigned base) AI {
#pragma unroll
        for (int i = 0; i < 4; ++i) pw[i] = (base + rg * 4096 + l15 * 128 + ((g ^ fsw(l15)) << 3)) ^ (i << 5);
    };
    const int row0 = rt * 128 + rg * 32 + kp * 16;
    u32x4 xr[2];
    u64 gg;
    unsigned xo = srow * 2048 + sch * 16;
    auto g_load = [&]() AI {
        asm volatile("global_load_dwordx2 %0, %1, %2 sc1" : "=&v"(gg) : "v"((unsigned)l15 * 8), "s"(ws + WS_GG + row0 * 8) : "memory");
    };
    auto xg_load = [&]() AI {
        asm volatile("global_load_dwordx4 %0, %3, %4\n\tglobal_load_dwordx4 %1, %3, %4 offset:64\n\tglobal_load_dwordx2 %2, %5, %6 sc1"
                     : "=&v"(xr[0]), "=&v"(xr[1]), "=&v"(gg)
                     : "v"((unsigned)((l >> 2) * 2048 + p * 16)), "s"(x + (size_t)row0 * DM + cb * 64),
                       "v"((unsigned)l15 * 8), "s"(ws + WS_GG + row0 * 8) : "memory");
    };
    f32x4 a0_[4];
    float og;
    auto x_init = [&](bool staged) AI {
        if (staged) asm volatile("" : "+v"(gg));
        else asm volatile("" : "+v"(xr[0]), "+v"(xr[1]), "+v"(gg));
#pragma unroll 1
        for (unsigned s = 0; s < SPIN_CAP && __ballot((gg >> 16) != (((u64)sq[1] << 32 | sq[0]) & 0xFFFFFFFFFFFFull)); ++s) {
            g_load();
            waitv<0>();
            asm volatile("" : "+v"(gg));
        }
        og = rbf(1.f - bf2f((u16)gg));
        if (staged) {
#pragma unroll
            for (int nb = 0; nb < 4; ++nb) {
                const u32x2 u = lds_read<u32x2>(lds0 + L_A + 2048 + rg * 4096 + l15 * 128 + (((2 * nb + (g >> 1)) ^ sch) * 16) + (g & 1) * 8);
                a0_[nb] = (f32x4){lo16(u[0]), hi16(u[0]), lo16(u[1]), hi16(u[1])} * og;
            }
            asm volatile("" : "+v"(a0_[0]), "+v"(a0_[1]), "+v"(a0_[2]), "+v"(a0_[3]), "+v"(og));
            SB0();
            return;
        }
        unsigned t[8];
#pragma unroll
        for (int i = 0; i < 8; ++i) t[i] = __builtin_amdgcn_ds_bpermute(l15 * 16 + (g >> 1) * 4 + (g & 1) * 8, xr[i >> 2][i & 3]);
#pragma unroll
        for (int j = 0; j < 2; ++j) {
            auto u = __builtin_amdgcn_permlane16_swap(t[4 * j], t[4 * j + 2], false, false);
            auto v = __builtin_amdgcn_permlane16_swap(t[4 * j + 1], t[4 * j + 3], false, false);
#pragma unroll
            for (int b = 0; b < 2; ++b) a0_[2 * j + b] = (f32x4){lo16(u[b]), hi16(u[b]), lo16(v[b]), hi16(v[b])} * og;
        }
        asm volatile("" : "+v"(a0_[0]), "+v"(a0_[1]), "+v"(a0_[2]), "+v"(a0_[3]), "+v"(og));
        SB0();
    };
    auto xg_def = [&]() AI { asm volatile("" : "=v"(xr[0]), "=v"(xr[1]), "=v"(gg)); };
    auto relane = [&]() AI {
        unsigned z = 0;
        asm volatile("" : "+v"(z));
        l = __builtin_amdgcn_mbcnt_hi(~0u, __builtin_amdgcn_mbcnt_lo(~0u, z));
        g = l >> 4, l15 = l & 15, q = l15 >> 2, p = l & 3, srow = l >> 3, sch = l & 7, l16 = l * 16;
        AX = lds0 + L_A + rg * 4096 + l16, W4 = lds0 + p * 4, PG = lds0 + L_PROG + p * 256, xo = srow * 2048 + sch * 16;
    };
    u32x4 kf[6];
    auto fd_def = [&]() AI {
#pragma unroll
        for (int i = 0; i < 16; ++i) asm volatile("" : "=v"(fd[i]));
        asm volatile("" : "=v"(gu[0]), "=v"(gu[1]));
    };
    auto kf_def = [&]() AI { asm volatile("" : "=v"(kf[0]), "=v"(kf[1]), "=v"(kf[2]), "=v"(kf[3]), "=v"(kf[4]), "=v"(kf[5])); };
    const int oh = hs ^ kp ^ 1;

    if (__builtin_expect(w >= 4, 1)) {
        const bool loader = !(w & 1);
        const unsigned kf_kv = WS_KF + (hp * 2 + (mt >> 3)) * 8192 + (mt & 7) * 512;
        const unsigned kf_z = WS_KF + (hp * 2 + (mt >> 3)) * 8192 + 4096 + (mt & 7) * 128;
        const unsigned fgo = ffn ? (unsigned)((256 * kq + 16 * srow) * 7168 + cs * 128 + sch * 16) : 0u;
        const char* fsrc = (const char*)(hs ? wu : wg);
        auto gu_w = [&](int q0, int q1) AI {
#pragma unroll
            for (int i = q0; i < q1; ++i) {
                unsigned t1, t2, t3;
                asm volatile("v_add_u32 %4, 0x1c00, %7\n\tv_add_u32 %5, 0x3800, %7\n\tv_add_u32 %6, 0x5400, %7\n\t"
                             "global_load_dwordx4 %0, %7, %8 nt\n\tglobal_load_dwordx4 %1, %4, %8 nt\n\t"
                             "global_load_dwordx4 %2, %5, %8 nt\n\tglobal_load_dwordx4 %3, %6, %8 nt"
                             : "=&v"(fw[4 * i]), "=&v"(fw[4 * i + 1]), "=&v"(fw[4 * i + 2]), "=&v"(fw[4 * i + 3]), "=&v"(t1), "=&v"(t2), "=&v"(t3)
                             : "v"(fgo), "s"(fsrc + (128 * (i >> 2) + 4 * (i & 3)) * 7168) : "memory");
            }
        };
        const unsigned fdo = cd < 56 ? (unsigned)((64 * cd + 8 * srow) * 2048 + 512 * kq + 256 * hs + sch * 16) : 0u;
        auto dn_w = [&](int q0, int q1) AI {
#pragma unroll
            for (int i = q0; i < q1; ++i)
                asm volatile("global_load_dwordx4 %0, %4, %5 offset:-4096 nt\n\tglobal_load_dwordx4 %1, %4, %5 offset:-2048 nt\n\t"
                             "global_load_dwordx4 %2, %4, %5 nt\n\tglobal_load_dwordx4 %3, %4, %5 offset:2048 nt"
                             : "=&v"(fd[4 * i]), "=&v"(fd[4 * i + 1]), "=&v"(fd[4 * i + 2]), "=&v"(fd[4 * i + 3])
                             : "v"(fdo), "s"((const char*)wd + 4096 + (i & 1) * 8192 + (i >> 1) * 128) : "memory");
        };
        auto gate_up = [&]() AI {
            sel_init();
            u32x4 xs[4];
            waitv<4 * (NF - NF1) + 1 + 4 * (8 - GQ1) + 16>();
#pragma unroll
            for (int i = 0; i < 4; ++i) xs[i] = lds_read<u32x4>(lds0 + L_XS + hs * 512 + (i >> 1) * 256 + srow * 32 + (i & 1) * 16);
            f32x4 a;
            bf16x8 t[2];
            t[0] = spread(xs[0], 0);
            sfor<0, 32>([&](auto I) {
                constexpr int i = I;
                SB0();
                if constexpr (i < 31) t[~i & 1] = spread(xs[(i + 1) >> 3], (i + 1) & 7);
                else asm volatile("s_nop 1");
                asm volatile("" : "+v"(t[~i & 1]));
                if constexpr (i == 4 * GQ1) waitv<16>();
                SB0();
                u32x4& f = fw[i];
                bf16x8& u = t[i & 1];
                f32x4& c = a;
                pin(f);
                if constexpr (i == 0) asm volatile("v_mfma_f32_16x16x32_bf16 %0, %1, %2, 0" : "=&v"(c) : "v"(u), "v"(f));
                else asm volatile("v_mfma_f32_16x16x32_bf16 %0, %1, %2, %0" : "+v"(c) : "v"(u), "v"(f));
            });
            asm volatile("s_nop 7" : "+v"(a));
            SB0();
            fold_store(a, WS_GU + cs * 4096 + kq * 512 + hs * 256);
            SB0();
        };
        u64 T;
        u32x4 sv[2];
        u32x2 sg;
        auto rg_load = [&]() AI {
            asm volatile("global_load_dwordx4 %0, %3, %4 sc1\n\tglobal_load_dwordx4 %1, %3, %4 offset:16 sc1\n\tglobal_load_dwordx2 %2, %5, %6 sc1"
                         : "=&v"(sv[0]), "=&v"(sv[1]), "=&v"(sg) : "v"(l * 32), "s"(ws + WS_RG), "v"(l * 8), "s"(ws + WS_GG + (mt * 2 + hs) * 512) : "memory");
        };
        auto select = [&]() AI {
            const unsigned tl = (unsigned)T & 0x1FFFu, thi = (unsigned)(T >> 13);
#pragma unroll 1
            for (unsigned s = 0; s < SPIN_CAP; ++s) {
                pin(sv[0]);
                pin(sv[1]);
                asm volatile("" : "+v"(sg));
                const unsigned bad = ((sg[0] >> 16) ^ ((unsigned)T & 0xFFFFu)) | (sg[1] ^ (unsigned)(T >> 16)) | ((sv[0][0] >> 19) ^ tl) | (sv[0][1] ^ thi) | ((sv[0][2] >> 19) ^ tl) | (sv[0][3] ^ thi) |
                                     ((sv[1][0] >> 19) ^ tl) | (sv[1][1] ^ thi) | ((sv[1][2] >> 19) ^ tl) | (sv[1][3] ^ thi);
                if (__all(bad == 0u)) break;
                __builtin_amdgcn_s_sleep(16);
                rg_load();
                waitv<0>();
            }
            pin(sv[0]);
            pin(sv[1]);
            asm volatile("" : "+v"(sg));
            lds_write<unsigned>(lds0 + L_G + hs * 512 + l * 4, sg[0] << 16);
            unsigned k2 = 0;
#pragma unroll
            for (int j = 0; j < 4; ++j) {
                const unsigned v = sv[j >> 1][(j & 1) * 2] & 0x7FFFFu;
                k2 = max(k2, ((v >> 3) << 11) | ((255 - (4 * l + j)) << 3) | (v & 7));
            }
            const unsigned key = __builtin_amdgcn_readfirstlane(wave_umax(k2));
            asm volatile("s_mov_b32 m0, %2\n\ts_mov_b64 exec, %3\n\tglobal_load_lds_dwordx4 %0, %1\n\ts_mov_b64 exec, -1"
                         ::"v"((unsigned)(512 * kq) + l16), "s"(x + (size_t)(2047 - (int)(key & 2047u)) * DM), "s"(lds0 + L_XS + hs * 512), "s"(0xFFFFFFFFull) : "memory");
            unsigned rb = key >> 11;
            rb ^= (rb & 0x8000u) ? 0x8000u : 0xFFFFu;
            const float rsel = rbf(1.f / (1.f + __expf(-bf2f((u16)rb))));
            lds_write<u32x4>(lds0 + L_SEL, (u32x4){(unsigned)T, (unsigned)(T >> 32), 2047u - (key & 2047u), __float_as_uint(rsel)});
            if (bid == 0 && hs) ST_MASK("global_store_dwordx2", 1ull, 0u, T, ws + WS_E);
        };
        auto k_soft = [&]() AI {
            bf16x8 a0[2], kb[2];
            f32x4 pk[2][2][2];
            unsigned pw[4];
            phi_w(a0, 0);
            phi_adr(pw, lds0 + P_U[0] * 8192);
            asm volatile("" : "+v"(pw[0]), "+v"(pw[1]), "+v"(pw[2]), "+v"(pw[3]));
            auto logits = [&](int h) AI {
                lds_wait(WR + L_FK0 + 4 * h, 1);
#pragma unroll
                for (int i = 0; i < 2; ++i) kb[i] = lds_read128(AX + (P_U[0] * 8192 - L_A) + h * 4096 + i * 1024);
#pragma unroll
                for (int i = 0; i < 4; ++i) pk[h][i >> 1][i & 1] = mfma16(a0[i & 1], kb[i >> 1], zero);
            };
            auto round = [&](int h) AI {
#pragma unroll
                for (int i = 0; i < 4; ++i) pk[h][i >> 1][i & 1] = rbf4(pk[h][i >> 1][i & 1]);
            };
            logits(0);
            round(0);
            SB0();
            phi_k(pk[0], pw, 0, [&]() AI {
                logits(1);
                SB0();
            });
            SB0();
            round(1);
            phi_k(pk[1], pw, 4096, []() {});
            SB0();
            flag(WR + L_FP);
        };
        auto q_soft = [&](auto hook) AI {
            bf16x8 a1[2];
            f32x4 pq[2][2];
            phi_w(a1, hs);
            lds_wait0(WR + L_FQ);
#pragma unroll
            for (int rb = 0; rb < 2; ++rb) {
                const bf16x8 b = lds_read128(AX + rb * 1024);
#pragma unroll
                for (int fb = 0; fb < 2; ++fb) pq[rb][fb] = rbf4(mfma16(a1[fb], b, zero));
            }
            softmax2(pq, ph, hook);
            asm volatile("" : "+v"(ph[0][0]), "+v"(ph[0][1]), "+v"(ph[1][0]), "+v"(ph[1][1]));
        };
        if (__builtin_expect(loader, 1)) {
            vx_init();
            vw_init();
            unsigned pv = 0;
            sfor<4, NF1>([&](auto N) {
                constexpr int n = N;
                if constexpr (F_N[n] > F_N[n - 1]) {
                    __builtin_amdgcn_s_setprio(3);
                    lds_wait(pv, PG, F_N[n]);
                    if (F_N[n] < RLD) lds_peek(pv, PG);
                    __builtin_amdgcn_s_setprio(0);
                }
                if constexpr (n == 12) {
                    asm volatile("s_mov_b32 m0, %2\n\ts_nop 0\n\tglobal_load_lds_dwordx4 %0, %1\n\tglobal_load_lds_dwordx4 %0, %1 offset:1024"
                                 ::"v"((unsigned)((l >> 2) * 64 + (((l & 3) ^ (2 * ((l >> 4) & 1))) * 16))),
                                   "s"(wphi + (hp * 2 + hs) * 1024), "s"(lds0 + L_WPHI + hs * 2048) : "memory");
                }
                fill(N);
            });
            flag(lds0 + L_FLD + hs * 4);
            __builtin_amdgcn_s_setprio(2);
            const char* cla = ws + WS_CL + ((l15 * 2 + (mt >> 3)) * 16 + hp) * 128;
            u64 cl;
            asm volatile("global_load_dwordx2 %0, %1, off sc1" : "=&v"(cl) : "v"(cla) : "memory");
            k_soft();
            lds_wait(W4 + L_FP, 1);
            sq = lds_read<u32x4>(lds0 + L_SEL);
            waitv<0>();
            asm volatile("" : "+v"(cl));
            {
                u32x4 kp_[6];
                const char* kb = ws + WS_KV + (hp * 8 + (mt >> 1)) * 16384 + (mt & 1) * 512 + hs * 256;
                const bool zw = !((mt & 3) | hs);
                const char* zb = zw ? ws + WS_Z + (hp * 4 + (mt >> 2)) * 2048 : kb;
                const unsigned kvo = g * 1024 + l15 * 16, zo = zw ? l16 : 0u;
                auto ko = [&](int i) { return i < 4 ? kvo : zo; };
                auto ka = [&](int i) { return i < 4 ? kb + i * 4096 : zb + (i - 4) * 1024; };
                const u64 T = (u64)sq[1] << 32 | sq[0];
#pragma unroll 1
                for (unsigned s = 0; s < SPIN_CAP && __ballot(cl != T); ++s)
                    asm volatile("global_load_dwordx2 %0, %1, off sc1\n\ts_waitcnt vmcnt(0)" : "=&v"(cl) : "v"(cla) : "memory");
                u32x4 a01;
#pragma unroll
                for (int i = 0; i < 4; ++i) a01[i] = (l15 >> 1) != i ? 0u : (l & 1) ? 0x3f800000u : 0x3f80u;
                unsigned po = kf_kv + hs * 256 + l15 * 16 + (g & 1) * 8, pz = kf_z + (l & 8) * 8 + p * 16 + (sch >> 2) * 8 + (g >> 1) * 4;
                pin(a01);
                asm volatile("" : "+v"(po), "+v"(pz));
                lds_wait0(W4 + L_FKS);
                __builtin_amdgcn_s_sleep(GDL);
#pragma unroll
                for (int i = 0; i < 6; ++i) ld_async(kp_[i], ko(i), ka(i));
                xg_load();
                SB0();
                waitv<3>();
                u32x2 o;
                auto sum = [&]() AI {
                    f32x4 d = zero;
#pragma unroll
                    for (int i = 0; i < 4; ++i) d = mfma16(__builtin_bit_cast(bf16x8, a01), __builtin_bit_cast(bf16x8, kp_[i]), d);
                    SB0();
                    unsigned m = least(kp_);
                    asm volatile("" : "+v"(m));
                    SB0();
                    o = (u32x2){nz1(pack2(d[0], d[1])), nz1(pack2(d[2], d[3]))};
                    asm volatile("" : "+v"(o));
                    return m;
                };
#pragma unroll
                for (int i = 0; i < 6; ++i) pin(kp_[i]);
                if (__builtin_expect(__ballot(sum() == 0u) != 0ull, 0)) {
                    again(kp_, ko, ka);
                    sum();
                }
                ST_MASK("global_store_dwordx2", 0xFFFFFFFFull, po, o, ws);
                if (zw) {
                    const f32x4 z = __builtin_bit_cast(f32x4, kp_[4]) + __builtin_bit_cast(f32x4, kp_[5]);
                    const unsigned t = __float_as_uint(dpp_add<0x128>(swap16_add(swap32_add(z[0], z[2]), swap32_add(z[1], z[3]))));
                    auto r = __builtin_amdgcn_permlane16_swap(t, t, false, false);
                    ST_MASK("global_store_dword", 0x0000FFFF0000FFFFull, pz, nz1(pack2(__uint_as_float(r[0]), __uint_as_float(r[1]))), ws);
                }
                flag(lds0 + L_FG + hs * 4);
            }
            const char* kfb = ws + WS_KF + (hp * 2 + hs) * 8192;
            auto fo = [&](int i) { return i < 4 ? l16 : (unsigned)g * 16; };
            auto fb = [&](int i) { return i < 4 ? kfb + i * 1024 : kfb + 4096 + (i - 4) * 512; };
            need(KFN);
            unsigned sa0 = lds0 + L_KF + hs * 4096 + l16, sa1 = lds0 + L_KZ + hs * 128 + g * 16, sf = lds0 + L_FKF + hs * 4;
            asm volatile("" : "+v"(sa0), "+v"(sa1), "+v"(sf));
            q_soft([]() {});
            SB0();
            __builtin_amdgcn_s_sleep(KFD);
#pragma unroll
            for (int i = 0; i < 6; ++i) ld_async(kf[i], fo(i), fb(i));
            SB0();
            waitv<6>();
            x_init(false);
            waitv<0>();
            auto stage = [&]() AI {
#pragma unroll
                for (int i = 0; i < 2; ++i) lds_write<u32x4>(sa1 + i * 64, kf[4 + i]);
#pragma unroll
                for (int i = 0; i < 4; ++i) lds_write<u32x4>(sa0 + i * 1024, kf[i]);
                SB0();
            };
#pragma unroll
            for (int i = 0; i < 6; ++i) pin(kf[i]);
            stage();
            if (__builtin_expect(__ballot(least(kf) == 0u) != 0ull, 0)) {
                again(kf, fo, fb);
                stage();
            }
            flag(sf);
            fd_def();
            xg_def();
        } else {
            const int row0 = (mt * 16 + hp) * 8 + 4 * hs;
            __builtin_amdgcn_s_setprio(2);
            asm volatile("s_waitcnt lgkmcnt(0)" : "+s"(sd));
            lds_wait(lds0 + L_FULL + p * 4, 3);
            u32x4 rv[2], gv[2], xv[4][2];
            u64 ev;
            asm volatile("global_load_dwordx2 %4, %5, %9 sc1\n\t"
                         "global_store_dwordx4 %10, %11, %9 sc1\n\t"
                         "global_store_dwordx2 %12, %13, %9 sc1\n\t"
                         "global_load_dwordx4 %0, %6, %7\n\tglobal_load_dwordx4 %1, %6, %7 offset:1024\n\t"
                         "global_load_dwordx4 %2, %6, %8\n\tglobal_load_dwordx4 %3, %6, %8 offset:1024"
                         : "=&v"(rv[0]), "=&v"(rv[1]), "=&v"(gv[0]), "=&v"(gv[1]), "=&v"(ev)
                         : "v"(0u), "v"(l16), "s"(wr), "s"(gw), "s"(ws),
                           "v"(l < 32 ? WS_Y + cd * 4096 + kq * 1024 + hs * 512 + l16 : WS_GU + cs * 4096 + kq * 512 + hs * 256 + l15 * 16), "v"(z4),
                           "v"(hs ? kf_z + l15 * 8 : kf_kv + l * 8), "v"(0ull)
                         : "memory");
#pragma unroll
            for (int r = 0; r < 2; ++r)
                asm volatile("global_load_dwordx4 %0, %4, %5\n\tglobal_load_dwordx4 %1, %4, %5 offset:1024\n\t"
                             "global_load_dwordx4 %2, %4, %5 offset:2048\n\tglobal_load_dwordx4 %3, %4, %5 offset:3072"
                             : "=&v"(xv[2 * r][0]), "=&v"(xv[2 * r][1]), "=&v"(xv[2 * r + 1][0]), "=&v"(xv[2 * r + 1][1])
                             : "v"(l16), "s"(x + (size_t)(row0 + 2 * r) * DM) : "memory");
            SB0();
            waitv<0>();
            asm volatile("" : "+v"(rv[0]), "+v"(rv[1]), "+v"(gv[0]), "+v"(gv[1]), "+v"(ev));
#pragma unroll
            for (int r = 0; r < 4; ++r) asm volatile("" : "+v"(xv[r][0]), "+v"(xv[r][1]));
            T = ((u64)__builtin_amdgcn_readfirstlane((unsigned)(ev >> 32)) << 32 | __builtin_amdgcn_readfirstlane((unsigned)ev)) + 1;
            float ra_[4] = {0.f, 0.f, 0.f, 0.f}, ga_[4] = {0.f, 0.f, 0.f, 0.f};
#pragma unroll
            for (int r = 0; r < 4; ++r)
#pragma unroll
                for (int i = 0; i < 2; ++i)
#pragma unroll
                    for (int j = 0; j < 4; ++j) {
                        ra_[r] = dot2(xv[r][i][j], rv[i][j], ra_[r]);
                        ga_[r] = dot2(xv[r][i][j], gv[i][j], ga_[r]);
                    }
            const int rr = (g >> 1) | ((g & 1) << 1);
            unsigned k = f2bf(sum4(ra_));
            k ^= (k & 0x8000u) ? 0xFFFFu : 0x8000u;
            k = (k << 3) | (7 - 4 * hs - rr);
            auto r1 = __builtin_amdgcn_permlane16_swap(k, k, false, false);
            k = max(r1[0], r1[1]);
            auto r2 = __builtin_amdgcn_permlane32_swap(k, k, false, false);
            k = max(r2[0], r2[1]);
            u64 gq = (T << 16) | f2bf(1.f / (1.f + __expf(-rbf(sum4(ga_)))));
            if (hs) lds_write<unsigned>(lds0 + L_BEST, k | 0x80000000u);
            asm volatile("" : "+v"(gq), "+v"(k));
            __builtin_amdgcn_s_setprio(0);
            lds_wait8(PG, CLR0 + ph4);
            if (!hs) {
                lds_wait(lds0 + L_BEST, 1);
                const u64 rq = (T << 19) | max(k, lds_read<unsigned>(lds0 + L_BEST) & 0x7FFFFu);
                ST_MASK("global_store_dwordx2", 1ull, 0u, rq, ws + WS_RG + (mt * 16 + hp) * 8);
            }
            ST_MASK("global_store_dwordx2", 0x0001000100010001ull, (unsigned)(rr * 8), gq, ws + WS_GG + row0 * 8);
#pragma unroll
            for (int n = 0; n < 8; ++n) st_wt(rsrc, WS_O + ((mt * 2 + (n >> 2)) * 32 + 2 * hp + hs) * 4096 + (n & 3) * 1024 + l16, z4);
#pragma unroll
            for (int n = 0; n < 4; ++n) st_wt(rsrc, WS_KV + ((hp * 8 + hs * 4 + n) * 16 + mt) * 1024 + l16, z4);
            ST_MASK("global_store_dwordx4", 0xFFFFull, WS_Z + ((hp * 4 + hs * 2 + (srow & 1)) * 16 + mt) * 128 + sch * 16, z4, ws);
            waitv<0>();
            ST_MASK("global_store_dwordx2", 0xFFFFull, (unsigned)l * 8, T, ws + WS_CL + ((mt * 2 + hs) * 16 + hp) * 128);
            lds_wait8(lds0 + L_FLD + (l & 1) * 4, 1);
            rg_load();
            gu_w(0, GQ1);
            waitv<4 * GQ1>();
            select();
            flag(WR + L_FP);
            vx_init();
            vw_init();
            sfor<NF1, NF>([&](auto N) {
                constexpr int n = N;
                if constexpr (n == NF1 || F_N[n] != F_N[n - 1]) need(F_N[n]);
                fill(N);
            });
            gu_w(GQ1, 8);
            dn_w(0, 4);
            __builtin_amdgcn_s_setprio(3);
            gate_up();
            __builtin_amdgcn_s_setprio(0);
            xg_load();
            lds_wait(W4 + L_FKS, 1);
            wo_load(0, WOE, false);
            sq = lds_read<u32x4>(lds0 + L_SEL);
            waitv<4 * WOE>();
            x_init(false);
            lds_wait(lds0 + L_FG + (l & 1) * 4, 1);
            gu_load();
            wo_load(WOE, WOM, false);
            __builtin_amdgcn_s_setprio(2);
            q_soft([&]() AI {
                wo_load(WOM, WON, true);
                SB0();
            });
            waitv<4 * (WON - WOE) + 1>();
#pragma unroll
            for (int i = 0; i < 16; ++i) pin(fd[i]);
            pin(gu[0]);
            pin(gu[1]);
            xg_def();
            kf_def();
        }
    } else {
        __builtin_amdgcn_s_setprio(1);
        unsigned ra[4], xt[2];
#pragma unroll
        for (int u = 0; u < 4; ++u) ra[u] = lds0 + J_B[0] * 8192 + (8 * g + q) * 128 + ((u ^ (q >> 1) ^ ((g & 1) << 1)) * 32) + 8 * p;
        unsigned xa = lds0 + (32 * (w & 1) + l15) * 128 + ((g ^ (l15 >> 1)) * 16), fa = lds0 + L_FULL + (l & 3) * 4;
        xt[0] = xa + (hs ? X_U1[0] : X_U0[0]) * 8192;
        xt[1] = xt[0] ^ 64;
        asm volatile("s_mov_b32 m0, %4" : "+v"(xa), "+v"(fa), "+v"(xt[0]), "+v"(xt[1]) : "s"(lds0 + L_PROG + w * 256 - 124));
        f32x4 acc[3][4][2];
        bf16x8 xf[ND][2][2];
        s16x4 wh[PD + 1][2];
        unsigned fv = 0;
        bf16x8 kown[2];
        u64 bad = ~0ull;
        auto chk = [&](int c) AI {
            SB0();
            if (__builtin_expect(bad != 0ull, 0)) {
                unsigned c_;
                asm volatile("s_mov_b32 %1, 0x400000\n"
                             ".Lp%=:\n\tds_read_b32 %0, %2 offset:%3\n\ts_waitcnt lgkmcnt(0)\n\tv_cmp_gt_u32 vcc, %4, %0\n\ts_cbranch_vccz .Lf%=\n\t"
                             "s_sub_u32 %1, %1, 1\n\ts_cbranch_scc0 .Lp%=\n.Lf%=:"
                             : "=&v"(fv), "=&s"(c_) : "v"(fa), "n"(c > NM1 ? 32 : 0), "n"(c) : "memory", "vcc", "scc");
            }
            SB0();
        };
        auto kv_part = [&]() AI {
            bf16x8 ones;
#pragma unroll
            for (int j = 0; j < 8; ++j) ones[j] = (short)0x3f80;
            unsigned pa = lds0 + (hs ? P_U[2] : P_U[0]) * 8192 + (4 * g + q) * 128 + (((8 * (rg & 1) + p) ^ fsw(4 * g + q)) << 3);
            unsigned pb = pa ^ 32, va = lds0 + L_A + hs * 2048 + l16, kv_st = WS_KV + ((hp * 8 + rg * 2) * 16 + mt) * 1024 + l16;
            unsigned kv_s1 = kv_st + 16384, z_st = WS_Z + ((hp * 4 + rg) * 16 + mt) * 128 + (l & 1) * 64 + g * 16, fk = WR + L_FKS;
            asm volatile("" : "+v"(pa), "+v"(pb), "+v"(va), "+v"(kv_st), "+v"(kv_s1), "+v"(z_st), "+v"(fk), "+v"(ones));
            bf16x8 b0[4], b1[4], af[2][4];
            lds_wait0(lds0 + L_FV + sch * 4);
#pragma unroll
            for (int i = 0; i < 4; ++i) {
                af[0][i] = cat8(tr_read(pa + i * 4096), tr_read(pa + i * 4096 + 2048));
                b0[i] = lds_read128(va + i * 4096);
                SB0();
                af[1][i] = cat8(tr_read(pb + i * 4096), tr_read(pb + i * 4096 + 2048));
                SB0();
            }
#pragma unroll
            for (int i = 0; i < 4; ++i) b1[i] = lds_read128(va + i * 4096 + 1024);
            SB0();
            f32x4 k0[2] = {zero, zero}, k1[2] = {zero, zero}, zt[2] = {zero, zero};
#pragma unroll
            for (int i = 0; i < 4; ++i)
#pragma unroll
                for (int e = 0; e < 2; ++e) {
                    k0[e] = mfma16(af[e][i], b0[i], k0[e]);
                    zt[e] = mfma16(af[e][i], ones, zt[e]);
                }
            SB0();
#pragma unroll
            for (int i = 0; i < 4; ++i)
#pragma unroll
                for (int e = 0; e < 2; ++e) k1[e] = mfma16(af[e][i], b1[i], k1[e]);
            st_wt(rsrc, kv_st, nz4(__builtin_bit_cast(u32x4, pack44(k0[0], k0[1]))));
            const u32x4 zo = nz4((l & 1) ? zt[1] : zt[0]);
            ST_MASK("global_store_dwordx4", 0x0003000300030003ull, z_st, zo, ws);
            st_wt(rsrc, kv_s1, nz4(__builtin_bit_cast(u32x4, pack44(k1[0], k1[1]))));
            SB0();
            flag(fk);
        };
        auto report = [&](int r) AI {
            unsigned t, a;
            if (r > RLD) asm volatile("v_mov_b32 %0, %3\n\tv_mov_b32 %1, %2\n\tds_write_b32 %1, %0" : "=&v"(t), "=&v"(a) : "s"(WR + L_PRL), "n"(r) : "memory");
            else asm volatile("global_load_lds_dword %0, %1 offset:%2" ::"v"(v0), "s"(one), "n"(4 * r) : "memory");
        };
        auto rd1 = [&](auto F, auto H) AI {
            constexpr int f = F, h = H, n = f >> 2, j = f >> 3, c = h ? E_B[f] : E_A[f], a = c % 100;
            if constexpr (h == 0 && (f & 3) == 0 && Q_C[n] > 0) chk(Q_C[n]);
            wh[f % (PD + 1)][h] = tr_read(ra[f & 3] + (J_U[j] - J_B[j]) * 8192 + (n & 1) * 4096 + h * 512);
            SB0();
            if constexpr (c / 100 == 1) {
                constexpr int jn = (f + 3 - a) >> 3;
                asm volatile("v_add_u32 %0, %1, %0" : "+v"(ra[a]) : "n"((J_B[jn] - J_B[jn - 1]) * 8192));
            } else if constexpr (c / 50 == 4) {
                asm volatile("v_add_u32 %0, %1, %2" : "=v"(xt[0]) : "s"((hs ? X_U1[a] : X_U0[a]) * 8192), "v"(xa));
            } else if constexpr (c / 50 == 5) {
                asm volatile("v_xor_b32 %0, 64, %1" : "=v"(xt[1]) : "v"(xt[0]));
            } else if constexpr (c / 100 == 3) {
                xf[a % 25 % ND][a / 25 & 1][a / 50] = lds_read128(xt[a / 50] + (a / 25 & 1) * 2048);
            } else if constexpr (c / 100 == 4) {
                fv = *(volatile AS3 unsigned*)(size_t)(fa + (a > NM1 ? 32 : 0));
            } else if constexpr (c / 100 == 5) {
                bad = __builtin_amdgcn_ballot_w64(fv < (unsigned)a);
                asm volatile("" : "+s"(bad));
            } else if constexpr (c / 100 == 6) {
                report(a);
            }
        };
        sfor<0, PD>([&](auto F) { rd1(F, IC<0>{}); rd1(F, IC<1>{}); });
        constexpr int K0F = 4 * KD, K1F = 4 * KD + 1;
        sfor<0, 96>([&](auto N) {
            sfor<0, 4>([&](auto E) {
                constexpr int n = N, e = E, f = 4 * n + e, j = n >> 1, ks = n & 1, u = e, s = J_X[j], m = J_M[j];
                const bf16x8 wv = cat8(wh[f % (PD + 1)][0], wh[f % (PD + 1)][1]);
                sfor<0, 2>([&](auto RB) {
                    constexpr int rb = RB;
                    const bf16x8 xv = xf[s % ND][rb][ks];
                    if (j_first(j) && ks == 0) asm volatile("v_mfma_f32_16x16x32_bf16 %0, %1, %2, 0" : "=&v"(acc[m][u][rb]) : "v"(m == 1 ? xv : wv), "v"(m == 1 ? wv : xv));
                    else asm volatile("v_mfma_f32_16x16x32_bf16 %0, %1, %2, %0" : "+v"(acc[m][u][rb]) : "v"(m == 1 ? xv : wv), "v"(m == 1 ? wv : xv));
                    SB0();
                    if constexpr (f + PD < 384 && (f + PD < 4 * VD || f >= 4 * VD)) rd1(IC<f + PD>{}, RB);
                    if constexpr (rb == 0 && f == K0F) relane();
                    if constexpr (rb == 0 && (f == K0F || f == K1F)) asm volatile("s_nop 7");
                    SB0();
                    if constexpr (f == K0F) lds_write<bf16x8>(AX + (P_U[0] * 8192 - L_A) + rb * 1024, pack44(acc[0][0][rb], acc[0][1][rb]));
                    if constexpr (f == K1F) {
                        kown[rb] = pack44(acc[0][2][rb], acc[0][3][rb]);
                        asm volatile("" : "+v"(kown[rb]));
                    }
                    SB0();
                });
                if constexpr (f == K0F) flag(WR + L_FK0);
                if constexpr (f == 4 * VD - 1) {
                    bf16x8 ak[2];
                    f32x4 pk[2][2];
                    unsigned pw[4];
                    __builtin_amdgcn_s_setprio(2);
                    phi_w(ak, 1);
                    asm volatile("s_nop 7");
                    SB0();
#pragma unroll
                    for (int i = 0; i < 4; ++i) lds_write<bf16x8>(AX + i * 1024, pack44(acc[1][i][0], acc[1][i][1]));
                    SB0();
#pragma unroll
                    for (int i = 0; i < 4; ++i) pk[i >> 1][i & 1] = rbf4(mfma16(ak[i & 1], kown[i >> 1], zero));
                    phi_adr(pw, lds0 + P_U[2] * 8192);
                    phi_k(pk, pw, 0, []() {});
                    SB0();
                    flag(WR + L_FV);
                    kv_part();
                    __builtin_amdgcn_s_setprio(1);
                    fv = 0;
                    bad = ~0ull;
                    sfor<4 * VD, 4 * VD + PD>([&](auto F) { rd1(F, IC<0>{}); rd1(F, IC<1>{}); });
                }
                if constexpr (f >= 24 && f < 56 && (f & 7) == 7) __builtin_amdgcn_s_sleep(5);
            });
        });
        asm volatile("s_nop 7");
        SB0();
        __builtin_amdgcn_s_setprio(0);
        flag(WR + L_FQD);
        bf16x8 a0[2];
        f32x4 pq[2][2];
        phi_w(a0, hs ^ 1);
        lds_wait(W4 + L_FKS, 1);
#pragma unroll
        for (int rb = 0; rb < 2; ++rb) {
            const bf16x8 b0 = pack44(acc[2][0][rb], acc[2][1][rb]), b1 = pack44(acc[2][2][rb], acc[2][3][rb]);
            lds_write<bf16x8>(AX + rb * 1024, hs ? b1 : b0);
#pragma unroll
            for (int fb = 0; fb < 2; ++fb) pq[rb][fb] = mfma16(a0[fb], hs ? b0 : b1, zero);
        }
        SB0();
        flag(WR + L_FQ);
        sq = lds_read<u32x4>(lds0 + L_SEL);
#pragma unroll
        for (int i = 0; i < 4; ++i) pq[i >> 1][i & 1] = rbf4(pq[i >> 1][i & 1]);
        softmax2(pq, ph, []() {});
        asm volatile("" : "+v"(ph[0][0]), "+v"(ph[0][1]), "+v"(ph[1][0]), "+v"(ph[1][1]));
        asm volatile("s_mov_b32 m0, %3\n\ts_nop 0\n\tglobal_load_lds_dwordx4 %0, %1\n\tglobal_load_lds_dwordx4 %0, %2 offset:1024"
                     ::"v"((unsigned)(srow * 2048 + (sch ^ srow) * 16)), "s"(x + (size_t)row0 * DM + cb * 64),
                       "s"(x + (size_t)(row0 + 8) * DM + cb * 64 - 512), "s"(lds0 + L_A + 2048 + rg * 4096) : "memory");
        g_load();
        asm volatile("" : "=v"(xr[0]), "=v"(xr[1]));
        asm volatile("" : "=v"(a0_[0]), "=v"(a0_[1]), "=v"(a0_[2]), "=v"(a0_[3]), "=v"(og));
        kf_def();
        fd_def();
    }
    relane();
    const char* cla = ws + WS_CL + ((rt * 2 + kp) * 16 + l15) * 128;
    const u64 T = (u64)sq[1] << 32 | sq[0];
    u64 cl;
    unsigned ka0 = lds0 + L_KF + oh * 4096 + l16, ka1 = lds0 + L_KZ + oh * 128 + g * 16;
    unsigned o_st = WS_O + ((mt * 2 + hs) * 32 + 2 * hp + oh) * 4096 + (rg & 1) * 2048 + l16;
    float gt[2];
#pragma unroll
    for (int i = 0; i < 2; ++i) gt[i] = lds_read<float>(lds0 + L_G + hs * 512 + (rg & 1) * 128 + i * 64 + l15 * 4);
    asm volatile("" : "+v"(ka0), "+v"(ka1), "+v"(o_st), "+v"(cla), "+v"(gt[0]), "+v"(gt[1]));
    if ((w & 5) != 4) {
        lds_wait0(lds0 + L_FKF + oh * 4);
#pragma unroll
        for (int i = 0; i < 2; ++i) kf[4 + i] = lds_read<u32x4>(ka1 + i * 64);
        SB0();
#pragma unroll
        for (int i = 0; i < 4; ++i) kf[i] = lds_read<u32x4>(ka0 + i * 1024);
    }
    asm volatile("global_load_dwordx2 %0, %1, off sc1" : "=&v"(cl) : "v"(cla) : "memory");
    SB0();
    o_store(kf, ph, gt, rsrc, o_st);
    flag(lds0 + L_FOS + w * 4);
    waitv<3>();
    __builtin_amdgcn_s_setprio(0);
    if ((w & 5) == 4) {
        lds_wait(lds0 + L_FOS + sch * 4, 1);
        wo_load(WON, 16, true);
    }
    if ((w & 5) == 5) {
        if (cd < 56) {
            sel_init();
            down();
        } else {
            __builtin_amdgcn_s_sleep(PDL);
            waitv<2>();
        }
    }
    flag(lds0 + L_FWO + w * 4);
    const u32x2 selv = {sq[2], sq[3]};

    const char* ob = ws + WS_O + (rt * 2 + (rg >> 1)) * 131072 + (rg & 1) * 2048;
    const unsigned ov0 = l16 + kp * 1024, ov1 = l16 + (kp ^ 1) * 1024;
    auto oa = [&](int i) { return ob + ((2 * i + kp + rot) & 31) * 4096; };
    u32x4 of[3][4];
    auto fetch1 = [&](int k, int j) AI { ld_async(of[k % 3][j], (j & 1) ? ov1 : ov0, oa(2 * k + (j >> 1))); };
    auto fix = [&](int k) AI { again(of[k % 3], [&](int j) { return (j & 1) ? ov1 : ov0; }, [&](int j) { return oa(2 * k + (j >> 1)); }); };
    const bool own = (__builtin_amdgcn_readfirstlane(selv[0]) >> 4) == (unsigned)(rt * 8 + rg * 2 + kp);
    if (!kp) {
        x_init(true);
        __builtin_amdgcn_s_sleep(PDL);
        waitv<2>();
    } else {
        __builtin_amdgcn_s_sleep(PD1);
        if (w & 1) waitv<4>();
        else waitv<3 + 4 * (16 - WON)>();
    }
    f32x4 acc[2][4];
#pragma unroll
    for (int nb = 0; nb < 4; ++nb) acc[0][nb] = a0_[nb];
    const float ys = __uint_as_float(__builtin_amdgcn_readlane(__float_as_uint(og), __builtin_amdgcn_readfirstlane(selv[0]) & 15));
    asm volatile("" : "+v"(cl));
#pragma unroll 1
    for (unsigned s = 0; s < SPIN_CAP && __ballot(cl != T); ++s)
        asm volatile("global_load_dwordx2 %0, %1, off sc1\n\ts_waitcnt vmcnt(0)" : "=&v"(cl) : "v"(cla) : "memory");
#pragma unroll
    for (int j = 0; j < 4; ++j) fetch1(0, j);
    unsigned wa[2][4];
#pragma unroll
    for (int i = 0; i < 8; ++i) {
        wa[i >> 2][i & 3] = ((lds0 + 4096 * kp + (4 * g + q) * 128 + ((((g & 1) << 1) | (q >> 1)) * 32) + 8 * p) ^ ((i & 3) << 5)) + (i >> 2) * 65536;
        asm volatile("" : "+v"(wa[i >> 2][i & 3]));
    }
    s16x4 wf[2][8][2];
    auto wrd = [&](int k, int i) AI {
        const int u = W_U[2 * k + (i >> 3)];
        wf[k & 1][i >> 1][i & 1] = tr_read(wa[u >> 3][(i >> 1) & 3] + (u & 7) * 8192 + (i & 1) * 2048);
    };
    u32x4 yp[14];
    const unsigned yo = srow * 4096 + cb * 256 + sch * 32;
    auto ya = [&](int i) { return ws + WS_Y + (i >> 1) * 32768 + (i & 1) * 16; };
    lds_wait(lds0 + L_FWD + (l & 1) * 4, 1);
#pragma unroll
    for (int i = 0; i < 16; ++i) wrd(0, i);
#pragma unroll
    for (int j = 0; j < 4; ++j) fetch1(1, j);
    const unsigned ea = lds0 + L_A + l16 + 4096 * w, eb = lds0 + L_A + 4096 * (w ^ 4);
    unsigned er = eb + l16, et = eb + srow * 128 + ((sch ^ srow) * 16), ew[4];
#pragma unroll
    for (int nb = 0; nb < 4; ++nb) ew[nb] = eb + l15 * 128 + (((2 * nb + (g >> 1)) ^ (l & 7)) * 16) + (g & 1) * 8;
    u16 *o0 = out + (size_t)row0 * DM + cb * 64, *o1 = o0 + 8 * DM;
    u64 km[2] = {__ballot(row0 + srow != (int)selv[0]), __ballot(row0 + 8 + srow != (int)selv[0])};
    asm volatile("" : "+s"(km[0]), "+s"(km[1]));
    asm volatile("" : "+v"(er), "+v"(et), "+v"(ew[0]), "+v"(ew[1]), "+v"(ew[2]), "+v"(ew[3]), "+s"(o0), "+s"(o1));
    __builtin_amdgcn_s_setprio(3);
    waitv<4>();
#pragma unroll
    for (int j = 0; j < 4; ++j) pin(of[0][j]);
    if (__builtin_expect(__ballot(least(of[0]) == 0u) != 0ull, 0)) fix(0);
    sfor<0, 8>([&](auto K) {
        constexpr int k = K;
        unsigned m;
        if constexpr (k > 4) __builtin_amdgcn_s_setprio(7 - k);
        if constexpr (2 * (k + 1) == WON) lds_wait(lds0 + ((l & 8) ? L_FWO + sch * 4 : L_FW2 + (l & 1) * 4), 1);
        if constexpr (k == 7) {
            if (__builtin_expect(own, 0)) {
#pragma unroll
                for (int i = 0; i < 14; ++i) ld_async(yp[i], yo, ya(i));
            }
        }
        SB0();
        sfor<0, 16>([&](auto I) {
            constexpr int i = I, j = k == 7 ? (0x2031 >> (i & 12)) & 3 : i >> 2, nb = i & 3;
            const bf16x8 a = cat8(wf[k & 1][(j >> 1) * 4 + nb][0], wf[k & 1][(j >> 1) * 4 + nb][1]);
            if (k == 0 && j == 1) asm volatile("v_mfma_f32_16x16x32_bf16 %0, %1, %2, 0" : "=&v"(acc[j & 1][nb]) : "v"(a), "v"(of[k % 3][j]));
            else asm volatile("v_mfma_f32_16x16x32_bf16 %0, %1, %2, %0" : "+v"(acc[j & 1][nb]) : "v"(a), "v"(of[k % 3][j]));
            SB0();
            if constexpr (k == 7 && i >= 12) {
                if constexpr (i == 12) lds_write<u32x4>(ea, nz4(acc[1][0]));
                else lds_write<f32x4>(ea + (i - 12) * 1024, acc[1][i - 12]);
            }
            if constexpr (k < 7) wrd(k + 1, i);
            if constexpr (k < 6 && i < 8 && !(i & 1)) fetch1(k + 2, i >> 1);
            if constexpr (k < 7 && i == 10) {
                waitv<(k < 6 ? 4 : 0)>();
#pragma unroll
                for (int n = 0; n < 4; ++n) pin(of[(k + 1) % 3][n]);
            }
            if constexpr (k < 7 && i > 10 && i <= 14) {
                const u32x4& v = of[(k + 1) % 3][i - 11];
                if constexpr (i == 11) asm volatile("v_min3_u32 %0, %1, %2, %3\n\tv_min_u32 %0, %0, %4" : "=&v"(m) : "v"(v[0]), "v"(v[1]), "v"(v[2]), "v"(v[3]));
                else asm volatile("v_min3_u32 %0, %0, %1, %2\n\tv_min3_u32 %0, %0, %3, %4" : "+v"(m) : "v"(v[0]), "v"(v[1]), "v"(v[2]), "v"(v[3]));
            }
            SB0();
        });
        if constexpr (k < 7) {
            if (__builtin_expect(__builtin_amdgcn_ballot_w64(m == 0u) != 0ull, 0)) fix(k + 1);
        }
        SB0();
    });
    asm volatile("s_nop 7");
    SB0();
    BAR();
    f32x4 pa[4];
#pragma unroll
    for (int nb = 0; nb < 4; ++nb) pa[nb] = lds_read<f32x4>(er + nb * 1024);
    SB0();
#pragma unroll
    for (int nb = 0; nb < 4; ++nb) acc[0][nb] += pa[nb];
#pragma unroll
    for (int nb = 0; nb < 4; ++nb) asm volatile("" : "+v"(acc[0][nb]));
#pragma unroll
    for (int nb = 0; nb < 4; ++nb) lds_write<u32x2>(ew[nb], (u32x2){pack2(acc[0][nb][0], acc[0][nb][1]), pack2(acc[0][nb][2], acc[0][nb][3])});
    u32x4 av[2];
#pragma unroll
    for (int j = 0; j < 2; ++j) av[j] = lds_read<u32x4>(et + j * 1024);
    SB0();
    asm volatile("s_mov_b64 exec, %5\n\tglobal_store_dwordx4 %0, %1, %3 sc1\n\ts_mov_b64 exec, %6\n\tglobal_store_dwordx4 %0, %2, %4 sc1\n\ts_mov_b64 exec, -1"
                 ::"v"(xo), "v"(av[0]), "v"(av[1]), "s"(o0), "s"(o1), "s"(km[0]), "s"(km[1]) : "memory");
    if (own) {
        const unsigned sr = __builtin_amdgcn_readfirstlane(selv[0]) & 15;
        const unsigned a2 = lds_read<unsigned>(eb + sr * 128 + ((sch ^ (sr & 7)) * 16) + g * 4);
        waitv<2>();
        settle(yp, [&](int) { return yo; }, ya);
        f32x4 yv[2];
#pragma unroll
        for (int h = 0; h < 2; ++h) {
            yv[h] = __builtin_bit_cast(f32x4, yp[h]);
#pragma unroll
            for (int i = 1; i < 7; ++i) yv[h] += __builtin_bit_cast(f32x4, yp[2 * i + h]);
        }
        float y2[2];
#pragma unroll
        for (int i = 0; i < 2; ++i) {
            const float t = dpp_add<0x128>(swap16_add(swap32_add(yv[0][i], yv[1][i]), swap32_add(yv[0][2 + i], yv[1][2 + i])));
            y2[i] = rbf(rbf(t) * __uint_as_float(selv[1])) * ys;
        }
        ST_MASK("global_store_dword", 0x00FF00FF00FF00FFull, sr * 2048 + sch * 16 + g * 4, pack2(lo16(a2) + y2[0], hi16(a2) + y2[1]), o0);
    }
}

extern "C" void kernel_launch(void* const* d_in, const int* in_sizes, int n_in, void* d_out, int out_size,
                              void* d_ws, size_t ws_size, hipStream_t stream) {
    fused_mix<<<256, 512, 0, stream>>>((const u16*)d_in[0], (const u16*)d_in[1], (const u16*)d_in[2], (const u16*)d_in[3],
                                       (const u16*)d_in[5], (const u16*)d_in[6], (const u16*)d_in[7], (const u16*)d_in[8],
                                       (const u16*)d_in[9], (char*)d_ws, (u16*)d_out);
}
```

```cpp
#include <hip/hip_runtime.h>
#include <hip/hip_bf16.h>
#include <stdint.h>
#include <type_traits>

typedef __attribute__((ext_vector_type(8))) short bf16x8;
typedef __attribute__((ext_vector_type(4))) short s16x4;
typedef __attribute__((ext_vector_type(4))) float f32x4;
typedef __attribute__((ext_vector_type(2))) float f32x2;
typedef __attribute__((ext_vector_type(2))) unsigned u32x2;
typedef __attribute__((ext_vector_type(4))) unsigned u32x4;
typedef __attribute__((ext_vector_type(2))) __bf16 bf16x2_t;
typedef unsigned short u16;
typedef unsigned long long u64;

#define DI __device__ __forceinline__
#define AS3 __attribute__((address_space(3)))

constexpr int DM = 1024;

DI float bf2f(u16 v) { return __uint_as_float(((unsigned)v) << 16); }
DI u16 f2bf(float f) { __bf16 b = (__bf16)f; return __builtin_bit_cast(u16, b); }
DI float rbf(float f) { return bf2f(f2bf(f)); }
DI unsigned pack2(float lo, float hi) {
    const f32x2 v = {lo, hi};
    return __builtin_bit_cast(unsigned, __builtin_convertvector(v, bf16x2_t));
}
DI float lo16(unsigned u) { return __uint_as_float(u << 16); }
DI float hi16(unsigned u) { return __uint_as_float(u & 0xffff0000u); }

DI s16x4 tr_read(unsigned a) { return __builtin_amdgcn_ds_read_tr16_b64_v4i16((AS3 s16x4*)(size_t)a); }
DI bf16x8 lds_read128(unsigned a) { return *(AS3 const bf16x8*)(size_t)a; }
template <class T> DI void lds_write(unsigned a, T v) { *(AS3 T*)(size_t)a = v; }
template <class T> DI T lds_read(unsigned a) { return *(AS3 const T*)(size_t)a; }
DI bf16x8 cat8(s16x4 a, s16x4 b) { return __builtin_shufflevector(a, b, 0, 1, 2, 3, 4, 5, 6, 7); }
DI f32x4 mfma16(bf16x8 a, bf16x8 b, f32x4 c) { return __builtin_amdgcn_mfma_f32_16x16x32_bf16(a, b, c, 0, 0, 0); }
DI bf16x8 pack44(const f32x4& a, const f32x4& b) {
    u32x4 r;
    r[0] = pack2(a[0], a[1]);
    r[1] = pack2(a[2], a[3]);
    r[2] = pack2(b[0], b[1]);
    r[3] = pack2(b[2], b[3]);
    return __builtin_bit_cast(bf16x8, r);
}
DI unsigned lds_of(const void* p) { return (unsigned)(size_t)(AS3 const char*)p; }

template <int CTRL>
DI unsigned dpp(unsigned x) { return __builtin_amdgcn_update_dpp(0u, x, CTRL, 0xf, 0xf, true); }
template <int CTRL>
DI float dpp_add(float x) { return x + __uint_as_float(dpp<CTRL>(__float_as_uint(x))); }
DI float swap32_add(float a, float b) {
    auto r = __builtin_amdgcn_permlane32_swap(__float_as_uint(a), __float_as_uint(b), false, false);
    return __uint_as_float(r[0]) + __uint_as_float(r[1]);
}
DI float swap16_add(float a, float b) {
    auto r = __builtin_amdgcn_permlane16_swap(__float_as_uint(a), __float_as_uint(b), false, false);
    return __uint_as_float(r[0]) + __uint_as_float(r[1]);
}
DI unsigned wave_umax(unsigned x) {
    x = max(x, dpp<0xB1>(x));
    x = max(x, dpp<0x4E>(x));
    x = max(x, dpp<0x141>(x));
    x = max(x, dpp<0x140>(x));
    auto r = __builtin_amdgcn_permlane16_swap(x, x, false, false);
    x = max(r[0], r[1]);
    auto s = __builtin_amdgcn_permlane32_swap(x, x, false, false);
    return max(s[0], s[1]);
}
constexpr unsigned WS_E = 0;
constexpr unsigned WS_RG = 256;
constexpr unsigned WS_GG = WS_RG + 2048;
constexpr unsigned WS_Z = 65536;
constexpr unsigned WS_KF = WS_Z + 131072;
constexpr unsigned WS_GU = WS_KF + 262144;
constexpr unsigned WS_Y = WS_GU + 262144;
constexpr unsigned WS_KV = WS_Y + 262144;
constexpr unsigned WS_O = WS_KV + (2u << 20);
constexpr unsigned WS_CL = WS_O + (4u << 20);
constexpr unsigned WS_END = WS_CL + 65536;

constexpr int NQ = 9, ND = 7, NX = 16, KD = 76, VD = 82, NF = 64, NF1 = 58, NF2 = 58, NP1 = 14, NP = 16, NM1 = 23, NM2 = 23, NM = 24, KFN = 100;
constexpr int J_S[48] = {0, 0, 0, 1, 1, 1, 2, 2, 2, 3, 3, 3, 4, 4, 4, 5, 5, 5, 6, 6, 6, 7, 7, 7, 8, 8, 8, 9, 9, 10, 10, 11, 11, 12, 12, 13, 14, 15, 13, 14, 15, 9, 10, 11, 12, 13, 14, 15};
constexpr int J_M[48] = {0, 1, 2, 0, 1, 2, 0, 1, 2, 0, 1, 2, 0, 1, 2, 0, 1, 2, 0, 1, 2, 0, 1, 2, 0, 1, 2, 0, 1, 0, 1, 0, 1, 0, 1, 0, 0, 0, 1, 1, 1, 2, 2, 2, 2, 2, 2, 2};
constexpr int J_X[48] = {0, 0, 0, 1, 1, 1, 2, 2, 2, 3, 3, 3, 4, 4, 4, 5, 5, 5, 6, 6, 6, 7, 7, 7, 8, 8, 8, 9, 9, 10, 10, 11, 11, 12, 12, 13, 14, 15, 13, 14, 15, 9, 10, 11, 12, 13, 14, 15};
constexpr int J_U[48] = {2, 6, 5, 0, 1, 15, 17, 14, 12, 4, 10, 9, 6, 5, 3, 18, 15, 16, 13, 12, 11, 7, 9, 8, 2, 3, 18, 15, 16, 13, 12, 10, 7, 6, 5, 18, 15, 14, 13, 12, 11, 4, 10, 7, 9, 8, 6, 5};
constexpr int J_B[48] = {0, 0, 0, 0, 0, 12, 12, 12, 12, 3, 3, 3, 3, 3, 3, 11, 11, 11, 11, 11, 11, 2, 2, 2, 2, 2, 12, 12, 12, 12, 12, 5, 5, 5, 5, 11, 11, 11, 11, 11, 11, 4, 4, 4, 4, 4, 4, 4};
constexpr int Q_C[96] = {1, 0, 2, 0, 3, 0, 4, 0, 5, 0, 6, 0, 7, 0, 0, 0, 0, 0, 8, 0, 0, 0, 9, 0, 0, 0, 0, 0, 10, 0, 0, 0, 0, 0, 0, 0, 11, 0, 0, 0, 0, 0, 12, 0, 0, 0, 0, 0, 13, 0, 0, 0, 0, 0, 14, 0, 0, 0, 15, 0, 16, 0, 0, 0, 0, 0, 17, 0, 0, 0, 0, 0, 18, 0, 19, 0, 20, 0, 21, 0, 22, 0, 23, 0, 24, 0, 0, 0, 0, 0, 0, 0, 0, 0, 0, 0};
constexpr int X_U0[16] = {9, 3, 16, 11, 8, 0, 17, 4, 6, 0, 17, 11, 9, 2, 0, 16};
constexpr int X_U1[16] = {8, 18, 13, 7, 2, 1, 14, 10, 5, 1, 14, 4, 8, 3, 1, 17};
constexpr int P_U[4] = {0, 1, 2, 3};
constexpr int W_U[16] = {15, 13, 14, 12, 11, 2, 3, 0, 1, 7, 4, 10, 9, 8, 6, 5};
constexpr int W_N[16] = {100, 100, 100, 100, 100, 101, 101, 101, 101, 15, 15, 15, 16, 16, 99, 99};
constexpr int F_J[64] = {-1, 0, 1, 2, -2, 3, 4, 5, -3, 6, 7, 8, -4, 9, 10, 11, -5, 12, 13, 14, 15, -6, 16, 17, 18, -7, 19, 20, 21, -8, 22, 23, 24, -9, 25, 26, -10, 27, 28, -11, 29, 30, -12, 31, 32, -13, 33, 34, -14, 35, -15, 36, -16, 37, 38, 39, 40, 41, 42, 43, 44, 45, 46, 47};
constexpr int F_N[64] = {0, 0, 0, 0, 0, 0, 0, 0, 0, 0, 0, 0, 0, 0, 0, 1, 1, 1, 1, 1, 1, 2, 2, 2, 2, 3, 3, 3, 3, 4, 4, 4, 4, 5, 5, 5, 5, 6, 6, 6, 7, 7, 8, 8, 9, 9, 9, 9, 10, 10, 10, 11, 12, 12, 13, 13, 13, 13, 13, 14, 14, 14, 14, 14};
constexpr int F_K[64] = {0, 1, 2, 3, 0, 4, 5, 6, 0, 0, 0, 7, 0, 0, 8, 0, 0, 0, 9, 0, 0, 0, 0, 10, 0, 0, 0, 11, 0, 0, 0, 12, 0, 0, 0, 13, 0, 0, 14, 0, 15, 0, 0, 0, 16, 0, 0, 0, 0, 17, 0, 18, 0, 19, 20, 21, 22, 23, 0, 0, 0, 0, 0, 24};
constexpr int E_A[384] = {300, 325, 350, 375, 0, 0, 0, 502, 0, 0, 0, 403, 0, 0, 0, 503, 201, 251, 0, 404, 0, 0, 0, 504, 301, 326, 351, 376, 0, 0, 0, 505, 601, 0, 0, 406, 0, 100, 101, 506, 102, 103, 202, 407, 252, 0, 0, 507, 302, 327, 352, 377, 0, 0, 0, 602, 0, 0, 0, 0, 0, 0, 0, 203, 253, 0, 0, 408, 0, 100, 101, 508, 303, 328, 353, 378, 0, 0, 0, 603, 0, 0, 0, 409, 0, 0, 0, 509, 204, 254, 0, 0, 0, 0, 0, 0, 304, 329, 354, 379, 0, 0, 0, 604, 0, 0, 0, 410, 0, 0, 0, 510, 205, 255, 0, 0, 0, 100, 101, 102, 305, 330, 355, 380, 0, 0, 0, 0, 0, 0, 0, 605, 0, 0, 0, 206, 256, 0, 0, 411, 0, 0, 0, 511, 306, 331, 356, 381, 0, 0, 0, 606, 0, 0, 0, 0, 0, 0, 0, 207, 257, 0, 0, 412, 607, 100, 101, 512, 307, 332, 357, 382, 0, 0, 0, 608, 0, 0, 0, 0, 0, 0, 0, 208, 258, 0, 0, 413, 0, 0, 0, 513, 308, 333, 358, 383, 0, 0, 0, 609, 0, 0, 0, 0, 0, 100, 101, 102, 103, 209, 259, 414, 0, 0, 0, 514, 309, 334, 359, 384, 0, 0, 0, 610, 210, 260, 0, 415, 0, 0, 0, 515, 310, 335, 360, 385, 0, 0, 0, 516, 612, 211, 261, 0, 0, 100, 101, 102, 311, 336, 361, 386, 0, 0, 0, 212, 262, 0, 0, 417, 613, 0, 0, 517, 312, 337, 362, 387, 0, 0, 0, 213, 263, 0, 0, 0, 0, 100, 101, 102, 313, 338, 363, 388, 214, 264, 0, 518, 314, 339, 364, 389, 215, 265, 0, 519, 315, 340, 365, 390, 0, 0, 0, 520, 0, 0, 0, 421, 0, 0, 0, 521, 0, 0, 0, 422, 0, 0, 0, 522, 0, 0, 0, 0, 0, 100, 101, 102, 103, 0, 0, 424, 0, 0, 0, 524, 0, 0, 0, 0, 0, 0, 0, 0, 0, 0, 0, 0, 0, 0, 0, 0, 0, 0, 0, 615, 0, 0, 0, 0, 0, 0, 0, 0, 0, 0, 0, 0, 0, 0, 0, 616, 0, 0, 0, 0, 0, 0, 0, 0, 0, 0, 0, 0};
constexpr int E_B[384] = {0, 0, 402, 0, 0, 0, 0, 0, 0, 0, 0, 0, 0, 0, 0, 0, 0, 0, 0, 0, 0, 0, 0, 0, 0, 0, 405, 0, 0, 0, 0, 0, 0, 0, 0, 0, 0, 0, 0, 0, 0, 0, 0, 0, 0, 0, 0, 0, 0, 0, 0, 0, 0, 0, 0, 0, 0, 0, 0, 0, 0, 0, 0, 0, 0, 0, 0, 0, 0, 0, 0, 102, 103, 0, 0, 0, 0, 0, 0, 0, 0, 0, 0, 0, 0, 0, 0, 0, 0, 0, 0, 0, 0, 0, 0, 0, 0, 0, 0, 0, 0, 0, 0, 0, 0, 0, 0, 0, 0, 0, 0, 0, 0, 0, 0, 0, 0, 0, 0, 0, 103, 0, 0, 0, 0, 0, 0, 0, 0, 0, 0, 0, 0, 0, 0, 0, 0, 0, 0, 0, 0, 0, 0, 0, 0, 0, 0, 0, 0, 0, 0, 0, 0, 0, 0, 0, 0, 0, 0, 0, 0, 0, 0, 0, 0, 0, 0, 102, 103, 0, 0, 0, 0, 0, 0, 0, 0, 0, 0, 0, 0, 0, 0, 0, 0, 0, 0, 0, 0, 0, 0, 0, 0, 0, 0, 0, 0, 0, 0, 0, 0, 0, 0, 0, 0, 0, 0, 0, 0, 0, 0, 0, 0, 0, 0, 0, 0, 0, 0, 0, 0, 0, 0, 0, 0, 0, 0, 0, 0, 0, 0, 611, 0, 0, 416, 0, 0, 0, 0, 0, 0, 0, 0, 0, 0, 0, 0, 0, 103, 0, 0, 0, 0, 0, 0, 0, 0, 0, 0, 0, 0, 0, 0, 0, 0, 0, 0, 0, 0, 0, 0, 0, 0, 0, 0, 0, 0, 0, 0, 0, 103, 0, 418, 0, 0, 0, 0, 614, 0, 0, 419, 0, 0, 0, 0, 0, 0, 0, 420, 0, 0, 0, 0, 0, 0, 0, 0, 0, 0, 0, 0, 0, 0, 0, 0, 0, 0, 0, 0, 0, 0, 0, 0, 0, 0, 0, 0, 0, 0, 0, 0, 0, 0, 0, 0, 0, 0, 0, 0, 0, 0, 0, 0, 0, 0, 0, 0, 0, 0, 0, 0, 0, 0, 0, 0, 0, 0, 0, 0, 0, 0, 0, 0, 0, 0, 0, 0, 0, 0, 0, 0, 0, 0, 0, 0, 0, 0, 0, 0, 0, 0, 0, 0, 0};
constexpr unsigned L_A = 16 * 8192;
constexpr unsigned L_KF = 18 * 8192;
constexpr unsigned L_SM = 19 * 8192;
constexpr unsigned L_FULL = L_SM + 256;
constexpr unsigned L_PROG = L_SM + 896;
constexpr unsigned L_PRL = L_SM + 336;
constexpr unsigned L_FW2 = L_SM + 512, L_FOS = L_SM + 480, L_FK0 = L_SM + 752, L_FV = L_SM + 560, L_FP = L_SM + 576, L_FKS = L_SM + 592;
constexpr unsigned L_FQD = L_SM + 608, L_FG = L_SM + 624, L_FKF = L_SM + 736, L_FQ = L_SM + 720, L_FWD = L_SM + 640, L_FWO = L_SM + 656;
constexpr unsigned L_FLD = L_SM + 688, L_BEST = L_SM + 696;
constexpr unsigned L_SEL = L_SM + 704;
constexpr unsigned L_XS = L_SM + 2048, L_KZ = L_SM + 3072, L_WPHI = L_SM + 4096;
constexpr unsigned L_G = L_SM + 3328;
static_assert(NP <= 31 && NM <= 63 && NF1 == NF2 && NX == 16, "");
static_assert(P_U[1] == P_U[0] + 1 && P_U[3] == P_U[2] + 1, "feature maps of a head lie side by side");
constexpr bool j_first(int j) {
    for (int i = 0; i < j; ++i)
        if (J_M[i] == J_M[j]) return false;
    return true;
}
constexpr unsigned SPIN_CAP = 1u << 16;
#define N8(a) a, a + 1, a + 2, a + 3, a + 4, a + 5, a + 6, a + 7
__device__ const unsigned k_num[64] = {N8(0), N8(8), N8(16), N8(24), N8(32), N8(40), N8(48), N8(56)};

constexpr int PD = 4;
constexpr int PACE = 16;
constexpr int RLD = F_N[NF1 - 1];
constexpr int CLR0 = 2;
constexpr int GQ1 = 6;
constexpr int WOE = 2, WOM = 7, WON = 10;
constexpr int GDL = 17, KFD = 6, PDL = 11, PD1 = 4;
constexpr unsigned CPF = 56832;
constexpr u64 KXB = 0xFEDBA9C768534210ull;
#define BAR() asm volatile("s_waitcnt lgkmcnt(0)\n\ts_barrier" ::: "memory")
template <int N>
DI void waitv() { asm volatile("s_waitcnt vmcnt(%0)" ::"n"(N) : "memory"); }

DI unsigned nz1(unsigned v) { return max(v, 1u); }
DI u32x4 nz4(u32x4 v) { return (u32x4){max(v[0], 1u), max(v[1], 1u), max(v[2], 1u), max(v[3], 1u)}; }
DI u32x4 nz4(f32x4 v) { return nz4(__builtin_bit_cast(u32x4, v)); }
DI void st_wt(__amdgpu_buffer_rsrc_t r, unsigned off, u32x4 v) { __builtin_amdgcn_raw_buffer_store_b128(v, r, off, 0, 16); }
DI void ld_async(u32x4& v, unsigned off, const char* base) {
    asm volatile("global_load_dwordx4 %0, %1, %2 sc1" : "=&v"(v) : "v"(off), "s"(base) : "memory");
}
#define ST_MASK(op, m, off, v, base) asm volatile("s_mov_b64 exec, %3\n\t" op " %0, %1, %2 sc1\n\ts_mov_b64 exec, -1\n\ts_nop 0" ::"v"(off), "v"(v), "s"(base), "s"((u64)(m)) : "memory")
DI void pin(u32x4& v) { asm volatile("" : "+v"(v)); }
template <int N>
DI unsigned least(const u32x4 (&v)[N]) {
    unsigned a = min(v[0][0], v[0][1]), b = min(v[0][2], v[0][3]);
#pragma unroll
    for (int i = 1; i < N; ++i) a = min(min(a, v[i][0]), v[i][1]), b = min(min(b, v[i][2]), v[i][3]);
    return min(a, b);
}
template <int N, class OFF, class BASE>
DI void again(u32x4 (&v)[N], OFF off, BASE base) {
    unsigned s = SPIN_CAP;
    do {
#pragma unroll
        for (int i = 0; i < N; ++i) {
            unsigned t;
            asm volatile("v_min3_u32 %1, %4, %5, %6\n\tv_min_u32 %1, %1, %7\n\tv_cmp_eq_u32 vcc, 0, %1\n\ts_mov_b64 exec, vcc\n\t"
                         "global_load_dwordx4 %0, %2, %3 sc1\n\ts_mov_b64 exec, -1"
                         : "+v"(v[i]), "=&v"(t) : "v"(off(i)), "s"(base(i)), "v"(v[i][0]), "v"(v[i][1]), "v"(v[i][2]), "v"(v[i][3]) : "memory", "vcc");
        }
        waitv<0>();
#pragma unroll
        for (int i = 0; i < N; ++i) pin(v[i]);
    } while (__ballot(least(v) == 0u) != 0ull && --s);
}
template <int N, class OFF, class BASE>
DI void settle(u32x4 (&v)[N], OFF off, BASE base) {
#pragma unroll
    for (int i = 0; i < N; ++i) pin(v[i]);
    if (__builtin_expect(__ballot(least(v) == 0u) != 0ull, 0)) again(v, off, base);
}
DI void lds_wait(unsigned a, unsigned n) {
    unsigned v, c;
    asm volatile("s_mov_b32 %1, 0x400000\n"
                 ".Lp%=:\n\tds_read_b32 %0, %2\n\ts_waitcnt lgkmcnt(0)\n\tv_cmp_gt_u32 vcc, %3, %0\n\ts_cbranch_vccz .Lf%=\n\t"
                 "s_sleep 1\n\ts_sub_u32 %1, %1, 1\n\ts_cbranch_scc0 .Lp%=\n.Lf%=:"
                 : "=&v"(v), "=&s"(c) : "v"(a), "s"(n) : "memory", "vcc", "scc");
}
DI void lds_wait0(unsigned a) {
    unsigned v, c;
    asm volatile("s_mov_b32 %1, 0x1000000\n"
                 ".Lp%=:\n\tds_read_b32 %0, %2\n\ts_sub_u32 %1, %1, 1\n\ts_cbranch_scc1 .Lf%=\n\ts_waitcnt lgkmcnt(0)\n\tv_cmp_eq_u32 vcc, 0, %0\n\ts_cbranch_vccnz .Lp%=\n"
                 ".Lf%=:\n\ts_waitcnt lgkmcnt(0)"
                 : "=&v"(v), "=&s"(c) : "v"(a) : "memory", "vcc", "scc");
}
DI void lds_wait8(unsigned a, unsigned n) {
    unsigned v, c;
    asm volatile("s_mov_b32 %1, 0x100000\n"
                 ".Lp%=:\n\tds_read_b32 %0, %2\n\ts_waitcnt lgkmcnt(0)\n\tv_cmp_gt_u32 vcc, %3, %0\n\ts_cbranch_vccz .Lf%=\n\t"
                 "s_sleep 4\n\ts_sub_u32 %1, %1, 1\n\ts_cbranch_scc0 .Lp%=\n.Lf%=:"
                 : "=&v"(v), "=&s"(c) : "v"(a), "s"(n) : "memory", "vcc", "scc");
}
DI void lds_peek(unsigned& v, unsigned a) { asm volatile("ds_read_b32 %0, %1" : "=v"(v) : "v"(a) : "memory"); }
DI void lds_wait(unsigned& v, unsigned a, unsigned n) {
    unsigned c;
    asm volatile("s_mov_b32 %1, 0x400000\n"
                 ".Lp%=:\n\ts_waitcnt lgkmcnt(0)\n\tv_cmp_gt_u32 vcc, %3, %0\n\ts_cbranch_vccz .Lf%=\n\t"
                 "ds_read_b32 %0, %2\n\ts_sub_u32 %1, %1, 1\n\ts_cbranch_scc0 .Lp%=\n.Lf%=:"
                 : "+v"(v), "=&s"(c) : "v"(a), "s"(n) : "memory", "vcc", "scc");
}
DI float dot2(unsigned a, unsigned b, float c) {
    return __builtin_amdgcn_fdot2_f32_bf16(__builtin_bit_cast(bf16x2_t, a), __builtin_bit_cast(bf16x2_t, b), c, false);
}
DI float sum4(const float (&a)[4]) {
    float c = swap16_add(swap32_add(a[0], a[1]), swap32_add(a[2], a[3]));
    c = dpp_add<0xB1>(c);
    c = dpp_add<0x4E>(c);
    c = dpp_add<0x141>(c);
    return dpp_add<0x140>(c);
}

template <class OP, class POST>
DI void red4(float& a, float& b, float& c, float& d, OP op, POST post) {
    auto x = __builtin_amdgcn_permlane32_swap(__float_as_uint(a), __float_as_uint(b), false, false);
    auto y = __builtin_amdgcn_permlane32_swap(__float_as_uint(c), __float_as_uint(d), false, false);
    const float m = op(__uint_as_float(x[0]), __uint_as_float(x[1])), n = op(__uint_as_float(y[0]), __uint_as_float(y[1]));
    auto z = __builtin_amdgcn_permlane16_swap(__float_as_uint(m), __float_as_uint(n), false, false);
    const unsigned f = __float_as_uint(post(op(__uint_as_float(z[0]), __uint_as_float(z[1]))));
    auto u = __builtin_amdgcn_permlane16_swap(f, f, false, false);
    auto v = __builtin_amdgcn_permlane32_swap(u[0], u[0], false, false);
    auto t = __builtin_amdgcn_permlane32_swap(u[1], u[1], false, false);
    a = __uint_as_float(v[0]);
    b = __uint_as_float(v[1]);
    c = __uint_as_float(t[0]);
    d = __uint_as_float(t[1]);
}
DI float mx2(float a, float b) { return __builtin_elementwise_maximum(a, b); }
template <class HOOK>
DI void softmax2(const f32x4 (&v)[2][2], u32x4 (&ph)[2][2], HOOK hook) {
    constexpr float L2E = 1.4426950408889634f;
    float mx[2], mn[2], sp_[2], sn_[2];
#pragma unroll
    for (int b = 0; b < 2; ++b) {
        const f32x4 s = v[b][0], t = v[b][1];
        mx[b] = mx2(mx2(mx2(mx2(s[0], s[1]), s[2]), mx2(mx2(s[3], t[0]), t[1])), mx2(t[2], t[3]));
        mn[b] = mx2(mx2(mx2(mx2(-s[0], -s[1]), -s[2]), mx2(mx2(-s[3], -t[0]), -t[1])), mx2(-t[2], -t[3]));
    }
    red4(mx[0], mx[1], mn[0], mn[1], [](float a, float b) { return mx2(a, b); }, [](float a) { return a * -L2E; });
    f32x2 ep[2][4], en[2][4];
#pragma unroll
    for (int b = 0; b < 2; ++b) {
#pragma unroll
        for (int e = 0; e < 4; ++e) {
            const f32x2 u = {v[b][e >> 1][2 * (e & 1)], v[b][e >> 1][2 * (e & 1) + 1]};
            const f32x2 a = __builtin_elementwise_fma(u, (f32x2){L2E, L2E}, (f32x2){mx[b], mx[b]});
            const f32x2 c = __builtin_elementwise_fma(u, (f32x2){-L2E, -L2E}, (f32x2){mn[b], mn[b]});
            ep[b][e] = (f32x2){__builtin_amdgcn_exp2f(a[0]), __builtin_amdgcn_exp2f(a[1])};
            en[b][e] = (f32x2){__builtin_amdgcn_exp2f(c[0]), __builtin_amdgcn_exp2f(c[1])};
        }
        const f32x2 s = (ep[b][0] + ep[b][1]) + (ep[b][2] + ep[b][3]), t = (en[b][0] + en[b][1]) + (en[b][2] + en[b][3]);
        sp_[b] = s[0] + s[1];
        sn_[b] = t[0] + t[1];
    }
    asm volatile("" : "+v"(sp_[0]), "+v"(sn_[0]), "+v"(sp_[1]), "+v"(sn_[1]));
    hook();
    red4(sp_[0], sp_[1], sn_[0], sn_[1], [](float a, float b) { return a + b; }, [](float a) { return __builtin_amdgcn_rcpf(a); });
#pragma unroll
    for (int b = 0; b < 2; ++b)
#pragma unroll
        for (int e = 0; e < 4; ++e) {
            const f32x2 a = ep[b][e] * (f32x2){sp_[b], sp_[b]}, c = en[b][e] * (f32x2){sn_[b], sn_[b]};
            ph[b][0][e] = pack2(a[0], a[1]);
            ph[b][1][e] = pack2(c[0], c[1]);
        }
}
DI f32x4 rbf4(const f32x4& a) {
    const unsigned u = pack2(a[0], a[1]), v = pack2(a[2], a[3]);
    return (f32x4){lo16(u), hi16(u), lo16(v), hi16(v)};
}
DI int fsw(int row) { return (((row >> 1) & 3) << 2) | (((row >> 3) & 1) << 1) | (row & 1); }
template <class HOOK>
DI void phi_k(const f32x4 (&pp)[2][2], const unsigned (&pw)[4], unsigned off, HOOK hook) {
    u32x4 ph[2][2];
    softmax2(pp, ph, hook);
#pragma unroll
    for (int b = 0; b < 2; ++b)
#pragma unroll
        for (int kf = 0; kf < 2; ++kf)
#pragma unroll
            for (int fb = 0; fb < 2; ++fb) {
                const u32x2 a = {ph[b][kf][2 * fb], ph[b][kf][2 * fb + 1]};
                lds_write<u32x2>(pw[2 * kf + fb] + off + b * 2048, a);
            }
}

#define SB0() __builtin_amdgcn_sched_barrier(0)
template <int A, int B, class F>
DI void sfor(F f) {
    if constexpr (A < B) {
        f(std::integral_constant<int, A>{});
        sfor<A + 1, B>(f);
    }
}
template <int N> using IC = std::integral_constant<int, N>;
#define AI __attribute__((always_inline))

DI unsigned min4(u32x4 v) { return min(min(v[0], v[1]), min(v[2], v[3])); }
DI unsigned o_sums(const u32x4 (&kf)[6], const u32x4 (&ph)[2][2], const float (&gt)[2], float (&rd_)[2], f32x4 (&nm)[2][2]) {
    const f32x4 zero = {0.f, 0.f, 0.f, 0.f};
    auto b8 = [](const u32x4& a) { return __builtin_bit_cast(bf16x8, a); };
    f32x4 dn[2];
#pragma unroll
    for (int i = 0; i < 2; ++i) dn[i] = mfma16(b8(kf[4]), b8(ph[i][0]), zero);
#pragma unroll
    for (int i = 0; i < 2; ++i) dn[i] = mfma16(b8(kf[5]), b8(ph[i][1]), dn[i]);
    SB0();
    unsigned m = min(min4(kf[4]), min4(kf[5]));
    asm volatile("" : "+v"(m));
#pragma unroll
    for (int i = 0; i < 4; ++i) nm[i & 1][i >> 1] = mfma16(b8(kf[i >> 1]), b8(ph[i & 1][0]), zero);
    m = min(m, min(min4(kf[0]), min4(kf[1])));
    asm volatile("" : "+v"(m));
    SB0();
#pragma unroll
    for (int i = 0; i < 4; ++i) nm[i >> 1][i & 1] = mfma16(b8(kf[2 + (i & 1)]), b8(ph[i >> 1][1]), nm[i >> 1][i & 1]);
    asm volatile("" : "+v"(dn[0]), "+v"(dn[1]));
    m = min(m, min(min4(kf[2]), min4(kf[3])));
#pragma unroll
    for (int i = 0; i < 2; ++i) rd_[i] = __builtin_amdgcn_rcpf(rbf(rbf(dn[i][0]) + 1e-6f)) * gt[i];
    asm volatile("" : "+v"(rd_[0]), "+v"(rd_[1]), "+v"(m));
    return m;
}
DI void o_store(const float (&rd_)[2], const f32x4 (&nm)[2][2], __amdgpu_buffer_rsrc_t rsrc, unsigned o_st) {
#pragma unroll
    for (int i = 0; i < 2; ++i) {
        u32x4 o;
        f32x2 t[4];
#pragma unroll
        for (int j = 0; j < 4; ++j) t[j] = (f32x2){nm[i][j >> 1][2 * (j & 1)], nm[i][j >> 1][2 * (j & 1) + 1]} * (f32x2){rd_[i], rd_[i]};
        SB0();
#pragma unroll
        for (int j = 0; j < 4; ++j) o[j] = pack2(t[j][0], t[j][1]);
        SB0();
        st_wt(rsrc, o_st + i * 1024, nz4(o));
        SB0();
    }
}

typedef __attribute__((ext_vector_type(16))) unsigned u32x16;
__global__ __launch_bounds__(512, 2) void fused_mix(const u16*, const u16*, const u16*, const u16*, const u16*, const u16*, const u16*, const u16*,
                                                    const u16*, char*, u16*) {
    __shared__ __attribute__((aligned(16))) char smem[163840];
    u32x4 ka;
    u32x16 kb_;
    u64 kz;
    asm volatile("s_load_dwordx4 %0, %3, 0x0\n\ts_load_dwordx16 %1, %3, 0x10\n\ts_load_dwordx2 %2, %3, 0x50"
                 : "=&s"(ka), "=&s"(kb_), "=&s"(kz) : "s"(__builtin_amdgcn_kernarg_segment_ptr()));
    SB0();
    const int tid = threadIdx.x;
    int l = tid & 63;
    const int w = __builtin_amdgcn_readfirstlane(tid >> 6) ^ 4;
    int bid = blockIdx.x;
    const unsigned lds0 = lds_of(smem);
    int g = l >> 4, l15 = l & 15, q = l15 >> 2, p = l & 3;
    int srow = l >> 3, sch = l & 7;
    unsigned l16 = l * 16;
    const int rg = w & 3, kp = w >> 2;
    const int hs = rg >> 1;
    SB0();
    {
        u64 pc;
        asm volatile("s_getpc_b64 %0\n\ts_mov_b32 m0, %2\n\ts_mov_b64 exec, 3\n\tglobal_load_lds_dword %1, %0\n\ts_mov_b64 exec, -1"
                     : "=&s"(pc) : "v"(min((unsigned)(((bid & ~7) + w) * 2 + l) * 128u, CPF)), "s"(lds0 + L_SM + 3584) : "memory");
    }
    lds_write<unsigned>(lds0 + L_SM + tid * 4, 0u);
    const int slot = bid >> 3;
    const int mt = (bid & 1) * 8 + (slot & 7);
    const int hp = ((bid >> 1) & 3) * 4 + (slot >> 3);
    unsigned vx[4], vw[4];
    auto vw_init = [&]() AI {
        int t = l;
        asm volatile("" : "+v"(t));
#pragma unroll
        for (int i = 0; i < 4; ++i) vw[i] = (unsigned)((8 * i + (t >> 3)) * 6144 + ((t & 7) ^ (((t >> 4) & 1) << 1) ^ ((i & 1) << 2)) * 16 + 4096 - i * 1024);
    };
    auto vx_init = [&]() AI {
        int t = l;
        asm volatile("" : "+v"(t));
#pragma unroll
        for (int i = 0; i < 4; ++i) vx[i] = (unsigned)((8 * i + (t >> 3)) * 2048 + (((t & 7) ^ ((4 * (i & 1) + (t >> 4)) & 7)) * 16) + 4096 - i * 1024);
    };
    vx_init();
    vw_init();
    asm volatile("" : "+v"(vx[0]), "+v"(vx[1]), "+v"(vx[2]), "+v"(vx[3]), "+v"(vw[0]), "+v"(vw[1]), "+v"(vw[2]), "+v"(vw[3]));
    int ks0 = ((slot + (slot >> 3)) & 3) ^ ((bid & 1) * 8), role = w & 5;
    long long xo0 = (mt * 128 + hs * 64) * 2048 - 4096, wo0 = hp * 128 + hs * (32 * 6144) - 4096;
    long long xo1 = xo0 + ks0 * 128, wo1 = wo0 + ks0 * (64 * 6144) + 2048;
    unsigned d0 = lds0 + (hs ? X_U1[0] : X_U0[0]) * 8192, d1 = lds0 + J_U[0] * 8192 + hs * 4096;
    const unsigned* one = k_num;
    asm volatile("s_waitcnt lgkmcnt(0)" : "+s"(ka), "+s"(kb_), "+s"(kz), "+s"(xo0), "+s"(wo0), "+s"(xo1), "+s"(wo1), "+s"(ks0), "+s"(role),
                 "+s"(d0), "+s"(d1), "+s"(one) :: "memory");
    const u16* x = (const u16*)((u64)ka[1] << 32 | ka[0]);
    const u16* wqkv = (const u16*)((u64)ka[3] << 32 | ka[2]);
    auto arg = [&](int i) { return (char*)((u64)kb_[2 * i + 1] << 32 | kb_[2 * i]); };
    const u16 *wphi = (const u16*)arg(0), *wo = (const u16*)arg(1), *wr = (const u16*)arg(2), *wg = (const u16*)arg(3);
    const u16 *wu = (const u16*)arg(4), *wd = (const u16*)arg(5), *gw = (const u16*)arg(6);
    char* ws = arg(7);
    u16* out = (u16*)kz;
    unsigned v0 = 0;
    asm volatile("" : "+v"(v0));
    auto mark = [&](unsigned dst, int n = 1, int lanes = 1) AI {
        asm volatile("s_mov_b32 m0, %2\n\ts_mov_b64 exec, %4\n\tglobal_load_lds_dword %0, %1 offset:%3\n\ts_mov_b64 exec, -1"
                     ::"v"(v0), "s"(one), "s"(dst - 4 * n), "n"(4 * n), "n"(lanes) : "memory");
    };
    auto four = [&](const unsigned (&v)[4], const char* src, unsigned dst) AI {
        asm volatile("s_mov_b32 m0, %5\n\ts_nop 0\n\t"
                     "global_load_lds_dwordx4 %0, %4\n\tglobal_load_lds_dwordx4 %1, %4 offset:1024\n\t"
                     "global_load_lds_dwordx4 %2, %4 offset:2048\n\tglobal_load_lds_dwordx4 %3, %4 offset:3072"
                     ::"v"(v[0]), "v"(v[1]), "v"(v[2]), "v"(v[3]), "s"(src), "s"(dst) : "memory");
    };
    static_assert(F_J[0] == -1 && F_J[1] == 0 && F_K[1] == 1 && (KXB & 15) == 0, "");
    if (__builtin_expect(role == 4, 1)) {
        four(vx, (const char*)x + xo1, d0);
        four(vx, (const char*)x + xo1 + 32 * 2048, d0 + 4096);
        four(vw, (const char*)wqkv + wo1, d1);
    }
    asm volatile("" : "+s"(xo0), "+s"(wo0));
    const char* xb0 = (const char*)x + xo0;
    const char* wb0 = (const char*)wqkv + wo0;
    auto fill = [&](auto N) AI {
        constexpr int n = N, j = F_J[n] < 0 ? 0 : F_J[n], xi = F_J[n] < 0 ? -1 - F_J[n] : 0, s = F_J[n] < 0 ? xi : J_S[j], m = J_M[j];
        const int ks = ks0 ^ (int)((KXB >> (4 * s)) & 15);
        if constexpr (F_N[n] > 0 && n < NF1) waitv<PACE>();
        if constexpr (F_J[n] >= 0) {
            four(vw, wb0 + ks * (64 * 6144) + (m == 2 ? 0 : 2048 * (m + 1)), lds0 + J_U[j] * 8192 + hs * 4096);
        } else {
            const char* xs_ = xb0 + ks * 128;
            const unsigned d = lds0 + (hs ? X_U1[xi] : X_U0[xi]) * 8192;
            four(vx, xs_, d);
            if constexpr (F_N[n] > 0) waitv<PACE>();
            four(vx, xs_ + 32 * 2048, d + 4096);
        }
        if constexpr (F_K[n] > 1) mark(lds0 + L_FULL + (n < NF1 ? 0 : 32) + hs * 8, F_K[n], 3);
    };
    unsigned sd = 0, st;
    asm volatile("s_cmp_lg_u32 %2, 5\n\ts_cbranch_scc1 .La%=\n\t"
                 "s_min_u32 %1, %3, 23\n\ts_lshl_b32 %1, %1, 18\n\ts_load_dword %0, %5, %1\n\ts_load_dword %0, %6, 0x0\n\t"
                 "s_and_b32 %1, %1, 0x3c0000\n\ts_load_dword %0, %4, %1\n\ts_load_dword %0, %10, %1\n\t"
                 "s_load_dword %0, %7, 0x0\n\ts_load_dword %0, %8, 0x0\n\ts_load_dword %0, %9, 0x0\n.La%=:"
                 : "+s"(sd), "=&s"(st) : "s"(w), "s"(slot), "s"(x), "s"(wqkv), "s"(one), "s"(wr), "s"(gw), "s"(wphi), "s"(out) : "memory", "scc");
    asm volatile("s_cmp_lg_u32 %2, 7\n\ts_cbranch_scc1 .La%=\n\t"
                 "s_min_u32 %1, %3, 27\n\ts_lshl_b32 %1, %1, 18\n\ts_load_dword %0, %4, 0x0\n\ts_load_dword %0, %4, %1\n\t"
                 "s_load_dword %0, %5, %1\n\ts_load_dword %0, %6, %1\n\ts_load_dword %0, %7, %1\n\t"
                 "s_and_b32 %1, %1, 0x1c0000\n\ts_load_dword %0, %8, %1\n.La%=:\n\ts_barrier"
                 : "+s"(sd), "=&s"(st) : "s"(w), "s"(slot), "s"(ws), "s"(wg), "s"(wu), "s"(wd), "s"(wo) : "memory", "scc");
    if (__builtin_expect((w & 5) == 4, 1)) {
        mark(lds0 + L_FULL + hs * 8, 1, 3);
        sfor<2, 4>(fill);
    }
    asm volatile("" : "+s"(bid));
    const int xcd = bid & 7;
    const int sc = (3 - (int)((unsigned)(size_t)wqkv >> 7)) & 7;
    const int rt = (xcd >> 1) * 4 + ((bid >> 3) & 3), cb = (xcd & 1) * 8 + (bid >> 5), rot = 8 * rt;
    const __amdgpu_buffer_rsrc_t rsrc = __builtin_amdgcn_make_buffer_rsrc(ws, 0, WS_END, 0x00020000);
    const u32x4 z4 = {0u, 0u, 0u, 0u};
    const f32x4 zero = {0.f, 0.f, 0.f, 0.f};
    unsigned AX = lds0 + L_A + rg * 4096 + l16;
    unsigned W4 = lds0 + p * 4;
    unsigned PG = lds0 + L_PROG + p * 256;
    const unsigned WR = lds0 + rg * 4;
    auto flag = [&](unsigned a) AI {
        asm volatile("ds_write_b32 %0, %0" ::"v"(a) : "memory");
        SB0();
    };
    auto phi_w = [&](bf16x8 (&a)[2], int h) AI {
        const unsigned ad = lds0 + L_WPHI + h * 2048 + (4 * g + q) * 64 + (((p >> 1) ^ (2 * (g & 1))) * 16) + 8 * (p & 1);
        a[0] = cat8(tr_read(ad), tr_read(ad + 1024));
        a[1] = cat8(tr_read(ad ^ 32), tr_read((ad ^ 32) + 1024));
    };
    u32x4 fw[32], fd[16], gu[2];
    unsigned sel_[4][2];
    u32x4 ph[2][2], sq;
    const int hp2 = ((bid >> 1) & 3) * 4 + (bid >> 6);
    const bool ffn = (hp2 & 7) != sc;
    const int ph4 = ((bid >> 3) + (bid >> 6)) & 3;
    const int kq = mt >> 2;
    const int cs = (ffn ? hp2 - (hp2 > sc) - (hp2 > sc + 8) : 14 + (hp2 >> 3)) * 4 + ((ph4 + 3 - (int)((unsigned)(size_t)wg >> 7)) & 3);
    const int c3 = (3 - (int)((unsigned)(size_t)wg >> 7)) & 7;
    const int cd = cs >= 56 ? (cs < 63 ? 8 * (cs - 56) + c3 : cs) : (cs & 7) == c3 ? 56 + (cs >> 3) : cs;
    auto spread = [&](const u32x4& v, int i) -> bf16x8 {
        const unsigned s = v[i >> 1];
        u32x4 a;
#pragma unroll
        for (int d = 0; d < 4; ++d) a[d] = __builtin_amdgcn_perm(s, s, sel_[d][i & 1]);
        return __builtin_bit_cast(bf16x8, a);
    };
    auto fold_store = [&](const f32x4& a, unsigned off) {
        f32x4 v;
#pragma unroll
        for (int i = 0; i < 4; ++i) {
            const unsigned u = __float_as_uint(a[i]);
            const float r = __uint_as_float(__builtin_amdgcn_update_dpp(u, u, 0x128, 0xc, 0xf, false));
            v[i] = swap32_add(r, r);
        }
        const u32x4 o = nz4(v);
        ST_MASK("global_store_dwordx4", 0x00FF00FFull, off + l15 * 32 + g * 16, o, ws);
    };
    auto sel_init = [&]() AI {
#pragma unroll
        for (int d = 0; d < 4; ++d)
#pragma unroll
            for (int hf = 0; hf < 2; ++hf) {
                const unsigned b = hf ? 0x0302u : 0x0100u;
                sel_[d][hf] = (sch >> 1) != d ? 0x0c0c0c0cu : (l & 1) ? (b << 16) | 0x0c0cu : 0x0c0c0000u | b;
            }
    };
    auto gua = [&](int i) { return ws + WS_GU + cd * 4096 + i * 1024; };
    auto gu_load = [&]() AI {
#pragma unroll
        for (int i = 0; i < 2; ++i) ld_async(gu[i], l16, gua(i));
    };
    auto down = [&]() AI {
#pragma unroll
        for (int i = 0; i < 16; ++i) pin(fd[i]);
        settle(gu, [&](int) { return l16; }, gua);
        const f32x4 v = __builtin_bit_cast(f32x4, gu[0]) + __builtin_bit_cast(f32x4, gu[1]);
        auto r = __builtin_amdgcn_permlane16_swap(__float_as_uint(swap32_add(v[0], v[2])), __float_as_uint(swap32_add(v[1], v[3])), false, false);
        const float gt = rbf(__uint_as_float(r[0])), up = rbf(__uint_as_float(r[1]));
        const unsigned ha = lds0 + L_XS + hs * 512;
        lds_write<u16>(ha + l15 * 8 + g * 2, f2bf(rbf(gt / (1.f + __expf(-gt))) * up));
        const u32x4 hv = lds_read<u32x4>(ha + srow * 16);
        f32x4 y0 = zero, y1 = zero;
#pragma unroll
        for (int i = 0; i < 8; ++i) {
            const bf16x8 a = spread(hv, i);
            y0 = mfma16(a, __builtin_bit_cast(bf16x8, fd[i]), y0);
            y1 = mfma16(a, __builtin_bit_cast(bf16x8, fd[8 + i]), y1);
        }
        fold_store(y0, WS_Y + cd * 4096 + kq * 1024 + hs * 512);
        fold_store(y1, WS_Y + cd * 4096 + kq * 1024 + hs * 512 + 256);
    };
    auto need = [&](int c) AI {
        if (c == 101) lds_wait(W4 + L_FKS, 1);
        else if (c == 100) lds_wait(W4 + L_FV, 1);
        else if (c == 99) lds_wait(W4 + L_FQD, 1);
        else if (c > 0) lds_wait(c > RLD ? W4 + L_PRL : PG, c);
    };
    auto wo_load = [&](int n0, int n1, bool mk) AI {
        unsigned vo[4];
#pragma unroll
        for (int i = 0; i < 4; ++i) vo[i] = (unsigned)((8 * i + srow) * 2048 + ((sch ^ ((g & 3) << 1)) * 16) + 4096 - i * 1024);
        const char* wob = (const char*)wo + cb * 128 - 4096;
#pragma unroll
        for (int n = n0; n < n1; ++n) {
            if (n < WON && (n == 0 || W_N[n] != W_N[n - 1])) need(W_N[n]);
            four(vo, wob + ((2 * n + hs + rot) & 31) * 65536, lds0 + W_U[n] * 8192 + hs * 4096);
        }
        if (mk) mark(lds0 + (n1 > WON ? L_FW2 : L_FWD) + hs * 4);
    };
    auto phi_adr = [&](unsigned (&pw)[4], unsigned base) AI {
#pragma unroll
        for (int i = 0; i < 4; ++i) pw[i] = (base + rg * 4096 + l15 * 128 + ((g ^ fsw(l15)) << 3)) ^ (i << 5);
    };
    const int row0 = rt * 128 + rg * 32 + kp * 16;
    u32x4 xr[2];
    u64 gg;
    unsigned xo = srow * 2048 + sch * 16;
    auto g_load = [&]() AI {
        asm volatile("global_load_dwordx2 %0, %1, %2 sc1" : "=&v"(gg) : "v"((unsigned)l15 * 8), "s"(ws + WS_GG + row0 * 8) : "memory");
    };
    auto xg_load = [&]() AI {
        asm volatile("global_load_dwordx4 %0, %3, %4\n\tglobal_load_dwordx4 %1, %3, %4 offset:64\n\tglobal_load_dwordx2 %2, %5, %6 sc1"
                     : "=&v"(xr[0]), "=&v"(xr[1]), "=&v"(gg)
                     : "v"((unsigned)((l >> 2) * 2048 + p * 16)), "s"(x + (size_t)row0 * DM + cb * 64),
                       "v"((unsigned)l15 * 8), "s"(ws + WS_GG + row0 * 8) : "memory");
    };
    f32x4 a0_[4];
    float og;
    auto x_init = [&](bool staged) AI {
        if (staged) asm volatile("" : "+v"(gg));
        else asm volatile("" : "+v"(xr[0]), "+v"(xr[1]), "+v"(gg));
#pragma unroll 1
        for (unsigned s = 0; s < SPIN_CAP && __ballot((gg >> 16) != (((u64)sq[1] << 32 | sq[0]) & 0xFFFFFFFFFFFFull)); ++s) {
            g_load();
            waitv<0>();
            asm volatile("" : "+v"(gg));
        }
        og = rbf(1.f - bf2f((u16)gg));
        if (staged) {
#pragma unroll
            for (int nb = 0; nb < 4; ++nb) {
                const u32x2 u = lds_read<u32x2>(lds0 + L_A + 2048 + rg * 4096 + l15 * 128 + (((2 * nb + (g >> 1)) ^ sch) * 16) + (g & 1) * 8);
                a0_[nb] = (f32x4){lo16(u[0]), hi16(u[0]), lo16(u[1]), hi16(u[1])} * og;
            }
            asm volatile("" : "+v"(a0_[0]), "+v"(a0_[1]), "+v"(a0_[2]), "+v"(a0_[3]), "+v"(og));
            SB0();
            return;
        }
        unsigned t[8];
#pragma unroll
        for (int i = 0; i < 8; ++i) t[i] = __builtin_amdgcn_ds_bpermute(l15 * 16 + (g >> 1) * 4 + (g & 1) * 8, xr[i >> 2][i & 3]);
#pragma unroll
        for (int j = 0; j < 2; ++j) {
            auto u = __builtin_amdgcn_permlane16_swap(t[4 * j], t[4 * j + 2], false, false);
            auto v = __builtin_amdgcn_permlane16_swap(t[4 * j + 1], t[4 * j + 3], false, false);
#pragma unroll
            for (int b = 0; b < 2; ++b) a0_[2 * j + b] = (f32x4){lo16(u[b]), hi16(u[b]), lo16(v[b]), hi16(v[b])} * og;
        }
        asm volatile("" : "+v"(a0_[0]), "+v"(a0_[1]), "+v"(a0_[2]), "+v"(a0_[3]), "+v"(og));
        SB0();
    };
    auto xg_def = [&]() AI { asm volatile("" : "=v"(xr[0]), "=v"(xr[1]), "=v"(gg)); };
    auto relane = [&]() AI {
        unsigned z = 0;
        asm volatile("" : "+v"(z));
        l = __builtin_amdgcn_mbcnt_hi(~0u, __builtin_amdgcn_mbcnt_lo(~0u, z));
        g = l >> 4, l15 = l & 15, q = l15 >> 2, p = l & 3, srow = l >> 3, sch = l & 7, l16 = l * 16;
        AX = lds0 + L_A + rg * 4096 + l16, W4 = lds0 + p * 4, PG = lds0 + L_PROG + p * 256, xo = srow * 2048 + sch * 16;
    };
    auto kf_fetch = [&]() AI {
        asm volatile("s_mov_b32 m0, %3\n\ts_mov_b64 exec, 0xff\n\tglobal_load_lds_dwordx4 %1, %2 sc1\n\ts_mov_b64 exec, -1\n\t"
                     "s_mov_b32 m0, %4\n\ts_nop 0\n\t"
                     "global_load_lds_dwordx4 %0, %2 sc1\n\tglobal_load_lds_dwordx4 %0, %2 offset:1024 sc1\n\t"
                     "global_load_lds_dwordx4 %0, %2 offset:2048 sc1\n\tglobal_load_lds_dwordx4 %0, %2 offset:3072 sc1"
                     ::"v"(l16), "v"((unsigned)(4096 + (l & 4) * 128 + p * 16)), "s"(ws + WS_KF + (hp * 2 + hs) * 8192),
                       "s"(lds0 + L_KZ + hs * 128), "s"(lds0 + L_KF + hs * 4096) : "memory");
    };
    auto fd_def = [&]() AI {
#pragma unroll
        for (int i = 0; i < 16; ++i) asm volatile("" : "=v"(fd[i]));
        asm volatile("" : "=v"(gu[0]), "=v"(gu[1]));
    };
    const int oh = hs ^ kp ^ 1;

    if (__builtin_expect(w >= 4, 1)) {
        const bool loader = !(w & 1);
        const unsigned kf_kv = WS_KF + (hp * 2 + (mt >> 3)) * 8192 + (mt & 7) * 512;
        const unsigned kf_z = WS_KF + (hp * 2 + (mt >> 3)) * 8192 + 4096 + (mt & 7) * 128;
        const unsigned fgo = ffn ? (unsigned)((256 * kq + 16 * srow) * 7168 + cs * 128 + sch * 16) : 0u;
        const char* fsrc = (const char*)(hs ? wu : wg);
        auto gu_w = [&](int q0, int q1) AI {
#pragma unroll
            for (int i = q0; i < q1; ++i) {
                unsigned t1, t2, t3;
                asm volatile("v_add_u32 %4, 0x1c00, %7\n\tv_add_u32 %5, 0x3800, %7\n\tv_add_u32 %6, 0x5400, %7\n\t"
                             "global_load_dwordx4 %0, %7, %8 nt\n\tglobal_load_dwordx4 %1, %4, %8 nt\n\t"
                             "global_load_dwordx4 %2, %5, %8 nt\n\tglobal_load_dwordx4 %3, %6, %8 nt"
                             : "=&v"(fw[4 * i]), "=&v"(fw[4 * i + 1]), "=&v"(fw[4 * i + 2]), "=&v"(fw[4 * i + 3]), "=&v"(t1), "=&v"(t2), "=&v"(t3)
                             : "v"(fgo), "s"(fsrc + (128 * (i >> 2) + 4 * (i & 3)) * 7168) : "memory");
            }
        };
        const unsigned fdo = cd < 56 ? (unsigned)((64 * cd + 8 * srow) * 2048 + 512 * kq + 256 * hs + sch * 16) : 0u;
        auto dn_w = [&](int q0, int q1) AI {
#pragma unroll
            for (int i = q0; i < q1; ++i)
                asm volatile("global_load_dwordx4 %0, %4, %5 offset:-4096 nt\n\tglobal_load_dwordx4 %1, %4, %5 offset:-2048 nt\n\t"
                             "global_load_dwordx4 %2, %4, %5 nt\n\tglobal_load_dwordx4 %3, %4, %5 offset:2048 nt"
                             : "=&v"(fd[4 * i]), "=&v"(fd[4 * i + 1]), "=&v"(fd[4 * i + 2]), "=&v"(fd[4 * i + 3])
                             : "v"(fdo), "s"((const char*)wd + 4096 + (i & 1) * 8192 + (i >> 1) * 128) : "memory");
        };
        auto gate_up = [&]() AI {
            sel_init();
            u32x4 xs[4];
            waitv<4 * (NF - NF1) + 1 + 4 * (8 - GQ1) + 16>();
#pragma unroll
            for (int i = 0; i < 4; ++i) xs[i] = lds_read<u32x4>(lds0 + L_XS + hs * 512 + (i >> 1) * 256 + srow * 32 + (i & 1) * 16);
            f32x4 a;
            bf16x8 t[2];
            t[0] = spread(xs[0], 0);
            sfor<0, 32>([&](auto I) {
                constexpr int i = I;
                SB0();
                if constexpr (i < 31) t[~i & 1] = spread(xs[(i + 1) >> 3], (i + 1) & 7);
                else asm volatile("s_nop 1");
                asm volatile("" : "+v"(t[~i & 1]));
                if constexpr (i == 4 * GQ1) waitv<16>();
                SB0();
                u32x4& f = fw[i];
                bf16x8& u = t[i & 1];
                f32x4& c = a;
                pin(f);
                if constexpr (i == 0) asm volatile("v_mfma_f32_16x16x32_bf16 %0, %1, %2, 0" : "=&v"(c) : "v"(u), "v"(f));
                else asm volatile("v_mfma_f32_16x16x32_bf16 %0, %1, %2, %0" : "+v"(c) : "v"(u), "v"(f));
            });
            asm volatile("s_nop 7" : "+v"(a));
            SB0();
            fold_store(a, WS_GU + cs * 4096 + kq * 512 + hs * 256);
            SB0();
        };
        u64 T;
        u32x4 sv[2];
        u32x2 sg;
        auto rg_load = [&]() AI {
            asm volatile("global_load_dwordx4 %0, %3, %4 sc1\n\tglobal_load_dwordx4 %1, %3, %4 offset:16 sc1\n\tglobal_load_dwordx2 %2, %5, %6 sc1"
                         : "=&v"(sv[0]), "=&v"(sv[1]), "=&v"(sg) : "v"(l * 32), "s"(ws + WS_RG), "v"(l * 8), "s"(ws + WS_GG + (mt * 2 + hs) * 512) : "memory");
        };
        auto select = [&]() AI {
            const unsigned tl = (unsigned)T & 0x1FFFu, thi = (unsigned)(T >> 13);
#pragma unroll 1
            for (unsigned s = 0; s < SPIN_CAP; ++s) {
                pin(sv[0]);
                pin(sv[1]);
                asm volatile("" : "+v"(sg));
                const unsigned bad = ((sg[0] >> 16) ^ ((unsigned)T & 0xFFFFu)) | (sg[1] ^ (unsigned)(T >> 16)) | ((sv[0][0] >> 19) ^ tl) | (sv[0][1] ^ thi) | ((sv[0][2] >> 19) ^ tl) | (sv[0][3] ^ thi) |
                                     ((sv[1][0] >> 19) ^ tl) | (sv[1][1] ^ thi) | ((sv[1][2] >> 19) ^ tl) | (sv[1][3] ^ thi);
                if (__all(bad == 0u)) break;
                __builtin_amdgcn_s_sleep(16);
                rg_load();
                waitv<0>();
            }
            pin(sv[0]);
            pin(sv[1]);
            asm volatile("" : "+v"(sg));
            lds_write<unsigned>(lds0 + L_G + hs * 512 + l * 4, sg[0] << 16);
            SB0();
            unsigned k2 = 0;
#pragma unroll
            for (int j = 0; j < 4; ++j) {
                const unsigned v = sv[j >> 1][(j & 1) * 2] & 0x7FFFFu;
                k2 = max(k2, ((v >> 3) << 11) | ((255 - (4 * l + j)) << 3) | (v & 7));
            }
            const unsigned key = __builtin_amdgcn_readfirstlane(wave_umax(k2));
            asm volatile("s_mov_b32 m0, %2\n\ts_mov_b64 exec, %3\n\tglobal_load_lds_dwordx4 %0, %1\n\ts_mov_b64 exec, -1"
                         ::"v"((unsigned)(512 * kq) + l16), "s"(x + (size_t)(2047 - (int)(key & 2047u)) * DM), "s"(lds0 + L_XS + hs * 512), "s"(0xFFFFFFFFull) : "memory");
            lds_write<u32x4>(lds0 + L_SEL, (u32x4){(unsigned)T, (unsigned)(T >> 32), 2047u - (key & 2047u), key >> 11});
            if (bid == 0 && hs) ST_MASK("global_store_dwordx2", 1ull, 0u, T, ws + WS_E);
            SB0();
        };
        auto k_soft = [&]() AI {
            bf16x8 a0[2], kb[2];
            f32x4 pk[2][2][2];
            unsigned pw[4];
            phi_w(a0, 0);
            phi_adr(pw, lds0 + P_U[0] * 8192);
            asm volatile("" : "+v"(pw[0]), "+v"(pw[1]), "+v"(pw[2]), "+v"(pw[3]));
            auto logits = [&](int h) AI {
                lds_wait(WR + L_FK0 + 4 * h, 1);
#pragma unroll
                for (int i = 0; i < 2; ++i) kb[i] = lds_read128(AX + (P_U[0] * 8192 - L_A) + h * 4096 + i * 1024);
#pragma unroll
                for (int i = 0; i < 4; ++i) pk[h][i >> 1][i & 1] = mfma16(a0[i & 1], kb[i >> 1], zero);
            };
            auto round = [&](int h) AI {
#pragma unroll
                for (int i = 0; i < 4; ++i) pk[h][i >> 1][i & 1] = rbf4(pk[h][i >> 1][i & 1]);
            };
            logits(0);
            SB0();
            round(0);
            SB0();
            phi_k(pk[0], pw, 0, [&]() AI {
                logits(1);
                SB0();
            });
            SB0();
            round(1);
            phi_k(pk[1], pw, 4096, []() {});
            SB0();
            flag(WR + L_FP);
        };
        auto q_soft = [&](auto hook) AI {
            bf16x8 a1[2];
            f32x4 pq[2][2];
            phi_w(a1, hs);
            lds_wait0(WR + L_FQ);
#pragma unroll
            for (int rb = 0; rb < 2; ++rb) {
                const bf16x8 b = lds_read128(AX + rb * 1024);
#pragma unroll
                for (int fb = 0; fb < 2; ++fb) pq[rb][fb] = rbf4(mfma16(a1[fb], b, zero));
            }
            softmax2(pq, ph, hook);
            asm volatile("" : "+v"(ph[0][0]), "+v"(ph[0][1]), "+v"(ph[1][0]), "+v"(ph[1][1]));
        };
        if (__builtin_expect(loader, 1)) {
            vx_init();
            vw_init();
            unsigned pv = 0;
            sfor<4, NF1>([&](auto N) {
                constexpr int n = N;
                if constexpr (F_N[n] > F_N[n - 1]) {
                    __builtin_amdgcn_s_setprio(3);
                    lds_wait(pv, PG, F_N[n]);
                    if (F_N[n] < RLD) lds_peek(pv, PG);
                    __builtin_amdgcn_s_setprio(0);
                }
                if constexpr (n == 12) {
                    asm volatile("s_mov_b32 m0, %2\n\ts_nop 0\n\tglobal_load_lds_dwordx4 %0, %1\n\tglobal_load_lds_dwordx4 %0, %1 offset:1024"
                                 ::"v"((unsigned)((l >> 2) * 64 + (((l & 3) ^ (2 * ((l >> 4) & 1))) * 16))),
                                   "s"(wphi + (hp * 2 + hs) * 1024), "s"(lds0 + L_WPHI + hs * 2048) : "memory");
                }
                fill(N);
            });
            flag(lds0 + L_FLD + hs * 4);
            __builtin_amdgcn_s_setprio(2);
            const char* cla = ws + WS_CL + ((l15 * 2 + (mt >> 3)) * 16 + hp) * 128;
            u64 cl;
            asm volatile("global_load_dwordx2 %0, %1, off sc1" : "=&v"(cl) : "v"(cla) : "memory");
            k_soft();
            SB0();
            lds_wait(W4 + L_FP, 1);
            sq = lds_read<u32x4>(lds0 + L_SEL);
            waitv<0>();
            asm volatile("" : "+v"(cl));
            SB0();
            {
                u32x4 kp_[6];
                const char* kb = ws + WS_KV + (hp * 8 + (mt >> 1)) * 16384 + (mt & 1) * 512 + hs * 256;
                const bool zw = !((mt & 3) | hs);
                const char* zb = zw ? ws + WS_Z + (hp * 4 + (mt >> 2)) * 2048 : kb;
                const unsigned kvo = g * 1024 + l15 * 16, zo = zw ? l16 : 0u;
                auto ko = [&](int i) { return i < 4 ? kvo : zo; };
                auto ka = [&](int i) { return i < 4 ? kb + i * 4096 : zb + (i - 4) * 1024; };
                const u64 T = (u64)sq[1] << 32 | sq[0];
#pragma unroll 1
                for (unsigned s = 0; s < SPIN_CAP && __ballot(cl != T); ++s)
                    asm volatile("global_load_dwordx2 %0, %1, off sc1\n\ts_waitcnt vmcnt(0)" : "=&v"(cl) : "v"(cla) : "memory");
                u32x4 a01;
#pragma unroll
                for (int i = 0; i < 4; ++i) a01[i] = (l15 >> 1) != i ? 0u : (l & 1) ? 0x3f800000u : 0x3f80u;
                unsigned po = kf_kv + hs * 256 + l15 * 16 + (g & 1) * 8, pz = kf_z + (l & 8) * 8 + p * 16 + (sch >> 2) * 8 + (g >> 1) * 4;
                pin(a01);
                asm volatile("" : "+v"(po), "+v"(pz));
                lds_wait0(W4 + L_FKS);
                __builtin_amdgcn_s_sleep(GDL);
#pragma unroll
                for (int i = 0; i < 6; ++i) ld_async(kp_[i], ko(i), ka(i));
                xg_load();
                SB0();
                waitv<3>();
                SB0();
                u32x2 o;
                auto sum = [&]() AI {
                    f32x4 d = zero;
#pragma unroll
                    for (int i = 0; i < 4; ++i) d = mfma16(__builtin_bit_cast(bf16x8, a01), __builtin_bit_cast(bf16x8, kp_[i]), d);
                    SB0();
                    unsigned m = least(kp_);
                    asm volatile("" : "+v"(m));
                    SB0();
                    o = (u32x2){nz1(pack2(d[0], d[1])), nz1(pack2(d[2], d[3]))};
                    asm volatile("" : "+v"(o));
                    return m;
                };
#pragma unroll
                for (int i = 0; i < 6; ++i) pin(kp_[i]);
                if (__builtin_expect(__ballot(sum() == 0u) != 0ull, 0)) {
                    again(kp_, ko, ka);
                    sum();
                }
                SB0();
                ST_MASK("global_store_dwordx2", 0xFFFFFFFFull, po, o, ws);
                if (zw) {
                    const f32x4 z = __builtin_bit_cast(f32x4, kp_[4]) + __builtin_bit_cast(f32x4, kp_[5]);
                    const unsigned t = __float_as_uint(dpp_add<0x128>(swap16_add(swap32_add(z[0], z[2]), swap32_add(z[1], z[3]))));
                    auto r = __builtin_amdgcn_permlane16_swap(t, t, false, false);
                    ST_MASK("global_store_dword", 0x0000FFFF0000FFFFull, pz, nz1(pack2(__uint_as_float(r[0]), __uint_as_float(r[1]))), ws);
                }
                flag(lds0 + L_FG + hs * 4);
            }
            SB0();
            need(KFN);
            q_soft([]() {});
            SB0();
            __builtin_amdgcn_s_sleep(KFD);
            SB0();
            kf_fetch();
            mark(lds0 + L_FKF + hs * 4);
            SB0();
            waitv<6>();
            x_init(false);
            fd_def();
            xg_def();
        } else {
            const int row0 = (mt * 16 + hp) * 8 + 4 * hs;
            __builtin_amdgcn_s_setprio(2);
            asm volatile("s_waitcnt lgkmcnt(0)" : "+s"(sd));
            lds_wait(lds0 + L_FULL + p * 4, 3);
            u32x4 rv[2], gv[2], xv[4][2];
            u64 ev;
            asm volatile("global_load_dwordx2 %4, %5, %9 sc1\n\t"
                         "global_store_dwordx4 %10, %11, %9 sc1\n\t"
                         "global_store_dwordx2 %12, %13, %9 sc1\n\t"
                         "global_load_dwordx4 %0, %6, %7\n\tglobal_load_dwordx4 %1, %6, %7 offset:1024\n\t"
                         "global_load_dwordx4 %2, %6, %8\n\tglobal_load_dwordx4 %3, %6, %8 offset:1024"
                         : "=&v"(rv[0]), "=&v"(rv[1]), "=&v"(gv[0]), "=&v"(gv[1]), "=&v"(ev)
                         : "v"(0u), "v"(l16), "s"(wr), "s"(gw), "s"(ws),
                           "v"(l < 32 ? WS_Y + cd * 4096 + kq * 1024 + hs * 512 + l16 : WS_GU + cs * 4096 + kq * 512 + hs * 256 + l15 * 16), "v"(z4),
                           "v"(hs ? kf_z + l15 * 8 : kf_kv + l * 8), "v"(0ull)
                         : "memory");
#pragma unroll
            for (int r = 0; r < 2; ++r)
                asm volatile("global_load_dwordx4 %0, %4, %5\n\tglobal_load_dwordx4 %1, %4, %5 offset:1024\n\t"
                             "global_load_dwordx4 %2, %4, %5 offset:2048\n\tglobal_load_dwordx4 %3, %4, %5 offset:3072"
                             : "=&v"(xv[2 * r][0]), "=&v"(xv[2 * r][1]), "=&v"(xv[2 * r + 1][0]), "=&v"(xv[2 * r + 1][1])
                             : "v"(l16), "s"(x + (size_t)(row0 + 2 * r) * DM) : "memory");
            SB0();
            waitv<0>();
            asm volatile("" : "+v"(rv[0]), "+v"(rv[1]), "+v"(gv[0]), "+v"(gv[1]), "+v"(ev));
#pragma unroll
            for (int r = 0; r < 4; ++r) asm volatile("" : "+v"(xv[r][0]), "+v"(xv[r][1]));
            SB0();
            T = ((u64)__builtin_amdgcn_readfirstlane((unsigned)(ev >> 32)) << 32 | __builtin_amdgcn_readfirstlane((unsigned)ev)) + 1;
            float ra_[4] = {0.f, 0.f, 0.f, 0.f}, ga_[4] = {0.f, 0.f, 0.f, 0.f};
#pragma unroll
            for (int r = 0; r < 4; ++r)
#pragma unroll
                for (int i = 0; i < 2; ++i)
#pragma unroll
                    for (int j = 0; j < 4; ++j) {
                        ra_[r] = dot2(xv[r][i][j], rv[i][j], ra_[r]);
                        ga_[r] = dot2(xv[r][i][j], gv[i][j], ga_[r]);
                    }
            const int rr = (g >> 1) | ((g & 1) << 1);
            unsigned k = f2bf(sum4(ra_));
            k ^= (k & 0x8000u) ? 0xFFFFu : 0x8000u;
            k = (k << 3) | (7 - 4 * hs - rr);
            auto r1 = __builtin_amdgcn_permlane16_swap(k, k, false, false);
            k = max(r1[0], r1[1]);
            auto r2 = __builtin_amdgcn_permlane32_swap(k, k, false, false);
            k = max(r2[0], r2[1]);
            u64 gq = (T << 16) | f2bf(1.f / (1.f + __expf(-rbf(sum4(ga_)))));
            if (hs) lds_write<unsigned>(lds0 + L_BEST, k | 0x80000000u);
            asm volatile("" : "+v"(gq), "+v"(k));
            __builtin_amdgcn_s_setprio(0);
            lds_wait8(PG, CLR0 + ph4);
            if (!hs) {
                lds_wait(lds0 + L_BEST, 1);
                const u64 rq = (T << 19) | max(k, lds_read<unsigned>(lds0 + L_BEST) & 0x7FFFFu);
                ST_MASK("global_store_dwordx2", 1ull, 0u, rq, ws + WS_RG + (mt * 16 + hp) * 8);
            }
            ST_MASK("global_store_dwordx2", 0x0001000100010001ull, (unsigned)(rr * 8), gq, ws + WS_GG + row0 * 8);
            SB0();
#pragma unroll
            for (int n = 0; n < 4; ++n) st_wt(rsrc, WS_KV + ((hp * 8 + hs * 4 + n) * 16 + mt) * 1024 + l16, z4);
            ST_MASK("global_store_dwordx4", 0xFFFFull, WS_Z + ((hp * 4 + hs * 2 + (srow & 1)) * 16 + mt) * 128 + sch * 16, z4, ws);
#pragma unroll
            for (int n = 0; n < 8; ++n) st_wt(rsrc, WS_O + ((mt * 2 + (n >> 2)) * 32 + 2 * hp + hs) * 4096 + (n & 3) * 1024 + l16, z4);
            waitv<0>();
            ST_MASK("global_store_dwordx2", 0xFFFFull, (unsigned)l * 8, T, ws + WS_CL + ((mt * 2 + hs) * 16 + hp) * 128);
            SB0();
            vw_init();
            asm volatile("" : "+v"(vw[0]), "+v"(vw[1]), "+v"(vw[2]), "+v"(vw[3]));
            lds_wait8(PG, RLD);
            __builtin_amdgcn_s_setprio(3);
            lds_wait(lds0 + L_FLD + (l & 1) * 4, 1);
            rg_load();
            SB0();
            gu_w(0, GQ1);
            waitv<4 * GQ1>();
            select();
            flag(WR + L_FP);
            sfor<NF1, NF>([&](auto N) {
                constexpr int n = N;
                if constexpr (n == NF1 || F_N[n] != F_N[n - 1]) need(F_N[n]);
                fill(N);
            });
            SB0();
            gu_w(GQ1, 8);
            dn_w(0, 4);
            SB0();
            gate_up();
            __builtin_amdgcn_s_setprio(0);
            SB0();
            xg_load();
            lds_wait(W4 + L_FKS, 1);
            wo_load(0, WOE, false);
            SB0();
            sq = lds_read<u32x4>(lds0 + L_SEL);
            waitv<4 * WOE>();
            x_init(false);
            lds_wait(lds0 + L_FG + (l & 1) * 4, 1);
            gu_load();
            wo_load(WOE, WOM, false);
            SB0();
            __builtin_amdgcn_s_setprio(2);
            q_soft([&]() AI {
                wo_load(WOM, WON, true);
                SB0();
            });
            waitv<4 * (WON - WOE) + 1>();
#pragma unroll
            for (int i = 0; i < 16; ++i) pin(fd[i]);
            pin(gu[0]);
            pin(gu[1]);
            SB0();
            xg_def();
        }
        SB0();
    } else {
        __builtin_amdgcn_s_setprio(1);
        unsigned ra[4], xt[2];
#pragma unroll
        for (int u = 0; u < 4; ++u) ra[u] = lds0 + J_B[0] * 8192 + (8 * g + q) * 128 + ((u ^ (q >> 1) ^ ((g & 1) << 1)) * 32) + 8 * p;
        unsigned xa = lds0 + (32 * (w & 1) + l15) * 128 + ((g ^ (l15 >> 1)) * 16), fa = lds0 + L_FULL + (l & 3) * 4;
        xt[0] = xa + (hs ? X_U1[0] : X_U0[0]) * 8192;
        xt[1] = xt[0] ^ 64;
        asm volatile("s_mov_b32 m0, %4" : "+v"(xa), "+v"(fa), "+v"(xt[0]), "+v"(xt[1]) : "s"(lds0 + L_PROG + w * 256 - 124));
        f32x4 acc[3][4][2];
        bf16x8 xf[ND][2][2];
        s16x4 wh[PD + 1][2];
        unsigned fv = 0;
        bf16x8 kown[2];
        u64 bad = ~0ull;
        auto chk = [&](int c) AI {
            SB0();
            if (__builtin_expect(bad != 0ull, 0)) {
                unsigned c_;
                asm volatile("s_mov_b32 %1, 0x400000\n"
                             ".Lp%=:\n\tds_read_b32 %0, %2 offset:%3\n\ts_waitcnt lgkmcnt(0)\n\tv_cmp_gt_u32 vcc, %4, %0\n\ts_cbranch_vccz .Lf%=\n\t"
                             "s_sub_u32 %1, %1, 1\n\ts_cbranch_scc0 .Lp%=\n.Lf%=:"
                             : "=&v"(fv), "=&s"(c_) : "v"(fa), "n"(c > NM1 ? 32 : 0), "n"(c) : "memory", "vcc", "scc");
            }
            SB0();
        };
        auto kv_part = [&]() AI {
            bf16x8 ones;
#pragma unroll
            for (int j = 0; j < 8; ++j) ones[j] = (short)0x3f80;
            unsigned pa = lds0 + (hs ? P_U[2] : P_U[0]) * 8192 + (4 * g + q) * 128 + (((8 * (rg & 1) + p) ^ fsw(4 * g + q)) << 3);
            unsigned pb = pa ^ 32, va = lds0 + L_A + hs * 2048 + l16, kv_st = WS_KV + ((hp * 8 + rg * 2) * 16 + mt) * 1024 + l16;
            unsigned kv_s1 = kv_st + 16384, z_st = WS_Z + ((hp * 4 + rg) * 16 + mt) * 128 + (l & 1) * 64 + g * 16, fk = WR + L_FKS;
            asm volatile("" : "+v"(pa), "+v"(pb), "+v"(va), "+v"(kv_st), "+v"(kv_s1), "+v"(z_st), "+v"(fk), "+v"(ones));
            bf16x8 b0[4], b1[4], af[2][4];
            lds_wait0(lds0 + L_FV + sch * 4);
            SB0();
#pragma unroll
            for (int i = 0; i < 4; ++i) {
                af[0][i] = cat8(tr_read(pa + i * 4096), tr_read(pa + i * 4096 + 2048));
                b0[i] = lds_read128(va + i * 4096);
                SB0();
                af[1][i] = cat8(tr_read(pb + i * 4096), tr_read(pb + i * 4096 + 2048));
                SB0();
            }
#pragma unroll
            for (int i = 0; i < 4; ++i) b1[i] = lds_read128(va + i * 4096 + 1024);
            SB0();
            f32x4 k0[2] = {zero, zero}, k1[2] = {zero, zero}, zt[2] = {zero, zero};
#pragma unroll
            for (int i = 0; i < 4; ++i)
#pragma unroll
                for (int e = 0; e < 2; ++e) {
                    k0[e] = mfma16(af[e][i], b0[i], k0[e]);
                    zt[e] = mfma16(af[e][i], ones, zt[e]);
                }
            SB0();
#pragma unroll
            for (int i = 0; i < 4; ++i)
#pragma unroll
                for (int e = 0; e < 2; ++e) k1[e] = mfma16(af[e][i], b1[i], k1[e]);
            st_wt(rsrc, kv_st, nz4(__builtin_bit_cast(u32x4, pack44(k0[0], k0[1]))));
            const u32x4 zo = nz4((l & 1) ? zt[1] : zt[0]);
            ST_MASK("global_store_dwordx4", 0x0003000300030003ull, z_st, zo, ws);
            st_wt(rsrc, kv_s1, nz4(__builtin_bit_cast(u32x4, pack44(k1[0], k1[1]))));
            SB0();
            flag(fk);
        };
        auto report = [&](int r) AI {
            unsigned t, a;
            if (r > RLD) asm volatile("v_mov_b32 %0, %3\n\tv_mov_b32 %1, %2\n\tds_write_b32 %1, %0" : "=&v"(t), "=&v"(a) : "s"(WR + L_PRL), "n"(r) : "memory");
            else asm volatile("global_load_lds_dword %0, %1 offset:%2" ::"v"(v0), "s"(one), "n"(4 * r) : "memory");
        };
        auto rd1 = [&](auto F, auto H) AI {
            constexpr int f = F, h = H, n = f >> 2, j = f >> 3, c = h ? E_B[f] : E_A[f], a = c % 100;
            if constexpr (h == 0 && (f & 3) == 0 && Q_C[n] > 0) chk(Q_C[n]);
            wh[f % (PD + 1)][h] = tr_read(ra[f & 3] + (J_U[j] - J_B[j]) * 8192 + (n & 1) * 4096 + h * 512);
            SB0();
            if constexpr (c / 100 == 1) {
                constexpr int jn = (f + 3 - a) >> 3;
                asm volatile("v_add_u32 %0, %1, %0" : "+v"(ra[a]) : "n"((J_B[jn] - J_B[jn - 1]) * 8192));
            } else if constexpr (c / 50 == 4) {
                asm volatile("v_add_u32 %0, %1, %2" : "=v"(xt[0]) : "s"((hs ? X_U1[a] : X_U0[a]) * 8192), "v"(xa));
            } else if constexpr (c / 50 == 5) {
                asm volatile("v_xor_b32 %0, 64, %1" : "=v"(xt[1]) : "v"(xt[0]));
            } else if constexpr (c / 100 == 3) {
                xf[a % 25 % ND][a / 25 & 1][a / 50] = lds_read128(xt[a / 50] + (a / 25 & 1) * 2048);
            } else if constexpr (c / 100 == 4) {
                fv = *(volatile AS3 unsigned*)(size_t)(fa + (a > NM1 ? 32 : 0));
            } else if constexpr (c / 100 == 5) {
                bad = __builtin_amdgcn_ballot_w64(fv < (unsigned)a);
                asm volatile("" : "+s"(bad));
            } else if constexpr (c / 100 == 6) {
                report(a);
            }
        };
        sfor<0, PD>([&](auto F) { rd1(F, IC<0>{}); rd1(F, IC<1>{}); });
        SB0();
        constexpr int K0F = 4 * KD, K1F = 4 * KD + 1;
        sfor<0, 96>([&](auto N) {
            sfor<0, 4>([&](auto E) {
                constexpr int n = N, e = E, f = 4 * n + e, j = n >> 1, ks = n & 1, u = e, s = J_X[j], m = J_M[j];
                const bf16x8 wv = cat8(wh[f % (PD + 1)][0], wh[f % (PD + 1)][1]);
                sfor<0, 2>([&](auto RB) {
                    constexpr int rb = RB;
                    const bf16x8 xv = xf[s % ND][rb][ks];
                    if (j_first(j) && ks == 0) asm volatile("v_mfma_f32_16x16x32_bf16 %0, %1, %2, 0" : "=&v"(acc[m][u][rb]) : "v"(m == 1 ? xv : wv), "v"(m == 1 ? wv : xv));
                    else asm volatile("v_mfma_f32_16x16x32_bf16 %0, %1, %2, %0" : "+v"(acc[m][u][rb]) : "v"(m == 1 ? xv : wv), "v"(m == 1 ? wv : xv));
                    SB0();
                    if constexpr (f + PD < 384 && (f + PD < 4 * VD || f >= 4 * VD)) rd1(IC<f + PD>{}, RB);
                    if constexpr (rb == 0 && f == K0F) relane();
                    if constexpr (rb == 0 && (f == K0F || f == K1F)) asm volatile("s_nop 7");
                    SB0();
                    if constexpr (f == K0F) lds_write<bf16x8>(AX + (P_U[0] * 8192 - L_A) + rb * 1024, pack44(acc[0][0][rb], acc[0][1][rb]));
                    if constexpr (f == K1F) {
                        kown[rb] = pack44(acc[0][2][rb], acc[0][3][rb]);
                        asm volatile("" : "+v"(kown[rb]));
                    }
                    SB0();
                });
                if constexpr (f == K0F) flag(WR + L_FK0);
                if constexpr (f == 4 * VD - 1) {
                    bf16x8 ak[2];
                    f32x4 pk[2][2];
                    unsigned pw[4];
                    __builtin_amdgcn_s_setprio(2);
                    phi_w(ak, 1);
                    asm volatile("s_nop 7");
                    SB0();
#pragma unroll
                    for (int i = 0; i < 4; ++i) lds_write<bf16x8>(AX + i * 1024, pack44(acc[1][i][0], acc[1][i][1]));
                    SB0();
#pragma unroll
                    for (int i = 0; i < 4; ++i) pk[i >> 1][i & 1] = rbf4(mfma16(ak[i & 1], kown[i >> 1], zero));
                    phi_adr(pw, lds0 + P_U[2] * 8192);
                    phi_k(pk, pw, 0, []() {});
                    SB0();
                    flag(WR + L_FV);
                    kv_part();
                    __builtin_amdgcn_s_setprio(1);
                    fv = 0;
                    bad = ~0ull;
                    sfor<4 * VD, 4 * VD + PD>([&](auto F) { rd1(F, IC<0>{}); rd1(F, IC<1>{}); });
                    SB0();
                }
                if constexpr (f >= 24 && f < 56 && (f & 7) == 7) __builtin_amdgcn_s_sleep(5);
            });
        });
        asm volatile("s_nop 7");
        SB0();
        __builtin_amdgcn_s_setprio(0);
        flag(WR + L_FQD);
        bf16x8 a0[2];
        f32x4 pq[2][2];
        phi_w(a0, hs ^ 1);
        lds_wait(W4 + L_FKS, 1);
#pragma unroll
        for (int rb = 0; rb < 2; ++rb) {
            const bf16x8 b0 = pack44(acc[2][0][rb], acc[2][1][rb]), b1 = pack44(acc[2][2][rb], acc[2][3][rb]);
            lds_write<bf16x8>(AX + rb * 1024, hs ? b1 : b0);
#pragma unroll
            for (int fb = 0; fb < 2; ++fb) pq[rb][fb] = mfma16(a0[fb], hs ? b0 : b1, zero);
        }
        SB0();
        flag(WR + L_FQ);
        sq = lds_read<u32x4>(lds0 + L_SEL);
        SB0();
#pragma unroll
        for (int i = 0; i < 4; ++i) pq[i >> 1][i & 1] = rbf4(pq[i >> 1][i & 1]);
        softmax2(pq, ph, []() {});
        asm volatile("" : "+v"(ph[0][0]), "+v"(ph[0][1]), "+v"(ph[1][0]), "+v"(ph[1][1]));
        SB0();
        asm volatile("s_mov_b32 m0, %3\n\ts_nop 0\n\tglobal_load_lds_dwordx4 %0, %1\n\tglobal_load_lds_dwordx4 %0, %2 offset:1024"
                     ::"v"((unsigned)(srow * 2048 + (sch ^ srow) * 16)), "s"(x + (size_t)row0 * DM + cb * 64),
                       "s"(x + (size_t)(row0 + 8) * DM + cb * 64 - 512), "s"(lds0 + L_A + 2048 + rg * 4096) : "memory");
        g_load();
        asm volatile("" : "=v"(xr[0]), "=v"(xr[1]));
        asm volatile("" : "=v"(a0_[0]), "=v"(a0_[1]), "=v"(a0_[2]), "=v"(a0_[3]), "=v"(og));
        fd_def();
    }
    relane();
    const char* cla = ws + WS_CL + ((rt * 2 + kp) * 16 + l15) * 128;
    const u64 T = (u64)sq[1] << 32 | sq[0];
    u64 cl;
    unsigned ka0 = lds0 + L_KF + oh * 4096 + l16, ka1 = lds0 + L_KZ + oh * 128 + g * 16;
    unsigned o_st = WS_O + ((mt * 2 + hs) * 32 + 2 * hp + oh) * 4096 + (rg & 1) * 2048 + l16;
    float gt[2];
#pragma unroll
    for (int i = 0; i < 2; ++i) gt[i] = lds_read<float>(lds0 + L_G + hs * 512 + (rg & 1) * 128 + i * 64 + l15 * 4);
    asm volatile("" : "+v"(ka0), "+v"(ka1), "+v"(o_st), "+v"(cla), "+v"(gt[0]), "+v"(gt[1]));
    u32x4 kf[6];
    auto kf_read = [&]() AI {
#pragma unroll
        for (int i = 0; i < 2; ++i) kf[4 + i] = lds_read<u32x4>(ka1 + i * 64);
        SB0();
#pragma unroll
        for (int i = 0; i < 4; ++i) kf[i] = lds_read<u32x4>(ka0 + i * 1024);
    };
    lds_wait0(lds0 + L_FKF + oh * 4);
    kf_read();
    asm volatile("global_load_dwordx2 %0, %1, off sc1" : "=&v"(cl) : "v"(cla) : "memory");
    SB0();
    f32x4 nm[2][2];
    float rd_[2];
    if (__builtin_expect(__ballot(o_sums(kf, ph, gt, rd_, nm) == 0u) != 0ull, 0)) {
#pragma unroll 1
        for (unsigned n = 2; n < SPIN_CAP; ++n) {
            if ((w & 5) == 4) {
                kf_fetch();
                waitv<0>();
                lds_write<unsigned>(lds0 + L_FKF + oh * 4, n);
            } else lds_wait(lds0 + L_FKF + oh * 4, n);
            asm volatile("" ::: "memory");
            kf_read();
            if (__ballot(o_sums(kf, ph, gt, rd_, nm) == 0u) == 0ull) break;
        }
        SB0();
    }
    SB0();
    o_store(rd_, nm, rsrc, o_st);
    SB0();
    flag(lds0 + L_FOS + w * 4);
    waitv<3>();
    __builtin_amdgcn_s_setprio(0);
    if ((w & 5) == 4) {
        lds_wait(lds0 + L_FOS + sch * 4, 1);
        wo_load(WON, 16, true);
    }
    if ((w & 5) == 5) {
        if (cd < 56) {
            sel_init();
            down();
        } else {
            __builtin_amdgcn_s_sleep(PDL);
            waitv<2>();
        }
    }
    flag(lds0 + L_FWO + w * 4);
    const u32x2 selv = {sq[2], sq[3]};

    SB0();
    const char* ob = ws + WS_O + (rt * 2 + (rg >> 1)) * 131072 + (rg & 1) * 2048;
    const unsigned ov0 = l16 + kp * 1024, ov1 = l16 + (kp ^ 1) * 1024;
    auto oa = [&](int i) { return ob + ((2 * i + kp + rot) & 31) * 4096; };
    u32x4 of[3][4];
    auto fetch1 = [&](int k, int j) AI { ld_async(of[k % 3][j], (j & 1) ? ov1 : ov0, oa(2 * k + (j >> 1))); };
    auto fix = [&](int k) AI { again(of[k % 3], [&](int j) { return (j & 1) ? ov1 : ov0; }, [&](int j) { return oa(2 * k + (j >> 1)); }); };
    const bool own = (__builtin_amdgcn_readfirstlane(selv[0]) >> 4) == (unsigned)(rt * 8 + rg * 2 + kp);
    if (!kp) {
        x_init(true);
        __builtin_amdgcn_s_sleep(PDL);
        waitv<2>();
    } else {
        __builtin_amdgcn_s_sleep(PD1);
        if (w & 1) waitv<4>();
        else waitv<3 + 4 * (16 - WON)>();
    }
    f32x4 acc[2][4];
#pragma unroll
    for (int nb = 0; nb < 4; ++nb) acc[0][nb] = a0_[nb];
    const float ys = __uint_as_float(__builtin_amdgcn_readlane(__float_as_uint(og), __builtin_amdgcn_readfirstlane(selv[0]) & 15));
    asm volatile("" : "+v"(cl));
#pragma unroll 1
    for (unsigned s = 0; s < SPIN_CAP && __ballot(cl != T); ++s)
        asm volatile("global_load_dwordx2 %0, %1, off sc1\n\ts_waitcnt vmcnt(0)" : "=&v"(cl) : "v"(cla) : "memory");
    SB0();
#pragma unroll
    for (int j = 0; j < 4; ++j) fetch1(0, j);
    SB0();
    unsigned wa[2][4];
#pragma unroll
    for (int i = 0; i < 8; ++i) {
        wa[i >> 2][i & 3] = ((lds0 + 4096 * kp + (4 * g + q) * 128 + ((((g & 1) << 1) | (q >> 1)) * 32) + 8 * p) ^ ((i & 3) << 5)) + (i >> 2) * 65536;
        asm volatile("" : "+v"(wa[i >> 2][i & 3]));
    }
    s16x4 wf[2][8][2];
    auto wrd = [&](int k, int i) AI {
        const int u = W_U[2 * k + (i >> 3)];
        wf[k & 1][i >> 1][i & 1] = tr_read(wa[u >> 3][(i >> 1) & 3] + (u & 7) * 8192 + (i & 1) * 2048);
    };
    u32x4 yp[14];
    const unsigned yo = srow * 4096 + cb * 256 + sch * 32;
    auto ya = [&](int i) { return ws + WS_Y + (i >> 1) * 32768 + (i & 1) * 16; };
    lds_wait(lds0 + L_FWD + (l & 1) * 4, 1);
    SB0();
#pragma unroll
    for (int i = 0; i < 16; ++i) wrd(0, i);
#pragma unroll
    for (int j = 0; j < 4; ++j) fetch1(1, j);
    const unsigned ea = lds0 + L_A + l16 + 4096 * w, eb = lds0 + L_A + 4096 * (w ^ 4);
    unsigned er = eb + l16, et = eb + srow * 128 + ((sch ^ srow) * 16), ew[4];
#pragma unroll
    for (int nb = 0; nb < 4; ++nb) ew[nb] = eb + l15 * 128 + (((2 * nb + (g >> 1)) ^ (l & 7)) * 16) + (g & 1) * 8;
    u16 *o0 = out + (size_t)row0 * DM + cb * 64, *o1 = o0 + 8 * DM;
    u64 km[2] = {__ballot(row0 + srow != (int)selv[0]), __ballot(row0 + 8 + srow != (int)selv[0])};
    asm volatile("" : "+s"(km[0]), "+s"(km[1]));
    asm volatile("" : "+v"(er), "+v"(et), "+v"(ew[0]), "+v"(ew[1]), "+v"(ew[2]), "+v"(ew[3]), "+s"(o0), "+s"(o1));
    __builtin_amdgcn_s_setprio(3);
    waitv<4>();
#pragma unroll
    for (int j = 0; j < 4; ++j) pin(of[0][j]);
    if (__builtin_expect(__ballot(least(of[0]) == 0u) != 0ull, 0)) fix(0);
    sfor<0, 8>([&](auto K) {
        constexpr int k = K;
        unsigned m;
        if constexpr (k > 4) __builtin_amdgcn_s_setprio(7 - k);
        if constexpr (2 * (k + 1) == WON) lds_wait(lds0 + ((l & 8) ? L_FWO + sch * 4 : L_FW2 + (l & 1) * 4), 1);
        if constexpr (k == 7) {
            if (__builtin_expect(own, 0)) {
#pragma unroll
                for (int i = 0; i < 14; ++i) ld_async(yp[i], yo, ya(i));
            }
        }
        SB0();
        sfor<0, 16>([&](auto I) {
            constexpr int i = I, j = k == 7 ? (0x2031 >> (i & 12)) & 3 : i >> 2, nb = i & 3;
            const bf16x8 a = cat8(wf[k & 1][(j >> 1) * 4 + nb][0], wf[k & 1][(j >> 1) * 4 + nb][1]);
            if (k == 0 && j == 1) asm volatile("v_mfma_f32_16x16x32_bf16 %0, %1, %2, 0" : "=&v"(acc[j & 1][nb]) : "v"(a), "v"(of[k % 3][j]));
            else asm volatile("v_mfma_f32_16x16x32_bf16 %0, %1, %2, %0" : "+v"(acc[j & 1][nb]) : "v"(a), "v"(of[k % 3][j]));
            SB0();
            if constexpr (k == 7 && i >= 12) {
                if constexpr (i == 12) lds_write<u32x4>(ea, nz4(acc[1][0]));
                else lds_write<f32x4>(ea + (i - 12) * 1024, acc[1][i - 12]);
            }
            if constexpr (k < 7) wrd(k + 1, i);
            if constexpr (k < 6 && i < 8 && !(i & 1)) fetch1(k + 2, i >> 1);
            if constexpr (k < 7 && i == 10) {
                waitv<(k < 6 ? 4 : 0)>();
#pragma unroll
                for (int n = 0; n < 4; ++n) pin(of[(k + 1) % 3][n]);
            }
            if constexpr (k < 7 && i > 10 && i <= 14) {
                const u32x4& v = of[(k + 1) % 3][i - 11];
                if constexpr (i == 11) asm volatile("v_min3_u32 %0, %1, %2, %3\n\tv_min_u32 %0, %0, %4" : "=&v"(m) : "v"(v[0]), "v"(v[1]), "v"(v[2]), "v"(v[3]));
                else asm volatile("v_min3_u32 %0, %0, %1, %2\n\tv_min3_u32 %0, %0, %3, %4" : "+v"(m) : "v"(v[0]), "v"(v[1]), "v"(v[2]), "v"(v[3]));
            }
            SB0();
        });
        if constexpr (k < 7) {
            if (__builtin_expect(__builtin_amdgcn_ballot_w64(m == 0u) != 0ull, 0)) fix(k + 1);
        }
        SB0();
    });
    asm volatile("s_nop 7");
    SB0();
    BAR();
    SB0();
    f32x4 pa[4];
#pragma unroll
    for (int nb = 0; nb < 4; ++nb) pa[nb] = lds_read<f32x4>(er + nb * 1024);
    SB0();
#pragma unroll
    for (int nb = 0; nb < 4; ++nb) acc[0][nb] += pa[nb];
#pragma unroll
    for (int nb = 0; nb < 4; ++nb) asm volatile("" : "+v"(acc[0][nb]));
#pragma unroll
    for (int nb = 0; nb < 4; ++nb) lds_write<u32x2>(ew[nb], (u32x2){pack2(acc[0][nb][0], acc[0][nb][1]), pack2(acc[0][nb][2], acc[0][nb][3])});
    u32x4 av[2];
#pragma unroll
    for (int j = 0; j < 2; ++j) av[j] = lds_read<u32x4>(et + j * 1024);
    SB0();
    asm volatile("s_mov_b64 exec, %5\n\tglobal_store_dwordx4 %0, %1, %3 sc1\n\ts_mov_b64 exec, %6\n\tglobal_store_dwordx4 %0, %2, %4 sc1\n\ts_mov_b64 exec, -1"
                 ::"v"(xo), "v"(av[0]), "v"(av[1]), "s"(o0), "s"(o1), "s"(km[0]), "s"(km[1]) : "memory");
    if (own) {
        const unsigned sr = __builtin_amdgcn_readfirstlane(selv[0]) & 15;
        const unsigned a2 = lds_read<unsigned>(eb + sr * 128 + ((sch ^ (sr & 7)) * 16) + g * 4);
        const float rsel = rbf(1.f / (1.f + __expf(-bf2f((u16)(selv[1] ^ ((selv[1] & 0x8000u) ? 0x8000u : 0xFFFFu))))));
        waitv<2>();
        settle(yp, [&](int) { return yo; }, ya);
        f32x4 yv[2];
#pragma unroll
        for (int h = 0; h < 2; ++h) {
            yv[h] = __builtin_bit_cast(f32x4, yp[h]);
#pragma unroll
            for (int i = 1; i < 7; ++i) yv[h] += __builtin_bit_cast(f32x4, yp[2 * i + h]);
        }
        float y2[2];
#pragma unroll
        for (int i = 0; i < 2; ++i) {
            const float t = dpp_add<0x128>(swap16_add(swap32_add(yv[0][i], yv[1][i]), swap32_add(yv[0][2 + i], yv[1][2 + i])));
            y2[i] = rbf(rbf(t) * rsel) * ys;
        }
        ST_MASK("global_store_dword", 0x00FF00FF00FF00FFull, sr * 2048 + sch * 16 + g * 4, pack2(lo16(a2) + y2[0], hi16(a2) + y2[1]), o0);
    }
    SB0();
}

extern "C" void kernel_launch(void* const* d_in, const int* in_sizes, int n_in, void* d_out, int out_size,
                              void* d_ws, size_t ws_size, hipStream_t stream) {
    fused_mix<<<256, 512, 0, stream>>>((const u16*)d_in[0], (const u16*)d_in[1], (const u16*)d_in[2], (const u16*)d_in[3],
                                       (const u16*)d_in[5], (const u16*)d_in[6], (const u16*)d_in[7], (const u16*)d_in[8],
                                       (const u16*)d_in[9], (char*)d_ws, (u16*)d_out);
}
```

```cpp
#include <hip/hip_runtime.h>
#include <hip/hip_bf16.h>
#include <stdint.h>
#include <type_traits>

typedef __attribute__((ext_vector_type(8))) short bf16x8;
typedef __attribute__((ext_vector_type(4))) short s16x4;
typedef __attribute__((ext_vector_type(4))) float f32x4;
typedef __attribute__((ext_vector_type(2))) float f32x2;
typedef __attribute__((ext_vector_type(2))) unsigned u32x2;
typedef __attribute__((ext_vector_type(4))) unsigned u32x4;
typedef __attribute__((ext_vector_type(2))) __bf16 bf16x2_t;
typedef unsigned short u16;
typedef unsigned long long u64;

#define DI __device__ __forceinline__
#define AS3 __attribute__((address_space(3)))

constexpr int DM = 1024;

DI float bf2f(u16 v) { return __uint_as_float(((unsigned)v) << 16); }
DI u16 f2bf(float f) { __bf16 b = (__bf16)f; return __builtin_bit_cast(u16, b); }
DI float rbf(float f) { return bf2f(f2bf(f)); }
DI unsigned pack2(float lo, float hi) {
    const f32x2 v = {lo, hi};
    return __builtin_bit_cast(unsigned, __builtin_convertvector(v, bf16x2_t));
}
DI float lo16(unsigned u) { return __uint_as_float(u << 16); }
DI float hi16(unsigned u) { return __uint_as_float(u & 0xffff0000u); }

DI s16x4 tr_read(unsigned a) { return __builtin_amdgcn_ds_read_tr16_b64_v4i16((AS3 s16x4*)(size_t)a); }
DI bf16x8 lds_read128(unsigned a) { return *(AS3 const bf16x8*)(size_t)a; }
template <class T> DI void lds_write(unsigned a, T v) { *(AS3 T*)(size_t)a = v; }
template <class T> DI T lds_read(unsigned a) { return *(AS3 const T*)(size_t)a; }
DI bf16x8 cat8(s16x4 a, s16x4 b) { return __builtin_shufflevector(a, b, 0, 1, 2, 3, 4, 5, 6, 7); }
DI f32x4 mfma16(bf16x8 a, bf16x8 b, f32x4 c) { return __builtin_amdgcn_mfma_f32_16x16x32_bf16(a, b, c, 0, 0, 0); }
DI bf16x8 pack44(const f32x4& a, const f32x4& b) {
    u32x4 r;
    r[0] = pack2(a[0], a[1]);
    r[1] = pack2(a[2], a[3]);
    r[2] = pack2(b[0], b[1]);
    r[3] = pack2(b[2], b[3]);
    return __builtin_bit_cast(bf16x8, r);
}
DI unsigned lds_of(const void* p) { return (unsigned)(size_t)(AS3 const char*)p; }

template <int CTRL>
DI unsigned dpp(unsigned x) { return __builtin_amdgcn_update_dpp(0u, x, CTRL, 0xf, 0xf, true); }
template <int CTRL>
DI float dpp_add(float x) { return x + __uint_as_float(dpp<CTRL>(__float_as_uint(x))); }
DI float swap32_add(float a, float b) {
    auto r = __builtin_amdgcn_permlane32_swap(__float_as_uint(a), __float_as_uint(b), false, false);
    return __uint_as_float(r[0]) + __uint_as_float(r[1]);
}
DI float swap16_add(float a, float b) {
    auto r = __builtin_amdgcn_permlane16_swap(__float_as_uint(a), __float_as_uint(b), false, false);
    return __uint_as_float(r[0]) + __uint_as_float(r[1]);
}
DI unsigned wave_umax(unsigned x) {
    x = max(x, dpp<0xB1>(x));
    x = max(x, dpp<0x4E>(x));
    x = max(x, dpp<0x141>(x));
    x = max(x, dpp<0x140>(x));
    auto r = __builtin_amdgcn_permlane16_swap(x, x, false, false);
    x = max(r[0], r[1]);
    auto s = __builtin_amdgcn_permlane32_swap(x, x, false, false);
    return max(s[0], s[1]);
}
constexpr unsigned WS_E = 0;
constexpr unsigned WS_RG = 256;
constexpr unsigned WS_GG = WS_RG + 2048;
constexpr unsigned WS_Z = 65536;
constexpr unsigned WS_KF = WS_Z + 131072;
constexpr unsigned WS_GU = WS_KF + 262144;
constexpr unsigned WS_Y = WS_GU + 262144;
constexpr unsigned WS_KV = WS_Y + 262144;
constexpr unsigned WS_O = WS_KV + (2u << 20);
constexpr unsigned WS_CL = WS_O + (4u << 20);
constexpr unsigned WS_END = WS_CL + 65536;

constexpr int NQ = 9, ND = 7, NX = 16, KD = 76, VD = 82, NF = 64, NF1 = 58, NF2 = 58, NP1 = 15, NP = 17, NM1 = 24, NM2 = 24, NM = 25, KFN = 100;
constexpr int J_S[48] = {0, 0, 0, 1, 1, 1, 2, 2, 2, 3, 3, 3, 4, 4, 4, 5, 5, 5, 6, 6, 6, 7, 7, 8, 8, 9, 9, 9, 10, 10, 11, 11, 12, 12, 12, 13, 14, 15, 13, 14, 15, 7, 8, 10, 11, 13, 14, 15};
constexpr int J_M[48] = {0, 1, 2, 0, 1, 2, 0, 1, 2, 0, 1, 2, 0, 1, 2, 0, 1, 2, 0, 1, 2, 0, 1, 0, 1, 0, 1, 2, 0, 1, 0, 1, 0, 1, 2, 0, 0, 0, 1, 1, 1, 2, 2, 2, 2, 2, 2, 2};
constexpr int J_X[48] = {0, 0, 0, 1, 1, 1, 2, 2, 2, 3, 3, 3, 4, 4, 4, 5, 5, 5, 6, 6, 6, 7, 7, 8, 8, 9, 9, 9, 10, 10, 11, 11, 12, 12, 12, 13, 14, 15, 13, 14, 15, 7, 8, 10, 11, 13, 14, 15};
constexpr int J_U[48] = {4, 6, 7, 9, 10, 11, 17, 14, 15, 2, 1, 3, 6, 7, 8, 18, 11, 16, 13, 15, 12, 0, 3, 6, 7, 9, 10, 11, 14, 13, 2, 1, 5, 4, 6, 18, 11, 14, 13, 15, 12, 2, 1, 0, 3, 5, 4, 6};
constexpr int J_B[48] = {4, 4, 4, 4, 4, 4, 14, 14, 14, 1, 1, 1, 1, 1, 1, 11, 11, 11, 11, 11, 11, 0, 0, 0, 0, 9, 9, 9, 9, 9, 1, 1, 1, 1, 1, 11, 11, 11, 11, 11, 11, 0, 0, 0, 0, 0, 0, 0};
constexpr int Q_C[96] = {1, 0, 2, 0, 3, 0, 4, 0, 5, 0, 6, 0, 7, 0, 0, 0, 0, 0, 8, 0, 0, 0, 9, 0, 0, 0, 0, 0, 10, 0, 0, 0, 0, 0, 0, 0, 11, 0, 0, 0, 0, 0, 12, 0, 0, 0, 13, 0, 0, 0, 14, 0, 0, 0, 0, 0, 15, 0, 0, 0, 16, 0, 0, 0, 17, 0, 0, 0, 0, 0, 18, 0, 19, 0, 20, 0, 21, 0, 22, 0, 23, 0, 24, 0, 25, 0, 0, 0, 0, 0, 0, 0, 0, 0, 0, 0};
constexpr int X_U0[16] = {3, 8, 16, 12, 5, 9, 17, 2, 5, 8, 16, 15, 0, 7, 9, 16};
constexpr int X_U1[16] = {5, 18, 13, 0, 4, 10, 14, 1, 4, 18, 17, 12, 3, 8, 10, 17};
constexpr int X_R[16] = {0, 1, 2, 3, 4, 5, 6, 0, 1, 2, 3, 4, 5, 6, 2, 5};
constexpr int P_U[4] = {9, 10, 7, 8};
constexpr int W_U[16] = {11, 13, 14, 15, 12, 7, 8, 9, 10, 0, 2, 1, 3, 5, 4, 6};
constexpr int W_N[16] = {100, 100, 100, 100, 100, 101, 101, 101, 101, 16, 16, 16, 17, 17, 99, 99};
constexpr int F_J[64] = {-1, 0, 1, 2, -2, 3, 4, 5, -3, 6, 7, 8, -4, 9, 10, 11, -5, 12, 13, 14, 15, -6, 16, 17, 18, -7, 19, 20, 21, -8, 22, -9, 23, 24, -10, 25, 26, 27, -11, 28, 29, -12, 30, 31, -13, 32, 33, 34, -14, 35, -15, 36, -16, 37, 38, 39, 40, 41, 42, 43, 44, 45, 46, 47};
constexpr int F_N[64] = {0, 0, 0, 0, 0, 0, 0, 0, 0, 0, 0, 0, 0, 0, 0, 1, 1, 1, 1, 1, 1, 2, 2, 2, 2, 3, 3, 3, 3, 4, 4, 4, 5, 5, 5, 5, 5, 6, 6, 6, 7, 8, 8, 8, 9, 9, 9, 10, 10, 10, 11, 12, 12, 13, 14, 14, 14, 14, 15, 15, 15, 15, 15, 15};
constexpr int F_K[64] = {0, 1, 2, 3, 0, 4, 5, 6, 0, 0, 0, 7, 0, 0, 8, 0, 0, 0, 9, 0, 0, 0, 0, 10, 0, 0, 0, 11, 0, 0, 12, 0, 0, 13, 0, 0, 0, 14, 0, 0, 15, 0, 0, 16, 0, 0, 0, 17, 0, 18, 0, 19, 0, 20, 21, 22, 23, 24, 0, 0, 0, 0, 0, 25};
constexpr int E_A[384] = {300, 325, 350, 375, 0, 0, 0, 502, 0, 0, 0, 403, 0, 0, 0, 503, 201, 251, 0, 404, 0, 0, 0, 504, 301, 326, 351, 376, 0, 0, 0, 505, 601, 0, 0, 406, 0, 0, 0, 506, 202, 252, 0, 407, 0, 100, 101, 507, 302, 327, 352, 377, 0, 0, 0, 602, 0, 0, 0, 0, 0, 0, 0, 203, 253, 0, 0, 408, 0, 100, 101, 508, 303, 328, 353, 378, 0, 0, 0, 603, 0, 0, 0, 409, 0, 0, 0, 509, 204, 254, 0, 0, 0, 0, 0, 0, 304, 329, 354, 379, 0, 0, 0, 604, 0, 0, 0, 410, 0, 0, 0, 510, 205, 255, 0, 0, 0, 100, 101, 102, 305, 330, 355, 380, 0, 0, 0, 0, 0, 0, 0, 605, 0, 0, 0, 206, 256, 0, 0, 411, 0, 0, 0, 511, 306, 331, 356, 381, 0, 0, 0, 606, 0, 0, 0, 0, 0, 0, 0, 207, 257, 0, 0, 412, 607, 100, 101, 512, 307, 332, 357, 382, 0, 0, 0, 608, 208, 258, 0, 413, 0, 0, 0, 513, 308, 333, 358, 383, 0, 0, 0, 609, 209, 259, 0, 414, 0, 100, 101, 514, 309, 334, 359, 384, 0, 0, 0, 610, 0, 0, 0, 0, 0, 0, 0, 210, 260, 0, 0, 415, 611, 0, 0, 515, 310, 335, 360, 385, 0, 0, 0, 612, 211, 261, 0, 416, 0, 100, 101, 516, 311, 336, 361, 386, 0, 0, 0, 212, 262, 0, 0, 417, 614, 0, 0, 517, 312, 337, 362, 387, 0, 0, 0, 0, 0, 0, 0, 0, 0, 0, 0, 213, 263, 0, 0, 418, 0, 100, 101, 518, 313, 338, 363, 388, 214, 264, 0, 519, 314, 339, 364, 389, 215, 265, 0, 520, 315, 340, 365, 390, 0, 0, 0, 521, 0, 0, 0, 422, 0, 0, 0, 522, 0, 0, 0, 423, 0, 0, 0, 523, 0, 0, 0, 0, 0, 100, 101, 102, 103, 0, 0, 425, 0, 0, 0, 525, 0, 0, 0, 0, 0, 0, 0, 0, 0, 0, 0, 0, 0, 0, 0, 0, 0, 0, 0, 616, 0, 0, 0, 0, 0, 0, 0, 0, 0, 0, 0, 0, 0, 0, 0, 617, 0, 0, 0, 0, 0, 0, 0, 0, 0, 0, 0, 0};
constexpr int E_B[384] = {0, 0, 402, 0, 0, 0, 0, 0, 0, 0, 0, 0, 0, 0, 0, 0, 0, 0, 0, 0, 0, 0, 0, 0, 0, 0, 405, 0, 0, 0, 0, 0, 0, 0, 0, 0, 0, 0, 0, 0, 0, 0, 0, 0, 0, 0, 0, 102, 103, 0, 0, 0, 0, 0, 0, 0, 0, 0, 0, 0, 0, 0, 0, 0, 0, 0, 0, 0, 0, 0, 0, 102, 103, 0, 0, 0, 0, 0, 0, 0, 0, 0, 0, 0, 0, 0, 0, 0, 0, 0, 0, 0, 0, 0, 0, 0, 0, 0, 0, 0, 0, 0, 0, 0, 0, 0, 0, 0, 0, 0, 0, 0, 0, 0, 0, 0, 0, 0, 0, 0, 103, 0, 0, 0, 0, 0, 0, 0, 0, 0, 0, 0, 0, 0, 0, 0, 0, 0, 0, 0, 0, 0, 0, 0, 0, 0, 0, 0, 0, 0, 0, 0, 0, 0, 0, 0, 0, 0, 0, 0, 0, 0, 0, 0, 0, 0, 0, 102, 103, 0, 0, 0, 0, 0, 0, 0, 0, 0, 0, 0, 0, 0, 0, 0, 0, 0, 0, 0, 0, 0, 0, 0, 0, 0, 0, 0, 0, 0, 0, 102, 103, 0, 0, 0, 0, 0, 0, 0, 0, 0, 0, 0, 0, 0, 0, 0, 0, 0, 0, 0, 0, 0, 0, 0, 0, 0, 0, 0, 0, 0, 0, 0, 0, 0, 0, 0, 0, 0, 0, 613, 102, 103, 0, 0, 0, 0, 0, 0, 0, 0, 0, 0, 0, 0, 0, 0, 0, 0, 0, 0, 0, 0, 0, 0, 0, 0, 0, 0, 0, 0, 0, 0, 0, 0, 0, 0, 0, 0, 0, 102, 103, 0, 419, 0, 0, 0, 0, 615, 0, 0, 420, 0, 0, 0, 0, 0, 0, 0, 421, 0, 0, 0, 0, 0, 0, 0, 0, 0, 0, 0, 0, 0, 0, 0, 0, 0, 0, 0, 0, 0, 0, 0, 0, 0, 0, 0, 0, 0, 0, 0, 0, 0, 0, 0, 0, 0, 0, 0, 0, 0, 0, 0, 0, 0, 0, 0, 0, 0, 0, 0, 0, 0, 0, 0, 0, 0, 0, 0, 0, 0, 0, 0, 0, 0, 0, 0, 0, 0, 0, 0, 0, 0, 0, 0, 0, 0, 0, 0, 0, 0, 0, 0, 0, 0};
constexpr unsigned L_A = 16 * 8192;
constexpr unsigned L_KF = 18 * 8192;
constexpr unsigned L_SM = 19 * 8192;
constexpr unsigned L_FULL = L_SM + 256;
constexpr unsigned L_PROG = L_SM + 896;
constexpr unsigned L_PRL = L_SM + 336;
constexpr unsigned L_FW2 = L_SM + 512, L_FOS = L_SM + 480, L_FK0 = L_SM + 752, L_FV = L_SM + 560, L_FP = L_SM + 576, L_FKS = L_SM + 592;
constexpr unsigned L_FQD = L_SM + 608, L_FG = L_SM + 624, L_FKF = L_SM + 736, L_FQ = L_SM + 720, L_FWD = L_SM + 640, L_FWO = L_SM + 656;
constexpr unsigned L_FLD = L_SM + 688, L_BEST = L_SM + 696;
constexpr unsigned L_SEL = L_SM + 704;
constexpr unsigned L_XS = L_SM + 2048, L_KZ = L_SM + 3072, L_WPHI = L_SM + 4096;
constexpr unsigned L_G = L_SM + 3328;
static_assert(NP <= 31 && NM <= 63 && NF1 == NF2 && NX == 16, "");
static_assert(P_U[1] == P_U[0] + 1 && P_U[3] == P_U[2] + 1, "feature maps of a head lie side by side");
constexpr bool j_first(int j) {
    for (int i = 0; i < j; ++i)
        if (J_M[i] == J_M[j]) return false;
    return true;
}
constexpr unsigned SPIN_CAP = 1u << 16;
#define N8(a) a, a + 1, a + 2, a + 3, a + 4, a + 5, a + 6, a + 7
__device__ const unsigned k_num[64] = {N8(0), N8(8), N8(16), N8(24), N8(32), N8(40), N8(48), N8(56)};

constexpr int PD = 4;
constexpr int PACE = 16;
constexpr int RLD = F_N[NF1 - 1];
constexpr int CLR0 = 2;
constexpr int GQ1 = 6;
constexpr int WOE = 2, WOM = 7, WON = 10;
constexpr int GDL = 17, KFD = 6, PDL = 11, PD1 = 4;
constexpr unsigned CPF = 56832;
constexpr u64 KXB = 0xFEDBA9C768534210ull;
#define BAR() asm volatile("s_waitcnt lgkmcnt(0)\n\ts_barrier" ::: "memory")
template <int N>
DI void waitv() { asm volatile("s_waitcnt vmcnt(%0)" ::"n"(N) : "memory"); }

DI unsigned nz1(unsigned v) { return max(v, 1u); }
DI u32x4 nz4(u32x4 v) { return (u32x4){max(v[0], 1u), max(v[1], 1u), max(v[2], 1u), max(v[3], 1u)}; }
DI u32x4 nz4(f32x4 v) { return nz4(__builtin_bit_cast(u32x4, v)); }
DI void st_wt(__amdgpu_buffer_rsrc_t r, unsigned off, u32x4 v) { __builtin_amdgcn_raw_buffer_store_b128(v, r, off, 0, 16); }
DI void ld_async(u32x4& v, unsigned off, const char* base) {
    asm volatile("global_load_dwordx4 %0, %1, %2 sc1" : "=&v"(v) : "v"(off), "s"(base) : "memory");
}
#define ST_MASK(op, m, off, v, base) asm volatile("s_mov_b64 exec, %3\n\t" op " %0, %1, %2 sc1\n\ts_mov_b64 exec, -1\n\ts_nop 0" ::"v"(off), "v"(v), "s"(base), "s"((u64)(m)) : "memory")
DI void pin(u32x4& v) { asm volatile("" : "+v"(v)); }
template <int N>
DI unsigned least(const u32x4 (&v)[N]) {
    unsigned a = min(v[0][0], v[0][1]), b = min(v[0][2], v[0][3]);
#pragma unroll
    for (int i = 1; i < N; ++i) a = min(min(a, v[i][0]), v[i][1]), b = min(min(b, v[i][2]), v[i][3]);
    return min(a, b);
}
template <int N, class OFF, class BASE>
DI void again(u32x4 (&v)[N], OFF off, BASE base) {
    unsigned s = SPIN_CAP;
    do {
#pragma unroll
        for (int i = 0; i < N; ++i) {
            unsigned t;
            asm volatile("v_min3_u32 %1, %4, %5, %6\n\tv_min_u32 %1, %1, %7\n\tv_cmp_eq_u32 vcc, 0, %1\n\ts_mov_b64 exec, vcc\n\t"
                         "global_load_dwordx4 %0, %2, %3 sc1\n\ts_mov_b64 exec, -1"
                         : "+v"(v[i]), "=&v"(t) : "v"(off(i)), "s"(base(i)), "v"(v[i][0]), "v"(v[i][1]), "v"(v[i][2]), "v"(v[i][3]) : "memory", "vcc");
        }
        waitv<0>();
#pragma unroll
        for (int i = 0; i < N; ++i) pin(v[i]);
    } while (__ballot(least(v) == 0u) != 0ull && --s);
}
template <int N, class OFF, class BASE>
DI void settle(u32x4 (&v)[N], OFF off, BASE base) {
#pragma unroll
    for (int i = 0; i < N; ++i) pin(v[i]);
    if (__builtin_expect(__ballot(least(v) == 0u) != 0ull, 0)) again(v, off, base);
}
DI void lds_wait(unsigned a, unsigned n) {
    unsigned v, c;
    asm volatile("s_mov_b32 %1, 0x400000\n"
                 ".Lp%=:\n\tds_read_b32 %0, %2\n\ts_waitcnt lgkmcnt(0)\n\tv_cmp_gt_u32 vcc, %3, %0\n\ts_cbranch_vccz .Lf%=\n\t"
                 "s_sleep 1\n\ts_sub_u32 %1, %1, 1\n\ts_cbranch_scc0 .Lp%=\n.Lf%=:"
                 : "=&v"(v), "=&s"(c) : "v"(a), "s"(n) : "memory", "vcc", "scc");
}
DI void lds_wait0(unsigned a) {
    unsigned v, c;
    asm volatile("s_mov_b32 %1, 0x1000000\n"
                 ".Lp%=:\n\tds_read_b32 %0, %2\n\ts_sub_u32 %1, %1, 1\n\ts_cbranch_scc1 .Lf%=\n\ts_waitcnt lgkmcnt(0)\n\tv_cmp_eq_u32 vcc, 0, %0\n\ts_cbranch_vccnz .Lp%=\n"
                 ".Lf%=:\n\ts_waitcnt lgkmcnt(0)"
                 : "=&v"(v), "=&s"(c) : "v"(a) : "memory", "vcc", "scc");
}
DI void lds_wait8(unsigned a, unsigned n) {
    unsigned v, c;
    asm volatile("s_mov_b32 %1, 0x100000\n"
                 ".Lp%=:\n\tds_read_b32 %0, %2\n\ts_waitcnt lgkmcnt(0)\n\tv_cmp_gt_u32 vcc, %3, %0\n\ts_cbranch_vccz .Lf%=\n\t"
                 "s_sleep 4\n\ts_sub_u32 %1, %1, 1\n\ts_cbranch_scc0 .Lp%=\n.Lf%=:"
                 : "=&v"(v), "=&s"(c) : "v"(a), "s"(n) : "memory", "vcc", "scc");
}
DI void lds_peek(unsigned& v, unsigned a) { asm volatile("ds_read_b32 %0, %1" : "=v"(v) : "v"(a) : "memory"); }
DI void lds_wait(unsigned& v, unsigned a, unsigned n) {
    unsigned c;
    asm volatile("s_mov_b32 %1, 0x400000\n"
                 ".Lp%=:\n\ts_waitcnt lgkmcnt(0)\n\tv_cmp_gt_u32 vcc, %3, %0\n\ts_cbranch_vccz .Lf%=\n\t"
                 "ds_read_b32 %0, %2\n\ts_sub_u32 %1, %1, 1\n\ts_cbranch_scc0 .Lp%=\n.Lf%=:"
                 : "+v"(v), "=&s"(c) : "v"(a), "s"(n) : "memory", "vcc", "scc");
}
DI float dot2(unsigned a, unsigned b, float c) {
    return __builtin_amdgcn_fdot2_f32_bf16(__builtin_bit_cast(bf16x2_t, a), __builtin_bit_cast(bf16x2_t, b), c, false);
}
DI float sum4(const float (&a)[4]) {
    float c = swap16_add(swap32_add(a[0], a[1]), swap32_add(a[2], a[3]));
    c = dpp_add<0xB1>(c);
    c = dpp_add<0x4E>(c);
    c = dpp_add<0x141>(c);
    return dpp_add<0x140>(c);
}

template <class OP, class POST>
DI void red4(float& a, float& b, float& c, float& d, OP op, POST post) {
    auto x = __builtin_amdgcn_permlane32_swap(__float_as_uint(a), __float_as_uint(b), false, false);
    auto y = __builtin_amdgcn_permlane32_swap(__float_as_uint(c), __float_as_uint(d), false, false);
    const float m = op(__uint_as_float(x[0]), __uint_as_float(x[1])), n = op(__uint_as_float(y[0]), __uint_as_float(y[1]));
    auto z = __builtin_amdgcn_permlane16_swap(__float_as_uint(m), __float_as_uint(n), false, false);
    const unsigned f = __float_as_uint(post(op(__uint_as_float(z[0]), __uint_as_float(z[1]))));
    auto u = __builtin_amdgcn_permlane16_swap(f, f, false, false);
    auto v = __builtin_amdgcn_permlane32_swap(u[0], u[0], false, false);
    auto t = __builtin_amdgcn_permlane32_swap(u[1], u[1], false, false);
    a = __uint_as_float(v[0]);
    b = __uint_as_float(v[1]);
    c = __uint_as_float(t[0]);
    d = __uint_as_float(t[1]);
}
DI float mx2(float a, float b) { return __builtin_elementwise_maximum(a, b); }
template <class HOOK>
DI void softmax2(const f32x4 (&v)[2][2], u32x4 (&ph)[2][2], HOOK hook) {
    constexpr float L2E = 1.4426950408889634f;
    float mx[2], mn[2], sp_[2], sn_[2];
#pragma unroll
    for (int b = 0; b < 2; ++b) {
        const f32x4 s = v[b][0], t = v[b][1];
        mx[b] = mx2(mx2(mx2(mx2(s[0], s[1]), s[2]), mx2(mx2(s[3], t[0]), t[1])), mx2(t[2], t[3]));
        mn[b] = mx2(mx2(mx2(mx2(-s[0], -s[1]), -s[2]), mx2(mx2(-s[3], -t[0]), -t[1])), mx2(-t[2], -t[3]));
    }
    red4(mx[0], mx[1], mn[0], mn[1], [](float a, float b) { return mx2(a, b); }, [](float a) { return a * -L2E; });
    f32x2 ep[2][4], en[2][4];
#pragma unroll
    for (int b = 0; b < 2; ++b) {
#pragma unroll
        for (int e = 0; e < 4; ++e) {
            const f32x2 u = {v[b][e >> 1][2 * (e & 1)], v[b][e >> 1][2 * (e & 1) + 1]};
            const f32x2 a = __builtin_elementwise_fma(u, (f32x2){L2E, L2E}, (f32x2){mx[b], mx[b]});
            const f32x2 c = __builtin_elementwise_fma(u, (f32x2){-L2E, -L2E}, (f32x2){mn[b], mn[b]});
            ep[b][e] = (f32x2){__builtin_amdgcn_exp2f(a[0]), __builtin_amdgcn_exp2f(a[1])};
            en[b][e] = (f32x2){__builtin_amdgcn_exp2f(c[0]), __builtin_amdgcn_exp2f(c[1])};
        }
        const f32x2 s = (ep[b][0] + ep[b][1]) + (ep[b][2] + ep[b][3]), t = (en[b][0] + en[b][1]) + (en[b][2] + en[b][3]);
        sp_[b] = s[0] + s[1];
        sn_[b] = t[0] + t[1];
    }
    asm volatile("" : "+v"(sp_[0]), "+v"(sn_[0]), "+v"(sp_[1]), "+v"(sn_[1]));
    hook();
    red4(sp_[0], sp_[1], sn_[0], sn_[1], [](float a, float b) { return a + b; }, [](float a) { return __builtin_amdgcn_rcpf(a); });
#pragma unroll
    for (int b = 0; b < 2; ++b)
#pragma unroll
        for (int e = 0; e < 4; ++e) {
            const f32x2 a = ep[b][e] * (f32x2){sp_[b], sp_[b]}, c = en[b][e] * (f32x2){sn_[b], sn_[b]};
            ph[b][0][e] = pack2(a[0], a[1]);
            ph[b][1][e] = pack2(c[0], c[1]);
        }
}
DI f32x4 rbf4(const f32x4& a) {
    const unsigned u = pack2(a[0], a[1]), v = pack2(a[2], a[3]);
    return (f32x4){lo16(u), hi16(u), lo16(v), hi16(v)};
}
DI int fsw(int row) { return (((row >> 1) & 3) << 2) | (((row >> 3) & 1) << 1) | (row & 1); }
template <class HOOK>
DI void phi_k(const f32x4 (&pp)[2][2], const unsigned (&pw)[4], unsigned off, HOOK hook) {
    u32x4 ph[2][2];
    softmax2(pp, ph, hook);
#pragma unroll
    for (int b = 0; b < 2; ++b)
#pragma unroll
        for (int kf = 0; kf < 2; ++kf)
#pragma unroll
            for (int fb = 0; fb < 2; ++fb) {
                const u32x2 a = {ph[b][kf][2 * fb], ph[b][kf][2 * fb + 1]};
                lds_write<u32x2>(pw[2 * kf + fb] + off + b * 2048, a);
            }
}

#define SB0() __builtin_amdgcn_sched_barrier(0)
template <int A, int B, class F>
DI void sfor(F f) {
    if constexpr (A < B) {
        f(std::integral_constant<int, A>{});
        sfor<A + 1, B>(f);
    }
}
template <int N> using IC = std::integral_constant<int, N>;
#define AI __attribute__((always_inline))

DI unsigned min4(u32x4 v) { return min(min(v[0], v[1]), min(v[2], v[3])); }
DI unsigned o_sums(const u32x4 (&kf)[6], const u32x4 (&ph)[2][2], const float (&gt)[2], float (&rd_)[2], f32x4 (&nm)[2][2]) {
    const f32x4 zero = {0.f, 0.f, 0.f, 0.f};
    auto b8 = [](const u32x4& a) { return __builtin_bit_cast(bf16x8, a); };
    f32x4 dn[2];
#pragma unroll
    for (int i = 0; i < 2; ++i) dn[i] = mfma16(b8(kf[4]), b8(ph[i][0]), zero);
#pragma unroll
    for (int i = 0; i < 2; ++i) dn[i] = mfma16(b8(kf[5]), b8(ph[i][1]), dn[i]);
    SB0();
    unsigned m = min(min4(kf[4]), min4(kf[5]));
    asm volatile("" : "+v"(m));
#pragma unroll
    for (int i = 0; i < 4; ++i) nm[i & 1][i >> 1] = mfma16(b8(kf[i >> 1]), b8(ph[i & 1][0]), zero);
    m = min(m, min(min4(kf[0]), min4(kf[1])));
    asm volatile("" : "+v"(m));
    SB0();
#pragma unroll
    for (int i = 0; i < 4; ++i) nm[i >> 1][i & 1] = mfma16(b8(kf[2 + (i & 1)]), b8(ph[i >> 1][1]), nm[i >> 1][i & 1]);
    asm volatile("" : "+v"(dn[0]), "+v"(dn[1]));
    m = min(m, min(min4(kf[2]), min4(kf[3])));
#pragma unroll
    for (int i = 0; i < 2; ++i) rd_[i] = __builtin_amdgcn_rcpf(rbf(rbf(dn[i][0]) + 1e-6f)) * gt[i];
    asm volatile("" : "+v"(rd_[0]), "+v"(rd_[1]), "+v"(m));
    return m;
}
DI void o_store(const float (&rd_)[2], const f32x4 (&nm)[2][2], __amdgpu_buffer_rsrc_t rsrc, unsigned o_st) {
#pragma unroll
    for (int i = 0; i < 2; ++i) {
        u32x4 o;
        f32x2 t[4];
#pragma unroll
        for (int j = 0; j < 4; ++j) t[j] = (f32x2){nm[i][j >> 1][2 * (j & 1)], nm[i][j >> 1][2 * (j & 1) + 1]} * (f32x2){rd_[i], rd_[i]};
        SB0();
#pragma unroll
        for (int j = 0; j < 4; ++j) o[j] = pack2(t[j][0], t[j][1]);
        SB0();
        st_wt(rsrc, o_st + i * 1024, nz4(o));
        SB0();
    }
}

typedef __attribute__((ext_vector_type(16))) unsigned u32x16;
__global__ __launch_bounds__(512, 2) void fused_mix(const u16*, const u16*, const u16*, const u16*, const u16*, const u16*, const u16*, const u16*,
                                                    const u16*, char*, u16*) {
    __shared__ __attribute__((aligned(16))) char smem[163840];
    u32x4 ka;
    u32x16 kb_;
    u64 kz;
    asm volatile("s_load_dwordx4 %0, %3, 0x0\n\ts_load_dwordx16 %1, %3, 0x10\n\ts_load_dwordx2 %2, %3, 0x50"
                 : "=&s"(ka), "=&s"(kb_), "=&s"(kz) : "s"(__builtin_amdgcn_kernarg_segment_ptr()));
    SB0();
    const int tid = threadIdx.x;
    int l = tid & 63;
    const int w = __builtin_amdgcn_readfirstlane(tid >> 6) ^ 4;
    int bid = blockIdx.x;
    const unsigned lds0 = lds_of(smem);
    int g = l >> 4, l15 = l & 15, q = l15 >> 2, p = l & 3;
    int srow = l >> 3, sch = l & 7;
    unsigned l16 = l * 16;
    const int rg = w & 3, kp = w >> 2;
    const int hs = rg >> 1;
    SB0();
    {
        u64 pc;
        asm volatile("s_getpc_b64 %0\n\ts_mov_b32 m0, %2\n\ts_mov_b64 exec, 3\n\tglobal_load_lds_dword %1, %0\n\ts_mov_b64 exec, -1"
                     : "=&s"(pc) : "v"(min((unsigned)(((bid & ~7) + w) * 2 + l) * 128u, CPF)), "s"(lds0 + L_SM + 3584) : "memory");
    }
    lds_write<unsigned>(lds0 + L_SM + tid * 4, 0u);
    const int slot = bid >> 3;
    const int mt = (bid & 1) * 8 + (slot & 7);
    const int hp = ((bid >> 1) & 3) * 4 + (slot >> 3);
    unsigned vx[4], vw[4];
    auto vw_init = [&]() AI {
        int t = l;
        asm volatile("" : "+v"(t));
#pragma unroll
        for (int i = 0; i < 4; ++i) vw[i] = (unsigned)((8 * i + (t >> 3)) * 6144 + ((t & 7) ^ (((t >> 4) & 1) << 1) ^ ((i & 1) << 2)) * 16 + 4096 - i * 1024);
    };
    auto vx_init = [&]() AI {
        int t = l;
        asm volatile("" : "+v"(t));
#pragma unroll
        for (int i = 0; i < 4; ++i) vx[i] = (unsigned)((8 * i + (t >> 3)) * 2048 + (((t & 7) ^ ((4 * (i & 1) + (t >> 4)) & 7)) * 16) + 4096 - i * 1024);
    };
    vx_init();
    vw_init();
    asm volatile("" : "+v"(vx[0]), "+v"(vx[1]), "+v"(vx[2]), "+v"(vx[3]), "+v"(vw[0]), "+v"(vw[1]), "+v"(vw[2]), "+v"(vw[3]));
    int ks0 = ((slot + (slot >> 3)) & 3) ^ ((bid & 1) * 8), role = w & 5;
    long long xo0 = (mt * 128 + hs * 64) * 2048 - 4096, wo0 = hp * 128 + hs * (32 * 6144) - 4096;
    long long xo1 = xo0 + ks0 * 128, wo1 = wo0 + ks0 * (64 * 6144) + 2048;
    unsigned d0 = lds0 + (hs ? X_U1[0] : X_U0[0]) * 8192, d1 = lds0 + J_U[0] * 8192 + hs * 4096;
    const unsigned* one = k_num;
    asm volatile("s_waitcnt lgkmcnt(0)" : "+s"(ka), "+s"(kb_), "+s"(kz), "+s"(xo0), "+s"(wo0), "+s"(xo1), "+s"(wo1), "+s"(ks0), "+s"(role),
                 "+s"(d0), "+s"(d1), "+s"(one) :: "memory");
    const u16* x = (const u16*)((u64)ka[1] << 32 | ka[0]);
    const u16* wqkv = (const u16*)((u64)ka[3] << 32 | ka[2]);
    auto arg = [&](int i) { return (char*)((u64)kb_[2 * i + 1] << 32 | kb_[2 * i]); };
    const u16 *wphi = (const u16*)arg(0), *wo = (const u16*)arg(1), *wr = (const u16*)arg(2), *wg = (const u16*)arg(3);
    const u16 *wu = (const u16*)arg(4), *wd = (const u16*)arg(5), *gw = (const u16*)arg(6);
    char* ws = arg(7);
    u16* out = (u16*)kz;
    unsigned v0 = 0;
    asm volatile("" : "+v"(v0));
    auto mark = [&](unsigned dst, int n = 1, int lanes = 1) AI {
        asm volatile("s_mov_b32 m0, %2\n\ts_mov_b64 exec, %4\n\tglobal_load_lds_dword %0, %1 offset:%3\n\ts_mov_b64 exec, -1"
                     ::"v"(v0), "s"(one), "s"(dst - 4 * n), "n"(4 * n), "n"(lanes) : "memory");
    };
    auto four = [&](const unsigned (&v)[4], const char* src, unsigned dst) AI {
        asm volatile("s_mov_b32 m0, %5\n\ts_nop 0\n\t"
                     "global_load_lds_dwordx4 %0, %4\n\tglobal_load_lds_dwordx4 %1, %4 offset:1024\n\t"
                     "global_load_lds_dwordx4 %2, %4 offset:2048\n\tglobal_load_lds_dwordx4 %3, %4 offset:3072"
                     ::"v"(v[0]), "v"(v[1]), "v"(v[2]), "v"(v[3]), "s"(src), "s"(dst) : "memory");
    };
    static_assert(F_J[0] == -1 && F_J[1] == 0 && F_K[1] == 1 && (KXB & 15) == 0, "");
    if (__builtin_expect(role == 4, 1)) {
        four(vx, (const char*)x + xo1, d0);
        four(vx, (const char*)x + xo1 + 32 * 2048, d0 + 4096);
        four(vw, (const char*)wqkv + wo1, d1);
    }
    asm volatile("" : "+s"(xo0), "+s"(wo0));
    const char* xb0 = (const char*)x + xo0;
    const char* wb0 = (const char*)wqkv + wo0;
    auto fill = [&](auto N) AI {
        constexpr int n = N, j = F_J[n] < 0 ? 0 : F_J[n], xi = F_J[n] < 0 ? -1 - F_J[n] : 0, s = F_J[n] < 0 ? xi : J_S[j], m = J_M[j];
        const int ks = ks0 ^ (int)((KXB >> (4 * s)) & 15);
        if constexpr (F_N[n] > 0 && n < NF1) waitv<PACE>();
        if constexpr (F_J[n] >= 0) {
            four(vw, wb0 + ks * (64 * 6144) + (m == 2 ? 0 : 2048 * (m + 1)), lds0 + J_U[j] * 8192 + hs * 4096);
        } else {
            const char* xs_ = xb0 + ks * 128;
            const unsigned d = lds0 + (hs ? X_U1[xi] : X_U0[xi]) * 8192;
            four(vx, xs_, d);
            if constexpr (F_N[n] > 0) waitv<PACE>();
            four(vx, xs_ + 32 * 2048, d + 4096);
        }
        if constexpr (F_K[n] > 1) mark(lds0 + L_FULL + (n < NF1 ? 0 : 32) + hs * 8, F_K[n], 3);
    };
    unsigned sd = 0, st;
    asm volatile("s_cmp_lg_u32 %2, 5\n\ts_cbranch_scc1 .La%=\n\t"
                 "s_min_u32 %1, %3, 23\n\ts_lshl_b32 %1, %1, 18\n\ts_load_dword %0, %5, %1\n\ts_load_dword %0, %6, 0x0\n\t"
                 "s_and_b32 %1, %1, 0x3c0000\n\ts_load_dword %0, %4, %1\n\ts_load_dword %0, %10, %1\n\t"
                 "s_load_dword %0, %7, 0x0\n\ts_load_dword %0, %8, 0x0\n\ts_load_dword %0, %9, 0x0\n.La%=:"
                 : "+s"(sd), "=&s"(st) : "s"(w), "s"(slot), "s"(x), "s"(wqkv), "s"(one), "s"(wr), "s"(gw), "s"(wphi), "s"(out) : "memory", "scc");
    asm volatile("s_cmp_lg_u32 %2, 7\n\ts_cbranch_scc1 .La%=\n\t"
                 "s_min_u32 %1, %3, 27\n\ts_lshl_b32 %1, %1, 18\n\ts_load_dword %0, %4, 0x0\n\ts_load_dword %0, %4, %1\n\t"
                 "s_load_dword %0, %5, %1\n\ts_load_dword %0, %6, %1\n\ts_load_dword %0, %7, %1\n\t"
                 "s_and_b32 %1, %1, 0x1c0000\n\ts_load_dword %0, %8, %1\n.La%=:\n\ts_barrier"
                 : "+s"(sd), "=&s"(st) : "s"(w), "s"(slot), "s"(ws), "s"(wg), "s"(wu), "s"(wd), "s"(wo) : "memory", "scc");
    if (__builtin_expect((w & 5) == 4, 1)) {
        mark(lds0 + L_FULL + hs * 8, 1, 3);
        sfor<2, 4>(fill);
    }
    asm volatile("" : "+s"(bid));
    const int xcd = bid & 7;
    const int sc = (3 - (int)((unsigned)(size_t)wqkv >> 7)) & 7;
    const int rt = (xcd >> 1) * 4 + ((bid >> 3) & 3), cb = (xcd & 1) * 8 + (bid >> 5), rot = 8 * rt;
    const __amdgpu_buffer_rsrc_t rsrc = __builtin_amdgcn_make_buffer_rsrc(ws, 0, WS_END, 0x00020000);
    const u32x4 z4 = {0u, 0u, 0u, 0u};
    const f32x4 zero = {0.f, 0.f, 0.f, 0.f};
    unsigned AX = lds0 + L_A + rg * 4096 + l16;
    unsigned W4 = lds0 + p * 4;
    unsigned PG = lds0 + L_PROG + p * 256;
    const unsigned WR = lds0 + rg * 4;
    auto flag = [&](unsigned a) AI {
        asm volatile("ds_write_b32 %0, %0" ::"v"(a) : "memory");
        SB0();
    };
    auto phi_w = [&](bf16x8 (&a)[2], int h) AI {
        const unsigned ad = lds0 + L_WPHI + h * 2048 + (4 * g + q) * 64 + (((p >> 1) ^ (2 * (g & 1))) * 16) + 8 * (p & 1);
        a[0] = cat8(tr_read(ad), tr_read(ad + 1024));
        a[1] = cat8(tr_read(ad ^ 32), tr_read((ad ^ 32) + 1024));
    };
    u32x4 fw[32], fd[16], gu[2];
    unsigned sel_[4][2];
    u32x4 ph[2][2], sq;
    const int hp2 = ((bid >> 1) & 3) * 4 + (bid >> 6);
    const bool ffn = (hp2 & 7) != sc;
    const int ph4 = ((bid >> 3) + (bid >> 6)) & 3;
    const int kq = mt >> 2;
    const int cs = (ffn ? hp2 - (hp2 > sc) - (hp2 > sc + 8) : 14 + (hp2 >> 3)) * 4 + ((ph4 + 3 - (int)((unsigned)(size_t)wg >> 7)) & 3);
    const int c3 = (3 - (int)((unsigned)(size_t)wg >> 7)) & 7;
    const int cd = cs >= 56 ? (cs < 63 ? 8 * (cs - 56) + c3 : cs) : (cs & 7) == c3 ? 56 + (cs >> 3) : cs;
    auto spread = [&](const u32x4& v, int i) -> bf16x8 {
        const unsigned s = v[i >> 1];
        u32x4 a;
#pragma unroll
        for (int d = 0; d < 4; ++d) a[d] = __builtin_amdgcn_perm(s, s, sel_[d][i & 1]);
        return __builtin_bit_cast(bf16x8, a);
    };
    auto fold_store = [&](const f32x4& a, unsigned off) {
        f32x4 v;
#pragma unroll
        for (int i = 0; i < 4; ++i) {
            const unsigned u = __float_as_uint(a[i]);
            const float r = __uint_as_float(__builtin_amdgcn_update_dpp(u, u, 0x128, 0xc, 0xf, false));
            v[i] = swap32_add(r, r);
        }
        const u32x4 o = nz4(v);
        ST_MASK("global_store_dwordx4", 0x00FF00FFull, off + l15 * 32 + g * 16, o, ws);
    };
    auto sel_init = [&]() AI {
#pragma unroll
        for (int d = 0; d < 4; ++d)
#pragma unroll
            for (int hf = 0; hf < 2; ++hf) {
                const unsigned b = hf ? 0x0302u : 0x0100u;
                sel_[d][hf] = (sch >> 1) != d ? 0x0c0c0c0cu : (l & 1) ? (b << 16) | 0x0c0cu : 0x0c0c0000u | b;
            }
    };
    auto gua = [&](int i) { return ws + WS_GU + cd * 4096 + i * 1024; };
    auto gu_load = [&]() AI {
#pragma unroll
        for (int i = 0; i < 2; ++i) ld_async(gu[i], l16, gua(i));
    };
    auto down = [&]() AI {
#pragma unroll
        for (int i = 0; i < 16; ++i) pin(fd[i]);
        settle(gu, [&](int) { return l16; }, gua);
        const f32x4 v = __builtin_bit_cast(f32x4, gu[0]) + __builtin_bit_cast(f32x4, gu[1]);
        auto r = __builtin_amdgcn_permlane16_swap(__float_as_uint(swap32_add(v[0], v[2])), __float_as_uint(swap32_add(v[1], v[3])), false, false);
        const float gt = rbf(__uint_as_float(r[0])), up = rbf(__uint_as_float(r[1]));
        const unsigned ha = lds0 + L_XS + hs * 512;
        lds_write<u16>(ha + l15 * 8 + g * 2, f2bf(rbf(gt / (1.f + __expf(-gt))) * up));
        const u32x4 hv = lds_read<u32x4>(ha + srow * 16);
        f32x4 y0 = zero, y1 = zero;
#pragma unroll
        for (int i = 0; i < 8; ++i) {
            const bf16x8 a = spread(hv, i);
            y0 = mfma16(a, __builtin_bit_cast(bf16x8, fd[i]), y0);
            y1 = mfma16(a, __builtin_bit_cast(bf16x8, fd[8 + i]), y1);
        }
        fold_store(y0, WS_Y + cd * 4096 + kq * 1024 + hs * 512);
        fold_store(y1, WS_Y + cd * 4096 + kq * 1024 + hs * 512 + 256);
    };
    auto need = [&](int c) AI {
        if (c == 101) lds_wait(W4 + L_FKS, 1);
        else if (c == 100) lds_wait(W4 + L_FV, 1);
        else if (c == 99) lds_wait(W4 + L_FQD, 1);
        else if (c > 0) lds_wait(c > RLD ? W4 + L_PRL : PG, c);
    };
    auto wo_load = [&](int n0, int n1, bool mk) AI {
        unsigned vo[4];
#pragma unroll
        for (int i = 0; i < 4; ++i) vo[i] = (unsigned)((8 * i + srow) * 2048 + ((sch ^ ((g & 3) << 1)) * 16) + 4096 - i * 1024);
        const char* wob = (const char*)wo + cb * 128 - 4096;
#pragma unroll
        for (int n = n0; n < n1; ++n) {
            if (n < WON && (n == 0 || W_N[n] != W_N[n - 1])) need(W_N[n]);
            four(vo, wob + ((2 * n + hs + rot) & 31) * 65536, lds0 + W_U[n] * 8192 + hs * 4096);
        }
        if (mk) mark(lds0 + (n1 > WON ? L_FW2 : L_FWD) + hs * 4);
    };
    auto phi_adr = [&](unsigned (&pw)[4], unsigned base) AI {
#pragma unroll
        for (int i = 0; i < 4; ++i) pw[i] = (base + rg * 4096 + l15 * 128 + ((g ^ fsw(l15)) << 3)) ^ (i << 5);
    };
    const int row0 = rt * 128 + rg * 32 + kp * 16;
    u32x4 xr[2];
    u64 gg;
    unsigned xo = srow * 2048 + sch * 16;
    auto g_load = [&]() AI {
        asm volatile("global_load_dwordx2 %0, %1, %2 sc1" : "=&v"(gg) : "v"((unsigned)l15 * 8), "s"(ws + WS_GG + row0 * 8) : "memory");
    };
    auto xg_load = [&]() AI {
        asm volatile("global_load_dwordx4 %0, %3, %4\n\tglobal_load_dwordx4 %1, %3, %4 offset:64\n\tglobal_load_dwordx2 %2, %5, %6 sc1"
                     : "=&v"(xr[0]), "=&v"(xr[1]), "=&v"(gg)
                     : "v"((unsigned)((l >> 2) * 2048 + p * 16)), "s"(x + (size_t)row0 * DM + cb * 64),
                       "v"((unsigned)l15 * 8), "s"(ws + WS_GG + row0 * 8) : "memory");
    };
    f32x4 a0_[4];
    float og;
    auto x_init = [&](bool staged) AI {
        if (staged) asm volatile("" : "+v"(gg));
        else asm volatile("" : "+v"(xr[0]), "+v"(xr[1]), "+v"(gg));
#pragma unroll 1
        for (unsigned s = 0; s < SPIN_CAP && __ballot((gg >> 16) != (((u64)sq[1] << 32 | sq[0]) & 0xFFFFFFFFFFFFull)); ++s) {
            g_load();
            waitv<0>();
            asm volatile("" : "+v"(gg));
        }
        og = rbf(1.f - bf2f((u16)gg));
        if (staged) {
#pragma unroll
            for (int nb = 0; nb < 4; ++nb) {
                const u32x2 u = lds_read<u32x2>(lds0 + L_A + 2048 + rg * 4096 + l15 * 128 + (((2 * nb + (g >> 1)) ^ sch) * 16) + (g & 1) * 8);
                a0_[nb] = (f32x4){lo16(u[0]), hi16(u[0]), lo16(u[1]), hi16(u[1])} * og;
            }
            asm volatile("" : "+v"(a0_[0]), "+v"(a0_[1]), "+v"(a0_[2]), "+v"(a0_[3]), "+v"(og));
            SB0();
            return;
        }
        unsigned t[8];
#pragma unroll
        for (int i = 0; i < 8; ++i) t[i] = __builtin_amdgcn_ds_bpermute(l15 * 16 + (g >> 1) * 4 + (g & 1) * 8, xr[i >> 2][i & 3]);
#pragma unroll
        for (int j = 0; j < 2; ++j) {
            auto u = __builtin_amdgcn_permlane16_swap(t[4 * j], t[4 * j + 2], false, false);
            auto v = __builtin_amdgcn_permlane16_swap(t[4 * j + 1], t[4 * j + 3], false, false);
#pragma unroll
            for (int b = 0; b < 2; ++b) a0_[2 * j + b] = (f32x4){lo16(u[b]), hi16(u[b]), lo16(v[b]), hi16(v[b])} * og;
        }
        asm volatile("" : "+v"(a0_[0]), "+v"(a0_[1]), "+v"(a0_[2]), "+v"(a0_[3]), "+v"(og));
        SB0();
    };
    auto xg_def = [&]() AI { asm volatile("" : "=v"(xr[0]), "=v"(xr[1]), "=v"(gg)); };
    auto relane = [&]() AI {
        unsigned z = 0;
        asm volatile("" : "+v"(z));
        l = __builtin_amdgcn_mbcnt_hi(~0u, __builtin_amdgcn_mbcnt_lo(~0u, z));
        g = l >> 4, l15 = l & 15, q = l15 >> 2, p = l & 3, srow = l >> 3, sch = l & 7, l16 = l * 16;
        AX = lds0 + L_A + rg * 4096 + l16, W4 = lds0 + p * 4, PG = lds0 + L_PROG + p * 256, xo = srow * 2048 + sch * 16;
    };
    auto kf_fetch = [&]() AI {
        asm volatile("s_mov_b32 m0, %3\n\ts_mov_b64 exec, 0xff\n\tglobal_load_lds_dwordx4 %1, %2 sc1\n\ts_mov_b64 exec, -1\n\t"
                     "s_mov_b32 m0, %4\n\ts_nop 0\n\t"
                     "global_load_lds_dwordx4 %0, %2 sc1\n\tglobal_load_lds_dwordx4 %0, %2 offset:1024 sc1\n\t"
                     "global_load_lds_dwordx4 %0, %2 offset:2048 sc1\n\tglobal_load_lds_dwordx4 %0, %2 offset:3072 sc1"
                     ::"v"(l16), "v"((unsigned)(4096 + (l & 4) * 128 + p * 16)), "s"(ws + WS_KF + (hp * 2 + hs) * 8192),
                       "s"(lds0 + L_KZ + hs * 128), "s"(lds0 + L_KF + hs * 4096) : "memory");
    };
    auto fd_def = [&]() AI {
#pragma unroll
        for (int i = 0; i < 16; ++i) asm volatile("" : "=v"(fd[i]));
        asm volatile("" : "=v"(gu[0]), "=v"(gu[1]));
    };
    const int oh = hs ^ kp ^ 1;

    if (__builtin_expect(w >= 4, 1)) {
        const bool loader = !(w & 1);
        const unsigned kf_kv = WS_KF + (hp * 2 + (mt >> 3)) * 8192 + (mt & 7) * 512;
        const unsigned kf_z = WS_KF + (hp * 2 + (mt >> 3)) * 8192 + 4096 + (mt & 7) * 128;
        const unsigned fgo = ffn ? (unsigned)((256 * kq + 16 * srow) * 7168 + cs * 128 + sch * 16) : 0u;
        const char* fsrc = (const char*)(hs ? wu : wg);
        auto gu_w = [&](int q0, int q1) AI {
#pragma unroll
            for (int i = q0; i < q1; ++i) {
                unsigned t1, t2, t3;
                asm volatile("v_add_u32 %4, 0x1c00, %7\n\tv_add_u32 %5, 0x3800, %7\n\tv_add_u32 %6, 0x5400, %7\n\t"
                             "global_load_dwordx4 %0, %7, %8 nt\n\tglobal_load_dwordx4 %1, %4, %8 nt\n\t"
                             "global_load_dwordx4 %2, %5, %8 nt\n\tglobal_load_dwordx4 %3, %6, %8 nt"
                             : "=&v"(fw[4 * i]), "=&v"(fw[4 * i + 1]), "=&v"(fw[4 * i + 2]), "=&v"(fw[4 * i + 3]), "=&v"(t1), "=&v"(t2), "=&v"(t3)
                             : "v"(fgo), "s"(fsrc + (128 * (i >> 2) + 4 * (i & 3)) * 7168) : "memory");
            }
        };
        const unsigned fdo = cd < 56 ? (unsigned)((64 * cd + 8 * srow) * 2048 + 512 * kq + 256 * hs + sch * 16) : 0u;
        auto dn_w = [&](int q0, int q1) AI {
#pragma unroll
            for (int i = q0; i < q1; ++i)
                asm volatile("global_load_dwordx4 %0, %4, %5 offset:-4096 nt\n\tglobal_load_dwordx4 %1, %4, %5 offset:-2048 nt\n\t"
                             "global_load_dwordx4 %2, %4, %5 nt\n\tglobal_load_dwordx4 %3, %4, %5 offset:2048 nt"
                             : "=&v"(fd[4 * i]), "=&v"(fd[4 * i + 1]), "=&v"(fd[4 * i + 2]), "=&v"(fd[4 * i + 3])
                             : "v"(fdo), "s"((const char*)wd + 4096 + (i & 1) * 8192 + (i >> 1) * 128) : "memory");
        };
        auto gate_up = [&]() AI {
            sel_init();
            u32x4 xs[4];
            waitv<4 * (NF - NF1) + 1 + 4 * (8 - GQ1) + 16>();
#pragma unroll
            for (int i = 0; i < 4; ++i) xs[i] = lds_read<u32x4>(lds0 + L_XS + hs * 512 + (i >> 1) * 256 + srow * 32 + (i & 1) * 16);
            f32x4 a;
            bf16x8 t[2];
            t[0] = spread(xs[0], 0);
            sfor<0, 32>([&](auto I) {
                constexpr int i = I;
                SB0();
                if constexpr (i < 31) t[~i & 1] = spread(xs[(i + 1) >> 3], (i + 1) & 7);
                else asm volatile("s_nop 1");
                asm volatile("" : "+v"(t[~i & 1]));
                if constexpr (i == 4 * GQ1) waitv<16>();
                SB0();
                u32x4& f = fw[i];
                bf16x8& u = t[i & 1];
                f32x4& c = a;
                pin(f);
                if constexpr (i == 0) asm volatile("v_mfma_f32_16x16x32_bf16 %0, %1, %2, 0" : "=&v"(c) : "v"(u), "v"(f));
                else asm volatile("v_mfma_f32_16x16x32_bf16 %0, %1, %2, %0" : "+v"(c) : "v"(u), "v"(f));
            });
            asm volatile("s_nop 7" : "+v"(a));
            SB0();
            fold_store(a, WS_GU + cs * 4096 + kq * 512 + hs * 256);
            SB0();
        };
        u64 T;
        u32x4 sv[2];
        u32x2 sg;
        auto rg_load = [&]() AI {
            asm volatile("global_load_dwordx4 %0, %3, %4 sc1\n\tglobal_load_dwordx4 %1, %3, %4 offset:16 sc1\n\tglobal_load_dwordx2 %2, %5, %6 sc1"
                         : "=&v"(sv[0]), "=&v"(sv[1]), "=&v"(sg) : "v"(l * 32), "s"(ws + WS_RG), "v"(l * 8), "s"(ws + WS_GG + (mt * 2 + hs) * 512) : "memory");
        };
        auto select = [&]() AI {
            const unsigned tl = (unsigned)T & 0x1FFFu, thi = (unsigned)(T >> 13);
#pragma unroll 1
            for (unsigned s = 0; s < SPIN_CAP; ++s) {
                pin(sv[0]);
                pin(sv[1]);
                asm volatile("" : "+v"(sg));
                const unsigned bad = ((sg[0] >> 16) ^ ((unsigned)T & 0xFFFFu)) | (sg[1] ^ (unsigned)(T >> 16)) | ((sv[0][0] >> 19) ^ tl) | (sv[0][1] ^ thi) | ((sv[0][2] >> 19) ^ tl) | (sv[0][3] ^ thi) |
                                     ((sv[1][0] >> 19) ^ tl) | (sv[1][1] ^ thi) | ((sv[1][2] >> 19) ^ tl) | (sv[1][3] ^ thi);
                if (__all(bad == 0u)) break;
                __builtin_amdgcn_s_sleep(16);
                rg_load();
                waitv<0>();
            }
            pin(sv[0]);
            pin(sv[1]);
            asm volatile("" : "+v"(sg));
            lds_write<unsigned>(lds0 + L_G + hs * 512 + l * 4, sg[0] << 16);
            SB0();
            unsigned k2 = 0;
#pragma unroll
            for (int j = 0; j < 4; ++j) {
                const unsigned v = sv[j >> 1][(j & 1) * 2] & 0x7FFFFu;
                k2 = max(k2, ((v >> 3) << 11) | ((255 - (4 * l + j)) << 3) | (v & 7));
            }
            const unsigned key = __builtin_amdgcn_readfirstlane(wave_umax(k2));
            asm volatile("s_mov_b32 m0, %2\n\ts_mov_b64 exec, %3\n\tglobal_load_lds_dwordx4 %0, %1\n\ts_mov_b64 exec, -1"
                         ::"v"((unsigned)(512 * kq) + l16), "s"(x + (size_t)(2047 - (int)(key & 2047u)) * DM), "s"(lds0 + L_XS + hs * 512), "s"(0xFFFFFFFFull) : "memory");
            lds_write<u32x4>(lds0 + L_SEL, (u32x4){(unsigned)T, (unsigned)(T >> 32), 2047u - (key & 2047u), key >> 11});
            if (bid == 0 && hs) ST_MASK("global_store_dwordx2", 1ull, 0u, T, ws + WS_E);
            SB0();
        };
        auto k_soft = [&]() AI {
            bf16x8 a0[2], kb[2];
            f32x4 pk[2][2][2];
            unsigned pw[4];
            phi_w(a0, 0);
            phi_adr(pw, lds0 + P_U[0] * 8192);
            asm volatile("" : "+v"(pw[0]), "+v"(pw[1]), "+v"(pw[2]), "+v"(pw[3]));
            auto logits = [&](int h) AI {
                lds_wait(WR + L_FK0 + 4 * h, 1);
#pragma unroll
                for (int i = 0; i < 2; ++i) kb[i] = lds_read128(AX + (P_U[0] * 8192 - L_A) + h * 4096 + i * 1024);
#pragma unroll
                for (int i = 0; i < 4; ++i) pk[h][i >> 1][i & 1] = mfma16(a0[i & 1], kb[i >> 1], zero);
            };
            auto round = [&](int h) AI {
#pragma unroll
                for (int i = 0; i < 4; ++i) pk[h][i >> 1][i & 1] = rbf4(pk[h][i >> 1][i & 1]);
            };
            logits(0);
            SB0();
            round(0);
            SB0();
            phi_k(pk[0], pw, 0, [&]() AI {
                logits(1);
                SB0();
            });
            SB0();
            round(1);
            phi_k(pk[1], pw, 4096, []() {});
            SB0();
            flag(WR + L_FP);
        };
        auto q_soft = [&](auto hook) AI {
            bf16x8 a1[2];
            f32x4 pq[2][2];
            phi_w(a1, hs);
            lds_wait0(WR + L_FQ);
#pragma unroll
            for (int rb = 0; rb < 2; ++rb) {
                const bf16x8 b = lds_read128(AX + rb * 1024);
#pragma unroll
                for (int fb = 0; fb < 2; ++fb) pq[rb][fb] = rbf4(mfma16(a1[fb], b, zero));
            }
            softmax2(pq, ph, hook);
            asm volatile("" : "+v"(ph[0][0]), "+v"(ph[0][1]), "+v"(ph[1][0]), "+v"(ph[1][1]));
        };
        if (__builtin_expect(loader, 1)) {
            vx_init();
            vw_init();
            unsigned pv = 0;
            sfor<4, NF1>([&](auto N) {
                constexpr int n = N;
                if constexpr (F_N[n] > F_N[n - 1]) {
                    __builtin_amdgcn_s_setprio(3);
                    lds_wait(pv, PG, F_N[n]);
                    if (F_N[n] < RLD) lds_peek(pv, PG);
                    __builtin_amdgcn_s_setprio(0);
                }
                if constexpr (n == 12) {
                    asm volatile("s_mov_b32 m0, %2\n\ts_nop 0\n\tglobal_load_lds_dwordx4 %0, %1\n\tglobal_load_lds_dwordx4 %0, %1 offset:1024"
                                 ::"v"((unsigned)((l >> 2) * 64 + (((l & 3) ^ (2 * ((l >> 4) & 1))) * 16))),
                                   "s"(wphi + (hp * 2 + hs) * 1024), "s"(lds0 + L_WPHI + hs * 2048) : "memory");
                }
                fill(N);
            });
            flag(lds0 + L_FLD + hs * 4);
            __builtin_amdgcn_s_setprio(2);
            const char* cla = ws + WS_CL + ((l15 * 2 + (mt >> 3)) * 16 + hp) * 128;
            u64 cl;
            asm volatile("global_load_dwordx2 %0, %1, off sc1" : "=&v"(cl) : "v"(cla) : "memory");
            k_soft();
            SB0();
            lds_wait(W4 + L_FP, 1);
            sq = lds_read<u32x4>(lds0 + L_SEL);
            waitv<0>();
            asm volatile("" : "+v"(cl));
            SB0();
            {
                u32x4 kp_[6];
                const char* kb = ws + WS_KV + (hp * 8 + (mt >> 1)) * 16384 + (mt & 1) * 512 + hs * 256;
                const bool zw = !((mt & 3) | hs);
                const char* zb = zw ? ws + WS_Z + (hp * 4 + (mt >> 2)) * 2048 : kb;
                const unsigned kvo = g * 1024 + l15 * 16, zo = zw ? l16 : 0u;
                auto ko = [&](int i) { return i < 4 ? kvo : zo; };
                auto ka = [&](int i) { return i < 4 ? kb + i * 4096 : zb + (i - 4) * 1024; };
                const u64 T = (u64)sq[1] << 32 | sq[0];
#pragma unroll 1
                for (unsigned s = 0; s < SPIN_CAP && __ballot(cl != T); ++s)
                    asm volatile("global_load_dwordx2 %0, %1, off sc1\n\ts_waitcnt vmcnt(0)" : "=&v"(cl) : "v"(cla) : "memory");
                u32x4 a01;
#pragma unroll
                for (int i = 0; i < 4; ++i) a01[i] = (l15 >> 1) != i ? 0u : (l & 1) ? 0x3f800000u : 0x3f80u;
                unsigned po = kf_kv + hs * 256 + l15 * 16 + (g & 1) * 8, pz = kf_z + (l & 8) * 8 + p * 16 + (sch >> 2) * 8 + (g >> 1) * 4;
                pin(a01);
                asm volatile("" : "+v"(po), "+v"(pz));
                lds_wait0(W4 + L_FKS);
                __builtin_amdgcn_s_sleep(GDL);
#pragma unroll
                for (int i = 0; i < 6; ++i) ld_async(kp_[i], ko(i), ka(i));
                xg_load();
                SB0();
                waitv<3>();
                SB0();
                u32x2 o;
                auto sum = [&]() AI {
                    f32x4 d = zero;
#pragma unroll
                    for (int i = 0; i < 4; ++i) d = mfma16(__builtin_bit_cast(bf16x8, a01), __builtin_bit_cast(bf16x8, kp_[i]), d);
                    SB0();
                    unsigned m = least(kp_);
                    asm volatile("" : "+v"(m));
                    SB0();
                    o = (u32x2){nz1(pack2(d[0], d[1])), nz1(pack2(d[2], d[3]))};
                    asm volatile("" : "+v"(o));
                    return m;
                };
#pragma unroll
                for (int i = 0; i < 6; ++i) pin(kp_[i]);
                if (__builtin_expect(__ballot(sum() == 0u) != 0ull, 0)) {
                    again(kp_, ko, ka);
                    sum();
                }
                SB0();
                ST_MASK("global_store_dwordx2", 0xFFFFFFFFull, po, o, ws);
                if (zw) {
                    const f32x4 z = __builtin_bit_cast(f32x4, kp_[4]) + __builtin_bit_cast(f32x4, kp_[5]);
                    const unsigned t = __float_as_uint(dpp_add<0x128>(swap16_add(swap32_add(z[0], z[2]), swap32_add(z[1], z[3]))));
                    auto r = __builtin_amdgcn_permlane16_swap(t, t, false, false);
                    ST_MASK("global_store_dword", 0x0000FFFF0000FFFFull, pz, nz1(pack2(__uint_as_float(r[0]), __uint_as_float(r[1]))), ws);
                }
                flag(lds0 + L_FG + hs * 4);
            }
            SB0();
            need(KFN);
            q_soft([]() {});
            SB0();
            __builtin_amdgcn_s_sleep(KFD);
            SB0();
            kf_fetch();
            mark(lds0 + L_FKF + hs * 4);
            SB0();
            waitv<6>();
            x_init(false);
            fd_def();
            xg_def();
        } else {
            const int row0 = (mt * 16 + hp) * 8 + 4 * hs;
            __builtin_amdgcn_s_setprio(2);
            asm volatile("s_waitcnt lgkmcnt(0)" : "+s"(sd));
            lds_wait(lds0 + L_FULL + p * 4, 3);
            u32x4 rv[2], gv[2], xv[4][2];
            u64 ev;
            asm volatile("global_load_dwordx2 %4, %5, %9 sc1\n\t"
                         "global_store_dwordx4 %10, %11, %9 sc1\n\t"
                         "global_store_dwordx2 %12, %13, %9 sc1\n\t"
                         "global_load_dwordx4 %0, %6, %7\n\tglobal_load_dwordx4 %1, %6, %7 offset:1024\n\t"
                         "global_load_dwordx4 %2, %6, %8\n\tglobal_load_dwordx4 %3, %6, %8 offset:1024"
                         : "=&v"(rv[0]), "=&v"(rv[1]), "=&v"(gv[0]), "=&v"(gv[1]), "=&v"(ev)
                         : "v"(0u), "v"(l16), "s"(wr), "s"(gw), "s"(ws),
                           "v"(l < 32 ? WS_Y + cd * 4096 + kq * 1024 + hs * 512 + l16 : WS_GU + cs * 4096 + kq * 512 + hs * 256 + l15 * 16), "v"(z4),
                           "v"(hs ? kf_z + l15 * 8 : kf_kv + l * 8), "v"(0ull)
                         : "memory");
#pragma unroll
            for (int r = 0; r < 2; ++r)
                asm volatile("global_load_dwordx4 %0, %4, %5\n\tglobal_load_dwordx4 %1, %4, %5 offset:1024\n\t"
                             "global_load_dwordx4 %2, %4, %5 offset:2048\n\tglobal_load_dwordx4 %3, %4, %5 offset:3072"
                             : "=&v"(xv[2 * r][0]), "=&v"(xv[2 * r][1]), "=&v"(xv[2 * r + 1][0]), "=&v"(xv[2 * r + 1][1])
                             : "v"(l16), "s"(x + (size_t)(row0 + 2 * r) * DM) : "memory");
            SB0();
            waitv<0>();
            asm volatile("" : "+v"(rv[0]), "+v"(rv[1]), "+v"(gv[0]), "+v"(gv[1]), "+v"(ev));
#pragma unroll
            for (int r = 0; r < 4; ++r) asm volatile("" : "+v"(xv[r][0]), "+v"(xv[r][1]));
            SB0();
            T = ((u64)__builtin_amdgcn_readfirstlane((unsigned)(ev >> 32)) << 32 | __builtin_amdgcn_readfirstlane((unsigned)ev)) + 1;
            float ra_[4] = {0.f, 0.f, 0.f, 0.f}, ga_[4] = {0.f, 0.f, 0.f, 0.f};
#pragma unroll
            for (int r = 0; r < 4; ++r)
#pragma unroll
                for (int i = 0; i < 2; ++i)
#pragma unroll
                    for (int j = 0; j < 4; ++j) {
                        ra_[r] = dot2(xv[r][i][j], rv[i][j], ra_[r]);
                        ga_[r] = dot2(xv[r][i][j], gv[i][j], ga_[r]);
                    }
            const int rr = (g >> 1) | ((g & 1) << 1);
            unsigned k = f2bf(sum4(ra_));
            k ^= (k & 0x8000u) ? 0xFFFFu : 0x8000u;
            k = (k << 3) | (7 - 4 * hs - rr);
            auto r1 = __builtin_amdgcn_permlane16_swap(k, k, false, false);
            k = max(r1[0], r1[1]);
            auto r2 = __builtin_amdgcn_permlane32_swap(k, k, false, false);
            k = max(r2[0], r2[1]);
            u64 gq = (T << 16) | f2bf(1.f / (1.f + __expf(-rbf(sum4(ga_)))));
            if (hs) lds_write<unsigned>(lds0 + L_BEST, k | 0x80000000u);
            asm volatile("" : "+v"(gq), "+v"(k));
            __builtin_amdgcn_s_setprio(0);
            lds_wait8(PG, CLR0 + ph4);
            if (!hs) {
                lds_wait(lds0 + L_BEST, 1);
                const u64 rq = (T << 19) | max(k, lds_read<unsigned>(lds0 + L_BEST) & 0x7FFFFu);
                ST_MASK("global_store_dwordx2", 1ull, 0u, rq, ws + WS_RG + (mt * 16 + hp) * 8);
            }
            ST_MASK("global_store_dwordx2", 0x0001000100010001ull, (unsigned)(rr * 8), gq, ws + WS_GG + row0 * 8);
            SB0();
#pragma unroll
            for (int n = 0; n < 4; ++n) st_wt(rsrc, WS_KV + ((hp * 8 + hs * 4 + n) * 16 + mt) * 1024 + l16, z4);
            ST_MASK("global_store_dwordx4", 0xFFFFull, WS_Z + ((hp * 4 + hs * 2 + (srow & 1)) * 16 + mt) * 128 + sch * 16, z4, ws);
#pragma unroll
            for (int n = 0; n < 8; ++n) st_wt(rsrc, WS_O + ((mt * 2 + (n >> 2)) * 32 + 2 * hp + hs) * 4096 + (n & 3) * 1024 + l16, z4);
            waitv<0>();
            ST_MASK("global_store_dwordx2", 0xFFFFull, (unsigned)l * 8, T, ws + WS_CL + ((mt * 2 + hs) * 16 + hp) * 128);
            SB0();
            vw_init();
            asm volatile("" : "+v"(vw[0]), "+v"(vw[1]), "+v"(vw[2]), "+v"(vw[3]));
            lds_wait8(PG, RLD);
            __builtin_amdgcn_s_setprio(3);
            lds_wait(lds0 + L_FLD + (l & 1) * 4, 1);
            rg_load();
            SB0();
            gu_w(0, GQ1);
            waitv<4 * GQ1>();
            select();
            flag(WR + L_FP);
            sfor<NF1, NF>([&](auto N) {
                constexpr int n = N;
                if constexpr (n == NF1 || F_N[n] != F_N[n - 1]) need(F_N[n]);
                fill(N);
            });
            SB0();
            gu_w(GQ1, 8);
            dn_w(0, 4);
            SB0();
            gate_up();
            __builtin_amdgcn_s_setprio(0);
            SB0();
            xg_load();
            lds_wait(W4 + L_FKS, 1);
            wo_load(0, WOE, false);
            SB0();
            sq = lds_read<u32x4>(lds0 + L_SEL);
            waitv<4 * WOE>();
            x_init(false);
            lds_wait(lds0 + L_FG + (l & 1) * 4, 1);
            gu_load();
            wo_load(WOE, WOM, false);
            SB0();
            __builtin_amdgcn_s_setprio(2);
            q_soft([&]() AI {
                wo_load(WOM, WON, true);
                SB0();
            });
            waitv<4 * (WON - WOE) + 1>();
#pragma unroll
            for (int i = 0; i < 16; ++i) pin(fd[i]);
            pin(gu[0]);
            pin(gu[1]);
            SB0();
            xg_def();
        }
        SB0();
    } else {
        __builtin_amdgcn_s_setprio(1);
        unsigned ra[4], xt[2];
#pragma unroll
        for (int u = 0; u < 4; ++u) ra[u] = lds0 + J_B[0] * 8192 + (8 * g + q) * 128 + ((u ^ (q >> 1) ^ ((g & 1) << 1)) * 32) + 8 * p;
        unsigned xa = lds0 + (32 * (w & 1) + l15) * 128 + ((g ^ (l15 >> 1)) * 16), fa = lds0 + L_FULL + (l & 3) * 4;
        xt[0] = xa + (hs ? X_U1[0] : X_U0[0]) * 8192;
        xt[1] = xt[0] ^ 64;
        asm volatile("s_mov_b32 m0, %4" : "+v"(xa), "+v"(fa), "+v"(xt[0]), "+v"(xt[1]) : "s"(lds0 + L_PROG + w * 256 - 124));
        f32x4 acc[3][4][2];
        bf16x8 xf[ND][2][2];
        s16x4 wh[PD + 1][2];
        unsigned fv = 0;
        bf16x8 kown[2];
        u64 bad = ~0ull;
        auto chk = [&](int c) AI {
            SB0();
            if (__builtin_expect(bad != 0ull, 0)) {
                unsigned c_;
                asm volatile("s_mov_b32 %1, 0x400000\n"
                             ".Lp%=:\n\tds_read_b32 %0, %2 offset:%3\n\ts_waitcnt lgkmcnt(0)\n\tv_cmp_gt_u32 vcc, %4, %0\n\ts_cbranch_vccz .Lf%=\n\t"
                             "s_sub_u32 %1, %1, 1\n\ts_cbranch_scc0 .Lp%=\n.Lf%=:"
                             : "=&v"(fv), "=&s"(c_) : "v"(fa), "n"(c > NM1 ? 32 : 0), "n"(c) : "memory", "vcc", "scc");
            }
            SB0();
        };
        auto kv_part = [&]() AI {
            bf16x8 ones;
#pragma unroll
            for (int j = 0; j < 8; ++j) ones[j] = (short)0x3f80;
            unsigned pa = lds0 + (hs ? P_U[2] : P_U[0]) * 8192 + (4 * g + q) * 128 + (((8 * (rg & 1) + p) ^ fsw(4 * g + q)) << 3);
            unsigned pb = pa ^ 32, va = lds0 + L_A + hs * 2048 + l16, kv_st = WS_KV + ((hp * 8 + rg * 2) * 16 + mt) * 1024 + l16;
            unsigned kv_s1 = kv_st + 16384, z_st = WS_Z + ((hp * 4 + rg) * 16 + mt) * 128 + (l & 1) * 64 + g * 16, fk = WR + L_FKS;
            asm volatile("" : "+v"(pa), "+v"(pb), "+v"(va), "+v"(kv_st), "+v"(kv_s1), "+v"(z_st), "+v"(fk), "+v"(ones));
            bf16x8 b0[4], b1[4], af[2][4];
            lds_wait0(lds0 + L_FV + sch * 4);
            SB0();
#pragma unroll
            for (int i = 0; i < 4; ++i) {
                af[0][i] = cat8(tr_read(pa + i * 4096), tr_read(pa + i * 4096 + 2048));
                b0[i] = lds_read128(va + i * 4096);
                SB0();
                af[1][i] = cat8(tr_read(pb + i * 4096), tr_read(pb + i * 4096 + 2048));
                SB0();
            }
#pragma unroll
            for (int i = 0; i < 4; ++i) b1[i] = lds_read128(va + i * 4096 + 1024);
            SB0();
            f32x4 k0[2] = {zero, zero}, k1[2] = {zero, zero}, zt[2] = {zero, zero};
#pragma unroll
            for (int i = 0; i < 4; ++i)
#pragma unroll
                for (int e = 0; e < 2; ++e) {
                    k0[e] = mfma16(af[e][i], b0[i], k0[e]);
                    zt[e] = mfma16(af[e][i], ones, zt[e]);
                }
            SB0();
#pragma unroll
            for (int i = 0; i < 4; ++i)
#pragma unroll
                for (int e = 0; e < 2; ++e) k1[e] = mfma16(af[e][i], b1[i], k1[e]);
            st_wt(rsrc, kv_st, nz4(__builtin_bit_cast(u32x4, pack44(k0[0], k0[1]))));
            const u32x4 zo = nz4((l & 1) ? zt[1] : zt[0]);
            ST_MASK("global_store_dwordx4", 0x0003000300030003ull, z_st, zo, ws);
            st_wt(rsrc, kv_s1, nz4(__builtin_bit_cast(u32x4, pack44(k1[0], k1[1]))));
            SB0();
            flag(fk);
        };
        auto report = [&](int r) AI {
            unsigned t, a;
            if (r > RLD) asm volatile("v_mov_b32 %0, %3\n\tv_mov_b32 %1, %2\n\tds_write_b32 %1, %0" : "=&v"(t), "=&v"(a) : "s"(WR + L_PRL), "n"(r) : "memory");
            else asm volatile("global_load_lds_dword %0, %1 offset:%2" ::"v"(v0), "s"(one), "n"(4 * r) : "memory");
        };
        auto rd1 = [&](auto F, auto H) AI {
            constexpr int f = F, h = H, n = f >> 2, j = f >> 3, c = h ? E_B[f] : E_A[f], a = c % 100;
            if constexpr (h == 0 && (f & 3) == 0 && Q_C[n] > 0) chk(Q_C[n]);
            wh[f % (PD + 1)][h] = tr_read(ra[f & 3] + (J_U[j] - J_B[j]) * 8192 + (n & 1) * 4096 + h * 512);
            SB0();
            if constexpr (c / 100 == 1) {
                constexpr int jn = (f + 3 - a) >> 3;
                asm volatile("v_add_u32 %0, %1, %0" : "+v"(ra[a]) : "n"((J_B[jn] - J_B[jn - 1]) * 8192));
            } else if constexpr (c / 50 == 4) {
                asm volatile("v_add_u32 %0, %1, %2" : "=v"(xt[0]) : "s"((hs ? X_U1[a] : X_U0[a]) * 8192), "v"(xa));
            } else if constexpr (c / 50 == 5) {
                asm volatile("v_xor_b32 %0, 64, %1" : "=v"(xt[1]) : "v"(xt[0]));
            } else if constexpr (c / 100 == 3) {
                xf[X_R[a % 25]][a / 25 & 1][a / 50] = lds_read128(xt[a / 50] + (a / 25 & 1) * 2048);
            } else if constexpr (c / 100 == 4) {
                fv = *(volatile AS3 unsigned*)(size_t)(fa + (a > NM1 ? 32 : 0));
            } else if constexpr (c / 100 == 5) {
                bad = __builtin_amdgcn_ballot_w64(fv < (unsigned)a);
                asm volatile("" : "+s"(bad));
            } else if constexpr (c / 100 == 6) {
                report(a);
            }
        };
        sfor<0, PD>([&](auto F) { rd1(F, IC<0>{}); rd1(F, IC<1>{}); });
        SB0();
        constexpr int K0F = 4 * KD, K1F = 4 * KD + 1;
        sfor<0, 96>([&](auto N) {
            sfor<0, 4>([&](auto E) {
                constexpr int n = N, e = E, f = 4 * n + e, j = n >> 1, ks = n & 1, u = e, s = J_X[j], m = J_M[j];
                const bf16x8 wv = cat8(wh[f % (PD + 1)][0], wh[f % (PD + 1)][1]);
                sfor<0, 2>([&](auto RB) {
                    constexpr int rb = RB;
                    const bf16x8 xv = xf[X_R[s]][rb][ks];
                    if (j_first(j) && ks == 0) asm volatile("v_mfma_f32_16x16x32_bf16 %0, %1, %2, 0" : "=&v"(acc[m][u][rb]) : "v"(m == 1 ? xv : wv), "v"(m == 1 ? wv : xv));
                    else asm volatile("v_mfma_f32_16x16x32_bf16 %0, %1, %2, %0" : "+v"(acc[m][u][rb]) : "v"(m == 1 ? xv : wv), "v"(m == 1 ? wv : xv));
                    SB0();
                    if constexpr (f + PD < 384 && (f + PD < 4 * VD || f >= 4 * VD)) rd1(IC<f + PD>{}, RB);
                    if constexpr (rb == 0 && f == K0F) relane();
                    if constexpr (rb == 0 && (f == K0F || f == K1F)) asm volatile("s_nop 7");
                    SB0();
                    if constexpr (f == K0F) lds_write<bf16x8>(AX + (P_U[0] * 8192 - L_A) + rb * 1024, pack44(acc[0][0][rb], acc[0][1][rb]));
                    if constexpr (f == K1F) {
                        kown[rb] = pack44(acc[0][2][rb], acc[0][3][rb]);
                        asm volatile("" : "+v"(kown[rb]));
                    }
                    SB0();
                });
                if constexpr (f == K0F) flag(WR + L_FK0);
                if constexpr (f == 4 * VD - 1) {
                    bf16x8 ak[2];
                    f32x4 pk[2][2];
                    unsigned pw[4];
                    __builtin_amdgcn_s_setprio(2);
                    phi_w(ak, 1);
                    asm volatile("s_nop 7");
                    SB0();
#pragma unroll
                    for (int i = 0; i < 4; ++i) lds_write<bf16x8>(AX + i * 1024, pack44(acc[1][i][0], acc[1][i][1]));
                    SB0();
#pragma unroll
                    for (int i = 0; i < 4; ++i) pk[i >> 1][i & 1] = rbf4(mfma16(ak[i & 1], kown[i >> 1], zero));
                    phi_adr(pw, lds0 + P_U[2] * 8192);
                    phi_k(pk, pw, 0, []() {});
                    SB0();
                    flag(WR + L_FV);
                    kv_part();
                    __builtin_amdgcn_s_setprio(1);
                    fv = 0;
                    bad = ~0ull;
                    sfor<4 * VD, 4 * VD + PD>([&](auto F) { rd1(F, IC<0>{}); rd1(F, IC<1>{}); });
                    SB0();
                }
                if constexpr (f >= 24 && f < 56 && (f & 7) == 7) __builtin_amdgcn_s_sleep(5);
            });
        });
        asm volatile("s_nop 7");
        SB0();
        __builtin_amdgcn_s_setprio(0);
        flag(WR + L_FQD);
        bf16x8 a0[2];
        f32x4 pq[2][2];
        phi_w(a0, hs ^ 1);
        lds_wait(W4 + L_FKS, 1);
#pragma unroll
        for (int rb = 0; rb < 2; ++rb) {
            const bf16x8 b0 = pack44(acc[2][0][rb], acc[2][1][rb]), b1 = pack44(acc[2][2][rb], acc[2][3][rb]);
            lds_write<bf16x8>(AX + rb * 1024, hs ? b1 : b0);
#pragma unroll
            for (int fb = 0; fb < 2; ++fb) pq[rb][fb] = mfma16(a0[fb], hs ? b0 : b1, zero);
        }
        SB0();
        flag(WR + L_FQ);
        sq = lds_read<u32x4>(lds0 + L_SEL);
        SB0();
#pragma unroll
        for (int i = 0; i < 4; ++i) pq[i >> 1][i & 1] = rbf4(pq[i >> 1][i & 1]);
        softmax2(pq, ph, []() {});
        asm volatile("" : "+v"(ph[0][0]), "+v"(ph[0][1]), "+v"(ph[1][0]), "+v"(ph[1][1]));
        SB0();
        asm volatile("s_mov_b32 m0, %3\n\ts_nop 0\n\tglobal_load_lds_dwordx4 %0, %1\n\tglobal_load_lds_dwordx4 %0, %2 offset:1024"
                     ::"v"((unsigned)(srow * 2048 + (sch ^ srow) * 16)), "s"(x + (size_t)row0 * DM + cb * 64),
                       "s"(x + (size_t)(row0 + 8) * DM + cb * 64 - 512), "s"(lds0 + L_A + 2048 + rg * 4096) : "memory");
        g_load();
        asm volatile("" : "=v"(xr[0]), "=v"(xr[1]));
        asm volatile("" : "=v"(a0_[0]), "=v"(a0_[1]), "=v"(a0_[2]), "=v"(a0_[3]), "=v"(og));
        fd_def();
    }
    relane();
    const char* cla = ws + WS_CL + ((rt * 2 + kp) * 16 + l15) * 128;
    const u64 T = (u64)sq[1] << 32 | sq[0];
    u64 cl;
    unsigned ka0 = lds0 + L_KF + oh * 4096 + l16, ka1 = lds0 + L_KZ + oh * 128 + g * 16;
    unsigned o_st = WS_O + ((mt * 2 + hs) * 32 + 2 * hp + oh) * 4096 + (rg & 1) * 2048 + l16;
    float gt[2];
#pragma unroll
    for (int i = 0; i < 2; ++i) gt[i] = lds_read<float>(lds0 + L_G + hs * 512 + (rg & 1) * 128 + i * 64 + l15 * 4);
    asm volatile("" : "+v"(ka0), "+v"(ka1), "+v"(o_st), "+v"(cla), "+v"(gt[0]), "+v"(gt[1]));
    u32x4 kf[6];
    auto kf_read = [&]() AI {
#pragma unroll
        for (int i = 0; i < 2; ++i) kf[4 + i] = lds_read<u32x4>(ka1 + i * 64);
        SB0();
#pragma unroll
        for (int i = 0; i < 4; ++i) kf[i] = lds_read<u32x4>(ka0 + i * 1024);
    };
    lds_wait0(lds0 + L_FKF + oh * 4);
    kf_read();
    asm volatile("global_load_dwordx2 %0, %1, off sc1" : "=&v"(cl) : "v"(cla) : "memory");
    SB0();
    f32x4 nm[2][2];
    float rd_[2];
    if (__builtin_expect(__ballot(o_sums(kf, ph, gt, rd_, nm) == 0u) != 0ull, 0)) {
#pragma unroll 1
        for (unsigned n = 2; n < SPIN_CAP; ++n) {
            if ((w & 5) == 4) {
                kf_fetch();
                waitv<0>();
                lds_write<unsigned>(lds0 + L_FKF + oh * 4, n);
            } else lds_wait(lds0 + L_FKF + oh * 4, n);
            asm volatile("" ::: "memory");
            kf_read();
            if (__ballot(o_sums(kf, ph, gt, rd_, nm) == 0u) == 0ull) break;
        }
        SB0();
    }
    SB0();
    o_store(rd_, nm, rsrc, o_st);
    SB0();
    flag(lds0 + L_FOS + w * 4);
    waitv<3>();
    __builtin_amdgcn_s_setprio(0);
    if ((w & 5) == 4) {
        lds_wait(lds0 + L_FOS + sch * 4, 1);
        wo_load(WON, 16, true);
    }
    if ((w & 5) == 5) {
        if (cd < 56) {
            sel_init();
            down();
        } else {
            __builtin_amdgcn_s_sleep(PDL);
            waitv<2>();
        }
    }
    flag(lds0 + L_FWO + w * 4);
    const u32x2 selv = {sq[2], sq[3]};

    SB0();
    const char* ob = ws + WS_O + (rt * 2 + (rg >> 1)) * 131072 + (rg & 1) * 2048;
    const unsigned ov0 = l16 + kp * 1024, ov1 = l16 + (kp ^ 1) * 1024;
    auto oa = [&](int i) { return ob + ((2 * i + kp + rot) & 31) * 4096; };
    u32x4 of[3][4];
    auto fetch1 = [&](int k, int j) AI { ld_async(of[k % 3][j], (j & 1) ? ov1 : ov0, oa(2 * k + (j >> 1))); };
    auto fix = [&](int k) AI { again(of[k % 3], [&](int j) { return (j & 1) ? ov1 : ov0; }, [&](int j) { return oa(2 * k + (j >> 1)); }); };
    const bool own = (__builtin_amdgcn_readfirstlane(selv[0]) >> 4) == (unsigned)(rt * 8 + rg * 2 + kp);
    if (!kp) {
        x_init(true);
        __builtin_amdgcn_s_sleep(PDL);
        waitv<2>();
    } else {
        __builtin_amdgcn_s_sleep(PD1);
        if (w & 1) waitv<4>();
        else waitv<3 + 4 * (16 - WON)>();
    }
    f32x4 acc[2][4];
#pragma unroll
    for (int nb = 0; nb < 4; ++nb) acc[0][nb] = a0_[nb];
    const float ys = __uint_as_float(__builtin_amdgcn_readlane(__float_as_uint(og), __builtin_amdgcn_readfirstlane(selv[0]) & 15));
    asm volatile("" : "+v"(cl));
#pragma unroll 1
    for (unsigned s = 0; s < SPIN_CAP && __ballot(cl != T); ++s)
        asm volatile("global_load_dwordx2 %0, %1, off sc1\n\ts_waitcnt vmcnt(0)" : "=&v"(cl) : "v"(cla) : "memory");
    SB0();
#pragma unroll
    for (int j = 0; j < 4; ++j) fetch1(0, j);
    SB0();
    unsigned wa[2][4];
#pragma unroll
    for (int i = 0; i < 8; ++i) {
        wa[i >> 2][i & 3] = ((lds0 + 4096 * kp + (4 * g + q) * 128 + ((((g & 1) << 1) | (q >> 1)) * 32) + 8 * p) ^ ((i & 3) << 5)) + (i >> 2) * 65536;
        asm volatile("" : "+v"(wa[i >> 2][i & 3]));
    }
    s16x4 wf[2][8][2];
    auto wrd = [&](int k, int i) AI {
        const int u = W_U[2 * k + (i >> 3)];
        wf[k & 1][i >> 1][i & 1] = tr_read(wa[u >> 3][(i >> 1) & 3] + (u & 7) * 8192 + (i & 1) * 2048);
    };
    u32x4 yp[14];
    const unsigned yo = srow * 4096 + cb * 256 + sch * 32;
    auto ya = [&](int i) { return ws + WS_Y + (i >> 1) * 32768 + (i & 1) * 16; };
    lds_wait(lds0 + L_FWD + (l & 1) * 4, 1);
    SB0();
#pragma unroll
    for (int i = 0; i < 16; ++i) wrd(0, i);
#pragma unroll
    for (int j = 0; j < 4; ++j) fetch1(1, j);
    const unsigned ea = lds0 + L_A + l16 + 4096 * w, eb = lds0 + L_A + 4096 * (w ^ 4);
    unsigned er = eb + l16, et = eb + srow * 128 + ((sch ^ srow) * 16), ew[4];
#pragma unroll
    for (int nb = 0; nb < 4; ++nb) ew[nb] = eb + l15 * 128 + (((2 * nb + (g >> 1)) ^ (l & 7)) * 16) + (g & 1) * 8;
    u16 *o0 = out + (size_t)row0 * DM + cb * 64, *o1 = o0 + 8 * DM;
    u64 km[2] = {__ballot(row0 + srow != (int)selv[0]), __ballot(row0 + 8 + srow != (int)selv[0])};
    asm volatile("" : "+s"(km[0]), "+s"(km[1]));
    asm volatile("" : "+v"(er), "+v"(et), "+v"(ew[0]), "+v"(ew[1]), "+v"(ew[2]), "+v"(ew[3]), "+s"(o0), "+s"(o1));
    __builtin_amdgcn_s_setprio(3);
    waitv<4>();
#pragma unroll
    for (int j = 0; j < 4; ++j) pin(of[0][j]);
    if (__builtin_expect(__ballot(least(of[0]) == 0u) != 0ull, 0)) fix(0);
    sfor<0, 8>([&](auto K) {
        constexpr int k = K;
        unsigned m;
        if constexpr (k > 4) __builtin_amdgcn_s_setprio(7 - k);
        if constexpr (2 * (k + 1) == WON) lds_wait(lds0 + ((l & 8) ? L_FWO + sch * 4 : L_FW2 + (l & 1) * 4), 1);
        if constexpr (k == 7) {
            if (__builtin_expect(own, 0)) {
#pragma unroll
                for (int i = 0; i < 14; ++i) ld_async(yp[i], yo, ya(i));
            }
        }
        SB0();
        sfor<0, 16>([&](auto I) {
            constexpr int i = I, j = k == 7 ? (0x2031 >> (i & 12)) & 3 : i >> 2, nb = i & 3;
            const bf16x8 a = cat8(wf[k & 1][(j >> 1) * 4 + nb][0], wf[k & 1][(j >> 1) * 4 + nb][1]);
            if (k == 0 && j == 1) asm volatile("v_mfma_f32_16x16x32_bf16 %0, %1, %2, 0" : "=&v"(acc[j & 1][nb]) : "v"(a), "v"(of[k % 3][j]));
            else asm volatile("v_mfma_f32_16x16x32_bf16 %0, %1, %2, %0" : "+v"(acc[j & 1][nb]) : "v"(a), "v"(of[k % 3][j]));
            SB0();
            if constexpr (k == 7 && i >= 12) {
                if constexpr (i == 12) lds_write<u32x4>(ea, nz4(acc[1][0]));
                else lds_write<f32x4>(ea + (i - 12) * 1024, acc[1][i - 12]);
            }
            if constexpr (k < 7) wrd(k + 1, i);
            if constexpr (k < 6 && i < 8 && !(i & 1)) fetch1(k + 2, i >> 1);
            if constexpr (k < 7 && i == 10) {
                waitv<(k < 6 ? 4 : 0)>();
#pragma unroll
                for (int n = 0; n < 4; ++n) pin(of[(k + 1) % 3][n]);
            }
            if constexpr (k < 7 && i > 10 && i <= 14) {
                const u32x4& v = of[(k + 1) % 3][i - 11];
                if constexpr (i == 11) asm volatile("v_min3_u32 %0, %1, %2, %3\n\tv_min_u32 %0, %0, %4" : "=&v"(m) : "v"(v[0]), "v"(v[1]), "v"(v[2]), "v"(v[3]));
                else asm volatile("v_min3_u32 %0, %0, %1, %2\n\tv_min3_u32 %0, %0, %3, %4" : "+v"(m) : "v"(v[0]), "v"(v[1]), "v"(v[2]), "v"(v[3]));
            }
            SB0();
        });
        if constexpr (k < 7) {
            if (__builtin_expect(__builtin_amdgcn_ballot_w64(m == 0u) != 0ull, 0)) fix(k + 1);
        }
        SB0();
    });
    asm volatile("s_nop 7");
    SB0();
    BAR();
    SB0();
    f32x4 pa[4];
#pragma unroll
    for (int nb = 0; nb < 4; ++nb) pa[nb] = lds_read<f32x4>(er + nb * 1024);
    SB0();
#pragma unroll
    for (int nb = 0; nb < 4; ++nb) acc[0][nb] += pa[nb];
#pragma unroll
    for (int nb = 0; nb < 4; ++nb) asm volatile("" : "+v"(acc[0][nb]));
#pragma unroll
    for (int nb = 0; nb < 4; ++nb) lds_write<u32x2>(ew[nb], (u32x2){pack2(acc[0][nb][0], acc[0][nb][1]), pack2(acc[0][nb][2], acc[0][nb][3])});
    u32x4 av[2];
#pragma unroll
    for (int j = 0; j < 2; ++j) av[j] = lds_read<u32x4>(et + j * 1024);
    SB0();
    asm volatile("s_mov_b64 exec, %5\n\tglobal_store_dwordx4 %0, %1, %3 sc1\n\ts_mov_b64 exec, %6\n\tglobal_store_dwordx4 %0, %2, %4 sc1\n\ts_mov_b64 exec, -1"
                 ::"v"(xo), "v"(av[0]), "v"(av[1]), "s"(o0), "s"(o1), "s"(km[0]), "s"(km[1]) : "memory");
    if (own) {
        const unsigned sr = __builtin_amdgcn_readfirstlane(selv[0]) & 15;
        const unsigned a2 = lds_read<unsigned>(eb + sr * 128 + ((sch ^ (sr & 7)) * 16) + g * 4);
        const float rsel = rbf(1.f / (1.f + __expf(-bf2f((u16)(selv[1] ^ ((selv[1] & 0x8000u) ? 0x8000u : 0xFFFFu))))));
        waitv<2>();
        settle(yp, [&](int) { return yo; }, ya);
        f32x4 yv[2];
#pragma unroll
        for (int h = 0; h < 2; ++h) {
            yv[h] = __builtin_bit_cast(f32x4, yp[h]);
#pragma unroll
            for (int i = 1; i < 7; ++i) yv[h] += __builtin_bit_cast(f32x4, yp[2 * i + h]);
        }
        float y2[2];
#pragma unroll
        for (int i = 0; i < 2; ++i) {
            const float t = dpp_add<0x128>(swap16_add(swap32_add(yv[0][i], yv[1][i]), swap32_add(yv[0][2 + i], yv[1][2 + i])));
            y2[i] = rbf(rbf(t) * rsel) * ys;
        }
        ST_MASK("global_store_dword", 0x00FF00FF00FF00FFull, sr * 2048 + sch * 16 + g * 4, pack2(lo16(a2) + y2[0], hi16(a2) + y2[1]), o0);
    }
    SB0();
}

extern "C" void kernel_launch(void* const* d_in, const int* in_sizes, int n_in, void* d_out, int out_size,
                              void* d_ws, size_t ws_size, hipStream_t stream) {
    fused_mix<<<256, 512, 0, stream>>>((const u16*)d_in[0], (const u16*)d_in[1], (const u16*)d_in[2], (const u16*)d_in[3],
                                       (const u16*)d_in[5], (const u16*)d_in[6], (const u16*)d_in[7], (const u16*)d_in[8],
                                       (const u16*)d_in[9], (char*)d_ws, (u16*)d_out);
}
```

```cpp
#include <hip/hip_runtime.h>
#include <hip/hip_bf16.h>
#include <stdint.h>
#include <type_traits>

typedef __attribute__((ext_vector_type(8))) short bf16x8;
typedef __attribute__((ext_vector_type(4))) short s16x4;
typedef __attribute__((ext_vector_type(4))) float f32x4;
typedef __attribute__((ext_vector_type(2))) float f32x2;
typedef __attribute__((ext_vector_type(2))) unsigned u32x2;
typedef __attribute__((ext_vector_type(4))) unsigned u32x4;
typedef __attribute__((ext_vector_type(2))) __bf16 bf16x2_t;
typedef unsigned short u16;
typedef unsigned long long u64;

#define DI __device__ __forceinline__
#define AS3 __attribute__((address_space(3)))

constexpr int DM = 1024;

DI float bf2f(u16 v) { return __uint_as_float(((unsigned)v) << 16); }
DI u16 f2bf(float f) { __bf16 b = (__bf16)f; return __builtin_bit_cast(u16, b); }
DI float rbf(float f) { return bf2f(f2bf(f)); }
DI unsigned pack2(float lo, float hi) {
    const f32x2 v = {lo, hi};
    return __builtin_bit_cast(unsigned, __builtin_convertvector(v, bf16x2_t));
}
DI float lo16(unsigned u) { return __uint_as_float(u << 16); }
DI float hi16(unsigned u) { return __uint_as_float(u & 0xffff0000u); }

DI s16x4 tr_read(unsigned a) { return __builtin_amdgcn_ds_read_tr16_b64_v4i16((AS3 s16x4*)(size_t)a); }
DI bf16x8 lds_read128(unsigned a) { return *(AS3 const bf16x8*)(size_t)a; }
template <class T> DI void lds_write(unsigned a, T v) { *(AS3 T*)(size_t)a = v; }
template <class T> DI T lds_read(unsigned a) { return *(AS3 const T*)(size_t)a; }
DI bf16x8 cat8(s16x4 a, s16x4 b) { return __builtin_shufflevector(a, b, 0, 1, 2, 3, 4, 5, 6, 7); }
DI f32x4 mfma16(bf16x8 a, bf16x8 b, f32x4 c) { return __builtin_amdgcn_mfma_f32_16x16x32_bf16(a, b, c, 0, 0, 0); }
DI bf16x8 pack44(const f32x4& a, const f32x4& b) {
    u32x4 r;
    r[0] = pack2(a[0], a[1]);
    r[1] = pack2(a[2], a[3]);
    r[2] = pack2(b[0], b[1]);
    r[3] = pack2(b[2], b[3]);
    return __builtin_bit_cast(bf16x8, r);
}
DI unsigned lds_of(const void* p) { return (unsigned)(size_t)(AS3 const char*)p; }

template <int CTRL>
DI unsigned dpp(unsigned x) { return __builtin_amdgcn_update_dpp(0u, x, CTRL, 0xf, 0xf, true); }
template <int CTRL>
DI float dpp_add(float x) { return x + __uint_as_float(dpp<CTRL>(__float_as_uint(x))); }
DI float swap32_add(float a, float b) {
    auto r = __builtin_amdgcn_permlane32_swap(__float_as_uint(a), __float_as_uint(b), false, false);
    return __uint_as_float(r[0]) + __uint_as_float(r[1]);
}
DI float swap16_add(float a, float b) {
    auto r = __builtin_amdgcn_permlane16_swap(__float_as_uint(a), __float_as_uint(b), false, false);
    return __uint_as_float(r[0]) + __uint_as_float(r[1]);
}
DI unsigned wave_umax(unsigned x) {
    x = max(x, dpp<0xB1>(x));
    x = max(x, dpp<0x4E>(x));
    x = max(x, dpp<0x141>(x));
    x = max(x, dpp<0x140>(x));
    auto r = __builtin_amdgcn_permlane16_swap(x, x, false, false);
    x = max(r[0], r[1]);
    auto s = __builtin_amdgcn_permlane32_swap(x, x, false, false);
    return max(s[0], s[1]);
}
constexpr unsigned WS_E = 0;
constexpr unsigned WS_RG = 256;
constexpr unsigned WS_GG = WS_RG + 2048;
constexpr unsigned WS_Z = 65536;
constexpr unsigned WS_KF = WS_Z + 131072;
constexpr unsigned WS_GU = WS_KF + 262144;
constexpr unsigned WS_Y = WS_GU + 262144;
constexpr unsigned WS_KV = WS_Y + 262144;
constexpr unsigned WS_O = WS_KV + (2u << 20);
constexpr unsigned WS_CL = WS_O + (4u << 20);
constexpr unsigned WS_END = WS_CL + 65536;

constexpr int NQ = 9, ND = 7, NX = 16, KD = 76, VD = 82, NF = 64, NF1 = 58, NF2 = 58, NP1 = 15, NP = 15, NM1 = 25, NM2 = 25, NM = 26, KFN = 100;
constexpr int J_S[48] = {0, 0, 0, 1, 1, 1, 2, 2, 2, 3, 3, 3, 4, 4, 4, 5, 5, 5, 6, 6, 6, 7, 7, 8, 8, 9, 9, 9, 10, 10, 11, 11, 12, 12, 12, 13, 14, 15, 13, 14, 15, 7, 8, 10, 11, 13, 14, 15};
constexpr int J_M[48] = {0, 1, 2, 0, 1, 2, 0, 1, 2, 0, 1, 2, 0, 1, 2, 0, 1, 2, 0, 1, 2, 0, 1, 0, 1, 0, 1, 2, 0, 1, 0, 1, 0, 1, 2, 0, 0, 0, 1, 1, 1, 2, 2, 2, 2, 2, 2, 2};
constexpr int J_X[48] = {0, 0, 0, 1, 1, 1, 2, 2, 2, 3, 3, 3, 4, 4, 4, 5, 5, 5, 6, 6, 6, 7, 7, 8, 8, 9, 9, 9, 10, 10, 11, 11, 12, 12, 12, 13, 14, 15, 13, 14, 15, 7, 8, 10, 11, 13, 14, 15};
constexpr int J_U[48] = {5, 3, 8, 10, 11, 15, 17, 12, 14, 7, 4, 2, 3, 8, 9, 18, 15, 16, 13, 14, 1, 6, 2, 3, 8, 10, 11, 15, 12, 1, 6, 2, 0, 5, 3, 18, 15, 12, 13, 14, 1, 7, 4, 6, 2, 0, 5, 3};
constexpr int J_B[48] = {3, 3, 3, 3, 11, 11, 11, 11, 11, 2, 2, 2, 2, 2, 2, 13, 13, 13, 13, 13, 1, 1, 1, 1, 1, 10, 10, 10, 10, 0, 0, 0, 0, 0, 0, 12, 12, 12, 12, 12, 0, 0, 0, 0, 0, 0, 0, 0};
constexpr int Q_C[96] = {1, 0, 2, 0, 3, 0, 4, 0, 5, 0, 6, 0, 7, 0, 0, 0, 0, 0, 8, 0, 0, 0, 9, 0, 0, 0, 0, 0, 10, 0, 0, 0, 0, 0, 0, 0, 11, 0, 0, 0, 0, 0, 12, 0, 0, 0, 13, 0, 0, 0, 14, 0, 0, 0, 0, 0, 15, 0, 0, 16, 0, 0, 17, 0, 0, 0, 18, 0, 0, 0, 19, 0, 20, 0, 21, 0, 22, 0, 23, 0, 24, 0, 25, 0, 26, 0, 0, 0, 0, 0, 0, 0, 0, 0, 0, 0};
constexpr int X_U0[16] = {2, 9, 16, 1, 0, 10, 17, 7, 0, 9, 16, 13, 7, 8, 10, 16};
constexpr int X_U1[16] = {0, 18, 13, 6, 5, 11, 12, 4, 5, 18, 17, 14, 4, 9, 11, 17};
constexpr int X_R[16] = {0, 1, 2, 3, 4, 5, 6, 0, 1, 2, 3, 4, 5, 6, 2, 5};
constexpr int P_U[4] = {10, 11, 8, 9};
constexpr int W_U[16] = {15, 13, 12, 14, 1, 8, 9, 10, 11, 2, 0, 5, 3, 6, 7, 4};
constexpr int W_N[16] = {100, 100, 100, 100, 100, 101, 101, 101, 101, 99, 99, 99, 99, 99, 99, 99};
constexpr int F_J[64] = {-1, 0, 1, 2, -2, 3, 4, 5, -3, 6, 7, 8, -4, 9, 10, 11, -5, 12, 13, 14, 15, -6, 16, 17, 18, -7, 19, 20, 21, -8, 22, -9, 23, 24, -10, 25, 26, 27, -11, 28, -12, 29, -13, 30, 31, 32, 33, 34, -14, 35, -15, 36, -16, 37, 38, 39, 40, 41, 42, 43, 44, 45, 46, 47};
constexpr int F_N[64] = {0, 0, 0, 0, 0, 0, 0, 0, 0, 0, 0, 0, 0, 0, 0, 1, 1, 1, 1, 1, 1, 2, 2, 2, 2, 3, 3, 3, 3, 4, 4, 4, 5, 5, 5, 5, 5, 6, 6, 6, 7, 8, 8, 9, 9, 9, 9, 10, 11, 11, 12, 13, 13, 14, 14, 14, 15, 15, 15, 102, 102, 102, 102, 102};
constexpr int F_K[64] = {0, 1, 2, 3, 0, 4, 5, 6, 0, 0, 0, 7, 0, 0, 8, 0, 0, 0, 9, 0, 0, 0, 0, 10, 0, 0, 0, 11, 0, 0, 12, 0, 0, 13, 0, 0, 0, 14, 0, 0, 0, 15, 0, 16, 0, 17, 0, 18, 0, 19, 0, 20, 0, 21, 22, 23, 24, 25, 0, 0, 0, 0, 0, 26};
constexpr int E_A[384] = {300, 325, 350, 375, 0, 0, 0, 502, 0, 0, 0, 403, 0, 0, 0, 503, 201, 251, 0, 404, 0, 0, 0, 504, 301, 326, 351, 376, 0, 100, 101, 505, 601, 102, 103, 406, 0, 0, 0, 506, 202, 252, 0, 407, 0, 0, 0, 507, 302, 327, 352, 377, 0, 0, 0, 602, 0, 0, 0, 0, 0, 0, 0, 203, 253, 0, 0, 408, 0, 100, 101, 508, 303, 328, 353, 378, 0, 0, 0, 603, 0, 0, 0, 409, 0, 0, 0, 509, 204, 254, 0, 0, 0, 0, 0, 0, 304, 329, 354, 379, 0, 0, 0, 604, 0, 0, 0, 410, 0, 0, 0, 510, 205, 255, 0, 0, 0, 100, 101, 102, 305, 330, 355, 380, 0, 0, 0, 0, 0, 0, 0, 605, 0, 0, 0, 206, 256, 0, 0, 411, 0, 0, 0, 511, 306, 331, 356, 381, 0, 0, 0, 606, 0, 0, 0, 0, 0, 100, 101, 102, 103, 207, 257, 412, 607, 0, 0, 512, 307, 332, 357, 382, 0, 0, 0, 608, 208, 258, 0, 413, 0, 0, 0, 513, 308, 333, 358, 383, 0, 0, 0, 609, 209, 259, 0, 414, 0, 100, 101, 514, 309, 334, 359, 384, 610, 0, 0, 0, 0, 0, 0, 611, 0, 0, 0, 210, 260, 0, 0, 415, 612, 0, 0, 515, 310, 335, 360, 385, 311, 336, 361, 386, 613, 102, 103, 516, 312, 337, 362, 387, 0, 0, 0, 417, 615, 0, 0, 517, 0, 0, 0, 0, 0, 0, 0, 0, 0, 0, 0, 418, 0, 0, 0, 518, 0, 0, 0, 0, 0, 0, 0, 213, 263, 0, 0, 419, 0, 100, 101, 519, 313, 338, 363, 388, 214, 264, 0, 520, 314, 339, 364, 389, 215, 265, 0, 521, 315, 340, 365, 390, 0, 0, 0, 522, 0, 0, 0, 423, 0, 0, 0, 523, 0, 0, 0, 424, 0, 100, 101, 524, 102, 103, 0, 0, 0, 0, 0, 0, 0, 0, 0, 426, 0, 0, 0, 526, 0, 0, 0, 0, 0, 0, 0, 0, 0, 0, 0, 0, 0, 0, 0, 0, 0, 0, 0, 0, 0, 0, 0, 0, 0, 0, 0, 0, 0, 0, 0, 0, 0, 0, 0, 0, 0, 0, 0, 0, 0, 0, 0, 0, 0, 0, 0, 0};
constexpr int E_B[384] = {0, 0, 402, 0, 0, 0, 0, 0, 0, 0, 0, 0, 0, 0, 0, 0, 0, 0, 0, 0, 0, 0, 0, 0, 0, 0, 405, 0, 0, 0, 0, 0, 0, 0, 0, 0, 0, 0, 0, 0, 0, 0, 0, 0, 0, 0, 0, 0, 0, 0, 0, 0, 0, 0, 0, 0, 0, 0, 0, 0, 0, 0, 0, 0, 0, 0, 0, 0, 0, 0, 0, 102, 103, 0, 0, 0, 0, 0, 0, 0, 0, 0, 0, 0, 0, 0, 0, 0, 0, 0, 0, 0, 0, 0, 0, 0, 0, 0, 0, 0, 0, 0, 0, 0, 0, 0, 0, 0, 0, 0, 0, 0, 0, 0, 0, 0, 0, 0, 0, 0, 103, 0, 0, 0, 0, 0, 0, 0, 0, 0, 0, 0, 0, 0, 0, 0, 0, 0, 0, 0, 0, 0, 0, 0, 0, 0, 0, 0, 0, 0, 0, 0, 0, 0, 0, 0, 0, 0, 0, 0, 0, 0, 0, 0, 0, 0, 0, 0, 0, 0, 0, 0, 0, 0, 0, 0, 0, 0, 0, 0, 0, 0, 0, 0, 0, 0, 0, 0, 0, 0, 0, 0, 0, 0, 0, 0, 0, 0, 0, 102, 103, 0, 0, 0, 0, 0, 0, 0, 0, 0, 0, 0, 0, 0, 0, 0, 0, 0, 0, 0, 0, 0, 0, 0, 0, 0, 211, 0, 261, 100, 416, 101, 212, 262, 0, 614, 0, 0, 0, 0, 0, 0, 0, 0, 0, 0, 0, 0, 0, 0, 0, 0, 0, 0, 0, 0, 0, 0, 0, 0, 0, 0, 0, 0, 0, 0, 0, 0, 0, 0, 0, 0, 0, 0, 0, 0, 0, 0, 0, 102, 103, 0, 420, 0, 0, 0, 0, 0, 0, 0, 421, 0, 0, 0, 0, 0, 0, 0, 422, 0, 0, 0, 0, 0, 0, 0, 0, 0, 0, 0, 0, 0, 0, 0, 0, 0, 0, 0, 0, 0, 0, 0, 0, 0, 0, 0, 0, 0, 0, 0, 0, 0, 0, 0, 0, 0, 0, 0, 0, 0, 0, 0, 0, 0, 0, 0, 0, 0, 0, 0, 0, 0, 0, 0, 0, 0, 0, 0, 0, 0, 0, 0, 0, 0, 0, 0, 0, 0, 0, 0, 0, 0, 0, 0, 0, 0, 0, 0, 0, 0, 0, 0, 0, 0};
constexpr unsigned L_A = 16 * 8192;
constexpr unsigned L_KF = 18 * 8192;
constexpr unsigned L_SM = 19 * 8192;
constexpr unsigned L_FULL = L_SM + 256;
constexpr unsigned L_PROG = L_SM + 896;
constexpr unsigned L_FW2 = L_SM + 512, L_FOS = L_SM + 480, L_FK0 = L_SM + 752, L_FV = L_SM + 560, L_FP = L_SM + 576, L_FKS = L_SM + 592;
constexpr unsigned L_FQD = L_SM + 608, L_FG = L_SM + 624, L_FKF = L_SM + 736, L_FQ = L_SM + 720, L_FWD = L_SM + 640, L_FWO = L_SM + 656;
constexpr unsigned L_BEST = L_SM + 696;
constexpr unsigned L_SEL = L_SM + 704;
constexpr unsigned L_XS = L_SM + 2048, L_KZ = L_SM + 3072, L_WPHI = L_SM + 4096;
constexpr unsigned L_G = L_SM + 3328;
static_assert(NP <= 31 && NM <= 63 && NF1 == NF2 && NX == 16, "");
static_assert(P_U[1] == P_U[0] + 1 && P_U[3] == P_U[2] + 1, "feature maps of a head lie side by side");
constexpr bool j_first(int j) {
    for (int i = 0; i < j; ++i)
        if (J_M[i] == J_M[j]) return false;
    return true;
}
constexpr unsigned SPIN_CAP = 1u << 16;
#define N8(a) a, a + 1, a + 2, a + 3, a + 4, a + 5, a + 6, a + 7
__device__ const unsigned k_num[64] = {N8(0), N8(8), N8(16), N8(24), N8(32), N8(40), N8(48), N8(56)};

constexpr int PD = 4;
constexpr int PACE = 16;
constexpr int RLD = F_N[NF1 - 1];
static_assert(RLD == NP, "");
constexpr int CLR0 = 2;
constexpr int GQ1 = 6;
constexpr int WOE = 2, WOM = 7, WON = 10;
constexpr int GDL = 17, KFD = 6, PDL = 11, PD1 = 4;
constexpr unsigned CPF = 57344;
constexpr u64 KXB = 0xFEDBA9C768534210ull;
#define BAR() asm volatile("s_waitcnt lgkmcnt(0)\n\ts_barrier" ::: "memory")
template <int N>
DI void waitv() { asm volatile("s_waitcnt vmcnt(%0)" ::"n"(N) : "memory"); }

DI unsigned nz1(unsigned v) { return max(v, 1u); }
DI u32x4 nz4(u32x4 v) { return (u32x4){max(v[0], 1u), max(v[1], 1u), max(v[2], 1u), max(v[3], 1u)}; }
DI u32x4 nz4(f32x4 v) { return nz4(__builtin_bit_cast(u32x4, v)); }
DI void st_wt(__amdgpu_buffer_rsrc_t r, unsigned off, u32x4 v) { __builtin_amdgcn_raw_buffer_store_b128(v, r, off, 0, 16); }
DI void ld_async(u32x4& v, unsigned off, const char* base) {
    asm volatile("global_load_dwordx4 %0, %1, %2 sc1" : "=&v"(v) : "v"(off), "s"(base) : "memory");
}
#define ST_MASK(op, m, off, v, base) asm volatile("s_mov_b64 exec, %3\n\t" op " %0, %1, %2 sc1\n\ts_mov_b64 exec, -1\n\ts_nop 0" ::"v"(off), "v"(v), "s"(base), "s"((u64)(m)) : "memory")
DI void pin(u32x4& v) { asm volatile("" : "+v"(v)); }
template <int N>
DI unsigned least(const u32x4 (&v)[N]) {
    unsigned a = min(v[0][0], v[0][1]), b = min(v[0][2], v[0][3]);
#pragma unroll
    for (int i = 1; i < N; ++i) a = min(min(a, v[i][0]), v[i][1]), b = min(min(b, v[i][2]), v[i][3]);
    return min(a, b);
}
template <int N, class OFF, class BASE>
DI void again(u32x4 (&v)[N], OFF off, BASE base) {
    unsigned s = SPIN_CAP;
    do {
#pragma unroll
        for (int i = 0; i < N; ++i) {
            unsigned t;
            asm volatile("v_min3_u32 %1, %4, %5, %6\n\tv_min_u32 %1, %1, %7\n\tv_cmp_eq_u32 vcc, 0, %1\n\ts_mov_b64 exec, vcc\n\t"
                         "global_load_dwordx4 %0, %2, %3 sc1\n\ts_mov_b64 exec, -1"
                         : "+v"(v[i]), "=&v"(t) : "v"(off(i)), "s"(base(i)), "v"(v[i][0]), "v"(v[i][1]), "v"(v[i][2]), "v"(v[i][3]) : "memory", "vcc");
        }
        waitv<0>();
#pragma unroll
        for (int i = 0; i < N; ++i) pin(v[i]);
    } while (__ballot(least(v) == 0u) != 0ull && --s);
}
template <int N, class OFF, class BASE>
DI void settle(u32x4 (&v)[N], OFF off, BASE base) {
#pragma unroll
    for (int i = 0; i < N; ++i) pin(v[i]);
    if (__builtin_expect(__ballot(least(v) == 0u) != 0ull, 0)) again(v, off, base);
}
DI void lds_wait(unsigned a, unsigned n) {
    unsigned v, c;
    asm volatile("s_mov_b32 %1, 0x400000\n"
                 ".Lp%=:\n\tds_read_b32 %0, %2\n\ts_waitcnt lgkmcnt(0)\n\tv_cmp_gt_u32 vcc, %3, %0\n\ts_cbranch_vccz .Lf%=\n\t"
                 "s_sleep 1\n\ts_sub_u32 %1, %1, 1\n\ts_cbranch_scc0 .Lp%=\n.Lf%=:"
                 : "=&v"(v), "=&s"(c) : "v"(a), "s"(n) : "memory", "vcc", "scc");
}
DI void lds_wait0(unsigned a) {
    unsigned v, c;
    asm volatile("s_mov_b32 %1, 0x1000000\n"
                 ".Lp%=:\n\tds_read_b32 %0, %2\n\ts_sub_u32 %1, %1, 1\n\ts_cbranch_scc1 .Lf%=\n\ts_waitcnt lgkmcnt(0)\n\tv_cmp_eq_u32 vcc, 0, %0\n\ts_cbranch_vccnz .Lp%=\n"
                 ".Lf%=:\n\ts_waitcnt lgkmcnt(0)"
                 : "=&v"(v), "=&s"(c) : "v"(a) : "memory", "vcc", "scc");
}
DI void lds_wait8(unsigned a, unsigned n) {
    unsigned v, c;
    asm volatile("s_mov_b32 %1, 0x100000\n"
                 ".Lp%=:\n\tds_read_b32 %0, %2\n\ts_waitcnt lgkmcnt(0)\n\tv_cmp_gt_u32 vcc, %3, %0\n\ts_cbranch_vccz .Lf%=\n\t"
                 "s_sleep 4\n\ts_sub_u32 %1, %1, 1\n\ts_cbranch_scc0 .Lp%=\n.Lf%=:"
                 : "=&v"(v), "=&s"(c) : "v"(a), "s"(n) : "memory", "vcc", "scc");
}
DI void lds_peek(unsigned& v, unsigned a) { asm volatile("ds_read_b32 %0, %1" : "=v"(v) : "v"(a) : "memory"); }
DI void lds_wait(unsigned& v, unsigned a, unsigned n) {
    unsigned c;
    asm volatile("s_mov_b32 %1, 0x400000\n"
                 ".Lp%=:\n\ts_waitcnt lgkmcnt(0)\n\tv_cmp_gt_u32 vcc, %3, %0\n\ts_cbranch_vccz .Lf%=\n\t"
                 "ds_read_b32 %0, %2\n\ts_sub_u32 %1, %1, 1\n\ts_cbranch_scc0 .Lp%=\n.Lf%=:"
                 : "+v"(v), "=&s"(c) : "v"(a), "s"(n) : "memory", "vcc", "scc");
}
DI float dot2(unsigned a, unsigned b, float c) {
    return __builtin_amdgcn_fdot2_f32_bf16(__builtin_bit_cast(bf16x2_t, a), __builtin_bit_cast(bf16x2_t, b), c, false);
}
DI float sum4(const float (&a)[4]) {
    float c = swap16_add(swap32_add(a[0], a[1]), swap32_add(a[2], a[3]));
    c = dpp_add<0xB1>(c);
    c = dpp_add<0x4E>(c);
    c = dpp_add<0x141>(c);
    return dpp_add<0x140>(c);
}

template <class OP, class POST>
DI void red4(float& a, float& b, float& c, float& d, OP op, POST post) {
    auto x = __builtin_amdgcn_permlane32_swap(__float_as_uint(a), __float_as_uint(b), false, false);
    auto y = __builtin_amdgcn_permlane32_swap(__float_as_uint(c), __float_as_uint(d), false, false);
    const float m = op(__uint_as_float(x[0]), __uint_as_float(x[1])), n = op(__uint_as_float(y[0]), __uint_as_float(y[1]));
    auto z = __builtin_amdgcn_permlane16_swap(__float_as_uint(m), __float_as_uint(n), false, false);
    const unsigned f = __float_as_uint(post(op(__uint_as_float(z[0]), __uint_as_float(z[1]))));
    auto u = __builtin_amdgcn_permlane16_swap(f, f, false, false);
    auto v = __builtin_amdgcn_permlane32_swap(u[0], u[0], false, false);
    auto t = __builtin_amdgcn_permlane32_swap(u[1], u[1], false, false);
    a = __uint_as_float(v[0]);
    b = __uint_as_float(v[1]);
    c = __uint_as_float(t[0]);
    d = __uint_as_float(t[1]);
}
DI float mx2(float a, float b) { return __builtin_elementwise_maximum(a, b); }
template <class HOOK>
DI void softmax2(const f32x4 (&v)[2][2], u32x4 (&ph)[2][2], HOOK hook) {
    constexpr float L2E = 1.4426950408889634f;
    float mx[2], mn[2], sp_[2], sn_[2];
#pragma unroll
    for (int b = 0; b < 2; ++b) {
        const f32x4 s = v[b][0], t = v[b][1];
        mx[b] = mx2(mx2(mx2(mx2(s[0], s[1]), s[2]), mx2(mx2(s[3], t[0]), t[1])), mx2(t[2], t[3]));
        mn[b] = mx2(mx2(mx2(mx2(-s[0], -s[1]), -s[2]), mx2(mx2(-s[3], -t[0]), -t[1])), mx2(-t[2], -t[3]));
    }
    red4(mx[0], mx[1], mn[0], mn[1], [](float a, float b) { return mx2(a, b); }, [](float a) { return a * -L2E; });
    f32x2 ep[2][4], en[2][4];
#pragma unroll
    for (int b = 0; b < 2; ++b) {
#pragma unroll
        for (int e = 0; e < 4; ++e) {
            const f32x2 u = {v[b][e >> 1][2 * (e & 1)], v[b][e >> 1][2 * (e & 1) + 1]};
            const f32x2 a = __builtin_elementwise_fma(u, (f32x2){L2E, L2E}, (f32x2){mx[b], mx[b]});
            const f32x2 c = __builtin_elementwise_fma(u, (f32x2){-L2E, -L2E}, (f32x2){mn[b], mn[b]});
            ep[b][e] = (f32x2){__builtin_amdgcn_exp2f(a[0]), __builtin_amdgcn_exp2f(a[1])};
            en[b][e] = (f32x2){__builtin_amdgcn_exp2f(c[0]), __builtin_amdgcn_exp2f(c[1])};
        }
        const f32x2 s = (ep[b][0] + ep[b][1]) + (ep[b][2] + ep[b][3]), t = (en[b][0] + en[b][1]) + (en[b][2] + en[b][3]);
        sp_[b] = s[0] + s[1];
        sn_[b] = t[0] + t[1];
    }
    asm volatile("" : "+v"(sp_[0]), "+v"(sn_[0]), "+v"(sp_[1]), "+v"(sn_[1]));
    hook();
    red4(sp_[0], sp_[1], sn_[0], sn_[1], [](float a, float b) { return a + b; }, [](float a) { return __builtin_amdgcn_rcpf(a); });
#pragma unroll
    for (int b = 0; b < 2; ++b)
#pragma unroll
        for (int e = 0; e < 4; ++e) {
            const f32x2 a = ep[b][e] * (f32x2){sp_[b], sp_[b]}, c = en[b][e] * (f32x2){sn_[b], sn_[b]};
            ph[b][0][e] = pack2(a[0], a[1]);
            ph[b][1][e] = pack2(c[0], c[1]);
        }
}
DI f32x4 rbf4(const f32x4& a) {
    const unsigned u = pack2(a[0], a[1]), v = pack2(a[2], a[3]);
    return (f32x4){lo16(u), hi16(u), lo16(v), hi16(v)};
}
DI int fsw(int row) { return (((row >> 1) & 3) << 2) | (((row >> 3) & 1) << 1) | (row & 1); }
template <class HOOK>
DI void phi_k(const f32x4 (&pp)[2][2], const unsigned (&pw)[4], unsigned off, HOOK hook) {
    u32x4 ph[2][2];
    softmax2(pp, ph, hook);
#pragma unroll
    for (int b = 0; b < 2; ++b)
#pragma unroll
        for (int kf = 0; kf < 2; ++kf)
#pragma unroll
            for (int fb = 0; fb < 2; ++fb) {
                const u32x2 a = {ph[b][kf][2 * fb], ph[b][kf][2 * fb + 1]};
                lds_write<u32x2>(pw[2 * kf + fb] + off + b * 2048, a);
            }
}

#define SB0() __builtin_amdgcn_sched_barrier(0)
template <int A, int B, class F>
DI void sfor(F f) {
    if constexpr (A < B) {
        f(std::integral_constant<int, A>{});
        sfor<A + 1, B>(f);
    }
}
template <int N> using IC = std::integral_constant<int, N>;
#define AI __attribute__((always_inline))

DI unsigned min4(u32x4 v) { return min(min(v[0], v[1]), min(v[2], v[3])); }
DI unsigned o_sums(const u32x4 (&kf)[6], const u32x4 (&ph)[2][2], const float (&gt)[2], float (&rd_)[2], f32x4 (&nm)[2][2]) {
    const f32x4 zero = {0.f, 0.f, 0.f, 0.f};
    auto b8 = [](const u32x4& a) { return __builtin_bit_cast(bf16x8, a); };
    f32x4 dn[2];
#pragma unroll
    for (int i = 0; i < 2; ++i) dn[i] = mfma16(b8(kf[4]), b8(ph[i][0]), zero);
#pragma unroll
    for (int i = 0; i < 2; ++i) dn[i] = mfma16(b8(kf[5]), b8(ph[i][1]), dn[i]);
    SB0();
    unsigned m = min(min4(kf[4]), min4(kf[5]));
    asm volatile("" : "+v"(m));
#pragma unroll
    for (int i = 0; i < 4; ++i) nm[i & 1][i >> 1] = mfma16(b8(kf[i >> 1]), b8(ph[i & 1][0]), zero);
    m = min(m, min(min4(kf[0]), min4(kf[1])));
    asm volatile("" : "+v"(m));
    SB0();
#pragma unroll
    for (int i = 0; i < 4; ++i) nm[i >> 1][i & 1] = mfma16(b8(kf[2 + (i & 1)]), b8(ph[i >> 1][1]), nm[i >> 1][i & 1]);
    asm volatile("" : "+v"(dn[0]), "+v"(dn[1]));
    m = min(m, min(min4(kf[2]), min4(kf[3])));
#pragma unroll
    for (int i = 0; i < 2; ++i) rd_[i] = __builtin_amdgcn_rcpf(rbf(rbf(dn[i][0]) + 1e-6f)) * gt[i];
    asm volatile("" : "+v"(rd_[0]), "+v"(rd_[1]), "+v"(m));
    return m;
}
DI void o_store(const float (&rd_)[2], const f32x4 (&nm)[2][2], __amdgpu_buffer_rsrc_t rsrc, unsigned o_st) {
#pragma unroll
    for (int i = 0; i < 2; ++i) {
        u32x4 o;
        f32x2 t[4];
#pragma unroll
        for (int j = 0; j < 4; ++j) t[j] = (f32x2){nm[i][j >> 1][2 * (j & 1)], nm[i][j >> 1][2 * (j & 1) + 1]} * (f32x2){rd_[i], rd_[i]};
        SB0();
#pragma unroll
        for (int j = 0; j < 4; ++j) o[j] = pack2(t[j][0], t[j][1]);
        SB0();
        st_wt(rsrc, o_st + i * 1024, nz4(o));
        SB0();
    }
}

typedef __attribute__((ext_vector_type(16))) unsigned u32x16;
__global__ __launch_bounds__(512, 2) void fused_mix(const u16*, const u16*, const u16*, const u16*, const u16*, const u16*, const u16*, const u16*,
                                                    const u16*, char*, u16*) {
    __shared__ __attribute__((aligned(16))) char smem[163840];
    u32x4 ka;
    u32x16 kb_;
    u64 kz;
    asm volatile("s_load_dwordx4 %0, %3, 0x0\n\ts_load_dwordx16 %1, %3, 0x10\n\ts_load_dwordx2 %2, %3, 0x50"
                 : "=&s"(ka), "=&s"(kb_), "=&s"(kz) : "s"(__builtin_amdgcn_kernarg_segment_ptr()));
    SB0();
    const int tid = threadIdx.x;
    int l = tid & 63;
    const int w = __builtin_amdgcn_readfirstlane(tid >> 6) ^ 4;
    int bid = blockIdx.x;
    const unsigned lds0 = lds_of(smem);
    int g = l >> 4, l15 = l & 15, q = l15 >> 2, p = l & 3;
    int srow = l >> 3, sch = l & 7;
    unsigned l16 = l * 16;
    const int rg = w & 3, kp = w >> 2;
    const int hs = rg >> 1;
    SB0();
    {
        u64 pc;
        asm volatile("s_getpc_b64 %0\n\ts_mov_b32 m0, %2\n\ts_mov_b64 exec, 3\n\tglobal_load_lds_dword %1, %0\n\ts_mov_b64 exec, -1"
                     : "=&s"(pc) : "v"(min((unsigned)(((bid & ~7) + w) * 2 + l) * 128u, CPF)), "s"(lds0 + L_SM + 3584) : "memory");
    }
    lds_write<unsigned>(lds0 + L_SM + tid * 4, 0u);
    const int slot = bid >> 3;
    const int mt = (bid & 1) * 8 + (slot & 7);
    const int hp = ((bid >> 1) & 3) * 4 + (slot >> 3);
    unsigned vx[4], vw[4];
    auto vw_init = [&]() AI {
        int t = l;
        asm volatile("" : "+v"(t));
#pragma unroll
        for (int i = 0; i < 4; ++i) vw[i] = (unsigned)((8 * i + (t >> 3)) * 6144 + ((t & 7) ^ (((t >> 4) & 1) << 1) ^ ((i & 1) << 2)) * 16 + 4096 - i * 1024);
    };
    auto vx_init = [&]() AI {
        int t = l;
        asm volatile("" : "+v"(t));
#pragma unroll
        for (int i = 0; i < 4; ++i) vx[i] = (unsigned)((8 * i + (t >> 3)) * 2048 + (((t & 7) ^ ((4 * (i & 1) + (t >> 4)) & 7)) * 16) + 4096 - i * 1024);
    };
    vx_init();
    vw_init();
    asm volatile("" : "+v"(vx[0]), "+v"(vx[1]), "+v"(vx[2]), "+v"(vx[3]), "+v"(vw[0]), "+v"(vw[1]), "+v"(vw[2]), "+v"(vw[3]));
    int ks0 = ((slot + (slot >> 3)) & 3) ^ ((bid & 1) * 8), role = w & 5;
    long long xo0 = (mt * 128 + hs * 64) * 2048 - 4096, wo0 = hp * 128 + hs * (32 * 6144) - 4096;
    long long xo1 = xo0 + ks0 * 128, wo1 = wo0 + ks0 * (64 * 6144) + 2048;
    unsigned d0 = lds0 + (hs ? X_U1[0] : X_U0[0]) * 8192, d1 = lds0 + J_U[0] * 8192 + hs * 4096;
    const unsigned* one = k_num;
    asm volatile("s_waitcnt lgkmcnt(0)" : "+s"(ka), "+s"(kb_), "+s"(kz), "+s"(xo0), "+s"(wo0), "+s"(xo1), "+s"(wo1), "+s"(ks0), "+s"(role),
                 "+s"(d0), "+s"(d1), "+s"(one) :: "memory");
    const u16* x = (const u16*)((u64)ka[1] << 32 | ka[0]);
    const u16* wqkv = (const u16*)((u64)ka[3] << 32 | ka[2]);
    auto arg = [&](int i) { return (char*)((u64)kb_[2 * i + 1] << 32 | kb_[2 * i]); };
    const u16 *wphi = (const u16*)arg(0), *wo = (const u16*)arg(1), *wr = (const u16*)arg(2), *wg = (const u16*)arg(3);
    const u16 *wu = (const u16*)arg(4), *wd = (const u16*)arg(5), *gw = (const u16*)arg(6);
    char* ws = arg(7);
    u16* out = (u16*)kz;
    unsigned v0 = 0;
    asm volatile("" : "+v"(v0));
    auto mark = [&](unsigned dst, int n = 1, int lanes = 1) AI {
        asm volatile("s_mov_b32 m0, %2\n\ts_mov_b64 exec, %4\n\tglobal_load_lds_dword %0, %1 offset:%3\n\ts_mov_b64 exec, -1"
                     ::"v"(v0), "s"(one), "s"(dst - 4 * n), "n"(4 * n), "n"(lanes) : "memory");
    };
    auto four = [&](const unsigned (&v)[4], const char* src, unsigned dst) AI {
        asm volatile("s_mov_b32 m0, %5\n\ts_nop 0\n\t"
                     "global_load_lds_dwordx4 %0, %4\n\tglobal_load_lds_dwordx4 %1, %4 offset:1024\n\t"
                     "global_load_lds_dwordx4 %2, %4 offset:2048\n\tglobal_load_lds_dwordx4 %3, %4 offset:3072"
                     ::"v"(v[0]), "v"(v[1]), "v"(v[2]), "v"(v[3]), "s"(src), "s"(dst) : "memory");
    };
    static_assert(F_J[0] == -1 && F_J[1] == 0 && F_K[1] == 1 && (KXB & 15) == 0, "");
    if (__builtin_expect(role == 4, 1)) {
        four(vx, (const char*)x + xo1, d0);
        four(vx, (const char*)x + xo1 + 32 * 2048, d0 + 4096);
        four(vw, (const char*)wqkv + wo1, d1);
    }
    asm volatile("" : "+s"(xo0), "+s"(wo0));
    const char* xb0 = (const char*)x + xo0;
    const char* wb0 = (const char*)wqkv + wo0;
    auto fill = [&](auto N) AI {
        constexpr int n = N, j = F_J[n] < 0 ? 0 : F_J[n], xi = F_J[n] < 0 ? -1 - F_J[n] : 0, s = F_J[n] < 0 ? xi : J_S[j], m = J_M[j];
        const int ks = ks0 ^ (int)((KXB >> (4 * s)) & 15);
        if constexpr (F_N[n] > 0 && n < NF1) waitv<PACE>();
        if constexpr (F_J[n] >= 0) {
            four(vw, wb0 + ks * (64 * 6144) + (m == 2 ? 0 : 2048 * (m + 1)), lds0 + J_U[j] * 8192 + hs * 4096);
        } else {
            const char* xs_ = xb0 + ks * 128;
            const unsigned d = lds0 + (hs ? X_U1[xi] : X_U0[xi]) * 8192;
            four(vx, xs_, d);
            if constexpr (F_N[n] > 0) waitv<PACE>();
            four(vx, xs_ + 32 * 2048, d + 4096);
        }
        if constexpr (F_K[n] > 1) mark(lds0 + L_FULL + (n < NF1 ? 0 : 32) + hs * 8, F_K[n], 3);
    };
    unsigned sd = 0, st;
    asm volatile("s_cmp_lg_u32 %2, 5\n\ts_cbranch_scc1 .La%=\n\t"
                 "s_min_u32 %1, %3, 23\n\ts_lshl_b32 %1, %1, 18\n\ts_load_dword %0, %5, %1\n\ts_load_dword %0, %6, 0x0\n\t"
                 "s_and_b32 %1, %1, 0x3c0000\n\ts_load_dword %0, %4, %1\n\ts_load_dword %0, %10, %1\n\t"
                 "s_load_dword %0, %7, 0x0\n\ts_load_dword %0, %8, 0x0\n\ts_load_dword %0, %9, 0x0\n.La%=:"
                 : "+s"(sd), "=&s"(st) : "s"(w), "s"(slot), "s"(x), "s"(wqkv), "s"(one), "s"(wr), "s"(gw), "s"(wphi), "s"(out) : "memory", "scc");
    asm volatile("s_cmp_lg_u32 %2, 7\n\ts_cbranch_scc1 .La%=\n\t"
                 "s_min_u32 %1, %3, 27\n\ts_lshl_b32 %1, %1, 18\n\ts_load_dword %0, %4, 0x0\n\ts_load_dword %0, %4, %1\n\t"
                 "s_load_dword %0, %5, %1\n\ts_load_dword %0, %6, %1\n\ts_load_dword %0, %7, %1\n\t"
                 "s_and_b32 %1, %1, 0x1c0000\n\ts_load_dword %0, %8, %1\n.La%=:\n\ts_barrier"
                 : "+s"(sd), "=&s"(st) : "s"(w), "s"(slot), "s"(ws), "s"(wg), "s"(wu), "s"(wd), "s"(wo) : "memory", "scc");
    if (__builtin_expect((w & 5) == 4, 1)) {
        mark(lds0 + L_FULL + hs * 8, 1, 3);
        sfor<2, 4>(fill);
    }
    asm volatile("" : "+s"(bid));
    const int xcd = bid & 7;
    const int sc = (3 - (int)((unsigned)(size_t)wqkv >> 7)) & 7;
    const int rt = (xcd >> 1) * 4 + ((bid >> 3) & 3), cb = (xcd & 1) * 8 + (bid >> 5), rot = 8 * rt;
    const __amdgpu_buffer_rsrc_t rsrc = __builtin_amdgcn_make_buffer_rsrc(ws, 0, WS_END, 0x00020000);
    const u32x4 z4 = {0u, 0u, 0u, 0u};
    const f32x4 zero = {0.f, 0.f, 0.f, 0.f};
    unsigned AX = lds0 + L_A + rg * 4096 + l16;
    unsigned W4 = lds0 + p * 4;
    unsigned PG = lds0 + L_PROG + p * 256;
    const unsigned WR = lds0 + rg * 4;
    auto flag = [&](unsigned a) AI {
        asm volatile("ds_write_b32 %0, %0" ::"v"(a) : "memory");
        SB0();
    };
    auto phi_w = [&](bf16x8 (&a)[2], int h) AI {
        const unsigned ad = lds0 + L_WPHI + h * 2048 + (4 * g + q) * 64 + (((p >> 1) ^ (2 * (g & 1))) * 16) + 8 * (p & 1);
        a[0] = cat8(tr_read(ad), tr_read(ad + 1024));
        a[1] = cat8(tr_read(ad ^ 32), tr_read((ad ^ 32) + 1024));
    };
    u32x4 fw[32], fd[16], gu[2];
    unsigned sel_[4][2];
    u32x4 ph[2][2], sq;
    const int hp2 = ((bid >> 1) & 3) * 4 + (bid >> 6);
    const bool ffn = (hp2 & 7) != sc;
    const int ph4 = ((bid >> 3) + (bid >> 6)) & 3;
    const int kq = mt >> 2;
    const int cs = (ffn ? hp2 - (hp2 > sc) - (hp2 > sc + 8) : 14 + (hp2 >> 3)) * 4 + ((ph4 + 3 - (int)((unsigned)(size_t)wg >> 7)) & 3);
    const int c3 = (3 - (int)((unsigned)(size_t)wg >> 7)) & 7;
    const int cd = cs >= 56 ? (cs < 63 ? 8 * (cs - 56) + c3 : cs) : (cs & 7) == c3 ? 56 + (cs >> 3) : cs;
    auto spread = [&](const u32x4& v, int i) -> bf16x8 {
        const unsigned s = v[i >> 1];
        u32x4 a;
#pragma unroll
        for (int d = 0; d < 4; ++d) a[d] = __builtin_amdgcn_perm(s, s, sel_[d][i & 1]);
        return __builtin_bit_cast(bf16x8, a);
    };
    auto fold_store = [&](const f32x4& a, unsigned off) {
        f32x4 v;
#pragma unroll
        for (int i = 0; i < 4; ++i) {
            const unsigned u = __float_as_uint(a[i]);
            const float r = __uint_as_float(__builtin_amdgcn_update_dpp(u, u, 0x128, 0xc, 0xf, false));
            v[i] = swap32_add(r, r);
        }
        const u32x4 o = nz4(v);
        ST_MASK("global_store_dwordx4", 0x00FF00FFull, off + l15 * 32 + g * 16, o, ws);
    };
    auto sel_init = [&]() AI {
#pragma unroll
        for (int d = 0; d < 4; ++d)
#pragma unroll
            for (int hf = 0; hf < 2; ++hf) {
                const unsigned b = hf ? 0x0302u : 0x0100u;
                sel_[d][hf] = (sch >> 1) != d ? 0x0c0c0c0cu : (l & 1) ? (b << 16) | 0x0c0cu : 0x0c0c0000u | b;
            }
    };
    auto gua = [&](int i) { return ws + WS_GU + cd * 4096 + i * 1024; };
    auto gu_load = [&]() AI {
#pragma unroll
        for (int i = 0; i < 2; ++i) ld_async(gu[i], l16, gua(i));
    };
    auto down = [&]() AI {
#pragma unroll
        for (int i = 0; i < 16; ++i) pin(fd[i]);
        settle(gu, [&](int) { return l16; }, gua);
        const f32x4 v = __builtin_bit_cast(f32x4, gu[0]) + __builtin_bit_cast(f32x4, gu[1]);
        auto r = __builtin_amdgcn_permlane16_swap(__float_as_uint(swap32_add(v[0], v[2])), __float_as_uint(swap32_add(v[1], v[3])), false, false);
        const float gt = rbf(__uint_as_float(r[0])), up = rbf(__uint_as_float(r[1]));
        const unsigned ha = lds0 + L_XS + hs * 512;
        lds_write<u16>(ha + l15 * 8 + g * 2, f2bf(rbf(gt / (1.f + __expf(-gt))) * up));
        const u32x4 hv = lds_read<u32x4>(ha + srow * 16);
        f32x4 y0 = zero, y1 = zero;
#pragma unroll
        for (int i = 0; i < 8; ++i) {
            const bf16x8 a = spread(hv, i);
            y0 = mfma16(a, __builtin_bit_cast(bf16x8, fd[i]), y0);
            y1 = mfma16(a, __builtin_bit_cast(bf16x8, fd[8 + i]), y1);
        }
        fold_store(y0, WS_Y + cd * 4096 + kq * 1024 + hs * 512);
        fold_store(y1, WS_Y + cd * 4096 + kq * 1024 + hs * 512 + 256);
    };
    auto need = [&](int c) AI {
        if (c == 102) lds_wait(W4 + L_FK0, 1);
        else if (c == 101) lds_wait(W4 + L_FKS, 1);
        else if (c == 100) lds_wait(W4 + L_FV, 1);
        else if (c == 99) lds_wait(W4 + L_FQD, 1);
        else if (c > 0) lds_wait(PG, c);
    };
    auto wo_load = [&](int n0, int n1, bool mk) AI {
        unsigned vo[4];
#pragma unroll
        for (int i = 0; i < 4; ++i) vo[i] = (unsigned)((8 * i + srow) * 2048 + ((sch ^ ((g & 3) << 1)) * 16) + 4096 - i * 1024);
        const char* wob = (const char*)wo + cb * 128 - 4096;
#pragma unroll
        for (int n = n0; n < n1; ++n) {
            if (n < WON && (n == 0 || W_N[n] != W_N[n - 1])) need(W_N[n]);
            four(vo, wob + ((2 * n + hs + rot) & 31) * 65536, lds0 + W_U[n] * 8192 + hs * 4096);
        }
        if (mk) mark(lds0 + (n1 > WON ? L_FW2 : L_FWD) + hs * 4);
    };
    auto phi_adr = [&](unsigned (&pw)[4], unsigned base) AI {
#pragma unroll
        for (int i = 0; i < 4; ++i) pw[i] = (base + rg * 4096 + l15 * 128 + ((g ^ fsw(l15)) << 3)) ^ (i << 5);
    };
    const int row0 = rt * 128 + rg * 32 + kp * 16;
    u32x4 xr[2];
    u64 gg;
    unsigned xo = srow * 2048 + sch * 16;
    auto g_load = [&]() AI {
        asm volatile("global_load_dwordx2 %0, %1, %2 sc1" : "=&v"(gg) : "v"((unsigned)l15 * 8), "s"(ws + WS_GG + row0 * 8) : "memory");
    };
    auto xg_load = [&]() AI {
        asm volatile("global_load_dwordx4 %0, %3, %4\n\tglobal_load_dwordx4 %1, %3, %4 offset:64\n\tglobal_load_dwordx2 %2, %5, %6 sc1"
                     : "=&v"(xr[0]), "=&v"(xr[1]), "=&v"(gg)
                     : "v"((unsigned)((l >> 2) * 2048 + p * 16)), "s"(x + (size_t)row0 * DM + cb * 64),
                       "v"((unsigned)l15 * 8), "s"(ws + WS_GG + row0 * 8) : "memory");
    };
    f32x4 a0_[4];
    float og;
    auto x_init = [&](bool staged) AI {
        if (staged) asm volatile("" : "+v"(gg));
        else asm volatile("" : "+v"(xr[0]), "+v"(xr[1]), "+v"(gg));
#pragma unroll 1
        for (unsigned s = 0; s < SPIN_CAP && __ballot((gg >> 16) != (((u64)sq[1] << 32 | sq[0]) & 0xFFFFFFFFFFFFull)); ++s) {
            g_load();
            waitv<0>();
            asm volatile("" : "+v"(gg));
        }
        og = rbf(1.f - bf2f((u16)gg));
        if (staged) {
#pragma unroll
            for (int nb = 0; nb < 4; ++nb) {
                const u32x2 u = lds_read<u32x2>(lds0 + L_A + 2048 + rg * 4096 + l15 * 128 + (((2 * nb + (g >> 1)) ^ sch) * 16) + (g & 1) * 8);
                a0_[nb] = (f32x4){lo16(u[0]), hi16(u[0]), lo16(u[1]), hi16(u[1])} * og;
            }
            asm volatile("" : "+v"(a0_[0]), "+v"(a0_[1]), "+v"(a0_[2]), "+v"(a0_[3]), "+v"(og));
            SB0();
            return;
        }
        unsigned t[8];
#pragma unroll
        for (int i = 0; i < 8; ++i) t[i] = __builtin_amdgcn_ds_bpermute(l15 * 16 + (g >> 1) * 4 + (g & 1) * 8, xr[i >> 2][i & 3]);
#pragma unroll
        for (int j = 0; j < 2; ++j) {
            auto u = __builtin_amdgcn_permlane16_swap(t[4 * j], t[4 * j + 2], false, false);
            auto v = __builtin_amdgcn_permlane16_swap(t[4 * j + 1], t[4 * j + 3], false, false);
#pragma unroll
            for (int b = 0; b < 2; ++b) a0_[2 * j + b] = (f32x4){lo16(u[b]), hi16(u[b]), lo16(v[b]), hi16(v[b])} * og;
        }
        asm volatile("" : "+v"(a0_[0]), "+v"(a0_[1]), "+v"(a0_[2]), "+v"(a0_[3]), "+v"(og));
        SB0();
    };
    auto xg_def = [&]() AI { asm volatile("" : "=v"(xr[0]), "=v"(xr[1]), "=v"(gg)); };
    auto relane = [&]() AI {
        unsigned z = 0;
        asm volatile("" : "+v"(z));
        l = __builtin_amdgcn_mbcnt_hi(~0u, __builtin_amdgcn_mbcnt_lo(~0u, z));
        g = l >> 4, l15 = l & 15, q = l15 >> 2, p = l & 3, srow = l >> 3, sch = l & 7, l16 = l * 16;
        AX = lds0 + L_A + rg * 4096 + l16, W4 = lds0 + p * 4, PG = lds0 + L_PROG + p * 256, xo = srow * 2048 + sch * 16;
    };
    auto kf_fetch = [&]() AI {
        asm volatile("s_mov_b32 m0, %3\n\ts_mov_b64 exec, 0xff\n\tglobal_load_lds_dwordx4 %1, %2 sc1\n\ts_mov_b64 exec, -1\n\t"
                     "s_mov_b32 m0, %4\n\ts_nop 0\n\t"
                     "global_load_lds_dwordx4 %0, %2 sc1\n\tglobal_load_lds_dwordx4 %0, %2 offset:1024 sc1\n\t"
                     "global_load_lds_dwordx4 %0, %2 offset:2048 sc1\n\tglobal_load_lds_dwordx4 %0, %2 offset:3072 sc1"
                     ::"v"(l16), "v"((unsigned)(4096 + (l & 4) * 128 + p * 16)), "s"(ws + WS_KF + (hp * 2 + hs) * 8192),
                       "s"(lds0 + L_KZ + hs * 128), "s"(lds0 + L_KF + hs * 4096) : "memory");
    };
    auto fd_def = [&]() AI {
#pragma unroll
        for (int i = 0; i < 16; ++i) asm volatile("" : "=v"(fd[i]));
        asm volatile("" : "=v"(gu[0]), "=v"(gu[1]));
    };
    const int oh = hs ^ kp ^ 1;

    if (__builtin_expect(w >= 4, 1)) {
        const bool loader = !(w & 1);
        const unsigned kf_kv = WS_KF + (hp * 2 + (mt >> 3)) * 8192 + (mt & 7) * 512;
        const unsigned kf_z = WS_KF + (hp * 2 + (mt >> 3)) * 8192 + 4096 + (mt & 7) * 128;
        const unsigned fgo = ffn ? (unsigned)((256 * kq + 16 * srow) * 7168 + cs * 128 + sch * 16) : 0u;
        const char* fsrc = (const char*)(hs ? wu : wg);
        auto gu_w = [&](int q0, int q1) AI {
#pragma unroll
            for (int i = q0; i < q1; ++i) {
                unsigned t1, t2, t3;
                asm volatile("v_add_u32 %4, 0x1c00, %7\n\tv_add_u32 %5, 0x3800, %7\n\tv_add_u32 %6, 0x5400, %7\n\t"
                             "global_load_dwordx4 %0, %7, %8 nt\n\tglobal_load_dwordx4 %1, %4, %8 nt\n\t"
                             "global_load_dwordx4 %2, %5, %8 nt\n\tglobal_load_dwordx4 %3, %6, %8 nt"
                             : "=&v"(fw[4 * i]), "=&v"(fw[4 * i + 1]), "=&v"(fw[4 * i + 2]), "=&v"(fw[4 * i + 3]), "=&v"(t1), "=&v"(t2), "=&v"(t3)
                             : "v"(fgo), "s"(fsrc + (128 * (i >> 2) + 4 * (i & 3)) * 7168) : "memory");
            }
        };
        const unsigned fdo = cd < 56 ? (unsigned)((64 * cd + 8 * srow) * 2048 + 512 * kq + 256 * hs + sch * 16) : 0u;
        auto dn_w = [&](int q0, int q1) AI {
#pragma unroll
            for (int i = q0; i < q1; ++i)
                asm volatile("global_load_dwordx4 %0, %4, %5 offset:-4096 nt\n\tglobal_load_dwordx4 %1, %4, %5 offset:-2048 nt\n\t"
                             "global_load_dwordx4 %2, %4, %5 nt\n\tglobal_load_dwordx4 %3, %4, %5 offset:2048 nt"
                             : "=&v"(fd[4 * i]), "=&v"(fd[4 * i + 1]), "=&v"(fd[4 * i + 2]), "=&v"(fd[4 * i + 3])
                             : "v"(fdo), "s"((const char*)wd + 4096 + (i & 1) * 8192 + (i >> 1) * 128) : "memory");
        };
        auto gate_up = [&]() AI {
            sel_init();
            u32x4 xs[4];
            waitv<4 * (NF - NF1) + 1 + 4 * (8 - GQ1) + 16>();
#pragma unroll
            for (int i = 0; i < 4; ++i) xs[i] = lds_read<u32x4>(lds0 + L_XS + hs * 512 + (i >> 1) * 256 + srow * 32 + (i & 1) * 16);
            f32x4 a;
            bf16x8 t[2];
            t[0] = spread(xs[0], 0);
            sfor<0, 32>([&](auto I) {
                constexpr int i = I;
                SB0();
                if constexpr (i < 31) t[~i & 1] = spread(xs[(i + 1) >> 3], (i + 1) & 7);
                else asm volatile("s_nop 1");
                asm volatile("" : "+v"(t[~i & 1]));
                if constexpr (i == 4 * GQ1) waitv<16>();
                SB0();
                u32x4& f = fw[i];
                bf16x8& u = t[i & 1];
                f32x4& c = a;
                pin(f);
                if constexpr (i == 0) asm volatile("v_mfma_f32_16x16x32_bf16 %0, %1, %2, 0" : "=&v"(c) : "v"(u), "v"(f));
                else asm volatile("v_mfma_f32_16x16x32_bf16 %0, %1, %2, %0" : "+v"(c) : "v"(u), "v"(f));
            });
            asm volatile("s_nop 7" : "+v"(a));
            SB0();
            fold_store(a, WS_GU + cs * 4096 + kq * 512 + hs * 256);
            SB0();
        };
        u64 T;
        u32x4 sv[2];
        u32x2 sg;
        auto rg_load = [&]() AI {
            asm volatile("global_load_dwordx4 %0, %3, %4 sc1\n\tglobal_load_dwordx4 %1, %3, %4 offset:16 sc1\n\tglobal_load_dwordx2 %2, %5, %6 sc1"
                         : "=&v"(sv[0]), "=&v"(sv[1]), "=&v"(sg) : "v"(l * 32), "s"(ws + WS_RG), "v"(l * 8), "s"(ws + WS_GG + (mt * 2 + hs) * 512) : "memory");
        };
        auto select = [&]() AI {
            const unsigned tl = (unsigned)T & 0x1FFFu, thi = (unsigned)(T >> 13);
#pragma unroll 1
            for (unsigned s = 0; s < SPIN_CAP; ++s) {
                pin(sv[0]);
                pin(sv[1]);
                asm volatile("" : "+v"(sg));
                const unsigned bad = ((sg[0] >> 16) ^ ((unsigned)T & 0xFFFFu)) | (sg[1] ^ (unsigned)(T >> 16)) | ((sv[0][0] >> 19) ^ tl) | (sv[0][1] ^ thi) | ((sv[0][2] >> 19) ^ tl) | (sv[0][3] ^ thi) |
                                     ((sv[1][0] >> 19) ^ tl) | (sv[1][1] ^ thi) | ((sv[1][2] >> 19) ^ tl) | (sv[1][3] ^ thi);
                if (__all(bad == 0u)) break;
                __builtin_amdgcn_s_sleep(16);
                rg_load();
                waitv<0>();
            }
            pin(sv[0]);
            pin(sv[1]);
            asm volatile("" : "+v"(sg));
            lds_write<unsigned>(lds0 + L_G + hs * 512 + l * 4, sg[0] << 16);
            SB0();
            unsigned k2 = 0;
#pragma unroll
            for (int j = 0; j < 4; ++j) {
                const unsigned v = sv[j >> 1][(j & 1) * 2] & 0x7FFFFu;
                k2 = max(k2, ((v >> 3) << 11) | ((255 - (4 * l + j)) << 3) | (v & 7));
            }
            const unsigned key = __builtin_amdgcn_readfirstlane(wave_umax(k2));
            asm volatile("s_mov_b32 m0, %2\n\ts_mov_b64 exec, %3\n\tglobal_load_lds_dwordx4 %0, %1\n\ts_mov_b64 exec, -1"
                         ::"v"((unsigned)(512 * kq) + l16), "s"(x + (size_t)(2047 - (int)(key & 2047u)) * DM), "s"(lds0 + L_XS + hs * 512), "s"(0xFFFFFFFFull) : "memory");
            lds_write<u32x4>(lds0 + L_SEL, (u32x4){(unsigned)T, (unsigned)(T >> 32), 2047u - (key & 2047u), key >> 11});
            if (bid == 0 && hs) ST_MASK("global_store_dwordx2", 1ull, 0u, T, ws + WS_E);
            SB0();
        };
        auto k_soft = [&]() AI {
            bf16x8 a0[2], kb[2];
            f32x4 pk[2][2][2];
            unsigned pw[4];
            phi_w(a0, 0);
            phi_adr(pw, lds0 + P_U[0] * 8192);
            asm volatile("" : "+v"(pw[0]), "+v"(pw[1]), "+v"(pw[2]), "+v"(pw[3]));
            auto logits = [&](int h) AI {
                lds_wait(WR + L_FK0 + 4 * h, 1);
#pragma unroll
                for (int i = 0; i < 2; ++i) kb[i] = lds_read128(AX + (P_U[0] * 8192 - L_A) + h * 4096 + i * 1024);
#pragma unroll
                for (int i = 0; i < 4; ++i) pk[h][i >> 1][i & 1] = mfma16(a0[i & 1], kb[i >> 1], zero);
            };
            auto round = [&](int h) AI {
#pragma unroll
                for (int i = 0; i < 4; ++i) pk[h][i >> 1][i & 1] = rbf4(pk[h][i >> 1][i & 1]);
            };
            logits(0);
            SB0();
            round(0);
            SB0();
            phi_k(pk[0], pw, 0, [&]() AI {
                logits(1);
                SB0();
            });
            SB0();
            round(1);
            phi_k(pk[1], pw, 4096, []() {});
            SB0();
            flag(WR + L_FP);
        };
        auto q_soft = [&](auto hook) AI {
            bf16x8 a1[2];
            f32x4 pq[2][2];
            phi_w(a1, hs);
            lds_wait0(WR + L_FQ);
#pragma unroll
            for (int rb = 0; rb < 2; ++rb) {
                const bf16x8 b = lds_read128(AX + rb * 1024);
#pragma unroll
                for (int fb = 0; fb < 2; ++fb) pq[rb][fb] = rbf4(mfma16(a1[fb], b, zero));
            }
            softmax2(pq, ph, hook);
            asm volatile("" : "+v"(ph[0][0]), "+v"(ph[0][1]), "+v"(ph[1][0]), "+v"(ph[1][1]));
        };
        if (__builtin_expect(loader, 1)) {
            vx_init();
            vw_init();
            unsigned pv = 0;
            sfor<4, NF1>([&](auto N) {
                constexpr int n = N;
                if constexpr (F_N[n] > F_N[n - 1]) {
                    __builtin_amdgcn_s_setprio(3);
                    lds_wait(pv, PG, F_N[n]);
                    if (F_N[n] < RLD) lds_peek(pv, PG);
                    __builtin_amdgcn_s_setprio(0);
                }
                if constexpr (n == 12) {
                    asm volatile("s_mov_b32 m0, %2\n\ts_nop 0\n\tglobal_load_lds_dwordx4 %0, %1\n\tglobal_load_lds_dwordx4 %0, %1 offset:1024"
                                 ::"v"((unsigned)((l >> 2) * 64 + (((l & 3) ^ (2 * ((l >> 4) & 1))) * 16))),
                                   "s"(wphi + (hp * 2 + hs) * 1024), "s"(lds0 + L_WPHI + hs * 2048) : "memory");
                }
                fill(N);
            });
            SB0();
            __builtin_amdgcn_s_setprio(2);
            const char* cla = ws + WS_CL + ((l15 * 2 + (mt >> 3)) * 16 + hp) * 128;
            u64 cl;
            asm volatile("global_load_dwordx2 %0, %1, off sc1" : "=&v"(cl) : "v"(cla) : "memory");
            k_soft();
            SB0();
            lds_wait(W4 + L_FP, 1);
            sq = lds_read<u32x4>(lds0 + L_SEL);
            waitv<0>();
            asm volatile("" : "+v"(cl));
            SB0();
            {
                u32x4 kp_[6];
                const char* kb = ws + WS_KV + (hp * 8 + (mt >> 1)) * 16384 + (mt & 1) * 512 + hs * 256;
                const bool zw = !((mt & 3) | hs);
                const char* zb = zw ? ws + WS_Z + (hp * 4 + (mt >> 2)) * 2048 : kb;
                const unsigned kvo = g * 1024 + l15 * 16, zo = zw ? l16 : 0u;
                auto ko = [&](int i) { return i < 4 ? kvo : zo; };
                auto ka = [&](int i) { return i < 4 ? kb + i * 4096 : zb + (i - 4) * 1024; };
                const u64 T = (u64)sq[1] << 32 | sq[0];
#pragma unroll 1
                for (unsigned s = 0; s < SPIN_CAP && __ballot(cl != T); ++s)
                    asm volatile("global_load_dwordx2 %0, %1, off sc1\n\ts_waitcnt vmcnt(0)" : "=&v"(cl) : "v"(cla) : "memory");
                u32x4 a01;
#pragma unroll
                for (int i = 0; i < 4; ++i) a01[i] = (l15 >> 1) != i ? 0u : (l & 1) ? 0x3f800000u : 0x3f80u;
                unsigned po = kf_kv + hs * 256 + l15 * 16 + (g & 1) * 8, pz = kf_z + (l & 8) * 8 + p * 16 + (sch >> 2) * 8 + (g >> 1) * 4;
                pin(a01);
                asm volatile("" : "+v"(po), "+v"(pz));
                lds_wait0(W4 + L_FKS);
                __builtin_amdgcn_s_sleep(GDL);
#pragma unroll
                for (int i = 0; i < 6; ++i) ld_async(kp_[i], ko(i), ka(i));
                xg_load();
                SB0();
                waitv<3>();
                SB0();
                u32x2 o;
                auto sum = [&]() AI {
                    f32x4 d = zero;
#pragma unroll
                    for (int i = 0; i < 4; ++i) d = mfma16(__builtin_bit_cast(bf16x8, a01), __builtin_bit_cast(bf16x8, kp_[i]), d);
                    SB0();
                    unsigned m = least(kp_);
                    asm volatile("" : "+v"(m));
                    SB0();
                    o = (u32x2){nz1(pack2(d[0], d[1])), nz1(pack2(d[2], d[3]))};
                    asm volatile("" : "+v"(o));
                    return m;
                };
#pragma unroll
                for (int i = 0; i < 6; ++i) pin(kp_[i]);
                if (__builtin_expect(__ballot(sum() == 0u) != 0ull, 0)) {
                    again(kp_, ko, ka);
                    sum();
                }
                SB0();
                ST_MASK("global_store_dwordx2", 0xFFFFFFFFull, po, o, ws);
                if (zw) {
                    const f32x4 z = __builtin_bit_cast(f32x4, kp_[4]) + __builtin_bit_cast(f32x4, kp_[5]);
                    const unsigned t = __float_as_uint(dpp_add<0x128>(swap16_add(swap32_add(z[0], z[2]), swap32_add(z[1], z[3]))));
                    auto r = __builtin_amdgcn_permlane16_swap(t, t, false, false);
                    ST_MASK("global_store_dword", 0x0000FFFF0000FFFFull, pz, nz1(pack2(__uint_as_float(r[0]), __uint_as_float(r[1]))), ws);
                }
                flag(lds0 + L_FG + hs * 4);
            }
            SB0();
            need(KFN);
            q_soft([]() {});
            SB0();
            __builtin_amdgcn_s_sleep(KFD);
            SB0();
            kf_fetch();
            mark(lds0 + L_FKF + hs * 4);
            SB0();
            waitv<6>();
            x_init(false);
            fd_def();
            xg_def();
        } else {
            const int row0 = (mt * 16 + hp) * 8 + 4 * hs;
            __builtin_amdgcn_s_setprio(2);
            asm volatile("s_waitcnt lgkmcnt(0)" : "+s"(sd));
            lds_wait(lds0 + L_FULL + p * 4, 3);
            u32x4 rv[2], gv[2], xv[4][2];
            u64 ev;
            asm volatile("global_load_dwordx2 %4, %5, %9 sc1\n\t"
                         "global_store_dwordx4 %10, %11, %9 sc1\n\t"
                         "global_store_dwordx2 %12, %13, %9 sc1\n\t"
                         "global_load_dwordx4 %0, %6, %7\n\tglobal_load_dwordx4 %1, %6, %7 offset:1024\n\t"
                         "global_load_dwordx4 %2, %6, %8\n\tglobal_load_dwordx4 %3, %6, %8 offset:1024"
                         : "=&v"(rv[0]), "=&v"(rv[1]), "=&v"(gv[0]), "=&v"(gv[1]), "=&v"(ev)
                         : "v"(0u), "v"(l16), "s"(wr), "s"(gw), "s"(ws),
                           "v"(l < 32 ? WS_Y + cd * 4096 + kq * 1024 + hs * 512 + l16 : WS_GU + cs * 4096 + kq * 512 + hs * 256 + l15 * 16), "v"(z4),
                           "v"(hs ? kf_z + l15 * 8 : kf_kv + l * 8), "v"(0ull)
                         : "memory");
#pragma unroll
            for (int r = 0; r < 2; ++r)
                asm volatile("global_load_dwordx4 %0, %4, %5\n\tglobal_load_dwordx4 %1, %4, %5 offset:1024\n\t"
                             "global_load_dwordx4 %2, %4, %5 offset:2048\n\tglobal_load_dwordx4 %3, %4, %5 offset:3072"
                             : "=&v"(xv[2 * r][0]), "=&v"(xv[2 * r][1]), "=&v"(xv[2 * r + 1][0]), "=&v"(xv[2 * r + 1][1])
                             : "v"(l16), "s"(x + (size_t)(row0 + 2 * r) * DM) : "memory");
            SB0();
            waitv<0>();
            asm volatile("" : "+v"(rv[0]), "+v"(rv[1]), "+v"(gv[0]), "+v"(gv[1]), "+v"(ev));
#pragma unroll
            for (int r = 0; r < 4; ++r) asm volatile("" : "+v"(xv[r][0]), "+v"(xv[r][1]));
            SB0();
            T = ((u64)__builtin_amdgcn_readfirstlane((unsigned)(ev >> 32)) << 32 | __builtin_amdgcn_readfirstlane((unsigned)ev)) + 1;
            float ra_[4] = {0.f, 0.f, 0.f, 0.f}, ga_[4] = {0.f, 0.f, 0.f, 0.f};
#pragma unroll
            for (int r = 0; r < 4; ++r)
#pragma unroll
                for (int i = 0; i < 2; ++i)
#pragma unroll
                    for (int j = 0; j < 4; ++j) {
                        ra_[r] = dot2(xv[r][i][j], rv[i][j], ra_[r]);
                        ga_[r] = dot2(xv[r][i][j], gv[i][j], ga_[r]);
                    }
            const int rr = (g >> 1) | ((g & 1) << 1);
            unsigned k = f2bf(sum4(ra_));
            k ^= (k & 0x8000u) ? 0xFFFFu : 0x8000u;
            k = (k << 3) | (7 - 4 * hs - rr);
            auto r1 = __builtin_amdgcn_permlane16_swap(k, k, false, false);
            k = max(r1[0], r1[1]);
            auto r2 = __builtin_amdgcn_permlane32_swap(k, k, false, false);
            k = max(r2[0], r2[1]);
            u64 gq = (T << 16) | f2bf(1.f / (1.f + __expf(-rbf(sum4(ga_)))));
            if (hs) lds_write<unsigned>(lds0 + L_BEST, k | 0x80000000u);
            asm volatile("" : "+v"(gq), "+v"(k));
            __builtin_amdgcn_s_setprio(0);
            lds_wait8(PG, CLR0 + ph4);
            if (!hs) {
                lds_wait(lds0 + L_BEST, 1);
                const u64 rq = (T << 19) | max(k, lds_read<unsigned>(lds0 + L_BEST) & 0x7FFFFu);
                ST_MASK("global_store_dwordx2", 1ull, 0u, rq, ws + WS_RG + (mt * 16 + hp) * 8);
            }
            ST_MASK("global_store_dwordx2", 0x0001000100010001ull, (unsigned)(rr * 8), gq, ws + WS_GG + row0 * 8);
            SB0();
#pragma unroll
            for (int n = 0; n < 4; ++n) st_wt(rsrc, WS_KV + ((hp * 8 + hs * 4 + n) * 16 + mt) * 1024 + l16, z4);
            ST_MASK("global_store_dwordx4", 0xFFFFull, WS_Z + ((hp * 4 + hs * 2 + (srow & 1)) * 16 + mt) * 128 + sch * 16, z4, ws);
#pragma unroll
            for (int n = 0; n < 8; ++n) st_wt(rsrc, WS_O + ((mt * 2 + (n >> 2)) * 32 + 2 * hp + hs) * 4096 + (n & 3) * 1024 + l16, z4);
            waitv<0>();
            ST_MASK("global_store_dwordx2", 0xFFFFull, (unsigned)l * 8, T, ws + WS_CL + ((mt * 2 + hs) * 16 + hp) * 128);
            SB0();
            vw_init();
            asm volatile("" : "+v"(vw[0]), "+v"(vw[1]), "+v"(vw[2]), "+v"(vw[3]));
            lds_wait8(PG, RLD - 1);
            lds_wait(PG, RLD);
            __builtin_amdgcn_s_setprio(3);
            __builtin_amdgcn_s_sleep(5);
            rg_load();
            SB0();
            gu_w(0, GQ1);
            waitv<4 * GQ1>();
            select();
            flag(WR + L_FP);
            sfor<NF1, NF>([&](auto N) {
                constexpr int n = N;
                if constexpr (n == NF1 || F_N[n] != F_N[n - 1]) need(F_N[n]);
                fill(N);
            });
            SB0();
            gu_w(GQ1, 8);
            dn_w(0, 4);
            SB0();
            gate_up();
            __builtin_amdgcn_s_setprio(0);
            SB0();
            xg_load();
            lds_wait(W4 + L_FKS, 1);
            wo_load(0, WOE, false);
            SB0();
            sq = lds_read<u32x4>(lds0 + L_SEL);
            waitv<4 * WOE>();
            x_init(false);
            lds_wait(lds0 + L_FG + (l & 1) * 4, 1);
            gu_load();
            wo_load(WOE, WOM, false);
            SB0();
            __builtin_amdgcn_s_setprio(2);
            q_soft([&]() AI {
                wo_load(WOM, WON, true);
                SB0();
            });
            waitv<4 * (WON - WOE) + 1>();
#pragma unroll
            for (int i = 0; i < 16; ++i) pin(fd[i]);
            pin(gu[0]);
            pin(gu[1]);
            SB0();
            xg_def();
        }
        SB0();
    } else {
        __builtin_amdgcn_s_setprio(1);
        unsigned ra[4], xt[2];
#pragma unroll
        for (int u = 0; u < 4; ++u) ra[u] = lds0 + J_B[0] * 8192 + (8 * g + q) * 128 + ((u ^ (q >> 1) ^ ((g & 1) << 1)) * 32) + 8 * p;
        unsigned xa = lds0 + (32 * (w & 1) + l15) * 128 + ((g ^ (l15 >> 1)) * 16), fa = lds0 + L_FULL + (l & 3) * 4;
        xt[0] = xa + (hs ? X_U1[0] : X_U0[0]) * 8192;
        xt[1] = xt[0] ^ 64;
        asm volatile("s_mov_b32 m0, %4" : "+v"(xa), "+v"(fa), "+v"(xt[0]), "+v"(xt[1]) : "s"(lds0 + L_PROG + w * 256 - 124));
        f32x4 acc[3][4][2];
        bf16x8 xf[ND][2][2];
        s16x4 wh[PD + 1][2];
        unsigned fv = 0;
        bf16x8 kown[2];
        u64 bad = ~0ull;
        auto chk = [&](int c) AI {
            SB0();
            if (__builtin_expect(bad != 0ull, 0)) {
                unsigned c_;
                asm volatile("s_mov_b32 %1, 0x400000\n"
                             ".Lp%=:\n\tds_read_b32 %0, %2 offset:%3\n\ts_waitcnt lgkmcnt(0)\n\tv_cmp_gt_u32 vcc, %4, %0\n\ts_cbranch_vccz .Lf%=\n\t"
                             "s_sub_u32 %1, %1, 1\n\ts_cbranch_scc0 .Lp%=\n.Lf%=:"
                             : "=&v"(fv), "=&s"(c_) : "v"(fa), "n"(c > NM1 ? 32 : 0), "n"(c) : "memory", "vcc", "scc");
            }
            SB0();
        };
        auto kv_part = [&]() AI {
            bf16x8 ones;
#pragma unroll
            for (int j = 0; j < 8; ++j) ones[j] = (short)0x3f80;
            unsigned pa = lds0 + (hs ? P_U[2] : P_U[0]) * 8192 + (4 * g + q) * 128 + (((8 * (rg & 1) + p) ^ fsw(4 * g + q)) << 3);
            unsigned pb = pa ^ 32, va = lds0 + L_A + hs * 2048 + l16, kv_st = WS_KV + ((hp * 8 + rg * 2) * 16 + mt) * 1024 + l16;
            unsigned kv_s1 = kv_st + 16384, z_st = WS_Z + ((hp * 4 + rg) * 16 + mt) * 128 + (l & 1) * 64 + g * 16, fk = WR + L_FKS;
            asm volatile("" : "+v"(pa), "+v"(pb), "+v"(va), "+v"(kv_st), "+v"(kv_s1), "+v"(z_st), "+v"(fk), "+v"(ones));
            bf16x8 b0[4], b1[4], af[2][4];
            lds_wait0(lds0 + L_FV + sch * 4);
            SB0();
#pragma unroll
            for (int i = 0; i < 4; ++i) {
                af[0][i] = cat8(tr_read(pa + i * 4096), tr_read(pa + i * 4096 + 2048));
                b0[i] = lds_read128(va + i * 4096);
                SB0();
                af[1][i] = cat8(tr_read(pb + i * 4096), tr_read(pb + i * 4096 + 2048));
                SB0();
            }
#pragma unroll
            for (int i = 0; i < 4; ++i) b1[i] = lds_read128(va + i * 4096 + 1024);
            SB0();
            f32x4 k0[2] = {zero, zero}, k1[2] = {zero, zero}, zt[2] = {zero, zero};
#pragma unroll
            for (int i = 0; i < 4; ++i)
#pragma unroll
                for (int e = 0; e < 2; ++e) {
                    k0[e] = mfma16(af[e][i], b0[i], k0[e]);
                    zt[e] = mfma16(af[e][i], ones, zt[e]);
                }
            SB0();
#pragma unroll
            for (int i = 0; i < 4; ++i)
#pragma unroll
                for (int e = 0; e < 2; ++e) k1[e] = mfma16(af[e][i], b1[i], k1[e]);
            st_wt(rsrc, kv_st, nz4(__builtin_bit_cast(u32x4, pack44(k0[0], k0[1]))));
            const u32x4 zo = nz4((l & 1) ? zt[1] : zt[0]);
            ST_MASK("global_store_dwordx4", 0x0003000300030003ull, z_st, zo, ws);
            st_wt(rsrc, kv_s1, nz4(__builtin_bit_cast(u32x4, pack44(k1[0], k1[1]))));
            SB0();
            flag(fk);
        };
        auto report = [&](int r) AI { asm volatile("global_load_lds_dword %0, %1 offset:%2" ::"v"(v0), "s"(one), "n"(4 * r) : "memory"); };
        auto rd1 = [&](auto F, auto H) AI {
            constexpr int f = F, h = H, n = f >> 2, j = f >> 3, c = h ? E_B[f] : E_A[f], a = c % 100;
            if constexpr (h == 0 && (f & 3) == 0 && Q_C[n] > 0) chk(Q_C[n]);
            wh[f % (PD + 1)][h] = tr_read(ra[f & 3] + (J_U[j] - J_B[j]) * 8192 + (n & 1) * 4096 + h * 512);
            SB0();
            if constexpr (c / 100 == 1) {
                constexpr int jn = (f + 3 - a) >> 3;
                asm volatile("v_add_u32 %0, %1, %0" : "+v"(ra[a]) : "n"((J_B[jn] - J_B[jn - 1]) * 8192));
            } else if constexpr (c / 50 == 4) {
                asm volatile("v_add_u32 %0, %1, %2" : "=v"(xt[0]) : "s"((hs ? X_U1[a] : X_U0[a]) * 8192), "v"(xa));
            } else if constexpr (c / 50 == 5) {
                asm volatile("v_xor_b32 %0, 64, %1" : "=v"(xt[1]) : "v"(xt[0]));
            } else if constexpr (c / 100 == 3) {
                xf[X_R[a % 25]][a / 25 & 1][a / 50] = lds_read128(xt[a / 50] + (a / 25 & 1) * 2048);
            } else if constexpr (c / 100 == 4) {
                fv = *(volatile AS3 unsigned*)(size_t)(fa + (a > NM1 ? 32 : 0));
            } else if constexpr (c / 100 == 5) {
                bad = __builtin_amdgcn_ballot_w64(fv < (unsigned)a);
                asm volatile("" : "+s"(bad));
            } else if constexpr (c / 100 == 6) {
                report(a);
            }
        };
        sfor<0, PD>([&](auto F) { rd1(F, IC<0>{}); rd1(F, IC<1>{}); });
        SB0();
        constexpr int K0F = 4 * KD, K1F = 4 * KD + 1;
        sfor<0, 96>([&](auto N) {
            sfor<0, 4>([&](auto E) {
                constexpr int n = N, e = E, f = 4 * n + e, j = n >> 1, ks = n & 1, u = e, s = J_X[j], m = J_M[j];
                const bf16x8 wv = cat8(wh[f % (PD + 1)][0], wh[f % (PD + 1)][1]);
                sfor<0, 2>([&](auto RB) {
                    constexpr int rb = RB;
                    const bf16x8 xv = xf[X_R[s]][rb][ks];
                    if (j_first(j) && ks == 0) asm volatile("v_mfma_f32_16x16x32_bf16 %0, %1, %2, 0" : "=&v"(acc[m][u][rb]) : "v"(m == 1 ? xv : wv), "v"(m == 1 ? wv : xv));
                    else asm volatile("v_mfma_f32_16x16x32_bf16 %0, %1, %2, %0" : "+v"(acc[m][u][rb]) : "v"(m == 1 ? xv : wv), "v"(m == 1 ? wv : xv));
                    SB0();
                    if constexpr (f + PD < 384 && (f + PD < 4 * VD || f >= 4 * VD)) rd1(IC<f + PD>{}, RB);
                    if constexpr (rb == 0 && f == K0F) relane();
                    if constexpr (rb == 0 && (f == K0F || f == K1F)) asm volatile("s_nop 7");
                    SB0();
                    if constexpr (f == K0F) lds_write<bf16x8>(AX + (P_U[0] * 8192 - L_A) + rb * 1024, pack44(acc[0][0][rb], acc[0][1][rb]));
                    if constexpr (f == K1F) {
                        kown[rb] = pack44(acc[0][2][rb], acc[0][3][rb]);
                        asm volatile("" : "+v"(kown[rb]));
                    }
                    SB0();
                });
                if constexpr (f == K0F) flag(WR + L_FK0);
                if constexpr (f == 4 * VD - 1) {
                    bf16x8 ak[2];
                    f32x4 pk[2][2];
                    unsigned pw[4];
                    __builtin_amdgcn_s_setprio(2);
                    phi_w(ak, 1);
                    asm volatile("s_nop 7");
                    SB0();
#pragma unroll
                    for (int i = 0; i < 4; ++i) lds_write<bf16x8>(AX + i * 1024, pack44(acc[1][i][0], acc[1][i][1]));
                    SB0();
#pragma unroll
                    for (int i = 0; i < 4; ++i) pk[i >> 1][i & 1] = rbf4(mfma16(ak[i & 1], kown[i >> 1], zero));
                    phi_adr(pw, lds0 + P_U[2] * 8192);
                    phi_k(pk, pw, 0, []() {});
                    SB0();
                    flag(WR + L_FV);
                    kv_part();
                    __builtin_amdgcn_s_setprio(1);
                    fv = 0;
                    bad = ~0ull;
                    sfor<4 * VD, 4 * VD + PD>([&](auto F) { rd1(F, IC<0>{}); rd1(F, IC<1>{}); });
                    SB0();
                }
                if constexpr (f >= 24 && f < 56 && (f & 7) == 7) __builtin_amdgcn_s_sleep(5);
            });
        });
        asm volatile("s_nop 7");
        SB0();
        __builtin_amdgcn_s_setprio(0);
        flag(WR + L_FQD);
        bf16x8 a0[2];
        f32x4 pq[2][2];
        phi_w(a0, hs ^ 1);
        lds_wait(W4 + L_FKS, 1);
#pragma unroll
        for (int rb = 0; rb < 2; ++rb) {
            const bf16x8 b0 = pack44(acc[2][0][rb], acc[2][1][rb]), b1 = pack44(acc[2][2][rb], acc[2][3][rb]);
            lds_write<bf16x8>(AX + rb * 1024, hs ? b1 : b0);
#pragma unroll
            for (int fb = 0; fb < 2; ++fb) pq[rb][fb] = mfma16(a0[fb], hs ? b0 : b1, zero);
        }
        SB0();
        flag(WR + L_FQ);
        sq = lds_read<u32x4>(lds0 + L_SEL);
        SB0();
#pragma unroll
        for (int i = 0; i < 4; ++i) pq[i >> 1][i & 1] = rbf4(pq[i >> 1][i & 1]);
        softmax2(pq, ph, []() {});
        asm volatile("" : "+v"(ph[0][0]), "+v"(ph[0][1]), "+v"(ph[1][0]), "+v"(ph[1][1]));
        SB0();
        asm volatile("s_mov_b32 m0, %3\n\ts_nop 0\n\tglobal_load_lds_dwordx4 %0, %1\n\tglobal_load_lds_dwordx4 %0, %2 offset:1024"
                     ::"v"((unsigned)(srow * 2048 + (sch ^ srow) * 16)), "s"(x + (size_t)row0 * DM + cb * 64),
                       "s"(x + (size_t)(row0 + 8) * DM + cb * 64 - 512), "s"(lds0 + L_A + 2048 + rg * 4096) : "memory");
        g_load();
        asm volatile("" : "=v"(xr[0]), "=v"(xr[1]));
        asm volatile("" : "=v"(a0_[0]), "=v"(a0_[1]), "=v"(a0_[2]), "=v"(a0_[3]), "=v"(og));
        fd_def();
    }
    relane();
    const char* cla = ws + WS_CL + ((rt * 2 + kp) * 16 + l15) * 128;
    const u64 T = (u64)sq[1] << 32 | sq[0];
    u64 cl;
    unsigned ka0 = lds0 + L_KF + oh * 4096 + l16, ka1 = lds0 + L_KZ + oh * 128 + g * 16;
    unsigned o_st = WS_O + ((mt * 2 + hs) * 32 + 2 * hp + oh) * 4096 + (rg & 1) * 2048 + l16;
    float gt[2];
#pragma unroll
    for (int i = 0; i < 2; ++i) gt[i] = lds_read<float>(lds0 + L_G + hs * 512 + (rg & 1) * 128 + i * 64 + l15 * 4);
    asm volatile("" : "+v"(ka0), "+v"(ka1), "+v"(o_st), "+v"(cla), "+v"(gt[0]), "+v"(gt[1]));
    u32x4 kf[6];
    auto kf_read = [&]() AI {
#pragma unroll
        for (int i = 0; i < 2; ++i) kf[4 + i] = lds_read<u32x4>(ka1 + i * 64);
        SB0();
#pragma unroll
        for (int i = 0; i < 4; ++i) kf[i] = lds_read<u32x4>(ka0 + i * 1024);
    };
    lds_wait0(lds0 + L_FKF + oh * 4);
    kf_read();
    asm volatile("global_load_dwordx2 %0, %1, off sc1" : "=&v"(cl) : "v"(cla) : "memory");
    SB0();
    f32x4 nm[2][2];
    float rd_[2];
    if (__builtin_expect(__ballot(o_sums(kf, ph, gt, rd_, nm) == 0u) != 0ull, 0)) {
#pragma unroll 1
        for (unsigned n = 2; n < SPIN_CAP; ++n) {
            if ((w & 5) == 4) {
                kf_fetch();
                waitv<0>();
                lds_write<unsigned>(lds0 + L_FKF + oh * 4, n);
            } else lds_wait(lds0 + L_FKF + oh * 4, n);
            asm volatile("" ::: "memory");
            kf_read();
            if (__ballot(o_sums(kf, ph, gt, rd_, nm) == 0u) == 0ull) break;
        }
        SB0();
    }
    SB0();
    o_store(rd_, nm, rsrc, o_st);
    SB0();
    flag(lds0 + L_FOS + w * 4);
    waitv<3>();
    __builtin_amdgcn_s_setprio(0);
    if ((w & 5) == 4) {
        lds_wait(lds0 + L_FOS + sch * 4, 1);
        wo_load(WON, 16, true);
    }
    if ((w & 5) == 5) {
        if (cd < 56) {
            sel_init();
            down();
        } else {
            __builtin_amdgcn_s_sleep(PDL);
            waitv<2>();
        }
    }
    const u32x2 selv = {sq[2], sq[3]};

    SB0();
    const char* ob = ws + WS_O + (rt * 2 + (rg >> 1)) * 131072 + (rg & 1) * 2048;
    const unsigned ov0 = l16 + kp * 1024, ov1 = l16 + (kp ^ 1) * 1024;
    auto oa = [&](int i) { return ob + ((2 * i + kp + rot) & 31) * 4096; };
    u32x4 of[3][4];
    auto fetch1 = [&](int k, int j) AI { ld_async(of[k % 3][j], (j & 1) ? ov1 : ov0, oa(2 * k + (j >> 1))); };
    auto fix = [&](int k) AI { again(of[k % 3], [&](int j) { return (j & 1) ? ov1 : ov0; }, [&](int j) { return oa(2 * k + (j >> 1)); }); };
    const bool own = (__builtin_amdgcn_readfirstlane(selv[0]) >> 4) == (unsigned)(rt * 8 + rg * 2 + kp);
    if (!kp) {
        x_init(true);
        __builtin_amdgcn_s_sleep(PDL);
        waitv<2>();
    } else {
        __builtin_amdgcn_s_sleep(PD1);
        if (w & 1) waitv<4>();
        else waitv<3 + 4 * (16 - WON)>();
    }
    f32x4 acc[2][4];
#pragma unroll
    for (int nb = 0; nb < 4; ++nb) acc[0][nb] = a0_[nb];
    const float ys = __uint_as_float(__builtin_amdgcn_readlane(__float_as_uint(og), __builtin_amdgcn_readfirstlane(selv[0]) & 15));
    asm volatile("" : "+v"(cl));
#pragma unroll 1
    for (unsigned s = 0; s < SPIN_CAP && __ballot(cl != T); ++s)
        asm volatile("global_load_dwordx2 %0, %1, off sc1\n\ts_waitcnt vmcnt(0)" : "=&v"(cl) : "v"(cla) : "memory");
    SB0();
#pragma unroll
    for (int j = 0; j < 4; ++j) fetch1(0, j);
    SB0();
    unsigned wa[2][4];
#pragma unroll
    for (int i = 0; i < 8; ++i) {
        wa[i >> 2][i & 3] = ((lds0 + 4096 * kp + (4 * g + q) * 128 + ((((g & 1) << 1) | (q >> 1)) * 32) + 8 * p) ^ ((i & 3) << 5)) + (i >> 2) * 65536;
        asm volatile("" : "+v"(wa[i >> 2][i & 3]));
    }
    s16x4 wf[2][8][2];
    auto wrd = [&](int k, int i) AI {
        const int u = W_U[2 * k + (i >> 3)];
        wf[k & 1][i >> 1][i & 1] = tr_read(wa[u >> 3][(i >> 1) & 3] + (u & 7) * 8192 + (i & 1) * 2048);
    };
    u32x4 yp[14];
    const unsigned yo = srow * 4096 + cb * 256 + sch * 32;
    auto ya = [&](int i) { return ws + WS_Y + (i >> 1) * 32768 + (i & 1) * 16; };
    lds_wait(lds0 + L_FWD + (l & 1) * 4, 1);
    SB0();
#pragma unroll
    for (int i = 0; i < 16; ++i) wrd(0, i);
#pragma unroll
    for (int j = 0; j < 4; ++j) fetch1(1, j);
    const unsigned ea = lds0 + L_A + l16 + 4096 * w, eb = lds0 + L_A + 4096 * (w ^ 4);
    unsigned er = eb + l16, et = eb + srow * 128 + ((sch ^ srow) * 16), ew[4];
#pragma unroll
    for (int nb = 0; nb < 4; ++nb) ew[nb] = eb + l15 * 128 + (((2 * nb + (g >> 1)) ^ (l & 7)) * 16) + (g & 1) * 8;
    u16 *o0 = out + (size_t)row0 * DM + cb * 64, *o1 = o0 + 8 * DM;
    u64 km[2] = {__ballot(row0 + srow != (int)selv[0]), __ballot(row0 + 8 + srow != (int)selv[0])};
    asm volatile("" : "+s"(km[0]), "+s"(km[1]));
    asm volatile("" : "+v"(er), "+v"(et), "+v"(ew[0]), "+v"(ew[1]), "+v"(ew[2]), "+v"(ew[3]), "+s"(o0), "+s"(o1));
    __builtin_amdgcn_s_setprio(3);
    waitv<4>();
#pragma unroll
    for (int j = 0; j < 4; ++j) pin(of[0][j]);
    if (__builtin_expect(__ballot(least(of[0]) == 0u) != 0ull, 0)) fix(0);
    sfor<0, 8>([&](auto K) {
        constexpr int k = K;
        unsigned m;
        if constexpr (k == 1) flag(lds0 + L_FWO + w * 4);
        if constexpr (k > 4) __builtin_amdgcn_s_setprio(7 - k);
        if constexpr (2 * (k + 1) == WON) lds_wait(lds0 + ((l & 8) ? L_FWO + sch * 4 : L_FW2 + (l & 1) * 4), 1);
        if constexpr (k == 7) {
            if (__builtin_expect(own, 0)) {
#pragma unroll
                for (int i = 0; i < 14; ++i) ld_async(yp[i], yo, ya(i));
            }
        }
        SB0();
        sfor<0, 16>([&](auto I) {
            constexpr int i = I, j = k == 7 ? (0x2031 >> (i & 12)) & 3 : i >> 2, nb = i & 3;
            const bf16x8 a = cat8(wf[k & 1][(j >> 1) * 4 + nb][0], wf[k & 1][(j >> 1) * 4 + nb][1]);
            if (k == 0 && j == 1) asm volatile("v_mfma_f32_16x16x32_bf16 %0, %1, %2, 0" : "=&v"(acc[j & 1][nb]) : "v"(a), "v"(of[k % 3][j]));
            else asm volatile("v_mfma_f32_16x16x32_bf16 %0, %1, %2, %0" : "+v"(acc[j & 1][nb]) : "v"(a), "v"(of[k % 3][j]));
            SB0();
            if constexpr (k == 7 && i >= 12) {
                if constexpr (i == 12) lds_write<u32x4>(ea, nz4(acc[1][0]));
                else lds_write<f32x4>(ea + (i - 12) * 1024, acc[1][i - 12]);
            }
            if constexpr (k < 7) wrd(k + 1, i);
            if constexpr (k < 6 && i < 8 && !(i & 1)) fetch1(k + 2, i >> 1);
            if constexpr (k < 7 && i == 10) {
                waitv<(k < 6 ? 4 : 0)>();
#pragma unroll
                for (int n = 0; n < 4; ++n) pin(of[(k + 1) % 3][n]);
            }
            if constexpr (k < 7 && i > 10 && i <= 14) {
                const u32x4& v = of[(k + 1) % 3][i - 11];
                if constexpr (i == 11) asm volatile("v_min3_u32 %0, %1, %2, %3\n\tv_min_u32 %0, %0, %4" : "=&v"(m) : "v"(v[0]), "v"(v[1]), "v"(v[2]), "v"(v[3]));
                else asm volatile("v_min3_u32 %0, %0, %1, %2\n\tv_min3_u32 %0, %0, %3, %4" : "+v"(m) : "v"(v[0]), "v"(v[1]), "v"(v[2]), "v"(v[3]));
            }
            SB0();
        });
        if constexpr (k < 7) {
            if (__builtin_expect(__builtin_amdgcn_ballot_w64(m == 0u) != 0ull, 0)) fix(k + 1);
        }
        SB0();
    });
    asm volatile("s_nop 7");
    SB0();
    BAR();
    SB0();
    f32x4 pa[4];
#pragma unroll
    for (int nb = 0; nb < 4; ++nb) pa[nb] = lds_read<f32x4>(er + nb * 1024);
    SB0();
#pragma unroll
    for (int nb = 0; nb < 4; ++nb) acc[0][nb] += pa[nb];
#pragma unroll
    for (int nb = 0; nb < 4; ++nb) asm volatile("" : "+v"(acc[0][nb]));
#pragma unroll
    for (int nb = 0; nb < 4; ++nb) lds_write<u32x2>(ew[nb], (u32x2){pack2(acc[0][nb][0], acc[0][nb][1]), pack2(acc[0][nb][2], acc[0][nb][3])});
    u32x4 av[2];
#pragma unroll
    for (int j = 0; j < 2; ++j) av[j] = lds_read<u32x4>(et + j * 1024);
    SB0();
    asm volatile("s_mov_b64 exec, %5\n\tglobal_store_dwordx4 %0, %1, %3 sc1\n\ts_mov_b64 exec, %6\n\tglobal_store_dwordx4 %0, %2, %4 sc1\n\ts_mov_b64 exec, -1"
                 ::"v"(xo), "v"(av[0]), "v"(av[1]), "s"(o0), "s"(o1), "s"(km[0]), "s"(km[1]) : "memory");
    if (own) {
        const unsigned sr = __builtin_amdgcn_readfirstlane(selv[0]) & 15;
        const unsigned a2 = lds_read<unsigned>(eb + sr * 128 + ((sch ^ (sr & 7)) * 16) + g * 4);
        const float rsel = rbf(1.f / (1.f + __expf(-bf2f((u16)(selv[1] ^ ((selv[1] & 0x8000u) ? 0x8000u : 0xFFFFu))))));
        waitv<2>();
        settle(yp, [&](int) { return yo; }, ya);
        f32x4 yv[2];
#pragma unroll
        for (int h = 0; h < 2; ++h) {
            yv[h] = __builtin_bit_cast(f32x4, yp[h]);
#pragma unroll
            for (int i = 1; i < 7; ++i) yv[h] += __builtin_bit_cast(f32x4, yp[2 * i + h]);
        }
        float y2[2];
#pragma unroll
        for (int i = 0; i < 2; ++i) {
            const float t = dpp_add<0x128>(swap16_add(swap32_add(yv[0][i], yv[1][i]), swap32_add(yv[0][2 + i], yv[1][2 + i])));
            y2[i] = rbf(rbf(t) * rsel) * ys;
        }
        ST_MASK("global_store_dword", 0x00FF00FF00FF00FFull, sr * 2048 + sch * 16 + g * 4, pack2(lo16(a2) + y2[0], hi16(a2) + y2[1]), o0);
    }
    SB0();
}

extern "C" void kernel_launch(void* const* d_in, const int* in_sizes, int n_in, void* d_out, int out_size,
                              void* d_ws, size_t ws_size, hipStream_t stream) {
    fused_mix<<<256, 512, 0, stream>>>((const u16*)d_in[0], (const u16*)d_in[1], (const u16*)d_in[2], (const u16*)d_in[3],
                                       (const u16*)d_in[5], (const u16*)d_in[6], (const u16*)d_in[7], (const u16*)d_in[8],
                                       (const u16*)d_in[9], (char*)d_ws, (u16*)d_out);
}
```
